```python
import torch

def _softmax(x, axis=-1):
    return torch.softmax(x, dim=axis)
def _gelu(x):
    return torch.nn.functional.gelu(x, approximate="tanh")
def _silu(x):
    return torch.nn.functional.silu(x)
def _sigmoid(x):
    return torch.sigmoid(x)
def _layernorm(x, axis=-1, eps=1e-5):
    m = x.mean(dim=axis, keepdim=True)
    v = x.var(dim=axis, keepdim=True, unbiased=False)
    return (x - m) / torch.sqrt(v + eps)
def _scatter_add(tgt, idx, src, axis=0):
    return tgt.clone().index_add_(axis, idx, src)
def _segment_sum(src, seg, n):
    out = torch.zeros((n,) + src.shape[1:], dtype=src.dtype)
    return out.index_add_(0, seg, src)
def _pad(x, pw):
    flat = tuple(v for pair in reversed(pw) for v in pair)
    return torch.nn.functional.pad(x, flat)

kernel_name = 'sequential_preLN__mha_block__glu_gated_reglu__prompt'

def setup_inputs():

    _w = (lambda a: (lambda t: t.to(torch.bfloat16) if t.is_floating_point() else t)(torch.as_tensor(a)))
    def _A_setup():
        B, S, D, H = 1, 2048, 640, 32
        return {
            "x": _w((lambda *s: torch.randn(*s, dtype=torch.bfloat16) * 0.05)(B, S, D)),
            "wqkv": _w((lambda *s: torch.randn(*s, dtype=torch.bfloat16) * 0.05)(D, 3 * D)),
            "wo": _w((lambda *s: torch.randn(*s, dtype=torch.bfloat16) * 0.05)(D, D)),
        }
    _A_in = _A_setup()

    def _B_setup():
        B, S, D, F = 1, 2048, 640, 1728
        return {
            "x": _w((lambda *s: torch.randn(*s, dtype=torch.bfloat16) * 0.05)(B, S, D)),
            "W_gate": _w((lambda *s: torch.randn(*s, dtype=torch.bfloat16) * 0.4)(D, F) * (2.0 / D) ** 0.5),
            "W_up": _w((lambda *s: torch.randn(*s, dtype=torch.bfloat16) * 0.4)(D, F) * (2.0 / D) ** 0.5),
            "W_down": _w((lambda *s: torch.randn(*s, dtype=torch.bfloat16) * 0.4)(F, D) * (1.0 / F) ** 0.5),
        }
    _B_in = _B_setup()

    return {
        "A_x": _A_in["x"],
        "A_wqkv": _A_in["wqkv"],
        "A_wo": _A_in["wo"],
        "B_x": _B_in["x"],
        "B_W_gate": _B_in["W_gate"],
        "B_W_up": _B_in["W_up"],
        "B_W_down": _B_in["W_down"],
    }

def reference(A_x, A_wqkv, A_wo, B_x, B_W_gate, B_W_up, B_W_down):
    def _A_ref(x, wqkv, wo):
        B, S, D = x.shape
        H = 32
        Dh = D // H
        qkv = (lambda x, *d: x.permute(*d))((x @ wqkv).reshape(B, S, 3, H, Dh), 2, 0, 3, 1, 4)
        q, k, v = qkv[0], qkv[1], qkv[2]
        scores = torch.einsum("bhqd,bhkd->bhqk", q, k) * (Dh ** -0.5)
        p = _softmax(scores, axis=-1)
        o = torch.einsum("bhqk,bhkd->bhqd", p, v)
        return (lambda x, *d: x.permute(*d))(o, 0, 2, 1, 3).reshape(B, S, D) @ wo

    def _B_ref(x, W_gate, W_up, W_down):
        gate = torch.einsum("bsd,df->bsf", x, W_gate)
        up = torch.einsum("bsd,df->bsf", x, W_up)
        gate = gate * (lambda x, dt: x.to(dt))(gate > 0, gate.dtype)
        h = gate * up
        return torch.einsum("bsf,fd->bsd", h, W_down)

    x = A_x
    h = x + _A_ref(_layernorm(x, axis=-1), A_wqkv, A_wo)
    h2 = h + _B_ref(_layernorm(h, axis=-1), B_W_gate, B_W_up, B_W_down)
    return h2
```

```cpp
#include <hip/hip_runtime.h>
#include <hip/hip_bf16.h>
#include <stdint.h>

#define DI __device__ __forceinline__
typedef __attribute__((ext_vector_type(2))) __bf16 bf2v;
typedef __attribute__((ext_vector_type(8))) __bf16 bf8v;
typedef __attribute__((ext_vector_type(16))) __bf16 bf16v;
typedef __attribute__((ext_vector_type(4))) short s4v;
typedef __attribute__((ext_vector_type(2))) float f32x2;
typedef __attribute__((ext_vector_type(4))) float f32x4;
typedef __attribute__((ext_vector_type(8))) float f32x8;
typedef __attribute__((ext_vector_type(16))) float f32x16;
typedef __attribute__((ext_vector_type(2))) unsigned u32x2;
typedef __attribute__((ext_vector_type(4))) unsigned u32x4;
typedef __amdgpu_buffer_rsrc_t rsrc_t;
#define LAS __attribute__((address_space(3)))

constexpr unsigned WS_Q = 0;
constexpr unsigned WS_KV = WS_Q + 32 * 32 * 2560;
constexpr unsigned WS_O = WS_KV + 32 * 163840;
constexpr unsigned WS_H = WS_O + 128 * 80 * 256;
constexpr unsigned WS_F = WS_H + 64 * 80 * 512;
constexpr unsigned WS_WO = WS_F + 128 * 216 * 256;
constexpr unsigned WS_WD = WS_WO + 8 * 102400;
constexpr unsigned WS_WGU = WS_WD + 8 * 276480;
constexpr unsigned WS_CS = WS_WGU + 108 * 40960;
constexpr unsigned WS_END = WS_CS + 5 * 3456 * 4;

struct Params {
    const char* x; const char* wqkv; const char* wo; const char* wg; const char* wu; const char* wd;
    char* out; char* ws;
};

#define SBAR() __builtin_amdgcn_sched_barrier(0)
#define PIN(x) asm volatile("" : "+v"(x))
#define WAITBAR(N) asm volatile("s_waitcnt vmcnt(" #N ") lgkmcnt(0)\n\ts_barrier" ::: "memory")
#define LBAR() asm volatile("s_waitcnt lgkmcnt(0)\n\ts_barrier" ::: "memory")

DI rsrc_t mkrs(const char* p, unsigned bytes) { return __builtin_amdgcn_make_buffer_rsrc((void*)p, (short)0, (int)bytes, 0x00020000); }
template <class T> DI T ldsr(unsigned a) { return *(const LAS T*)(uintptr_t)a; }
template <class T> DI void ldsw(unsigned a, T v) { *(LAS T*)(uintptr_t)a = v; }
DI s4v ldstr(unsigned a) { return __builtin_amdgcn_ds_read_tr16_b64_v4i16((LAS s4v*)(uintptr_t)a); }
DI bf8v ldstr2(unsigned a, unsigned d) {
    s4v lo = ldstr(a), hi = ldstr(a + d);
    return __builtin_bit_cast(bf8v, __builtin_shufflevector(lo, hi, 0, 1, 2, 3, 4, 5, 6, 7));
}
DI f32x16 mfma32(bf8v a, bf8v b, f32x16 c) { return __builtin_amdgcn_mfma_f32_32x32x16_bf16(a, b, c, 0, 0, 0); }
DI f32x4 mfma16(bf8v a, bf8v b, f32x4 c) { return __builtin_amdgcn_mfma_f32_16x16x32_bf16(a, b, c, 0, 0, 0); }
DI unsigned pk2(float lo, float hi) { f32x2 v = {lo, hi}; return __builtin_bit_cast(unsigned, __builtin_convertvector(v, bf2v)); }
DI float bflo(unsigned u) { return __uint_as_float(u << 16); }
DI float bfhi(unsigned u) { return __uint_as_float(u & 0xffff0000u); }
DI float dot2(unsigned a, unsigned b, float c) { return __builtin_amdgcn_fdot2_f32_bf16(__builtin_bit_cast(bf2v, a), __builtin_bit_cast(bf2v, b), c, false); }
DI float sum8(bf8v f, float c) { u32x4 u = __builtin_bit_cast(u32x4, f);
#pragma unroll
    for (int i = 0; i < 4; ++i) c = dot2(u[i], 0x3f803f80u, c);
    return c; }
DI float sq8(bf8v f, float c) { u32x4 u = __builtin_bit_cast(u32x4, f);
#pragma unroll
    for (int i = 0; i < 4; ++i) c = dot2(u[i], u[i], c);
    return c; }
DI f32x2 swap32(float a, float b) { auto r = __builtin_amdgcn_permlane32_swap(__float_as_uint(a), __float_as_uint(b), false, false); return f32x2{__uint_as_float(r[0]), __uint_as_float(r[1])}; }
DI void st16(rsrc_t r, unsigned voff, unsigned soff, u32x4 v) { asm volatile("buffer_store_dwordx4 %0, %1, %2, %3 offen\n\ts_nop 1" :: "v"(v), "v"(voff), "s"(r), "s"(soff)); }
DI void st8(rsrc_t r, unsigned voff, unsigned soff, u32x2 v) { __builtin_amdgcn_raw_buffer_store_b64(v, r, voff, soff, 0); }
DI u32x4 ld16(rsrc_t r, unsigned voff, unsigned soff) { return __builtin_amdgcn_raw_buffer_load_b128(r, voff, soff, 0); }
DI u32x2 ld8(rsrc_t r, unsigned voff, unsigned soff) { return __builtin_amdgcn_raw_buffer_load_b64(r, voff, soff, 0); }
DI void dma1(unsigned m0v, unsigned voff, rsrc_t r, unsigned soff) {
    asm volatile("s_mov_b32 m0, %0\n\ts_nop 0\n\tbuffer_load_dwordx4 %1, %2, %3 offen lds" :: "s"(m0v), "v"(voff), "s"(r), "s"(soff) : "memory"); }
DI void dma4(unsigned m0v, unsigned voff, rsrc_t r, unsigned soff) {
    asm volatile("s_mov_b32 m0, %0\n\ts_nop 0\n\tbuffer_load_dwordx4 %1, %2, %3 offen lds\n\tbuffer_load_dwordx4 %1, %2, %3 offen offset:1024 lds\n\t"
                 "buffer_load_dwordx4 %1, %2, %3 offen offset:2048 lds\n\tbuffer_load_dwordx4 %1, %2, %3 offen offset:3072 lds" :: "s"(m0v), "v"(voff), "s"(r), "s"(soff) : "memory"); }
DI unsigned usel(bool c, unsigned a, unsigned b) { return c ? a : b; }

template <bool ISV>
DI void qkv_body(const Params& p, int mt, int nt, int w, int lane) {
    const int t = lane & 31, hh = lane >> 5, T = w & 1, cb = w >> 1;
    const rsrc_t rx = mkrs(p.x, 2048 * 1280), rw = mkrs(p.wqkv - 3072, 640 * 3840 + 3072), rws = mkrs(p.ws, WS_END);
    unsigned xo[2], wo[2];
#pragma unroll
    for (int j = 0; j < 2; ++j) {
        const int rho = 16 * w + 8 * j + (lane >> 3), r = rho & 63, bb = r >> 5, tt = r & 31;
        const int tok = (rho & 64) + (ISV ? 32 * bb + (tt & 16) + ((tt & 4) << 1) + ((tt & 8) >> 1) + (tt & 3) : 2 * tt + bb);
        xo[j] = (mt * 128 + tok) * 1280 + (((lane & 7) ^ ((rho >> 1) & 7)) << 4) - 1024 * j;
        wo[j] = (8 * w + 4 * j + ((lane >> 2) & 3)) * 3840 + nt * 256 + (lane >> 4) * 64 + (lane & 3) * 16 + 1024 - 1024 * j;
    }
#define QKV_DMA(st, slot) asm volatile("s_mov_b32 m0, %0\n\ts_nop 0\n\tbuffer_load_dwordx4 %1, %5, %7 offen lds\n\tbuffer_load_dwordx4 %2, %5, %7 offen offset:1024 lds\n\t" \
        "buffer_load_dwordx4 %3, %6, %8 offen offset:2048 lds\n\tbuffer_load_dwordx4 %4, %6, %8 offen offset:3072 lds" \
        :: "s"((slot) * 32768 + w * 4096), "v"(xo[0]), "v"(xo[1]), "v"(wo[0]), "v"(wo[1]), "s"(rx), "s"(rw), "s"((st) * 128), "s"((st) * 245760) : "memory")
#pragma unroll 1
    for (int s = 0; s < 5; ++s) QKV_DMA(s, s);
    unsigned xa[4], wa[4];
    {
        const int rot = cb ^ (2 * T), rho = 64 * T + t;
        const unsigned x0 = (rho >> 4) * 4096 + (rho & 15) * 128 + ((hh ^ ((t >> 1) & 7)) << 4);
        const unsigned w0 = hh * 4096 + 2048 + cb * 256 + ((lane >> 2) & 3) * 64 + ((lane >> 4) & 1) * 32 + (lane & 3) * 8;
#pragma unroll
        for (int j = 0; j < 4; ++j) { xa[j] = x0 ^ ((j ^ rot) << 5); wa[j] = w0 + ((j ^ rot) << 13); }
    }
    f32x16 acc0 = {}, acc1 = {};
    float sx0 = 0.f, sq0 = 0.f, sx1 = 0.f, sq1 = 0.f, cs = 0.f;
    bf8v xA0, xA1, wA, xB0, xB1, wB;
#define QKV_RD(X0, X1, W, j, sl) do { W = ldstr2(wa[j] + (sl), 1024); X0 = ldsr<bf8v>(xa[j] + (sl)); X1 = ldsr<bf8v>(xa[j] + (sl) + 8192); } while (0)
#define QKV_MM(X0, X1, W) do { acc0 = ISV ? mfma32(X0, W, acc0) : mfma32(W, X0, acc0); acc1 = ISV ? mfma32(X1, W, acc1) : mfma32(W, X1, acc1); } while (0)
    WAITBAR(16);
    QKV_RD(xA0, xA1, wA, 0, 0);
    unsigned sl = 0; int slot = 0;
#pragma unroll 1
    for (int s = 0; s < 10; ++s) {
        SBAR();
        QKV_RD(xB0, xB1, wB, 1, sl); SBAR();
        QKV_MM(xA0, xA1, wA); sx0 = sum8(xA0, sx0); sq0 = sq8(xA0, sq0); PIN(sx0); PIN(sq0); SBAR();
        QKV_RD(xA0, xA1, wA, 2, sl); SBAR();
        QKV_MM(xB0, xB1, wB); sx1 = sum8(xB1, sx1); sq1 = sq8(xB1, sq1); PIN(sx1); PIN(sq1); SBAR();
        QKV_RD(xB0, xB1, wB, 3, sl); SBAR();
        QKV_MM(xA0, xA1, wA); cs = sum8(wA, cs); PIN(cs); SBAR();
        if (s <= 5) WAITBAR(12); else if (s == 6) WAITBAR(8); else if (s == 7) WAITBAR(4); else WAITBAR(0);
        const int ps = slot;
        slot = slot == 4 ? 0 : slot + 1; sl = slot * 32768;
        QKV_RD(xA0, xA1, wA, 0, sl); SBAR();
        if (s < 5) QKV_DMA(s + 5, ps);
        SBAR();
        QKV_MM(xB0, xB1, wB); cs = sum8(wB, cs); PIN(cs);
    }
    constexpr unsigned L_ST = 32768, L_CS = L_ST + 4096, L_PR = L_CS + 1024;
    {
        const f32x2 a = swap32(sx0, sx1), b = swap32(sq0, sq1), c = swap32(cs, cs);
        ldsw<f32x2>(L_ST + (64 * T + lane) * 32 + cb * 8, f32x2{a[0] + a[1], b[0] + b[1]});
        ldsw<float>(L_CS + T * 512 + (cb * 32 + t) * 4, c[0] + c[1]);
    }
    LBAR();
    float ra, rb;
    {
        const f32x4 u = ldsr<f32x4>(L_ST + (64 * T + lane) * 32), v = ldsr<f32x4>(L_ST + (64 * T + lane) * 32 + 16);
        const float mean = ((u[0] + u[2]) + (v[0] + v[2])) * (1.f / 640.f);
        const float var = __builtin_fmaxf(((u[1] + u[3]) + (v[1] + v[3])) * (1.f / 640.f) - mean * mean, 0.f);
        ra = __builtin_amdgcn_rsqf(var + 1e-5f);
        if (nt < 5) ra *= 0.22360679774997896f * 1.4426950408889634f;
        rb = -mean * ra;
    }
    const int g64 = 2 * mt + T;
    if (!ISV) {
        const f32x2 a = swap32(ra, ra), b = swap32(rb, rb);
        const bool isq = nt < 5;
        const unsigned hs = isq ? 81920 : 163840;
        const unsigned sbase = isq ? WS_Q + g64 * 2560 : WS_KV + g64 * 5120;
        const int G0 = (isq ? nt : nt - 5) * 32 + cb * 8 + hh;
#pragma unroll
        for (int q = 0; q < 4; ++q) {
            const unsigned ca = L_CS + (cb * 32 + 8 * q + 4 * hh) * 4;
            const f32x4 c = ldsr<f32x4>(ca) + ldsr<f32x4>(ca + 512);
            float o0[4], o1[4];
#pragma unroll
            for (int i = 0; i < 4; ++i) { o0[i] = acc0[4 * q + i] * a[0] + b[0] * c[i]; o1[i] = acc1[4 * q + i] * a[1] + b[1] * c[i]; }
            const unsigned G = G0 + 2 * q, head = (G * 205) >> 10, dq = G - 5 * head;
            st16(rws, head * hs + dq * 512 + t * 16, sbase, u32x4{pk2(o0[0], o0[1]), pk2(o0[2], o0[3]), pk2(o1[0], o1[1]), pk2(o1[2], o1[3])});
        }
    } else {
        const unsigned pr = L_PR + w * 512;
        ldsw<float>(pr + lane * 4, ra); ldsw<float>(pr + 256 + lane * 4, rb);
        const float c = ldsr<float>(L_CS + (cb * 32 + t) * 4) + ldsr<float>(L_CS + 512 + (cb * 32 + t) * 4);
        const unsigned col = (nt - 10) * 128 + cb * 32 + t, head = (col * 3277) >> 16, d = col - 20 * head;
        const unsigned vo = head * 163840 + hh * 320 + d * 16;
#pragma unroll
        for (int bb = 0; bb < 2; ++bb)
#pragma unroll
            for (int k2 = 0; k2 < 2; ++k2) {
                float o[8];
#pragma unroll
                for (int q = 0; q < 2; ++q) {
                    const unsigned aa = pr + (32 * bb + 16 * k2 + 8 * q + 4 * hh) * 4;
                    const f32x4 a = ldsr<f32x4>(aa), b = ldsr<f32x4>(aa + 256);
#pragma unroll
                    for (int i = 0; i < 4; ++i) o[4 * q + i] = (bb ? acc1 : acc0)[8 * k2 + 4 * q + i] * a[i] + b[i] * c;
                }
                st16(rws, vo + (2 * bb + k2) * 640, WS_KV + g64 * 5120 + 2560, u32x4{pk2(o[0], o[1]), pk2(o[2], o[3]), pk2(o[4], o[5]), pk2(o[6], o[7])});
            }
    }
}
DI void phase_qkv(const Params& p, int b, int w, int lane) {
    const int xcd = b & 7, i = b >> 3, mt = (xcd >> 1) * 4 + (i & 3), nt = (xcd & 1) * 8 + (i >> 2);
    if (nt >= 15) return;
    if (nt >= 10) qkv_body<true>(p, mt, nt, w, lane); else qkv_body<false>(p, mt, nt, w, lane);
}

DI f32x16 qk_tile(u32x4 km, u32x2 kt, u32x4 qm, u32x2 qt, f32x16 c) {
    c = mfma32(__builtin_bit_cast(bf8v, km), __builtin_bit_cast(bf8v, qm), c);
    return __builtin_amdgcn_mfma_f32_32x32x8bf16_1k(__builtin_bit_cast(s4v, kt), __builtin_bit_cast(s4v, qt), c, 0, 0, 0);
}
DI float max16(const f32x16& s, float m) {
#pragma unroll
    for (int r = 0; r < 16; r += 2) m = __builtin_fmaxf(__builtin_fmaxf(s[r], s[r + 1]), m);
    return m;
}
DI f32x16 pv16(u32x4 v, f32x8 pr, f32x16 o) {
    return __builtin_amdgcn_smfmac_f32_32x32x32_bf16(__builtin_bit_cast(bf8v, v), __builtin_bit_cast(bf16v, pr), o, 0xDDDD, 0, 0);
}

#define EXP4(P, S, i) "v_exp_f32 v[" #P "+" #i "], v[" #S "+" #i "]\n\tv_exp_f32 v[" #P "+" #i "+1], v[" #S "+" #i "+1]\n\t" \
                      "v_exp_f32 v[" #P "+" #i "+2], v[" #S "+" #i "+2]\n\tv_exp_f32 v[" #P "+" #i "+3], v[" #S "+" #i "+3]\n\t"
#define ATILE(SC, SN, KA, KO, PRE, VH, MID, VL) \
    "s_waitcnt lgkmcnt(2)\n\t" \
    "v_mfma_f32_32x32x16_bf16 v[" #SN ":" #SN "+15], v[152:155], v[144:147], v[64:79]\n\t" PRE \
    "ds_read2st64_b64 v[152:155], " KA " offset0:" #KO " offset1:" #KO "+1\n\t" \
    EXP4(112, SC, 0) \
    "v_mfma_f32_32x32x8_bf16 v[" #SN ":" #SN "+15], v[150:151], v[148:149], v[" #SN ":" #SN "+15]\n\t" \
    "ds_read_b64 v[150:151], " KA " offset:" #KO "*512+2048\n\t" \
    EXP4(112, SC, 4) \
    "s_waitcnt lgkmcnt(2)\n\t" \
    "v_smfmac_f32_32x32x32_bf16 v[128:143], v[160:163], v[120:127], %[idx]\n\t" \
    EXP4(112, SC, 8) \
    "v_smfmac_f32_32x32x32_bf16 v[128:143], v[156:159], v[112:119], %[idx]\n\t" \
    "s_mov_b64 exec, %[msk]\n\t" \
    "ds_read_b128 v[160:163], %[av] offset:" #VH "\n\t" MID \
    "ds_read_b128 v[156:159], %[av] offset:" #VL "\n\t" \
    "s_mov_b64 exec, -1\n\t" \
    EXP4(112, SC, 12)

DI void phase_attn(const Params& p, int b, int w, int lane) {
    const int t = lane & 31, hh = lane >> 5;
    const int head = (b & 7) * 4 + (b >> 6), qb = (b >> 3) & 7;
    const rsrc_t rws = mkrs(p.ws, WS_END);
    const unsigned kvb = WS_KV + head * 163840, l16 = lane * 16;
    constexpr unsigned L_RT = 122880, L_CP = L_RT + 32768;
    u32x2 qa, qc, qe;
    {
        const int tok = qb * 256 + w * 32;
        const unsigned qs = WS_Q + head * 81920 + (tok >> 6) * 2560 + (tok & 32) * 8;
        asm volatile("buffer_load_dwordx2 %0, %3, %5, %6 offen\n\tbuffer_load_dwordx2 %1, %3, %5, %6 offen offset:512\n\tbuffer_load_dwordx2 %2, %4, %5, %6 offen offset:2048"
                     : "=&v"(qa), "=&v"(qc), "=&v"(qe) : "v"(hh * 1024 + t * 8), "v"(t * 8), "s"(rws), "s"(qs) : "memory");
    }
#pragma unroll 1
    for (int j = 0; j < 10; ++j) dma1((j * 8 + w) * 1024, l16, rws, kvb + (j * 8 + w) * 1024);
    const bool gu = b < 135;
    const int v = (b - 135) * 8 + w;
    const bool isWo = v < 200, have = gu || v < 740;
    unsigned ra[4], so[4];
    {
        const int e = lane >> 3, pc = (lane & 7) * 16, q = (lane >> 2) & 3, pp = lane & 3, gi = lane >> 4;
        const unsigned lb = L_RT + w * 4096;
        if (gu) {
            const int fg = b / 5, ks = (b - fg * 5) * 8 + w;
            const rsrc_t rg = mkrs(p.wg, 640 * 3456), ru = mkrs(p.wu, 640 * 3456);
            const unsigned vo = e * 3456 + pc, sb = ks * 16 * 3456 + fg * 128;
            dma1(lb, vo, rg, sb); dma1(lb + 1024, vo, rg, sb + 8 * 3456); dma1(lb + 2048, vo, ru, sb); dma1(lb + 3072, vo, ru, sb + 8 * 3456);
#pragma unroll
            for (int f = 0; f < 4; ++f) {
                ra[f] = lb + (gi & 1) * 2048 + (8 * (gi >> 1) + q) * 128 + f * 32 + (pp & 1) * 16 + (pp >> 1) * 8;
                so[f] = WS_WGU + (fg * 4 + f) * 40960 + ks * 1024;
            }
        } else {
            const int vv = have ? (isWo ? v : v - 200) : 0, ks = vv / 10, cu = vv - ks * 10, NK = isWo ? 10 : 27;
            const rsrc_t rs = mkrs(isWo ? p.wo : p.wd, isWo ? 640 * 1280 : 1728 * 1280);
            const bool tail = ks >= 16;
            const int ke = !isWo ? e : (tail ? 20 : 8) * (e >> 2) + (e & 3);
#pragma unroll
            for (int j = 0; j < 4; ++j) {
                const int c = 4 * ks + j;
                const int k0 = !isWo ? 8 * c : tail ? 40 * (c - 64) + 16 : 20 * (c >> 1) + 4 * (c & 1);
                dma1(lb + j * 1024, ke * 1280 + pc, rs, k0 * 1280 + cu * 128);
            }
            const int half = ks >= NK, kk = ks - half * NK;
#pragma unroll
            for (int f = 0; f < 4; ++f) {
                const int seg = 2 * cu + (f >> 1), a = seg / 5, sm = seg - 5 * a, fi = f & 1;
                const int tile = 2 * a + (sm == 2 ? fi : sm > 2), blk = sm == 2 ? 4 : (sm == 1 || sm == 4) ? 2 + fi : fi;
                ra[f] = lb + (8 * gi + q) * 128 + (f >> 1) * 64 + (sm == 2 ? 4 * fi + pp : 2 * pp + fi) * 8;
                so[f] = (isWo ? WS_WO : WS_WD) + (tile * NK * 10 + kk * 10 + half * 5 + blk) * 1024;
            }
        }
    }
#pragma unroll 1
    for (int j = 10; j < 15; ++j) dma1((j * 8 + w) * 1024, l16, rws, kvb + (j * 8 + w) * 1024);
    unsigned a0 = hh * 1024 + t * 8, a1 = a0 + 256, av = 2560 + hh * 320 + (t < 20 ? t : 0) * 16;
    const unsigned ones = t < 20 ? 0u : 0x3f803f80u;
    asm volatile("s_waitcnt vmcnt(17)\n\ts_barrier" : "+v"(qa), "+v"(qc), "+v"(qe) :: "memory");
    const u32x4 qm = {qa[0], qa[1], qc[0], qc[1]}; const u32x2 qt = {hh ? 0u : qe[0], hh ? 0u : qe[1]};
    f32x16 S0, S1 = {}, P = {}, O = {}, NM;
    u32x4 km, vlo, vhi = {ones, ones, ones, ones}; u32x2 kt;
    {
        const u32x2 k0 = ldsr<u32x2>(a0), k1 = ldsr<u32x2>(a0 + 512), k2 = ldsr<u32x2>(a0 + 2048);
        S0 = qk_tile(u32x4{k0[0], k0[1], k1[0], k1[1]}, k2, qm, qt, f32x16{});
        const u32x2 n0 = ldsr<u32x2>(a1), n1 = ldsr<u32x2>(a1 + 512);
        km = u32x4{n0[0], n0[1], n1[0], n1[1]}; kt = ldsr<u32x2>(a1 + 2048);
        const u32x4 v0 = ldsr<u32x4>(av);
#pragma unroll
        for (int i = 0; i < 4; ++i) vlo[i] = t < 20 ? v0[i] : ones;
        float m = max16(S0, S0[0]);
        const f32x2 mm = swap32(m, m);
        m = __builtin_fmaxf(mm[0], mm[1]);
#pragma unroll
        for (int r = 0; r < 16; ++r) { S0[r] -= m; NM[r] = -m; }
    }
    const unsigned idx = 0xDDDD;
    const unsigned long long msk = 0x000FFFFF000FFFFFull;
#pragma unroll 1
    for (int seg = 0; seg < 5; ++seg) {
        if (seg == 1) WAITBAR(14);
        else if (seg == 2) {
            WAITBAR(5);
            float cp[4];
#pragma unroll
            for (int f = 0; f < 4; ++f) {
                const bf8v fr = ldstr2(ra[f], 512);
                if (have) st16(rws, l16, so[f], __builtin_bit_cast(u32x4, fr));
                cp[f] = sum8(fr, 0.f);
            }
            const f32x2 c0 = swap32(cp[0], cp[1]), c1 = swap32(cp[2], cp[3]);
            ldsw<float>(L_CP + w * 512 + lane * 4, c0[0] + c0[1]); ldsw<float>(L_CP + w * 512 + 256 + lane * 4, c1[0] + c1[1]);
        } else if (seg == 3) {
            WAITBAR(0);
#pragma unroll 1
            for (int j = 0; j < 5; ++j) dma1((j * 8 + w) * 1024, l16, rws, kvb + 122880 + (j * 8 + w) * 1024);
            if (gu && w < 2) {
                float c = 0.f;
#pragma unroll
                for (int i = 0; i < 8; ++i) c += ldsr<float>(L_CP + i * 512 + w * 256 + lane * 4);
                const int fg = b / 5;
                __builtin_amdgcn_raw_buffer_store_b32(__float_as_uint(c), rws, lane * 4, WS_CS + ((b - fg * 5) * 3456 + fg * 128 + w * 64) * 4, 0);
            }
        } else if (seg == 4) WAITBAR(0);
        int cnt = seg == 0 ? 1 : seg == 1 ? 2 : seg == 4 ? 5 : 4;
        int inc = seg == 4 ? -112640 : 10240;
        asm volatile(
            "s_waitcnt lgkmcnt(0)\n\ts_nop 1\n"
            "1:\n\t"
            ATILE(80, 96, "%[a0]", 10, "", 640, "", 1280)
            ATILE(96, 80, "%[a1]", 10, "", 1920, "", 5120)
            ATILE(80, 96, "%[a0]", 0, "v_add_u32 %[a0], %[inc], %[a0]\n\t", 5760, "", 6400)
            ATILE(96, 80, "%[a1]", 0, "v_add_u32 %[a1], %[inc], %[a1]\n\t", 7040, "v_add_u32 %[av], %[inc], %[av]\n\t", 0)
            "s_movk_i32 %[inc], 10240\n\t"
            "s_add_i32 %[cnt], %[cnt], -1\n\t"
            "s_cmp_lg_u32 %[cnt], 0\n\t"
            "s_cbranch_scc1 1b\n\t"
            "s_waitcnt lgkmcnt(0)\n\ts_nop 7"
            : "+{v[80:95]}"(S0), "+{v[96:111]}"(S1), "+{v[112:127]}"(P), "+{v[128:143]}"(O), "+{v[150:151]}"(kt), "+{v[152:155]}"(km),
              "+{v[156:159]}"(vlo), "+{v[160:163]}"(vhi), [a0] "+v"(a0), [a1] "+v"(a1), [av] "+v"(av), [cnt] "+s"(cnt), [inc] "+s"(inc)
            : "{v[64:79]}"(NM), "{v[144:147]}"(qm), "{v[148:149]}"(qt), [idx] "v"(idx), [msk] "s"(msk)
            : "memory", "scc");
    }
    O = pv16(vhi, __builtin_shufflevector(P, P, 8, 9, 10, 11, 12, 13, 14, 15), O);
    {
        float mx = 0.f;
#pragma unroll
        for (int r = 0; r < 12; ++r) mx += __builtin_fabsf(O[r]);
        const bool bad = !(O[12] < 1.8446744e19f) || !(mx < __builtin_inff());
        if (__builtin_expect(__any(bad), 0)) {
            float m = -__builtin_inff();
            const unsigned ko = hh * 1024 + t * 8, vo = 2560 + hh * 320 + (t < 20 ? t : 0) * 16;
            f32x16 c = {};
#pragma unroll 1
            for (int it = 0; it < 128; ++it) {
                const unsigned sb = kvb + ((it & 63) >> 1) * 5120;
                const u32x2 k0 = ld8(rws, ko, sb + (it & 1) * 256), k1 = ld8(rws, ko + 512, sb + (it & 1) * 256), k2 = ld8(rws, t * 8 + 2048, sb + (it & 1) * 256);
                f32x16 s = qk_tile(u32x4{k0[0], k0[1], k1[0], k1[1]}, k2, qm, qt, c);
                if (it < 64) {
                    m = max16(s, m);
                    if (it == 63) {
                        const f32x2 mm = swap32(m, m);
                        m = __builtin_fmaxf(mm[0], mm[1]);
#pragma unroll
                        for (int r = 0; r < 16; ++r) { c[r] = -m; O[r] = 0.f; }
                    }
                } else {
                    u32x4 v0 = ld16(rws, vo, sb + (it & 1) * 1280), v1 = ld16(rws, vo + 640, sb + (it & 1) * 1280);
#pragma unroll
                    for (int i = 0; i < 4; ++i) { v0[i] = t < 20 ? v0[i] : ones; v1[i] = t < 20 ? v1[i] : ones; }
#pragma unroll
                    for (int r = 0; r < 16; ++r) s[r] = __builtin_amdgcn_exp2f(s[r]);
                    O = pv16(v0, __builtin_shufflevector(s, s, 0, 1, 2, 3, 4, 5, 6, 7), O);
                    O = pv16(v1, __builtin_shufflevector(s, s, 8, 9, 10, 11, 12, 13, 14, 15), O);
                }
            }
        }
    }
    const float inv = __builtin_amdgcn_rcpf(O[12]);
    const unsigned ob = WS_O + (qb * 16 + w * 2) * 20480, ov = (t >> 4) * 20480 + (t & 15) * 16;
    st16(rws, ov + hh * 256, ob + head * 512, u32x4{pk2(O[0] * inv, O[1] * inv), pk2(O[2] * inv, O[3] * inv), pk2(O[4] * inv, O[5] * inv), pk2(O[6] * inv, O[7] * inv)});
    if (hh == 0) st8(rws, ov, ob + (64 + (head >> 1)) * 256 + (head & 1) * 8, u32x2{pk2(O[8] * inv, O[9] * inv), pk2(O[10] * inv, O[11] * inv)});
}

template <bool DOWN>
DI void phase_g80(const Params& p, int b, int w, int lane) {
    constexpr int NK = DOWN ? 27 : 10;
    const int xcd = b & 7, i = b >> 3, mt = (xcd >> 1) * 8 + (i & 7), nt = (xcd & 1) * 4 + (i >> 3);
    const int tg = w & 3, kh = w >> 2, t = lane & 15, g = lane >> 4;
    const rsrc_t rws = mkrs(p.ws, WS_END);
    const unsigned l16 = lane * 16;
    const unsigned wb = (DOWN ? WS_WD : WS_WO) + nt * NK * 10240 + w * 5120;
    const unsigned ab = (DOWN ? WS_F : WS_O) + (mt * 4 + tg) * (NK * 2048) + kh * NK * 1024;
    constexpr unsigned L_EX = 122880, L_XT = L_EX + 20480;
    u32x4 B0[4], B1[4], B2[4];
#define G80_ISSUE(slot, B, WS_, AS_) do { dma4((slot) * 40960 + w * 5120, l16, rws, WS_); dma1((slot) * 40960 + w * 5120 + 4096, l16, rws, (WS_) + 4096); \
        asm volatile("buffer_load_dwordx4 %0, %4, %5, %6 offen\n\tbuffer_load_dwordx4 %1, %4, %5, %6 offen offset:1024\n\t" \
                     "buffer_load_dwordx4 %2, %4, %5, %6 offen offset:2048\n\tbuffer_load_dwordx4 %3, %4, %5, %6 offen offset:3072" \
                     : "=&v"(B[0]), "=&v"(B[1]), "=&v"(B[2]), "=&v"(B[3]) : "v"(l16), "s"(rws), "s"(AS_) : "memory"); } while (0)
    const unsigned pairoff = (nt & 1) * 32 + kh * 64, b4off = (nt & 1) ? 0 : 128;
    const int tok = mt * 64 + tg * 16;
    const unsigned hs = WS_H + (tok >> 5) * 40960 + nt * 5120 + (tok & 16) * 16;
    const unsigned hv0 = (pairoff / 16 + g) * 512 + t * 16, hv1 = (b4off / 16 + (g >> 1)) * 512 + t * 16 + (g & 1) * 8;
    u32x4 r0; u32x2 r1;
    G80_ISSUE(0, B0, wb, ab);
    G80_ISSUE(1, B1, wb + 40960, ab + 4096);
    if (DOWN) {
        asm volatile("buffer_load_dwordx4 %0, %2, %4, %5 offen\n\tbuffer_load_dwordx2 %1, %3, %4, %5 offen" : "=&v"(r0), "=&v"(r1) : "v"(hv0), "v"(hv1), "s"(rws), "s"(hs) : "memory");
    } else {
        const rsrc_t rx = mkrs(p.x, 2048 * 1280);
#pragma unroll
        for (int j = 0; j < 2; ++j) {
            const int pj = j == 0 ? w : 8 + (w & 1);
            const unsigned e = pj * 64 + lane, row = (e * 6554) >> 16;
            dma1(L_XT + pj * 1024, row * 1280 + (e - row * 10) * 16, rx, mt * 64 * 1280 + nt * 160);
        }
    }
    G80_ISSUE(2, B2, wb + 81920, ab + 8192);
    f32x4 acc[5] = {};
    unsigned la0 = kh * 5120 + l16, la1 = la0 + 40960, la2 = la0 + 81920;
    PIN(la0); PIN(la1); PIN(la2);
    bf8v wA[5], wB[5];
    int rem = NK;
    asm volatile("" : "+s"(rem));
    unsigned wn = wb + 3 * 40960, an = ab + 3 * 4096;
#define G80_RD(W, la, kk) do { _Pragma("unroll") for (int n = 0; n < 5; ++n) W[n] = ldsr<bf8v>(la + (kk) * 10240 + n * 1024); } while (0)
#define G80_MM(W, Bk) do { _Pragma("unroll") for (int n = 0; n < 5; ++n) acc[n] = mfma16(W[n], __builtin_bit_cast(bf8v, Bk), acc[n]); } while (0)
#define G80_STAGE(slot, la, B, lan, BN) \
        SBAR(); G80_RD(wB, la, 1); SBAR(); G80_MM(wA, B[0]); SBAR(); \
        G80_RD(wA, la, 2); SBAR(); G80_MM(wB, B[1]); SBAR(); \
        if (!DOWN && rem == 2) break; \
        G80_RD(wB, la, 3); SBAR(); G80_MM(wA, B[2]); SBAR(); \
        if (DOWN && rem == 3) break; \
        if (rem > 8) asm volatile("s_waitcnt vmcnt(9)" ::: "memory"); else asm volatile("s_waitcnt vmcnt(0)" ::: "memory"); \
        asm volatile("s_waitcnt lgkmcnt(0)\n\ts_barrier" : "+v"(BN[0]), "+v"(BN[1]), "+v"(BN[2]), "+v"(BN[3]) :: "memory"); \
        G80_RD(wA, lan, 0); SBAR(); G80_MM(wB, B[3]); SBAR(); \
        if (DOWN && rem > 12) { G80_ISSUE(slot, B, wn, an); wn += 40960; an += 4096; } \
        rem -= 4;
    asm volatile("s_waitcnt vmcnt(20)\n\ts_barrier" : "+v"(B0[0]), "+v"(B0[1]), "+v"(B0[2]), "+v"(B0[3]) :: "memory");
    G80_RD(wA, la0, 0);
#pragma unroll 1
    for (;;) {
        G80_STAGE(0, la0, B0, la1, B1)
        G80_STAGE(1, la1, B1, la2, B2)
        G80_STAGE(2, la2, B2, la0, B0)
    }
    if (DOWN) asm volatile("" : "+v"(r0), "+v"(r1));
    const unsigned ex = L_EX + tg * 5120 + l16;
    if (kh == 0) { ldsw<f32x4>(ex + 2048, acc[2]); ldsw<f32x4>(ex + 3072, acc[3]); }
    else { ldsw<f32x4>(ex, acc[0]); ldsw<f32x4>(ex + 1024, acc[1]); ldsw<f32x4>(ex + 4096, acc[4]); acc[0] = acc[2]; acc[1] = acc[3]; }
    LBAR();
    const unsigned xr = L_XT + (tg * 16 + t) * 160;
    acc[0] += ldsr<f32x4>(ex + kh * 2048); acc[1] += ldsr<f32x4>(ex + kh * 2048 + 1024); acc[4] += ldsr<f32x4>(ex + 4096);
    if (!DOWN) { r0 = ldsr<u32x4>(xr + pairoff + g * 16); r1 = ldsr<u32x2>(xr + b4off + g * 8); }
    const u32x4 o0 = {pk2(acc[0][0] + bflo(r0[0]), acc[0][1] + bfhi(r0[0])), pk2(acc[0][2] + bflo(r0[1]), acc[0][3] + bfhi(r0[1])),
                      pk2(acc[1][0] + bflo(r0[2]), acc[1][1] + bfhi(r0[2])), pk2(acc[1][2] + bflo(r0[3]), acc[1][3] + bfhi(r0[3]))};
    const u32x2 o1 = {pk2(acc[4][0] + bflo(r1[0]), acc[4][1] + bfhi(r1[0])), pk2(acc[4][2] + bflo(r1[1]), acc[4][3] + bfhi(r1[1]))};
    if (!DOWN) {
        st16(rws, hv0, hs, o0);
        if (kh == 0) st8(rws, hv1, hs, o1);
    } else {
        ldsw<u32x4>(xr + pairoff + g * 16, o0);
        if (kh == 0) ldsw<u32x2>(xr + b4off + g * 8, o1);
        LBAR();
        const rsrc_t ro = mkrs(p.out, 2048 * 1280);
#pragma unroll
        for (int j = 0; j < 2; ++j) {
            if (j == 1 && w >= 2) break;
            const unsigned e = (j * 8 + w) * 64 + lane, row = (e * 6554) >> 16;
            st16(ro, row * 1280 + (e - row * 10) * 16, mt * 64 * 1280 + nt * 160, ldsr<u32x4>(L_XT + e * 16));
        }
    }
}

template <int NB>
DI void gu_body(const Params& p, int mt, int b0, int w, int lane) {
    const int tb = w & 3, t = lane & 31, hh = lane >> 5;
    const rsrc_t rws = mkrs(p.ws, WS_END);
    const unsigned l16 = lane * 16;
    constexpr unsigned SLOT = 45056, L_CS = 3 * SLOT;
    const unsigned hb = WS_H + (mt * 4 + tb) * 40960, wb = WS_WGU + b0 * 40960 + tb * 1024;
#define GU_ISSUE(st, sl) do { if (NB == 4) dma4((sl) + tb * 4096, l16, rws, hb + (st) * 4096); \
        else { _Pragma("unroll") for (int n = 0; n < 7; ++n) dma1((sl) + 16384 + tb * 7168 + n * 1024, l16, rws, wb + n * 40960 + (st) * 4096); } } while (0)
#pragma unroll 1
    for (int s = 0; s < 3; ++s) GU_ISSUE(s, s * SLOT);
    float csv[5];
    if (NB == 4) {
        const unsigned e = tb * 64 + lane;
#pragma unroll
        for (int k = 0; k < 5; ++k) asm volatile("buffer_load_dword %0, %1, %2, %3 offen" : "=&v"(csv[k]) : "v"((e < 224 ? e : 0) * 4), "s"(rws), "s"(WS_CS + (k * 3456 + b0 * 32) * 4) : "memory");
    }
    f32x16 acc[NB] = {};
    float sx = 0.f, sq = 0.f;
    bf8v hA, hB, wA[NB], wB[NB];
    const unsigned ha = tb * 4096 + l16, wa = 16384 + (NB == 4 ? 0 : 4096) + l16;
#define GU_RD(Hf, Wf, j, sl) do { Hf = ldsr<bf8v>(ha + (sl) + (j) * 1024); _Pragma("unroll") for (int n = 0; n < NB; ++n) Wf[n] = ldsr<bf8v>(wa + (sl) + (j) * 7168 + n * 1024); } while (0)
#define GU_MM(Hf, Wf) do { _Pragma("unroll") for (int n = 0; n < NB; ++n) acc[n] = mfma32(Wf[n], Hf, acc[n]); sx = sum8(Hf, sx); sq = sq8(Hf, sq); PIN(sx); PIN(sq); } while (0)
    if (NB == 4) WAITBAR(13); else WAITBAR(14);
    GU_RD(hA, wA, 0, 0);
    unsigned sl = 0;
#pragma unroll 1
    for (int s = 0; s < 10; ++s) {
        SBAR();
        GU_RD(hB, wB, 1, sl); SBAR(); GU_MM(hA, wA); SBAR();
        GU_RD(hA, wA, 2, sl); SBAR(); GU_MM(hB, wB); SBAR();
        GU_RD(hB, wB, 3, sl); SBAR(); GU_MM(hA, wA); SBAR();
        if (NB == 4 && s == 2) {
            asm volatile("" : "+v"(csv[0]), "+v"(csv[1]), "+v"(csv[2]), "+v"(csv[3]), "+v"(csv[4]));
            ldsw<float>(L_CS + (tb * 64 + lane) * 4, ((csv[0] + csv[1]) + (csv[2] + csv[3])) + csv[4]);
        }
        if (s >= 8) WAITBAR(0); else if (NB == 3) WAITBAR(7); else if (s == 0) WAITBAR(9); else WAITBAR(4);
        const unsigned ps = sl;
        sl = sl == 2 * SLOT ? 0 : sl + SLOT;
        GU_RD(hA, wA, 0, sl); SBAR();
        if (s < 7) GU_ISSUE(s + 3, ps);
        SBAR();
        GU_MM(hB, wB);
    }
    float ra, rb;
    {
        const f32x2 a = swap32(sx, sx), c = swap32(sq, sq);
        const float mean = (a[0] + a[1]) * (1.f / 640.f);
        const float var = __builtin_fmaxf((c[0] + c[1]) * (1.f / 640.f) - mean * mean, 0.f);
        ra = __builtin_amdgcn_rsqf(var + 1e-5f); rb = -mean * ra;
    }
    const unsigned fv = (t >> 4) * (216 * 256) + hh * 256 + (t & 15) * 16;
    const unsigned fs = WS_F + (mt * 8 + tb * 2) * (216 * 256) + (b0 + (NB == 4 ? 0 : 4)) * 512;
#pragma unroll
    for (int n = 0; n < NB; ++n) {
        float o[8];
#pragma unroll
        for (int a = 0; a < 2; ++a) {
            const unsigned ca = L_CS + (((NB == 4 ? 0 : 4) + n) * 32 + 8 * a + 4 * hh) * 4;
            const f32x4 cg = ldsr<f32x4>(ca), cu = ldsr<f32x4>(ca + 64);
#pragma unroll
            for (int i = 0; i < 4; ++i) {
                const float gt = acc[n][4 * a + i] * ra + rb * cg[i], up = acc[n][8 + 4 * a + i] * ra + rb * cu[i];
                o[4 * a + i] = (gt > 0.f ? gt : gt * 0.f) * up;
            }
        }
        st16(rws, fv, fs + n * 512, u32x4{pk2(o[0], o[1]), pk2(o[2], o[3]), pk2(o[4], o[5]), pk2(o[6], o[7])});
    }
}
DI void phase_gu(const Params& p, int b, int w, int lane) {
    const int xcd = b & 7, i = b >> 3, mt = (xcd & 1) * 8 + (i & 7), nt = (xcd >> 1) * 4 + (i >> 3), b0 = nt * 101 / 15;
    if (w < 4) gu_body<4>(p, mt, b0, w, lane); else gu_body<3>(p, mt, b0, w, lane);
}

template <int PH>
__global__ void __launch_bounds__(512, 2) block_kernel(Params p) {
    __shared__ __attribute__((aligned(16))) char lds[163840];
    asm volatile("" :: "v"((LAS char*)lds) : "memory");
    const int lane = threadIdx.x & 63, w = __builtin_amdgcn_readfirstlane(threadIdx.x >> 6), b = blockIdx.x;
    if (PH == 1) phase_qkv(p, b, w, lane);
    if (PH == 2) phase_attn(p, b, w, lane);
    if (PH == 3) phase_g80<false>(p, b, w, lane);
    if (PH == 4) phase_gu(p, b, w, lane);
    if (PH == 5) phase_g80<true>(p, b, w, lane);
}

extern "C" void kernel_launch(void* const* d_in, const int* in_sizes, int n_in, void* d_out, int out_size, void* d_ws, size_t ws_size, hipStream_t stream) {
    Params p;
    p.x = (const char*)d_in[0]; p.wqkv = (const char*)d_in[1]; p.wo = (const char*)d_in[2];
    p.wg = (const char*)d_in[4]; p.wu = (const char*)d_in[5]; p.wd = (const char*)d_in[6];
    p.out = (char*)d_out; p.ws = (char*)d_ws;
    block_kernel<1><<<256, 512, 0, stream>>>(p);
    block_kernel<2><<<256, 512, 0, stream>>>(p);
    block_kernel<3><<<256, 512, 0, stream>>>(p);
    block_kernel<4><<<256, 512, 0, stream>>>(p);
    block_kernel<5><<<256, 512, 0, stream>>>(p);
}
```

```cpp
#include <hip/hip_runtime.h>
#include <hip/hip_bf16.h>
#include <stdint.h>

#define DI __device__ __forceinline__
typedef __attribute__((ext_vector_type(2))) __bf16 bf2v;
typedef __attribute__((ext_vector_type(8))) __bf16 bf8v;
typedef __attribute__((ext_vector_type(16))) __bf16 bf16v;
typedef __attribute__((ext_vector_type(4))) short s4v;
typedef __attribute__((ext_vector_type(2))) float f32x2;
typedef __attribute__((ext_vector_type(4))) float f32x4;
typedef __attribute__((ext_vector_type(8))) float f32x8;
typedef __attribute__((ext_vector_type(16))) float f32x16;
typedef __attribute__((ext_vector_type(32))) float f32x32;
typedef __attribute__((ext_vector_type(2))) unsigned u32x2;
typedef __attribute__((ext_vector_type(4))) unsigned u32x4;
typedef __attribute__((ext_vector_type(8))) unsigned u32x8;
typedef __amdgpu_buffer_rsrc_t rsrc_t;
#define LAS __attribute__((address_space(3)))

constexpr unsigned WS_Q = 0;
constexpr unsigned WS_KV = WS_Q + 32 * 81920;
constexpr unsigned WS_O = WS_KV + 32 * 163840;
constexpr unsigned WS_B = WS_O + 128 * 20480;
constexpr unsigned QBS = 32 * 40960, OFF_HS = 16 * 20480, OFF_F = OFF_HS + 16 * 2048;
constexpr unsigned WS_WO = WS_B + 8 * QBS;
constexpr unsigned WS_WD = WS_WO + 8 * 102400;
constexpr unsigned WS_WGU = WS_WD + 8 * 276480;
constexpr unsigned WS_CS = WS_WGU + 32 * 143360;
constexpr unsigned WS_FQ = WS_CS + 5 * 3584 * 4;
constexpr unsigned WS_FR = WS_FQ + 256 * 128;
constexpr unsigned WS_E = WS_FR + 2048;
constexpr unsigned WS_END = WS_E + 128;
constexpr unsigned OOB = 0x40000000u;
constexpr int SPIN = 1 << 14;

struct Params {
    const char* x; const char* wqkv; const char* wo; const char* wg; const char* wu; const char* wd;
    char* out; char* ws;
};

#define SBAR() __builtin_amdgcn_sched_barrier(0)
#define PIN(x) asm volatile("" : "+v"(x))
#define WAITBAR(N) asm volatile("s_waitcnt vmcnt(" #N ") lgkmcnt(0)\n\ts_barrier" ::: "memory")
#define VBAR(N) asm volatile("s_waitcnt vmcnt(" #N ")\n\ts_barrier" ::: "memory")
#define LBAR() asm volatile("s_waitcnt lgkmcnt(0)\n\ts_barrier" ::: "memory")
#define BAR() asm volatile("s_barrier" ::: "memory")

DI rsrc_t mkrs(const char* p, unsigned bytes) { return __builtin_amdgcn_make_buffer_rsrc((void*)p, (short)0, (int)bytes, 0x00020000); }
template <class T> DI T ldsr(unsigned a) { return *(const LAS T*)(uintptr_t)a; }
template <class T> DI void ldsw(unsigned a, T v) { *(LAS T*)(uintptr_t)a = v; }
DI s4v ldstr(unsigned a) { return __builtin_amdgcn_ds_read_tr16_b64_v4i16((LAS s4v*)(uintptr_t)a); }
DI bf8v ldstr2(unsigned a, unsigned d) {
    s4v lo = ldstr(a), hi = ldstr(a + d);
    return __builtin_bit_cast(bf8v, __builtin_shufflevector(lo, hi, 0, 1, 2, 3, 4, 5, 6, 7));
}
DI f32x16 mfma32(bf8v a, bf8v b, f32x16 c) { return __builtin_amdgcn_mfma_f32_32x32x16_bf16(a, b, c, 0, 0, 0); }
DI f32x4 mfma16(bf8v a, bf8v b, f32x4 c) { return __builtin_amdgcn_mfma_f32_16x16x32_bf16(a, b, c, 0, 0, 0); }
DI bf8v asbf(u32x4 v) { return __builtin_bit_cast(bf8v, v); }
DI unsigned pk2(float lo, float hi) { f32x2 v = {lo, hi}; return __builtin_bit_cast(unsigned, __builtin_convertvector(v, bf2v)); }
DI unsigned nomark(unsigned u) { return u < 0xfffffffeu ? u : 0xfffffffeu; }
DI unsigned pk2m(float lo, float hi) { return nomark(pk2(lo, hi)); }
DI float bflo(unsigned u) { return __uint_as_float(u << 16); }
DI float bfhi(unsigned u) { return __uint_as_float(u & 0xffff0000u); }
DI f32x2 swap32(float a, float b) { auto r = __builtin_amdgcn_permlane32_swap(__float_as_uint(a), __float_as_uint(b), false, false); return f32x2{__uint_as_float(r[0]), __uint_as_float(r[1])}; }
DI float sum16(float a) { auto r = __builtin_amdgcn_permlane16_swap(__float_as_uint(a), __float_as_uint(a), false, false); return __uint_as_float(r[0]) + __uint_as_float(r[1]); }
DI bool anyl(bool c) { return __builtin_amdgcn_ballot_w64(c) != 0; }
DI void st16(rsrc_t r, unsigned voff, unsigned soff, u32x4 v) { asm volatile("buffer_store_dwordx4 %0, %1, %2, %3 offen sc1\n\ts_nop 1" :: "v"(v), "v"(voff), "s"(r), "s"(soff) : "memory"); }
DI void st8(rsrc_t r, unsigned voff, unsigned soff, u32x2 v) { asm volatile("buffer_store_dwordx2 %0, %1, %2, %3 offen sc1" :: "v"(v), "v"(voff), "s"(r), "s"(soff) : "memory"); }
DI void st4(rsrc_t r, unsigned voff, unsigned soff, unsigned v) { asm volatile("buffer_store_dword %0, %1, %2, %3 offen sc1" :: "v"(v), "v"(voff), "s"(r), "s"(soff) : "memory"); }
DI u32x4 ld16c(rsrc_t r, unsigned voff, unsigned soff) { return __builtin_amdgcn_raw_buffer_load_b128(r, voff, soff, 16); }
DI u32x2 ld8c(rsrc_t r, unsigned voff, unsigned soff) { return __builtin_amdgcn_raw_buffer_load_b64(r, voff, soff, 16); }
DI void dma1(unsigned m0v, unsigned voff, rsrc_t r, unsigned soff) {
    asm volatile("s_mov_b32 m0, %0\n\ts_nop 0\n\tbuffer_load_dwordx4 %1, %2, %3 offen lds" :: "s"(m0v), "v"(voff), "s"(r), "s"(soff) : "memory"); }
DI void dma1c(unsigned m0v, unsigned voff, rsrc_t r, unsigned soff) {
    asm volatile("s_mov_b32 m0, %0\n\ts_nop 0\n\tbuffer_load_dwordx4 %1, %2, %3 offen sc1 lds" :: "s"(m0v), "v"(voff), "s"(r), "s"(soff) : "memory"); }
DI void dma3c(unsigned m0v, unsigned voff, rsrc_t r, unsigned soff) {
    asm volatile("s_mov_b32 m0, %0\n\ts_nop 0\n\tbuffer_load_dwordx4 %1, %2, %3 offen sc1 lds\n\tbuffer_load_dwordx4 %1, %2, %3 offen offset:1024 sc1 lds\n\t"
                 "buffer_load_dwordx4 %1, %2, %3 offen offset:2048 sc1 lds" :: "s"(m0v), "v"(voff), "s"(r), "s"(soff) : "memory"); }
DI void dma4c(unsigned m0v, unsigned voff, rsrc_t r, unsigned soff) {
    asm volatile("s_mov_b32 m0, %0\n\ts_nop 0\n\tbuffer_load_dwordx4 %1, %2, %3 offen sc1 lds\n\tbuffer_load_dwordx4 %1, %2, %3 offen offset:1024 sc1 lds\n\t"
                 "buffer_load_dwordx4 %1, %2, %3 offen offset:2048 sc1 lds\n\tbuffer_load_dwordx4 %1, %2, %3 offen offset:3072 sc1 lds" :: "s"(m0v), "v"(voff), "s"(r), "s"(soff) : "memory"); }
#define SPINS "0x4000"
#define PINS(x) asm volatile("" : "+s"(x))
#define CHK(B, TMP, DST) \
    "v_max3_u32 v" #TMP ", v[" #B "], v[" #B "+1], v[" #B "+2]\n\tv_max3_u32 v" #TMP ", v[" #B "+3], v[" #B "+4], v" #TMP "\n\t" \
    "v_max3_u32 v" #TMP ", v[" #B "+5], v[" #B "+6], v" #TMP "\n\tv_max3_u32 v" #TMP ", v[" #B "+7], v[" #B "+8], v" #TMP "\n\t" \
    "v_max3_u32 v" #TMP ", v[" #B "+9], v[" #B "+10], v" #TMP "\n\tv_max3_u32 v" #TMP ", v[" #B "+11], v[" #B "+12], v" #TMP "\n\t" \
    "v_max3_u32 v" #TMP ", v[" #B "+13], v[" #B "+14], v" #TMP "\n\tv_max_u32 v" #TMP ", v[" #B "+15], v" #TMP "\n\t" \
    "v_cmp_eq_u32 " DST ", -1, v" #TMP "\n\t"
#define LD1(R, V, S, O) "buffer_load_dwordx4 v[" R ":" R "+3], " V ", s[40:43], " S " offen offset:" O " sc1\n\t"
#define DM1(V, S, O) "buffer_load_dwordx4 " V ", s[40:43], " S " offen offset:" #O " sc1 lds\n\t"
#define DM3(V, S) DM1(V, S, 0) DM1(V, S, 1024) DM1(V, S, 2048)
#define DM4(V, S) DM3(V, S) DM1(V, S, 3072)
#define POLL3(L0, L1, L2, B0, B1, B2, TMP, FILL) \
    "s_mov_b64 s[60:61], -1\n\ts_mov_b64 s[62:63], -1\n\ts_mov_b64 s[64:65], -1\n\ts_movk_i32 s59, " SPINS "\n" \
    "1:\n\ts_mov_b64 exec, s[60:61]\n\ts_cbranch_execz 2f\n\t" L0 \
    "2:\n\ts_mov_b64 exec, s[62:63]\n\ts_cbranch_execz 3f\n\t" L1 \
    "3:\n\ts_mov_b64 exec, s[64:65]\n\ts_cbranch_execz 4f\n\t" L2 \
    "4:\n\ts_mov_b64 exec, -1\n\t" FILL "s_waitcnt vmcnt(0)\n\t" \
    CHK(B0, TMP, "s[60:61]") CHK(B1, TMP, "s[62:63]") CHK(B2, TMP, "s[64:65]") \
    "s_or_b64 vcc, s[60:61], s[62:63]\n\ts_or_b64 vcc, vcc, s[64:65]\n\ts_cbranch_scc0 5f\n\t" \
    "s_sub_u32 s59, s59, 1\n\ts_cbranch_scc0 1b\n" \
    "5:\n\t"
#define Z2(R) "v_mov_b64 v[" R ":" R "+1], 0\n\t"
#define Z4(R) Z2(R) Z2(R "+2")
#define Z8(R) Z4(R) Z4(R "+4")

#define VCL32 "v32", "v33", "v34", "v35", "v36", "v37", "v38", "v39", "v40", "v41", "v42", "v43", "v44", "v45", "v46", "v47"
#define VCL48 "v48", "v49", "v50", "v51", "v52", "v53", "v54", "v55", "v56", "v57", "v58", "v59", "v60", "v61", "v62", "v63"
#define VCL64 "v64", "v65", "v66", "v67", "v68", "v69", "v70", "v71", "v72", "v73", "v74", "v75", "v76", "v77", "v78", "v79"
#define VCL80 "v80", "v81", "v82", "v83", "v84", "v85", "v86", "v87", "v88", "v89", "v90", "v91", "v92", "v93", "v94", "v95"
#define VCL96 "v96", "v97", "v98", "v99", "v100", "v101", "v102", "v103", "v104", "v105", "v106", "v107", "v108", "v109", "v110", "v111"
#define VCL112 "v112", "v113", "v114", "v115", "v116", "v117", "v118", "v119", "v120", "v121", "v122", "v123", "v124", "v125", "v126", "v127"
#define VCL128 "v128", "v129", "v130", "v131", "v132", "v133", "v134", "v135", "v136", "v137", "v138", "v139"
#define VCL112L "v112", "v113", "v114", "v115", "v116", "v117", "v118", "v119"

#define PROFARG
#define PROFPASS
#define TSA(i)
#define PFIO
#define PFCL
#define TSM
#define PFM(i)
#define PFT
#define TS(i)
#define TC(i)

constexpr unsigned L_RT = 131072, L_CP = 122880;
struct Retile { rsrc_t ra, rb; unsigned vo[4], rd[4], so[4], dl; };
template <int F> DI unsigned quad(unsigned v) { return __builtin_amdgcn_mov_dpp(v, F * 0x55, 0xf, 0xf, true); }
DI void retile_setup(Retile& r, const Params& p, unsigned b, unsigned w, unsigned lane) {
    const unsigned f = lane & 3, q = (lane >> 2) & 3, gi = lane >> 4;
    unsigned A, M, SO, R0, R1, vlo, rb;
    if (b < 135) {
        const unsigned fg = (b * 205) >> 10, ks = (b - fg * 5) * 4 + (w >> 1), j = ks >> 1;
        r.ra = mkrs(p.wg - 3072, 640 * 3456 + 3072); r.rb = mkrs(p.wu - 3072, 640 * 3456 + 3072); r.dl = 256;
        A = (320 * (ks & 1) + (j < 8 ? (j & 1) * 32 + (j >> 1) * 8 : 8 * j) + (f & 1) * 160) * 3456 + fg * 128 + (w & 1) * 64;
        M = (j < 8 ? 96 : 16) * 3456;
        vlo = ((lane >> 2) & 7) * 3456 + f * 16;
        const unsigned o = fg * 8 + (w & 1) * 4 + f, nt = (o * 293) >> 11;
        SO = WS_WGU + nt * (143360 - 7168) + ks * 7168 + o * 1024;
        rb = (f >> 1) * 2048 + (8 * gi + q) * 64 + (f & 1) * 8; R0 = 16 * f; R1 = 0;
    } else {
        const unsigned v = (b - 135) * 8 + w;
        const bool isWo = v < 200;
        const unsigned vv = v < 740 ? (isWo ? v : v - 200) : 0, ks = (vv * 205) >> 11, cu = vv - ks * 10, NK = isWo ? 10 : 27;
        r.ra = r.rb = mkrs((isWo ? p.wo : p.wd) - 3072, NK * 64 * 1280 + 3072); r.dl = 512;
        const unsigned c = 4 * ks + f, j = (c * 2115) >> 16, ng = c - 31 * j;
        A = (isWo ? (ks >= 16 ? 40 * c - 2544 : 20 * (c >> 1) + 4 * (c & 1)) : 56 * ng + 16 * (j >> 1) + 4 * (j & 1)) * 1280 + cu * 128;
        M = (isWo ? (ks >= 16 ? 20 : 8) : (j < 6 ? 8 : 4)) * 1280;
        vlo = ((lane >> 3) & 3) * 1280 + (lane & 7) * 16;
        const unsigned seg = 2 * cu + (f >> 1), a = (seg * 205) >> 10, sm = seg - 5 * a, fi = f & 1;
        const unsigned tile = 2 * a + (sm == 2 ? fi : sm > 2), blk = sm == 2 ? 4 : (sm == 1 || sm == 4) ? 2 + fi : fi;
        SO = (v >= 740 ? OOB : isWo ? WS_WO : WS_WD) + (tile * NK * 10 + (ks >= NK ? (ks - NK) * 10 + 5 : ks * 10) + blk) * 1024;
        rb = (8 * gi + q) * 128; R0 = (f >> 1) * 64 + (sm == 2 ? 32 : 8) * fi; R1 = sm == 2 ? 8 : 16;
    }
    A += M * (lane >> 5) + 3072 - 1024 * f;
    rb += L_RT + w * 4096;
#define RT_F(F) r.vo[F] = quad<F>(A) + vlo; r.rd[F] = rb + quad<F>(R0) + quad<F>(R1) * f; r.so[F] = quad<F>(SO) + lane * 16; PIN(r.vo[F]); PIN(r.rd[F]); PIN(r.so[F]);
    RT_F(0) RT_F(1) RT_F(2) RT_F(3)
}
DI void retile_issue(const Retile& r, int w) {
    asm volatile("s_mov_b32 m0, %0\n\ts_nop 0\n\tbuffer_load_dwordx4 %1, %5, 0 offen lds\n\tbuffer_load_dwordx4 %2, %5, 0 offen offset:1024 lds\n\t"
                 "buffer_load_dwordx4 %3, %6, 0 offen offset:2048 lds\n\tbuffer_load_dwordx4 %4, %6, 0 offen offset:3072 lds"
                 :: "s"(L_RT + w * 4096), "v"(r.vo[0]), "v"(r.vo[1]), "v"(r.vo[2]), "v"(r.vo[3]), "s"(r.ra), "s"(r.rb) : "memory");
}
DI void retile_store(const Retile& r, rsrc_t rws, int w, int lane) {
    const u32x4 one = {0x3f803f80u, 0x3f803f80u, 0x3f803f80u, 0x3f803f80u};
    bf8v fr[4];
#pragma unroll
    for (int f = 0; f < 4; ++f) fr[f] = ldstr2(r.rd[f], r.dl);
    f32x4 cp[4];
#pragma unroll
    for (int f = 0; f < 4; ++f) {
        asm volatile("buffer_store_dwordx4 %0, %1, %2, 0 offen sc1\n\ts_nop 1" :: "v"(fr[f]), "v"(r.so[f]), "s"(rws) : "memory");
        cp[f] = mfma16(fr[f], asbf(one), f32x4{});
    }
    if ((lane & 15) == 0) {
#pragma unroll
        for (int f = 0; f < 4; ++f) ldsw<f32x4>(L_CP + w * 256 + f * 64 + lane, cp[f]);
    }
}

struct QkvSrc { rsrc_t rx, rw; unsigned xo[2], wo[2]; };
#define QKV_DMA(st, sl) asm volatile("s_mov_b32 m0, %0\n\ts_nop 0\n\tbuffer_load_dwordx4 %1, %5, %7 offen lds\n\tbuffer_load_dwordx4 %2, %5, %7 offen offset:1024 lds\n\t" \
        "buffer_load_dwordx4 %3, %6, %8 offen offset:2048 lds\n\tbuffer_load_dwordx4 %4, %6, %8 offen offset:3072 lds" \
        :: "s"((sl) + w * 4096), "v"(c.xo[0]), "v"(c.xo[1]), "v"(c.wo[0]), "v"(c.wo[1]), "s"(c.rx), "s"(c.rw), "s"((st) * 128), "s"((st) * 245760) : "memory")
DI void qkv_start(QkvSrc& c, const Params& p, int mt, int nt, int w, int lane) {
    c.rx = mkrs(p.x, 2048 * 1280); c.rw = mkrs(p.wqkv - 3072, 640 * 3840 + 3072);
    const int rr = lane >> 3;
    const int tok = (w >> 1) * 32 + (nt >= 10 ? 8 * (rr >> 2) + 4 * (w & 1) + (rr & 3) : 2 * rr + (w & 1));
    c.xo[0] = (mt * 128 + tok) * 1280 + (((lane & 7) ^ (rr >> 1)) << 4);
    c.xo[1] = (c.xo[0] ^ 64) + 16 * 1280 - 1024;
    c.wo[0] = (8 * w + ((lane >> 2) & 3)) * 3840 + nt * 256 + (lane >> 4) * 64 + (((lane & 3) ^ ((w & 1) << 1)) << 4) + 1024;
    c.wo[1] = c.wo[0] + 4 * 3840 - 1024;
    if (nt < 15) {
#pragma unroll 1
        for (int s = 0; s < 5; ++s) { QKV_DMA(s, s == 0 ? 131072 : s * 32768 - 32768); BAR(); }
    }
}
template <bool ISV>
DI void qkv_body(const QkvSrc& c, const Retile& rt, rsrc_t rws, int mt, int nt, int w, int lane PROFARG) {
    const int t = lane & 15, g = lane >> 4, T = w & 1, cb = w >> 1;
    const unsigned x0 = (4 * T + (cb & 2)) * 4096 + t * 128 + (((g ^ (t >> 1)) << 4) ^ ((cb & 1) << 6));
    const unsigned w0 = (4 * (cb & 1) + g) * 4096 + 2048 + cb * 256 + ((lane >> 2) & 3) * 64 + (g & 1) * 32 + (lane & 3) * 8;
    f32x4 acc[2][4] = {}, cs[2] = {}, sx[2] = {}, sq[2] = {};
    bf8v xA[4], wA[2], xB[4], wB[2];
    u32x4 one = {0x3f803f80u, 0x3f803f80u, 0x3f803f80u, 0x3f803f80u};
    PIN(one);
#define QKV_RD(X, W, j, sl) do { const unsigned xa_ = ((sl) + x0) ^ ((j) << 6), wa_ = ((sl) + w0) ^ ((j) << 14); \
        W[0] = ldstr2(wa_, 1024); X[0] = ldsr<bf8v>(xa_); X[1] = ldsr<bf8v>(xa_ + 4096); W[1] = ldstr2(wa_ ^ 32, 1024); \
        X[2] = ldsr<bf8v>(xa_ ^ 8192); X[3] = ldsr<bf8v>((xa_ ^ 8192) + 4096); } while (0)
#define QKV_MM(X, W) do { _Pragma("unroll") for (int i = 0; i < 4; ++i) _Pragma("unroll") for (int n = 0; n < 2; ++n) \
            acc[n][i] = ISV ? mfma16(X[i], W[n], acc[n][i]) : mfma16(W[n], X[i], acc[n][i]); \
        _Pragma("unroll") for (int n = 0; n < 2; ++n) cs[n] = ISV ? mfma16(asbf(one), W[n], cs[n]) : mfma16(W[n], asbf(one), cs[n]); } while (0)
    WAITBAR(16);
    TS(1);
    unsigned sl = 131072;
    QKV_RD(xA, wA, 0, sl);
#pragma unroll 1
    for (int s = 0; s < 10; ++s) {
        SBAR();
        QKV_RD(xB, wB, 1, sl); SBAR();
        QKV_MM(xA, wA);
#pragma unroll
        for (int i = 0; i < 2; ++i) { sx[i] = mfma16(asbf(one), xA[i], sx[i]); sq[i] = mfma16(xA[i], xA[i], sq[i]); }
        SBAR();
        if (s <= 6) WAITBAR(12); else if (s == 7) WAITBAR(8); else WAITBAR(4);
        const unsigned ps = sl;
        sl = sl == 131072 ? 0 : sl + 32768;
        QKV_RD(xA, wA, 0, sl); SBAR();
        if (s < 5) QKV_DMA(s + 5, ps); else if (s == 5) retile_issue(rt, w);
        SBAR();
        QKV_MM(xB, wB);
    }
    TS(2);
    constexpr unsigned L_ST = 0, L_PR = 4096;
    if (g == (t >> 2)) {
#pragma unroll
        for (int i = 0; i < 2; ++i) {
            const float q01 = (t & 1) ? sq[i][1] : sq[i][0], q23 = (t & 1) ? sq[i][3] : sq[i][2];
            ldsw<f32x2>(L_ST + (64 * T + 16 * (cb & 2) + 16 * i + t) * 16 + (cb & 1) * 8, f32x2{sx[i][0], (t & 2) ? q23 : q01});
        }
    }
    LBAR();
    const unsigned pr = L_PR + w * 512;
    {
        const f32x4 u = ldsr<f32x4>(L_ST + (64 * T + lane) * 16);
        const float mean = (u[0] + u[2]) * (1.f / 640.f);
        const float var = __builtin_fmaxf((u[1] + u[3]) * (1.f / 640.f) - mean * mean, 0.f);
        float ra = __builtin_amdgcn_rsqf(var + 1e-5f);
        if (nt < 5) ra *= 0.22360679774997896f * 1.4426950408889634f;
        ldsw<f32x2>(pr + ((lane * 8) ^ ((cb & 2) << 7)), f32x2{ra, -mean * ra});
    }
    const int g64 = 2 * mt + T;
    if (!ISV) {
        f32x2 r[4];
#pragma unroll
        for (int i = 0; i < 4; ++i) r[i] = ldsr<f32x2>(pr + i * 128 + t * 8);
        const bool isq = nt < 5;
        const unsigned hs = isq ? 81920 : 163840;
        const unsigned sbase = isq ? WS_Q + g64 * 2560 : WS_KV + g64 * 5120;
        const int G0 = (isq ? nt : nt - 5) * 32 + cb * 8 + g;
#pragma unroll
        for (int n = 0; n < 2; ++n) {
            const unsigned G = G0 + 4 * n, head = (G * 205) >> 10, dq = G - 5 * head;
#pragma unroll
            for (int q = 0; q < 2; ++q) {
                float o[8];
#pragma unroll
                for (int i = 0; i < 8; ++i) o[i] = acc[n][2 * q + (i >> 2)][i & 3] * r[2 * q + (i >> 2)][0] + r[2 * q + (i >> 2)][1] * cs[n][i & 3];
                st16(rws, head * hs + dq * 512 + t * 16, sbase + (q ^ (cb >> 1)) * 256, u32x4{pk2(o[0], o[1]), pk2(o[2], o[3]), pk2(o[4], o[5]), pk2(o[6], o[7])});
            }
        }
    } else {
#pragma unroll
        for (int q = 0; q < 2; ++q) {
            f32x4 r[4];
#pragma unroll
            for (int i = 0; i < 4; ++i) r[i] = ldsr<f32x4>(pr + (2 * q + (i >> 1)) * 128 + g * 32 + (i & 1) * 16);
#pragma unroll
            for (int n = 0; n < 2; ++n) {
                const unsigned col = (nt - 10) * 128 + cb * 32 + 16 * n + t, head = (col * 3277) >> 16, d = col - 20 * head;
                float o[8];
#pragma unroll
                for (int i = 0; i < 8; ++i) o[i] = acc[n][2 * q + (i >> 2)][i & 3] * r[i >> 1][2 * (i & 1)] + r[i >> 1][2 * (i & 1) + 1] * cs[n][0];
                st16(rws, head * 163840 + g * 320 + d * 16, WS_KV + g64 * 5120 + 2560 + (q ^ (cb >> 1)) * 1280, u32x4{pk2(o[0], o[1]), pk2(o[2], o[3]), pk2(o[4], o[5]), pk2(o[6], o[7])});
            }
        }
    }
}

DI f32x16 qk_tile(u32x4 km, u32x2 kt, u32x4 qm, u32x2 qt, f32x16 c) {
    c = mfma32(asbf(km), asbf(qm), c);
    return __builtin_amdgcn_mfma_f32_32x32x8bf16_1k(__builtin_bit_cast(s4v, kt), __builtin_bit_cast(s4v, qt), c, 0, 0, 0);
}
DI f32x16 lo16(const f32x32& s) { return __builtin_shufflevector(s, s, 0, 1, 2, 3, 4, 5, 6, 7, 8, 9, 10, 11, 12, 13, 14, 15); }
DI f32x16 hi16(const f32x32& s) { return __builtin_shufflevector(s, s, 16, 17, 18, 19, 20, 21, 22, 23, 24, 25, 26, 27, 28, 29, 30, 31); }
DI f32x32 qk_pair(unsigned a0, unsigned at, const u32x8& qm, u32x2 qt, f32x32 c) {
    const u32x2 k0 = ldsr<u32x2>(a0), k1 = ldsr<u32x2>(a0 + 512), kt = ldsr<u32x2>(at);
    const u32x4 km = {k0[0], k0[1], k1[0], k1[1]};
    c = __builtin_amdgcn_mfma_f32_32x32x4bf16_1k(__builtin_bit_cast(s4v, kt), __builtin_bit_cast(s4v, qt), c, 1, 0, 0);
    const f32x16 a = mfma32(asbf(km), asbf(__builtin_shufflevector(qm, qm, 0, 1, 2, 3)), lo16(c));
    const f32x16 b = mfma32(asbf(km), asbf(__builtin_shufflevector(qm, qm, 4, 5, 6, 7)), hi16(c));
    return __builtin_shufflevector(a, b, 0, 1, 2, 3, 4, 5, 6, 7, 8, 9, 10, 11, 12, 13, 14, 15, 16, 17, 18, 19, 20, 21, 22, 23, 24, 25, 26, 27, 28, 29, 30, 31);
}
DI float max16(const f32x16& s, float m) {
#pragma unroll
    for (int r = 0; r < 16; r += 2) m = __builtin_fmaxf(__builtin_fmaxf(s[r], s[r + 1]), m);
    return m;
}
DI f32x16 pv16(u32x4 v, f32x8 pr, f32x16 o) {
    return __builtin_amdgcn_smfmac_f32_32x32x32_bf16(asbf(v), __builtin_bit_cast(bf16v, pr), o, 0xDDDD, 0, 0);
}
#define EX4(P, S) "v_exp_f32 v[" #P "], v[" S "]\n\tv_exp_f32 v[" #P "+1], v[" S "+1]\n\tv_exp_f32 v[" #P "+2], v[" S "+2]\n\tv_exp_f32 v[" #P "+3], v[" S "+3]\n\t"
#define SMF(O, V, P) "v_smfmac_f32_32x32x32_bf16 v[" #O ":" #O "+15], v[" #V ":" #V "+3], v[" #P ":" #P "+7], %[idx]\n\t"
#define STEP(SC, SN, AB, VP, VC, KTRD, VRD, KRD, DOFS) \
    "v_mfma_f32_32x32x4_2b_bf16 v[" #SN ":" #SN "+31], v[188:189], v[184:185], v[32:63] cbsz:1 abid:" #AB "\n\t" KTRD \
    EX4(160, #SC) EX4(164, #SC "+4") \
    SMF(144, VP, 168) \
    EX4(168, #SC "+16") \
    "s_waitcnt lgkmcnt(0)\n\t" \
    SMF(128, 196, 160) \
    EX4(172, #SC "+20") \
    "v_mfma_f32_32x32x16_bf16 v[" #SN ":" #SN "+15], v[192:195], v[176:179], v[" #SN ":" #SN "+15]\n\t" \
    "buffer_load_dwordx4 %[dv], s[40:43], s45 offen offset:" #DOFS " sc1 lds\n\t" \
    EX4(160, #SC "+8") \
    SMF(144, 196, 168) VRD \
    EX4(164, #SC "+12") \
    "v_mfma_f32_32x32x16_bf16 v[" #SN "+16:" #SN "+31], v[192:195], v[180:183], v[" #SN "+16:" #SN "+31]\n\t" KRD \
    EX4(168, #SC "+24") \
    SMF(128, VC, 160) \
    EX4(172, #SC "+28")
#define VR(VH, OL, PRE) PRE "s_mov_b64 exec, s[56:57]\n\tds_read_b128 v[196:199], %[av] offset:" #OL "\n\tds_read_b128 v[" #VH ":" #VH "+3], %[av] offset:" #OL "+640\n\ts_mov_b64 exec, -1\n\t"
#define KR(O, PRE) PRE "ds_read_b64 v[192:193], %[a0] offset:" #O "\n\tds_read_b64 v[194:195], %[a0] offset:" #O "+512\n\t"
#define KT(O, PRE) PRE "ds_read_b64 v[188:189], %[at] offset:" #O "\n\t"
#define ADD(r) "v_add_u32 %[" #r "], s55, %[" #r "]\n\t"
#define CALL "s_call_b64 s[58:59], 8b\n\t"
#define ST16(O) "buffer_store_dwordx4 %[mk], %[l16], s[40:43], s54 offen offset:" #O " sc1\n\t"

DI void phase_attn(const Params& p, const Retile& rt, rsrc_t rws, unsigned T, int b, int w, int lane, int nt80, int mt64, int ntg,
                   unsigned rv0, unsigned rv1, unsigned rsb, u32x4& r0, u32x2& r1 PROFARG) {
    const int t = lane & 31, hh = lane >> 5, wl = w & 3, grp = w >> 2;
    const int head = (b & 7) * 4 + (b >> 6), qb = (b >> 3) & 7;
    const unsigned l16 = lane * 16, gb = grp * 61440, kvb = WS_KV + head * 163840, kvg = kvb + grp * 81920;
    u32x2 tag = {T, ~T};
    if (w < 2) {
        const int n0 = (head * 20 + ((lane & 16) ? 19 : 0)) >> 7;
        const unsigned fo = WS_FQ + (w == 0 ? (5 + 5 * hh + n0) * 16 + (lane & 15) : n0 * 16 + 2 * qb + (lane & 1)) * 128;
        u32x2 f; int s;
        asm volatile("s_movk_i32 %1, " SPINS "\n1:\n\tbuffer_load_dwordx2 %0, %2, %3, 0 offen sc1\n\ts_waitcnt vmcnt(0)\n\tv_cmp_ne_u64 vcc, %0, %4\n\t"
                     "s_cbranch_vccz 2f\n\ts_sub_u32 %1, %1, 1\n\ts_cbranch_scc0 1b\n2:" : "=&v"(f), "=&s"(s) : "v"(fo), "s"(rws), "v"(tag) : "memory", "vcc", "scc");
    }
    u32x8 qm; u32x2 qt;
    {
        unsigned qs = WS_Q + head * 81920 + (qb * 4 + wl) * 2560, va = hh * 1024 + grp * 256 + t * 8, vb = va ^ 256, vc = (lane * 8) ^ (grp * 256);
        unsigned dm = gb + wl * 1024, ds = kvg + wl * 1024;
#define KVP "buffer_load_dwordx4 %[l16], %[rs], %[ds] offen sc1 lds\n\ts_add_u32 m0, m0, 4096\n\ts_add_u32 %[ds], %[ds], 4096\n\t"
        asm volatile("s_mov_b32 m0, %[dm]\n\ts_barrier\n\t" TSA("4")
                     "buffer_load_dwordx2 v[176:177], %[va], %[rs], %[qs] offen sc1\n\tbuffer_load_dwordx2 v[178:179], %[va], %[rs], %[qs] offen offset:512 sc1\n\t"
                     "buffer_load_dwordx2 v[180:181], %[vb], %[rs], %[qs] offen sc1\n\tbuffer_load_dwordx2 v[182:183], %[vb], %[rs], %[qs] offen offset:512 sc1\n\t"
                     "buffer_load_dwordx2 v[184:185], %[vc], %[rs], %[qs] offen offset:2048 sc1\n\t"
                     KVP KVP KVP KVP "s_barrier\n\t" KVP KVP KVP
                     : "=&{v[176:183]}"(qm), "=&{v[184:185]}"(qt), [ds] "+&s"(ds) PFIO
                     : [va] "v"(va), [vb] "v"(vb), [vc] "v"(vc), [rs] "s"(rws), [qs] "s"(qs), [dm] "s"(dm), [l16] "v"(l16) : "memory", "scc" PFCL);
    }
    retile_store(rt, rws, w, lane);
    u32x4 mark = {~0u, ~0u, ~0u, ~0u};
    PIN(mark);
    st16(rws, l16, WS_O + b * 10240 + w * 1280, mark);
    st16(rws, lane < 16 ? l16 : OOB, WS_O + b * 10240 + w * 1280 + 1024, mark);
    unsigned a0 = hh * 1024 + t * 8, at = lane * 8 + 2048, av = gb + 2560 + hh * 320 + (t < 20 ? t : 0) * 16;
    const unsigned ones = t == 20 ? 0x3f803f80u : 0u;
    const unsigned wob = WS_WO + nt80 * 102400;
    u32x8 sa;
    sa[0] = gb + (28 + 4 * wl) * 1024; sa[1] = kvg + (28 + 4 * wl) * 1024;
    sa[2] = 122880 + (w ? w + 1 : 0) * 4096;
    sa[3] = w == 0 ? wob : w < 4 ? WS_WGU + ntg * 143360 + (mt64 & 4) * 17920 + (w - 1) * 19456 : WS_WD + nt80 * 276480 + (mt64 & 7) * 34560 + (w & 1) * 1792;
    sa[4] = 122880 + (w + 1) * 4096; sa[5] = wob + (w + 1) * 4096;
    sa[6] = (w + 9) * 4096 + (w == 0 ? 122880 : -20480);
    sa[7] = (w + 17) * 4096 + (w < 3 ? -20480 : 0);
    u32x4 sb = {(unsigned)grp, rsb, WS_B + qb * QBS + head * 40960 + w * 5120, 10240};
    unsigned dv4 = w == 0 ? l16 : w < 6 ? (lane & 7) * 128 + (lane >> 3) * 4096 : OOB;
    unsigned vfs = (w == 2 && lane == 0) ? WS_FR + b * 8 : OOB, vfe = (b == 0 && w == 0 && lane == 0) ? WS_E : OOB, vfp = WS_FR + w * 256 + t * 8;
    const unsigned fg = (b * 205) >> 10;
    unsigned vcs = (b < 135 && w < 2) ? WS_CS + ((b - fg * 5) * 3584 + fg * 128 + w * 64 + lane) * 4 : OOB;
    unsigned idx = 0xDDDD;
    rsrc_t rx = mkrs(p.x, 2048 * 1280);
    f32x32 S0, S1, O, NM; f32x16 P;
    u32x4 km, vlo, vh0, vh1 = {ones, ones, ones, ones}; u32x2 kt;
    asm volatile(Z8("128") Z8("136") Z8("144") Z8("152") Z8("168") "s_waitcnt vmcnt(9) lgkmcnt(0)\n\ts_barrier"
                 : "+{v[176:183]}"(qm), "+{v[184:185]}"(qt), "={v[96:127]}"(S1), "={v[128:159]}"(O), "={v[160:175]}"(P), "+{v[204:207]}"(vh1), "+{s[44:51]}"(sa), "+{s[52:55]}"(sb),
                   "+v"(dv4), "+v"(vfs), "+v"(vfe), "+v"(vfp), "+v"(vcs), "+v"(idx), "+v"(a0), "+v"(at), "+v"(av), "+v"(rv0), "+v"(rv1), "+s"(rx) :: "memory");
    TS(5);
    {
        const f32x32 r = qk_pair(a0, at, qm, qt, f32x32{});
        a0 += gb; at += gb;
        const f32x2 mm = swap32(max16(lo16(r), r[0]), max16(hi16(r), r[16]));
        const float m = __builtin_fmaxf(mm[0], mm[1]);
        const f32x2 m2 = swap32(m, m);
#pragma unroll
        for (int i = 0; i < 16; ++i) { NM[i] = -m2[0]; NM[16 + i] = -m2[1]; }
        S0 = qk_pair(a0, at, qm, qt, NM);
        kt = ldsr<u32x2>(at);
        const u32x2 n0 = ldsr<u32x2>(a0 + 256), n1 = ldsr<u32x2>(a0 + 768);
        km = u32x4{n0[0], n0[1], n1[0], n1[1]};
        const u32x4 v0 = ldsr<u32x4>(av), v1 = ldsr<u32x4>(av + 640);
#pragma unroll
        for (int i = 0; i < 4; ++i) { vlo[i] = t < 20 ? v0[i] : ones; vh0[i] = t < 20 ? v1[i] : ones; }
        float c = 0.f;
#pragma unroll
        for (int i = 0; i < 4; ++i) c += ldsr<float>(L_CP + (2 * i + (w & 1)) * 256 + lane * 4);
        asm volatile("buffer_store_dword %0, %1, %2, 0 offen sc1" :: "v"(c), "v"(vcs), "s"(rws) : "memory");
    }
    unsigned dv; u32x2 fr;
    asm volatile(
        "s_mov_b32 s56, 0xfffff\n\ts_mov_b32 s57, s56\n\tv_mov_b32 %[dv], %[l16]\n\ts_mov_b32 m0, s44\n\ts_nop 7\n\ts_branch 9f\n"
        "8:\n\ts_cmp_eq_u32 s52, 0\n\ts_cbranch_scc1 2f\n\ts_setprio 1\n2:\n\t"
        STEP(64, 96, 1, 204, 200, KT(5120, ""), VR(204, 1280, ""), KR(5120, ""), 0)
        STEP(96, 64, 0, 200, 204, "", VR(200, 5120, ""), KR(5376, ""), 1024)
        STEP(64, 96, 1, 204, 200, KT(0, ADD(at)), VR(204, 6400, ""), KR(0, ADD(a0)), 2048) "s_setprio 0\n\t"
        STEP(96, 64, 0, 200, 204, "", VR(200, 0, ADD(av)), KR(256, ""), 3072)
        "s_setpc_b64 s[58:59]\n"
        "9:\n\ts_nop 7\n\t" CALL
        "s_add_u32 m0, m0, 16384\n\ts_add_u32 s45, s45, 16384\n\ts_waitcnt vmcnt(11)\n\ts_barrier\n\t" CALL
        "s_sub_u32 m0, m0, 45056\n\ts_add_u32 s45, s45, 16384\n\ts_waitcnt vmcnt(4)\n\ts_barrier\n\t"
        "buffer_store_dwordx2 %[tag], %[vfs], s[40:43], 0 offen sc1\n\t" CALL
        "s_add_u32 m0, m0, 4096\n\ts_add_u32 s45, s45, 4096\n\tbuffer_load_dwordx2 %[fr], %[vfp], s[40:43], 0 offen sc1\n\t" CALL
        "s_mov_b32 m0, s46\n\ts_mov_b32 s45, s47\n\tv_mov_b32 %[dv], %[dv4]\n\ts_movk_i32 s44, " SPINS "\n\ts_waitcnt vmcnt(4)\n"
        "3:\n\tv_cmp_ne_u64 vcc, %[fr], %[tag]\n\ts_cbranch_vccz 4f\n\t"
        "buffer_load_dwordx2 %[fr], %[vfp], s[40:43], 0 offen sc1\n\ts_waitcnt vmcnt(0)\n\ts_sub_u32 s44, s44, 1\n\ts_cbranch_scc0 3b\n"
        "4:\n\ts_barrier\n\t" TSA("6")
        "buffer_store_dwordx2 %[tag], %[vfe], s[40:43], 0 offen sc1\n\t" ST16(0) "s_add_u32 s54, s54, 1024\n\t" ST16(0) ST16(1024) ST16(2048) ST16(3072) CALL
        "s_mov_b32 m0, s48\n\ts_mov_b32 s45, s49\n\tv_mov_b32 %[dv], %[l16]\n\ts_mov_b32 s55, -51200\n\t" CALL
        "s_mov_b32 m0, s50\n\ts_add_u32 s45, s45, 32768\n\ts_movk_i32 s55, 10240\n\ts_waitcnt vmcnt(14)\n\ts_barrier\n\t"
        "buffer_load_dwordx4 %[r0], %[rv0], %[rx], s53 offen\n\tbuffer_load_dwordx2 %[r1], %[rv1], %[rx], s53 offen\n\t" CALL
        "s_mov_b32 m0, s51\n\ts_add_u32 s45, s45, 32768\n\t" CALL
        "s_nop 1\n\t" SMF(144, 204, 168) "s_waitcnt vmcnt(8) lgkmcnt(0)\n\ts_barrier\n\ts_nop 7\n\ts_nop 7\n\ts_nop 7"
        : "+{v[64:95]}"(S0), "+{v[96:127]}"(S1), "+{v[128:159]}"(O), "+{v[160:175]}"(P), "+{v[188:189]}"(kt), "+{v[192:195]}"(km),
          "+{v[196:199]}"(vlo), "+{v[200:203]}"(vh0), "+{v[204:207]}"(vh1), "+{s[44:51]}"(sa), "+{s[52:55]}"(sb),
          [a0] "+&v"(a0), [at] "+&v"(at), [av] "+&v"(av), [dv] "=&v"(dv), [fr] "=&v"(fr), [r0] "=&v"(r0), [r1] "=&v"(r1) PFIO
        : "{v[32:63]}"(NM), "{v[176:183]}"(qm), "{v[184:185]}"(qt), "{s[40:43]}"(rws), [idx] "v"(idx), [l16] "v"(l16), [dv4] "v"(dv4), [mk] "v"(mark), [tag] "v"(tag),
          [vfs] "v"(vfs), [vfe] "v"(vfe), [vfp] "v"(vfp), [rv0] "v"(rv0), [rv1] "v"(rv1), [rx] "s"(rx)
        : "memory", "scc", "vcc", "s56", "s57", "s58", "s59" PFCL);
    TS(7);
    float o[12];
    {
        const unsigned e0 = (w < 6 ? w * 3072 : w * 40960 - 184320) + l16, w2 = w ^ 4, e1 = (w2 < 6 ? w2 * 3072 : w2 * 40960 - 184320) + l16;
#pragma unroll
        for (int i = 0; i < 3; ++i) ldsw<f32x4>(e0 + i * 1024, f32x4{O[16 + 4 * i], O[17 + 4 * i], O[18 + 4 * i], O[19 + 4 * i]});
        LBAR();
#pragma unroll
        for (int i = 0; i < 3; ++i) {
            const f32x4 v = ldsr<f32x4>(e1 + i * 1024);
#pragma unroll
            for (int k = 0; k < 4; ++k) o[4 * i + k] = O[4 * i + k] + v[k];
        }
    }
    {
        float mx = 0.f;
#pragma unroll
        for (int r = 0; r < 12; ++r) mx += __builtin_fabsf(o[r]);
        const bool bad = hh ? !(o[8] < 1.8446744e19f) || !(mx < __builtin_inff()) : !(mx < __builtin_inff());
        if (__builtin_expect(anyl(bad), 0)) {
            float m = -__builtin_inff();
            const unsigned ko = hh * 1024 + t * 8, vo = 2560 + hh * 320 + (t < 20 ? t : 0) * 16;
            const u32x4 qa = {qm[0], qm[1], qm[2], qm[3]}; const u32x2 qe = {hh ? 0u : qt[0], hh ? 0u : qt[1]};
            f32x16 c = {}, A = {};
#pragma unroll 1
            for (int it = 0; it < 128; ++it) {
                const unsigned sb = kvb + ((it & 63) >> 1) * 5120;
                const u32x2 k0 = ld8c(rws, ko, sb + (it & 1) * 256), k1 = ld8c(rws, ko + 512, sb + (it & 1) * 256), k2 = ld8c(rws, t * 8 + 2048, sb + (it & 1) * 256);
                f32x16 s = qk_tile(u32x4{k0[0], k0[1], k1[0], k1[1]}, k2, qa, qe, c);
                if (it < 64) {
                    m = max16(s, m);
                    if (it == 63) {
                        const f32x2 mm = swap32(m, m);
                        m = __builtin_fmaxf(mm[0], mm[1]);
#pragma unroll
                        for (int r = 0; r < 16; ++r) c[r] = -m;
                    }
                } else {
                    u32x4 v0 = ld16c(rws, vo, sb + (it & 1) * 1280), v1 = ld16c(rws, vo + 640, sb + (it & 1) * 1280);
#pragma unroll
                    for (int i = 0; i < 4; ++i) { v0[i] = t < 20 ? v0[i] : ones; v1[i] = t < 20 ? v1[i] : ones; }
#pragma unroll
                    for (int r = 0; r < 16; ++r) s[r] = __builtin_amdgcn_exp2f(s[r]);
                    A = pv16(v0, __builtin_shufflevector(s, s, 0, 1, 2, 3, 4, 5, 6, 7), A);
                    A = pv16(v1, __builtin_shufflevector(s, s, 8, 9, 10, 11, 12, 13, 14, 15), A);
                }
            }
#pragma unroll
            for (int r = 0; r < 12; ++r) o[r] = A[r];
        }
    }
    const float inv = __builtin_amdgcn_rcpf(swap32(o[8], o[8])[1]);
    const unsigned ob = WS_O + (qb * 16 + wl * 4 + grp * 2) * 20480, ov = (t >> 4) * 20480 + (t & 15) * 16;
    st16(rws, ov + hh * 256, ob + head * 512, u32x4{pk2m(o[0] * inv, o[1] * inv), pk2m(o[2] * inv, o[3] * inv), pk2m(o[4] * inv, o[5] * inv), pk2m(o[6] * inv, o[7] * inv)});
    st8(rws, hh ? OOB : ov, ob + (64 + (head >> 1)) * 256 + (head & 1) * 8, u32x2{pk2m(o[8] * inv, o[9] * inv), pk2m(o[10] * inv, o[11] * inv)});
    const unsigned u = (grp ? 21 : 11) + wl;
    dma4c(20480 + u * 4096, l16, rws, WS_WGU + ntg * 143360 + u * 4096);
}

#define MF16(A, W, B) "v_mfma_f32_16x16x32_bf16 v[" A ":" A "+3], v[" W ":" W "+3], v[" B ":" B "+3], v[" A ":" A "+3]\n\t"
#define RD1(W, V, O) "ds_read_b128 v[" W ":" W "+3], " V " offset:" O "\n\t"
#define GUN(n, H, V, O) "s_waitcnt lgkmcnt(6)\n\t" MF16("140+8*" #n, "112+4*" #n, H) MF16("144+8*" #n, "112+4*" #n, H "+8") RD1("112+4*" #n, V, O "+1024*" #n)
#define GUK(H, V, O) GUN(0, H, V, O) GUN(1, H, V, O) GUN(2, H, V, O) GUN(3, H, V, O) GUN(4, H, V, O) GUN(5, H, V, O) GUN(6, H, V, O)
#define LDH(H, S, O) LD1(#H, "%[l16]", S, O) LD1(#H "+4", "%[l16]", S, O "+1024") LD1(#H "+8", "%[lb]", S, O) LD1(#H "+12", "%[lb]", S, O "+1024")
#define RETRY(H, TMP, L) "1:\n\t" CHK(H, TMP, "vcc") "s_cbranch_vccz 2f\n\t" L "s_waitcnt vmcnt(0)\n\ts_sub_u32 s59, s59, 1\n\ts_cbranch_scc0 1b\n\ts_mov_b32 s59, 0\n2:\n\t"
#define GUS(H, PRE, O1, V2, O2, TAIL) \
    "s_waitcnt vmcnt(16)\n" RETRY(H, 196, LDH(H, "s44", "0")) PRE \
    GUK(#H, "%[wa]", O1) GUK(#H "+4", V2, O2) \
    "s_add_u32 s45, s44, 10240\n\ts_add_u32 s44, s44, 2048\n\ts_cmp_lt_u32 s45, s47\n\ts_cbranch_scc0 3f\n\t" LDH(H, "s45", "0") "s_branch 4f\n3:\n\t" TAIL "4:\n\t"
#define LDST LD1("32", "%[l16]", "s51", "0") LD1("36", "%[l16]", "s51", "1024") LD1("40", "%[l16]", "s51", "2048") LD1("44", "%[l16]", "s51", "3072")
DI void phase_gu(rsrc_t rws, int w, int lane, int qb, int nt80, int ntg PROFARG) {
    const int t = lane & 15, g = lane >> 4;
    const unsigned l16 = lane * 16, wd = WS_WD + nt80 * 276480;
    if (ntg == 31) {
#pragma unroll 1
        for (int i = 0; i < 5; ++i) dma3c(20480 + (i * 8 + w) * 3072, l16, rws, wd + (i * 8 + w) * 3072);
        return;
    }
#pragma unroll 1
    for (int u1 = w; u1 < 27; u1 += 8) {
        const unsigned u = u1 < 11 ? u1 : u1 < 17 ? u1 + 4 : u1 + 8;
        dma4c(20480 + u * 4096, l16, rws, WS_WGU + ntg * 143360 + u * 4096);
    }
    const unsigned qbb = WS_B + qb * QBS, hb = qbb + w * 40960, e = w * 64 + lane;
    u32x8 sa = {hb, WS_CS + ntg * 448, wd + w * 4096, hb + 20480, 20480u + w * 4096, wd + 65536 + w * 7168, 86016u + w * 7168, qbb + OFF_HS + w * 4096};
    unsigned wa = 20480 + l16;
    f32x32 A0; f32x16 A1, ST; f32x8 A2;
#define CSL(R) "buffer_load_dword v" #R ", %[ve], s[40:43], s45 offen sc1\n\ts_add_u32 s45, s45, 14336\n\t"
    asm volatile(
        CSL(112) CSL(113) CSL(114) CSL(115) CSL(116) "s_add_u32 s45, s44, 4096\n\t"
        POLL3(LDH(32, "s44", "0"), LDH(48, "s44", "2048"), LDH(64, "s45", "0"), 32, 48, 64, 196, Z8("140") Z8("148") Z8("156") Z8("164") Z8("172") Z8("180") Z8("188"))
        "s_add_u32 s59, s44, 8192\n\t" LDH(80, "s45", "2048") LDH(96, "s59", "0")
        "v_add_f32 v112, v112, v113\n\tv_add_f32 v114, v114, v115\n\tv_add_f32 v112, v112, v116\n\tv_add_f32 v196, v112, v114\n\ts_movk_i32 s59, " SPINS "\n\ts_barrier\n\t" TSA("11")
        "ds_write_b32 %[vl], v196\n\t"
        RD1("112", "%[wa]", "0") RD1("116", "%[wa]", "1024") RD1("120", "%[wa]", "2048") RD1("124", "%[wa]", "3072") RD1("128", "%[wa]", "4096") RD1("132", "%[wa]", "5120") RD1("136", "%[wa]", "6144")
        "6:\n\t"
        GUS(32, "", "7168", "%[wa]", "14336", LDST)
        GUS(48, "", "21504", "%[wa]", "28672", "s_mov_b32 m0, s48\n\ts_barrier\n\t" DM4("%[l16]", "s46"))
        GUS(64, "", "35840", "%[wa]", "43008", "s_add_u32 m0, s48, 32768\n\ts_add_u32 s46, s46, 32768\n\t" DM4("%[l16]", "s46"))
        GUS(80, "", "50176", "%[wa]", "57344", "s_mov_b32 m0, s50\n\ts_barrier\n\t" DM4("%[l16]", "s49") "s_add_u32 m0, s50, 4096\n\ts_add_u32 s49, s49, 4096\n\t" DM3("%[l16]", "s49"))
        GUS(96, "v_add_u32 %[wa], 0xfc00, %[wa]\n\t", "0", "%[wn]", "0", "s_branch 7f\n")
        "v_mov_b32 %[wa], %[wn]\n\ts_branch 6b\n"
        "7:\n\t" TSA("12") "s_waitcnt vmcnt(15)\n" RETRY(32, 196, LDST) "s_waitcnt lgkmcnt(0)\n\ts_nop 7"
        : "=&{v[140:171]}"(A0), "=&{v[172:187]}"(A1), "=&{v[188:195]}"(A2), "=&{v[32:47]}"(ST), "+{s[44:51]}"(sa), [wa] "+&v"(wa) PFIO
        : "{s[40:43]}"(rws), [l16] "v"(l16), [lb] "v"(l16 + 20480), [wn] "v"(l16 + 92160), [ve] "v"(e < 112 ? e * 4 : OOB), [vl] "v"(e * 4)
        : "memory", "scc", "vcc", "s59", "s60", "s61", "s62", "s63", "s64", "s65", VCL48, VCL64, VCL80, VCL96, VCL112, VCL128, "v196" PFCL);
    float ra, rb;
    {
        float s[2], q[2];
#pragma unroll
        for (int i = 0; i < 2; ++i) {
            s[i] = (ST[8 * i] + ST[8 * i + 2]) + (ST[8 * i + 4] + ST[8 * i + 6]);
            q[i] = (ST[8 * i + 1] + ST[8 * i + 3]) + (ST[8 * i + 5] + ST[8 * i + 7]);
        }
        const f32x2 ss = swap32(s[0], s[1]), qq = swap32(q[0], q[1]);
        const float mean = sum16(ss[0] + ss[1]) * (1.f / 640.f);
        const float var = __builtin_fmaxf(sum16(qq[0] + qq[1]) * (1.f / 640.f) - mean * mean, 0.f);
        ra = __builtin_amdgcn_rsqf(var + 1e-5f); rb = -mean * ra;
    }
    unsigned fo[14];
#pragma unroll
    for (int n = 0; n < 7; ++n) {
        const f32x4 cg = ldsr<f32x4>(n * 64 + (g & 1) * 16), cu = ldsr<f32x4>(n * 64 + 32 + (g & 1) * 16);
        float o[4];
#pragma unroll
        for (int i = 0; i < 4; ++i) {
            const f32x2 gu = n < 4 ? swap32(A0[8 * (n & 3) + i], A0[8 * (n & 3) + 4 + i]) : n < 6 ? swap32(A1[8 * (n & 1) + i], A1[8 * (n & 1) + 4 + i]) : swap32(A2[i], A2[4 + i]);
            const float gt = gu[0] * ra + rb * cg[i], up = gu[1] * ra + rb * cu[i];
            o[i] = (gt > 0.f ? gt : gt * 0.f) * up;
        }
        fo[2 * n] = pk2m(o[0], o[1]); fo[2 * n + 1] = pk2m(o[2], o[3]);
        const unsigned fv = (g >> 1) * 55296 + t * 16, fs = qbb + OFF_F + w * 110592 + ntg * 256;
        if (n & 1) st16(rws, fv + (g & 1) * 7936, fs + (n - 1) * 7936, u32x4{fo[2 * n - 2], fo[2 * n - 1], fo[2 * n], fo[2 * n + 1]});
        if (n == 6) st8(rws, ntg < 30 ? fv + (g & 1) * 8 : OOB, fs + 6 * 7936, u32x2{fo[12], fo[13]});
    }
}

#define G8N(n, W, B, V, O) "s_waitcnt lgkmcnt(9)\n\t" MF16("120+4*" #n, W "+4*" #n, B) RD1(W "+4*" #n, V, O "+1024*" #n)
#define G8K(W, B, V, O) G8N(0, W, B, V, O) G8N(1, W, B, V, O) G8N(2, W, B, V, O) G8N(3, W, B, V, O) G8N(4, W, B, V, O)
#define LDB(B, S, S2, S3) LD1(#B, "v141", S, "0") LD1(#B "+4", "v141", S, "1024") LD1(#B "+8", "v141", S2, "2048") LD1(#B "+12", "v141", S3, "3072")
#define DUP(N, S) "s_cmp_eq_u32 s45, " #N "\n\ts_cselect_b32 s51, 1024, 0\n\ts_sub_u32 s51, " S ", s51\n\t"
#define BRK(N) "s_cmp_eq_u32 s45, " #N "\n\ts_cbranch_scc1 9f\n\t"
#define G8S(SLOT, B, BN, LA, LN, BRK1, BRK2, LN4, LR4) \
    G8K("80", #B, LA, "20480") G8K("100", #B "+4", LA, "30720") BRK1 \
    "s_cmp_lt_u32 s45, s47\n\ts_cbranch_scc1 7f\n\ts_cmp_gt_u32 s45, 8\n\ts_cbranch_scc1 3f\n\ts_waitcnt vmcnt(0)\n3:\n\ts_waitcnt vmcnt(9)\n" \
    RETRY(BN, 140, LN4) \
    "s_waitcnt lgkmcnt(0)\n\ts_barrier\n\ts_cmp_gt_u32 s45, 12\n\ts_cbranch_scc0 7f\n\t" \
    "s_add_u32 m0, s48, " #SLOT "*40960\n\ts_add_u32 s50, s46, 4096\n\t" DM4("v141", "s46") "s_add_u32 m0, m0, 4096\n\ts_add_u32 s46, s46, 40960\n\t" DM1("v141", "s50", 0) \
    "7:\n\t" G8K("80", #B "+8", LN, "0") BRK2 G8K("100", #B "+12", LN, "10240") \
    "s_add_u32 s50, s49, 8192\n\ts_add_u32 s49, s49, 4096\n\ts_sub_u32 s45, s45, 4\n\ts_cmp_gt_u32 s45, 8\n\ts_cbranch_scc0 8f\n\t" LR4 "8:\n\t"
#define RD5(W, V, O) RD1(W, V, O) RD1(W "+4", V, O "+1024") RD1(W "+8", V, O "+2048") RD1(W "+12", V, O "+3072") RD1(W "+16", V, O "+4096")
#define G80_BODY \
    ".Lg80:\n\ts_add_u32 s49, s44, 4096\n\ts_add_u32 s50, s44, 8192\n\ts_cmp_gt_u32 s45, 12\n\ts_cselect_b32 s51, 0, 2048\n\ts_sub_u32 s51, s50, s51\n\t" \
    POLL3(LDB(32, "s44", "s44", "s44"), LDB(48, "s49", "s49", "s49"), LDB(64, "s50", "s51", "s51"), 32, 48, 64, 140, Z8("120") Z8("128") Z4("136")) \
    "s_movk_i32 s59, " SPINS "\n\ts_barrier\n\t" TSM RD5("80", "v142", "0") RD5("100", "v142", "10240") \
    "6:\n\t" \
    G8S(0, 32, 48, "v142", "v143", "", BRK(3), LDB(48, "s49", "s49", "s49"), DUP(11, "s50") LDB(32, "s50", "s50", "s51")) \
    G8S(1, 48, 64, "v143", "v144", "", "", LDB(64, "s49", "s49", "s49"), LDB(48, "s50", "s50", "s50")) \
    G8S(2, 64, 32, "v144", "v142", BRK(2), "", DUP(7, "s49") LDB(32, "s49", "s49", "s51"), LDB(64, "s50", "s50", "s50")) \
    "s_branch 6b\n9:\n\ts_waitcnt lgkmcnt(0)\n\ts_nop 7\n\ts_setpc_b64 s[56:57]\n"
template <int PS>
DI void g80(rsrc_t rws, int w, int lane, int nt, unsigned ab, u32x4& r0, u32x2& r1 PROFARG) {
    const int tg = w & 3, kh = w >> 2;
    const unsigned l16 = lane * 16, la = kh * 5120 + l16;
    u32x4 sa = {ab, PS ? 27u : 10u, WS_WD + nt * 276480 + w * 5120 + 3 * 40960, PS ? 5u : 100u};
    f32x16 A; f32x4 a4;
    asm volatile(PFT "s_call_b64 s[56:57], .Lg80"
        : "=&{v[120:135]}"(A), "=&{v[136:139]}"(a4), "+{s[44:47]}"(sa) PFIO
        : "{s[40:43]}"(rws), "{s48}"(20480 + w * 5120), "{v141}"(l16), "{v142}"(la + (PS ? 20480 : 122880)), "{v143}"(la + (PS ? 61440 : 20480)), "{v144}"(la + (PS ? 102400 : 81920)) PFM(PS ? 13 : 8)
        : "memory", "scc", "vcc", "s49", "s50", "s51", "s56", "s57", "s59", "s60", "s61", "s62", "s63", "s64", "s65", VCL32, VCL48, VCL64, VCL80, VCL96, VCL112L, "v140" PFCL);
    if (PS) TS(14); else TS(9);
    const unsigned ex = tg * 5120 + l16;
    f32x4 a0 = {A[0], A[1], A[2], A[3]}, a1 = {A[4], A[5], A[6], A[7]};
    const f32x4 a2 = {A[8], A[9], A[10], A[11]}, a3 = {A[12], A[13], A[14], A[15]};
    if (kh == 0) { ldsw<f32x4>(ex + 2048, a2); ldsw<f32x4>(ex + 3072, a3); }
    else { ldsw<f32x4>(ex, a0); ldsw<f32x4>(ex + 1024, a1); ldsw<f32x4>(ex + 4096, a4); a0 = a2; a1 = a3; }
    LBAR();
    a0 += ldsr<f32x4>(ex + kh * 2048); a1 += ldsr<f32x4>(ex + kh * 2048 + 1024); a4 += ldsr<f32x4>(ex + 4096);
    r0 = u32x4{pk2(a0[0] + bflo(r0[0]), a0[1] + bfhi(r0[0])), pk2(a0[2] + bflo(r0[1]), a0[3] + bfhi(r0[1])),
               pk2(a1[0] + bflo(r0[2]), a1[1] + bfhi(r0[2])), pk2(a1[2] + bflo(r0[3]), a1[3] + bfhi(r0[3]))};
    r1 = u32x2{pk2(a4[0] + bflo(r1[0]), a4[1] + bfhi(r1[0])), pk2(a4[2] + bflo(r1[1]), a4[3] + bfhi(r1[1]))};
}

__global__ void __launch_bounds__(512, 2) block_kernel(Params p) {
    __shared__ __attribute__((aligned(16))) char lds[163840];
    asm volatile("" :: "v"((LAS char*)lds) : "memory");
    const int lane = threadIdx.x & 63, w = __builtin_amdgcn_readfirstlane(threadIdx.x >> 6), b = blockIdx.x;
    const rsrc_t rws = mkrs(p.ws, WS_END);
    const int xcd = b & 7, i = b >> 3;
    unsigned T;
    Retile rt;
    {
        const int mt = (xcd >> 1) * 4 + (i & 3), nt = (xcd & 1) * 8 + (i >> 2);
        QkvSrc c;
        qkv_start(c, p, mt, nt, w, lane);
        asm volatile("s_load_dword %0, %1, 0 glc\n\ts_waitcnt lgkmcnt(0)\n\ts_add_u32 %0, %0, 1" : "=&s"(T) : "s"(p.ws + WS_E) : "memory", "scc");
        retile_setup(rt, p, b, w, lane);
        if (nt == 15) retile_issue(rt, w);
        else if (nt >= 10) qkv_body<true>(c, rt, rws, mt, nt, w, lane PROFPASS);
        else qkv_body<false>(c, rt, rws, mt, nt, w, lane PROFPASS);
        VBAR(0);
        if (w == 2) st8(rws, lane == 0 ? 0 : OOB, WS_FQ + (nt * 16 + mt) * 128, u32x2{T, ~T});
        TS(3);
    }
    const int mt = (xcd >> 1) * 8 + (i & 7), nt = (xcd & 1) * 4 + (i >> 3), ntg = (xcd & 1) * 16 + (i & 3) * 4 + (i >> 3);
    const int tg = w & 3, kh = w >> 2, t = lane & 15, g = lane >> 4, qb = mt >> 2, tk = (mt & 3) * 4 + tg;
    const unsigned rv0 = t * 1280 + (nt & 1) * 32 + kh * 64 + g * 16, rv1 = kh ? OOB : t * 1280 + ((nt & 1) ? 0 : 128) + g * 8;
    const unsigned rsb = (mt * 64 + tg * 16) * 1280 + nt * 160, qbb = WS_B + qb * QBS;
    u32x4 r0; u32x2 r1;
    phase_attn(p, rt, rws, T, b, w, lane, nt, mt, ntg, rv0, rv1, rsb, r0, r1 PROFPASS);
    g80<0>(rws, w, lane, nt, WS_O + (mt * 4 + tg) * 20480 + kh * 10240, r0, r1 PROFPASS);
    {
#pragma unroll
        for (int k = 0; k < 4; ++k) r0[k] = nomark(r0[k]);
        r1[0] = nomark(r1[0]); r1[1] = nomark(r1[1]);
        const unsigned hs = qbb + tk * 20480 + nt * 256;
        st16(rws, (2 * g + kh) * 2048 + t * 16, hs, r0);
        st8(rws, kh ? OOB : (8 + (g >> 1)) * 2048 + t * 16 + (g & 1) * 8, hs, r1);
        float s = 0.f, q = 0.f;
#pragma unroll
        for (int k = 0; k < 6; ++k) {
            const unsigned u = k < 4 ? r0[k] : kh ? 0u : r1[k - 4];
            const float a = bflo(u), c = bfhi(u);
            s += a + c; q += a * a + c * c;
        }
        const f32x2 y = swap32(s, q);
        const float z = sum16(y[0] + y[1]);
        const f32x2 v = swap32(z, z);
        st8(rws, g == 0 ? t * 16 + kh * 8 : OOB, qbb + OFF_HS + tk * 2048 + nt * 256, u32x2{nomark(__float_as_uint(v[0])), nomark(__float_as_uint(v[1]))});
    }
    TS(10);
    unsigned fb = qbb + OFF_F + tk * 55296 + kh * 27648;
    PINS(fb);
    phase_gu(rws, w, lane, qb, nt, ntg PROFPASS);
    g80<1>(rws, w, lane, nt, fb, r0, r1 PROFPASS);
    asm volatile("buffer_store_dwordx4 %0, %2, %4, %5 offen\n\tbuffer_store_dwordx2 %1, %3, %4, %5 offen" :: "v"(r0), "v"(r1), "v"(rv0), "v"(rv1), "s"(mkrs(p.out, 2048 * 1280)), "s"(rsb) : "memory");
    asm volatile("s_endpgm\n" G80_BODY ::: "memory");
}

extern "C" void kernel_launch(void* const* d_in, const int* in_sizes, int n_in, void* d_out, int out_size, void* d_ws, size_t ws_size, hipStream_t stream) {
    Params p;
    p.x = (const char*)d_in[0]; p.wqkv = (const char*)d_in[1]; p.wo = (const char*)d_in[2];
    p.wg = (const char*)d_in[4]; p.wu = (const char*)d_in[5]; p.wd = (const char*)d_in[6];
    p.out = (char*)d_out; p.ws = (char*)d_ws;
    block_kernel<<<256, 512, 0, stream>>>(p);
}
```

```cpp
#include <hip/hip_runtime.h>
#include <hip/hip_bf16.h>
#include <stdint.h>

#define DI __device__ __forceinline__
typedef __attribute__((ext_vector_type(2))) __bf16 bf2v;
typedef __attribute__((ext_vector_type(8))) __bf16 bf8v;
typedef __attribute__((ext_vector_type(16))) __bf16 bf16v;
typedef __attribute__((ext_vector_type(4))) short s4v;
typedef __attribute__((ext_vector_type(2))) float f32x2;
typedef __attribute__((ext_vector_type(4))) float f32x4;
typedef __attribute__((ext_vector_type(8))) float f32x8;
typedef __attribute__((ext_vector_type(16))) float f32x16;
typedef __attribute__((ext_vector_type(32))) float f32x32;
typedef __attribute__((ext_vector_type(2))) unsigned u32x2;
typedef __attribute__((ext_vector_type(4))) unsigned u32x4;
typedef __attribute__((ext_vector_type(8))) unsigned u32x8;
typedef __amdgpu_buffer_rsrc_t rsrc_t;
#define LAS __attribute__((address_space(3)))

constexpr unsigned WS_Q = 0;
constexpr unsigned WS_KV = WS_Q + 32 * 81920;
constexpr unsigned WS_O = WS_KV + 32 * 163840;
constexpr unsigned WS_B = WS_O + 128 * 20480;
constexpr unsigned QBS = 32 * 40960, OFF_HS = 16 * 20480, OFF_F = OFF_HS + 16 * 2048;
constexpr unsigned WS_WO = WS_B + 8 * QBS;
constexpr unsigned WS_WD = WS_WO + 8 * 102400;
constexpr unsigned WS_WGU = WS_WD + 8 * 276480;
constexpr unsigned WS_CS = WS_WGU + 32 * 143360;
constexpr unsigned WS_FQ = WS_CS + 5 * 3584 * 4;
constexpr unsigned WS_FR = WS_FQ + 256 * 128;
constexpr unsigned WS_E = WS_FR + 2048;
constexpr unsigned WS_END = WS_E + 128;
constexpr unsigned OOB = 0x40000000u;
constexpr int SPIN = 1 << 14;

struct Params {
    const char* x; const char* wqkv; const char* wo; const char* wg; const char* wu; const char* wd;
    char* out; char* ws;
};

#define SBAR() __builtin_amdgcn_sched_barrier(0)
#define PIN(x) asm volatile("" : "+v"(x))
#define WAITBAR(N) asm volatile("s_waitcnt vmcnt(" #N ") lgkmcnt(0)\n\ts_barrier" ::: "memory")
#define VBAR(N) asm volatile("s_waitcnt vmcnt(" #N ")\n\ts_barrier" ::: "memory")
#define LBAR() asm volatile("s_waitcnt lgkmcnt(0)\n\ts_barrier" ::: "memory")
#define BAR() asm volatile("s_barrier" ::: "memory")

DI rsrc_t mkrs(const char* p, unsigned bytes) { return __builtin_amdgcn_make_buffer_rsrc((void*)p, (short)0, (int)bytes, 0x00020000); }
template <class T> DI T ldsr(unsigned a) { return *(const LAS T*)(uintptr_t)a; }
template <class T> DI void ldsw(unsigned a, T v) { *(LAS T*)(uintptr_t)a = v; }
DI s4v ldstr(unsigned a) { return __builtin_amdgcn_ds_read_tr16_b64_v4i16((LAS s4v*)(uintptr_t)a); }
DI bf8v ldstr2(unsigned a, unsigned d) {
    s4v lo = ldstr(a), hi = ldstr(a + d);
    return __builtin_bit_cast(bf8v, __builtin_shufflevector(lo, hi, 0, 1, 2, 3, 4, 5, 6, 7));
}
DI f32x16 mfma32(bf8v a, bf8v b, f32x16 c) { return __builtin_amdgcn_mfma_f32_32x32x16_bf16(a, b, c, 0, 0, 0); }
DI f32x4 mfma16(bf8v a, bf8v b, f32x4 c) { return __builtin_amdgcn_mfma_f32_16x16x32_bf16(a, b, c, 0, 0, 0); }
DI bf8v asbf(u32x4 v) { return __builtin_bit_cast(bf8v, v); }
DI unsigned pk2(float lo, float hi) { f32x2 v = {lo, hi}; return __builtin_bit_cast(unsigned, __builtin_convertvector(v, bf2v)); }
DI unsigned nomark(unsigned u) { return u < 0xfffffffeu ? u : 0xfffffffeu; }
DI unsigned pk2m(float lo, float hi) { return nomark(pk2(lo, hi)); }
DI float bflo(unsigned u) { return __uint_as_float(u << 16); }
DI float bfhi(unsigned u) { return __uint_as_float(u & 0xffff0000u); }
DI f32x2 swap32(float a, float b) { auto r = __builtin_amdgcn_permlane32_swap(__float_as_uint(a), __float_as_uint(b), false, false); return f32x2{__uint_as_float(r[0]), __uint_as_float(r[1])}; }
DI float sum16(float a) { auto r = __builtin_amdgcn_permlane16_swap(__float_as_uint(a), __float_as_uint(a), false, false); return __uint_as_float(r[0]) + __uint_as_float(r[1]); }
DI bool anyl(bool c) { return __builtin_amdgcn_ballot_w64(c) != 0; }
DI void st16(rsrc_t r, unsigned voff, unsigned soff, u32x4 v) { asm volatile("buffer_store_dwordx4 %0, %1, %2, %3 offen sc1\n\ts_nop 1" :: "v"(v), "v"(voff), "s"(r), "s"(soff) : "memory"); }
DI void st8(rsrc_t r, unsigned voff, unsigned soff, u32x2 v) { asm volatile("buffer_store_dwordx2 %0, %1, %2, %3 offen sc1" :: "v"(v), "v"(voff), "s"(r), "s"(soff) : "memory"); }
DI void st4(rsrc_t r, unsigned voff, unsigned soff, unsigned v) { asm volatile("buffer_store_dword %0, %1, %2, %3 offen sc1" :: "v"(v), "v"(voff), "s"(r), "s"(soff) : "memory"); }
DI u32x4 ld16c(rsrc_t r, unsigned voff, unsigned soff) { return __builtin_amdgcn_raw_buffer_load_b128(r, voff, soff, 16); }
DI u32x2 ld8c(rsrc_t r, unsigned voff, unsigned soff) { return __builtin_amdgcn_raw_buffer_load_b64(r, voff, soff, 16); }
DI void dma1(unsigned m0v, unsigned voff, rsrc_t r, unsigned soff) {
    asm volatile("s_mov_b32 m0, %0\n\ts_nop 0\n\tbuffer_load_dwordx4 %1, %2, %3 offen lds" :: "s"(m0v), "v"(voff), "s"(r), "s"(soff) : "memory"); }
DI void dma1c(unsigned m0v, unsigned voff, rsrc_t r, unsigned soff) {
    asm volatile("s_mov_b32 m0, %0\n\ts_nop 0\n\tbuffer_load_dwordx4 %1, %2, %3 offen sc1 lds" :: "s"(m0v), "v"(voff), "s"(r), "s"(soff) : "memory"); }
DI void dma3c(unsigned m0v, unsigned voff, rsrc_t r, unsigned soff) {
    asm volatile("s_mov_b32 m0, %0\n\ts_nop 0\n\tbuffer_load_dwordx4 %1, %2, %3 offen sc1 lds\n\tbuffer_load_dwordx4 %1, %2, %3 offen offset:1024 sc1 lds\n\t"
                 "buffer_load_dwordx4 %1, %2, %3 offen offset:2048 sc1 lds" :: "s"(m0v), "v"(voff), "s"(r), "s"(soff) : "memory"); }
DI void dma4c(unsigned m0v, unsigned voff, rsrc_t r, unsigned soff) {
    asm volatile("s_mov_b32 m0, %0\n\ts_nop 0\n\tbuffer_load_dwordx4 %1, %2, %3 offen sc1 lds\n\tbuffer_load_dwordx4 %1, %2, %3 offen offset:1024 sc1 lds\n\t"
                 "buffer_load_dwordx4 %1, %2, %3 offen offset:2048 sc1 lds\n\tbuffer_load_dwordx4 %1, %2, %3 offen offset:3072 sc1 lds" :: "s"(m0v), "v"(voff), "s"(r), "s"(soff) : "memory"); }
#define SPINS "0x4000"
#define PINS(x) asm volatile("" : "+s"(x))
#define CHK(B, TMP, DST) \
    "v_max3_u32 v" #TMP ", v[" #B "], v[" #B "+1], v[" #B "+2]\n\tv_max3_u32 v" #TMP ", v[" #B "+3], v[" #B "+4], v" #TMP "\n\t" \
    "v_max3_u32 v" #TMP ", v[" #B "+5], v[" #B "+6], v" #TMP "\n\tv_max3_u32 v" #TMP ", v[" #B "+7], v[" #B "+8], v" #TMP "\n\t" \
    "v_max3_u32 v" #TMP ", v[" #B "+9], v[" #B "+10], v" #TMP "\n\tv_max3_u32 v" #TMP ", v[" #B "+11], v[" #B "+12], v" #TMP "\n\t" \
    "v_max3_u32 v" #TMP ", v[" #B "+13], v[" #B "+14], v" #TMP "\n\tv_max_u32 v" #TMP ", v[" #B "+15], v" #TMP "\n\t" \
    "v_cmp_eq_u32 " DST ", -1, v" #TMP "\n\t"
#define LD1(R, V, S, O) "buffer_load_dwordx4 v[" R ":" R "+3], " V ", s[40:43], " S " offen offset:" O " sc1\n\t"
#define DM1(V, S, O) "buffer_load_dwordx4 " V ", s[40:43], " S " offen offset:" #O " sc1 lds\n\t"
#define DM3(V, S) DM1(V, S, 0) DM1(V, S, 1024) DM1(V, S, 2048)
#define DM4(V, S) DM3(V, S) DM1(V, S, 3072)
#define POLL3(L0, L1, L2, B0, B1, B2, TMP, FILL) \
    "s_mov_b64 s[60:61], -1\n\ts_mov_b64 s[62:63], -1\n\ts_mov_b64 s[64:65], -1\n\ts_movk_i32 s59, " SPINS "\n" \
    "1:\n\ts_mov_b64 exec, s[60:61]\n\ts_cbranch_execz 2f\n\t" L0 \
    "2:\n\ts_mov_b64 exec, s[62:63]\n\ts_cbranch_execz 3f\n\t" L1 \
    "3:\n\ts_mov_b64 exec, s[64:65]\n\ts_cbranch_execz 4f\n\t" L2 \
    "4:\n\ts_mov_b64 exec, -1\n\t" FILL "s_waitcnt vmcnt(0)\n\t" TSP \
    CHK(B0, TMP, "s[60:61]") CHK(B1, TMP, "s[62:63]") CHK(B2, TMP, "s[64:65]") \
    "s_or_b64 vcc, s[60:61], s[62:63]\n\ts_or_b64 vcc, vcc, s[64:65]\n\ts_cbranch_scc0 5f\n\t" \
    "s_sub_u32 s59, s59, 1\n\ts_cbranch_scc0 1b\n" \
    "5:\n\t"
#define Z2(R) "v_mov_b64 v[" R ":" R "+1], 0\n\t"
#define Z4(R) Z2(R) Z2(R "+2")
#define Z8(R) Z4(R) Z4(R "+4")

#define VCL32 "v32", "v33", "v34", "v35", "v36", "v37", "v38", "v39", "v40", "v41", "v42", "v43", "v44", "v45", "v46", "v47"
#define VCL48 "v48", "v49", "v50", "v51", "v52", "v53", "v54", "v55", "v56", "v57", "v58", "v59", "v60", "v61", "v62", "v63"
#define VCL64 "v64", "v65", "v66", "v67", "v68", "v69", "v70", "v71", "v72", "v73", "v74", "v75", "v76", "v77", "v78", "v79"
#define VCL80 "v80", "v81", "v82", "v83", "v84", "v85", "v86", "v87", "v88", "v89", "v90", "v91", "v92", "v93", "v94", "v95"
#define VCL96 "v96", "v97", "v98", "v99", "v100", "v101", "v102", "v103", "v104", "v105", "v106", "v107", "v108", "v109", "v110", "v111"
#define VCL112 "v112", "v113", "v114", "v115", "v116", "v117", "v118", "v119", "v120", "v121", "v122", "v123", "v124", "v125", "v126", "v127"
#define VCL128 "v128", "v129", "v130", "v131", "v132", "v133", "v134", "v135", "v136", "v137", "v138", "v139"
#define VCL112L "v112", "v113", "v114", "v115", "v116", "v117", "v118", "v119"

#define PROFARG
#define PROFPASS
#define TSA(i)
#define PFIO
#define PFCL
#define TSM
#define PFM(i)
#define PFT
#define TS(i)

#define TSF(i)
#define TSI
#define TSS(i)
#define TSW(i)
#define TSP
#define TSQ(a, b)
constexpr unsigned L_RT = 131072, L_CP = 122880;
struct Retile { rsrc_t ra, rb; unsigned vo[4], rd[4], so[4], dl; };
template <int F> DI unsigned quad(unsigned v) { return __builtin_amdgcn_mov_dpp(v, F * 0x55, 0xf, 0xf, true); }
DI void retile_setup(Retile& r, const Params& p, unsigned b, unsigned w, unsigned lane) {
    const unsigned f = lane & 3, q = (lane >> 2) & 3, gi = lane >> 4;
    unsigned A, M, SO, R0, R1, vlo, rb;
    if (b < 135) {
        const unsigned fg = (b * 205) >> 10, ks = (b - fg * 5) * 4 + (w >> 1), j = ks >> 1;
        r.ra = mkrs(p.wg - 3072, 640 * 3456 + 3072); r.rb = mkrs(p.wu - 3072, 640 * 3456 + 3072); r.dl = 256;
        A = (320 * (ks & 1) + (j < 8 ? (j & 1) * 32 + (j >> 1) * 8 : 8 * j) + (f & 1) * 160) * 3456 + fg * 128 + (w & 1) * 64;
        M = (j < 8 ? 96 : 16) * 3456;
        vlo = ((lane >> 2) & 7) * 3456 + f * 16;
        const unsigned o = fg * 8 + (w & 1) * 4 + f, nt = (o * 293) >> 11;
        SO = WS_WGU + nt * (143360 - 7168) + ks * 7168 + o * 1024;
        rb = (f >> 1) * 2048 + (8 * gi + q) * 64 + (f & 1) * 8; R0 = 16 * f; R1 = 0;
    } else {
        const unsigned v = (b - 135) * 8 + w;
        const bool isWo = v < 200;
        const unsigned vv = v < 740 ? (isWo ? v : v - 200) : 0, ks = (vv * 205) >> 11, cu = vv - ks * 10, NK = isWo ? 10 : 27;
        r.ra = r.rb = mkrs((isWo ? p.wo : p.wd) - 3072, NK * 64 * 1280 + 3072); r.dl = 512;
        const unsigned c = 4 * ks + f, j = (c * 2115) >> 16, ng = c - 31 * j;
        A = (isWo ? (ks >= 16 ? 40 * c - 2544 : 20 * (c >> 1) + 4 * (c & 1)) : 56 * ng + 16 * (j >> 1) + 4 * (j & 1)) * 1280 + cu * 128;
        M = (isWo ? (ks >= 16 ? 20 : 8) : (j < 6 ? 8 : 4)) * 1280;
        vlo = ((lane >> 3) & 3) * 1280 + (lane & 7) * 16;
        const unsigned seg = 2 * cu + (f >> 1), a = (seg * 205) >> 10, sm = seg - 5 * a, fi = f & 1;
        const unsigned tile = 2 * a + (sm == 2 ? fi : sm > 2), blk = sm == 2 ? 4 : (sm == 1 || sm == 4) ? 2 + fi : fi;
        SO = (v >= 740 ? OOB : isWo ? WS_WO : WS_WD) + (tile * NK * 10 + (ks >= NK ? (ks - NK) * 10 + 5 : ks * 10) + blk) * 1024;
        rb = (8 * gi + q) * 128; R0 = (f >> 1) * 64 + (sm == 2 ? 32 : 8) * fi; R1 = sm == 2 ? 8 : 16;
    }
    A += M * (lane >> 5) + 3072 - 1024 * f;
    rb += L_RT + w * 4096;
#define RT_F(F) r.vo[F] = quad<F>(A) + vlo; r.rd[F] = rb + quad<F>(R0) + quad<F>(R1) * f; r.so[F] = quad<F>(SO) + lane * 16; PIN(r.vo[F]); PIN(r.rd[F]); PIN(r.so[F]);
    RT_F(0) RT_F(1) RT_F(2) RT_F(3)
}
DI void retile_issue(const Retile& r, int w) {
    asm volatile("s_mov_b32 m0, %0\n\ts_nop 0\n\tbuffer_load_dwordx4 %1, %5, 0 offen lds\n\tbuffer_load_dwordx4 %2, %5, 0 offen offset:1024 lds\n\t"
                 "buffer_load_dwordx4 %3, %6, 0 offen offset:2048 lds\n\tbuffer_load_dwordx4 %4, %6, 0 offen offset:3072 lds"
                 :: "s"(L_RT + w * 4096), "v"(r.vo[0]), "v"(r.vo[1]), "v"(r.vo[2]), "v"(r.vo[3]), "s"(r.ra), "s"(r.rb) : "memory");
}
DI void retile_store(const Retile& r, rsrc_t rws, int w, int lane) {
    const u32x4 one = {0x3f803f80u, 0x3f803f80u, 0x3f803f80u, 0x3f803f80u};
    bf8v fr[4];
#pragma unroll
    for (int f = 0; f < 4; ++f) fr[f] = ldstr2(r.rd[f], r.dl);
    f32x4 cp[4];
#pragma unroll
    for (int f = 0; f < 4; ++f) {
        asm volatile("buffer_store_dwordx4 %0, %1, %2, 0 offen sc1\n\ts_nop 1" :: "v"(fr[f]), "v"(r.so[f]), "s"(rws) : "memory");
        cp[f] = mfma16(fr[f], asbf(one), f32x4{});
    }
    if ((lane & 15) == 0) {
#pragma unroll
        for (int f = 0; f < 4; ++f) ldsw<f32x4>(L_CP + w * 256 + f * 64 + lane, cp[f]);
    }
}

struct QkvSrc { rsrc_t rx, rw; unsigned xo[2], wo[2]; };
#define QKV_DMA(st, sl) asm volatile("s_mov_b32 m0, %0\n\ts_nop 0\n\tbuffer_load_dwordx4 %1, %5, %7 offen lds\n\tbuffer_load_dwordx4 %2, %5, %7 offen offset:1024 lds\n\t" \
        "buffer_load_dwordx4 %3, %6, %8 offen offset:2048 lds\n\tbuffer_load_dwordx4 %4, %6, %8 offen offset:3072 lds" \
        :: "s"((sl) + w * 4096), "v"(c.xo[0]), "v"(c.xo[1]), "v"(c.wo[0]), "v"(c.wo[1]), "s"(c.rx), "s"(c.rw), "s"((st) * 128), "s"((st) * 245760) : "memory")
DI void qkv_start(QkvSrc& c, const Params& p, int mt, int nt, int w, int lane PROFARG) {
    c.rx = mkrs(p.x, 2048 * 1280); c.rw = mkrs(p.wqkv - 3072, 640 * 3840 + 3072);
    const int rr = lane >> 3;
    const int tok = (w >> 1) * 32 + (nt >= 10 ? 8 * (rr >> 2) + 4 * (w & 1) + (rr & 3) : 2 * rr + (w & 1));
    c.xo[0] = (mt * 128 + tok) * 1280 + (((lane & 7) ^ (rr >> 1)) << 4);
    c.xo[1] = (c.xo[0] ^ 64) + 16 * 1280 - 1024;
    c.wo[0] = (8 * w + ((lane >> 2) & 3)) * 3840 + nt * 256 + (lane >> 4) * 64 + (((lane & 3) ^ ((w & 1) << 1)) << 4) + 1024;
    c.wo[1] = c.wo[0] + 4 * 3840 - 1024;
    PIN(c.xo[1]); PIN(c.wo[1]); PINS(c.rx); PINS(c.rw);
    TS(40);
    if (nt < 15) {
#pragma unroll 1
        for (int s = 0; s < 5; ++s) { QKV_DMA(s, s == 0 ? 131072 : s * 32768 - 32768); BAR(); }
    }
    TS(41);
}
template <bool ISV>
DI void qkv_body(const QkvSrc& c, const Retile& rt, rsrc_t rws, int mt, int nt, int w, int lane PROFARG) {
    const int t = lane & 15, g = lane >> 4, T = w & 1, cb = w >> 1;
    const unsigned x0 = (4 * T + (cb & 2)) * 4096 + t * 128 + (((g ^ (t >> 1)) << 4) ^ ((cb & 1) << 6));
    const unsigned w0 = (4 * (cb & 1) + g) * 4096 + 2048 + cb * 256 + ((lane >> 2) & 3) * 64 + (g & 1) * 32 + (lane & 3) * 8;
    f32x4 acc[2][4] = {}, cs[2] = {}, sx[2] = {}, sq[2] = {};
    bf8v xA[4], wA[2], xB[4], wB[2];
    u32x4 one = {0x3f803f80u, 0x3f803f80u, 0x3f803f80u, 0x3f803f80u};
    PIN(one);
#define QKV_RD(X, W, j, sl) do { const unsigned xa_ = ((sl) + x0) ^ ((j) << 6), wa_ = ((sl) + w0) ^ ((j) << 14); \
        W[0] = ldstr2(wa_, 1024); X[0] = ldsr<bf8v>(xa_); X[1] = ldsr<bf8v>(xa_ + 4096); W[1] = ldstr2(wa_ ^ 32, 1024); \
        X[2] = ldsr<bf8v>(xa_ ^ 8192); X[3] = ldsr<bf8v>((xa_ ^ 8192) + 4096); } while (0)
#define QKV_MM(X, W) do { _Pragma("unroll") for (int i = 0; i < 4; ++i) _Pragma("unroll") for (int n = 0; n < 2; ++n) \
            acc[n][i] = ISV ? mfma16(X[i], W[n], acc[n][i]) : mfma16(W[n], X[i], acc[n][i]); \
        _Pragma("unroll") for (int n = 0; n < 2; ++n) cs[n] = ISV ? mfma16(asbf(one), W[n], cs[n]) : mfma16(W[n], asbf(one), cs[n]); } while (0)
    WAITBAR(16);
    TS(1);
    unsigned sl = 131072;
    QKV_RD(xA, wA, 0, sl);
#pragma unroll 1
    for (int s = 0; s < 10; ++s) {
        SBAR();
        QKV_RD(xB, wB, 1, sl); SBAR();
        QKV_MM(xA, wA);
#pragma unroll
        for (int i = 0; i < 2; ++i) { sx[i] = mfma16(asbf(one), xA[i], sx[i]); sq[i] = mfma16(xA[i], xA[i], sq[i]); }
        SBAR();
        if (s <= 6) WAITBAR(12); else if (s == 7) WAITBAR(8); else WAITBAR(4);
        const unsigned ps = sl;
        sl = sl == 131072 ? 0 : sl + 32768;
        QKV_RD(xA, wA, 0, sl); SBAR();
        if (s < 5) QKV_DMA(s + 5, ps); else if (s == 5) retile_issue(rt, w);
        SBAR();
        QKV_MM(xB, wB);
        TSW(26 + s);
    }
    TS(2);
    constexpr unsigned L_ST = 0, L_PR = 4096;
    if (g == (t >> 2)) {
#pragma unroll
        for (int i = 0; i < 2; ++i) {
            const float q01 = (t & 1) ? sq[i][1] : sq[i][0], q23 = (t & 1) ? sq[i][3] : sq[i][2];
            ldsw<f32x2>(L_ST + (64 * T + 16 * (cb & 2) + 16 * i + t) * 16 + (cb & 1) * 8, f32x2{sx[i][0], (t & 2) ? q23 : q01});
        }
    }
    LBAR();
    const unsigned pr = L_PR + w * 512;
    {
        const f32x4 u = ldsr<f32x4>(L_ST + (64 * T + lane) * 16);
        const float mean = (u[0] + u[2]) * (1.f / 640.f);
        const float var = __builtin_fmaxf((u[1] + u[3]) * (1.f / 640.f) - mean * mean, 0.f);
        float ra = __builtin_amdgcn_rsqf(var + 1e-5f);
        if (nt < 5) ra *= 0.22360679774997896f * 1.4426950408889634f;
        ldsw<f32x2>(pr + ((lane * 8) ^ ((cb & 2) << 7)), f32x2{ra, -mean * ra});
    }
    const int g64 = 2 * mt + T;
    if (!ISV) {
        f32x2 r[4];
#pragma unroll
        for (int i = 0; i < 4; ++i) r[i] = ldsr<f32x2>(pr + i * 128 + t * 8);
        const bool isq = nt < 5;
        const unsigned hs = isq ? 81920 : 163840;
        const unsigned sbase = isq ? WS_Q + g64 * 2560 : WS_KV + g64 * 5120;
        const int G0 = (isq ? nt : nt - 5) * 32 + cb * 8 + g;
#pragma unroll
        for (int n = 0; n < 2; ++n) {
            const unsigned G = G0 + 4 * n, head = (G * 205) >> 10, dq = G - 5 * head;
#pragma unroll
            for (int q = 0; q < 2; ++q) {
                float o[8];
#pragma unroll
                for (int i = 0; i < 8; ++i) o[i] = acc[n][2 * q + (i >> 2)][i & 3] * r[2 * q + (i >> 2)][0] + r[2 * q + (i >> 2)][1] * cs[n][i & 3];
                st16(rws, head * hs + dq * 512 + t * 16, sbase + (q ^ (cb >> 1)) * 256, u32x4{pk2(o[0], o[1]), pk2(o[2], o[3]), pk2(o[4], o[5]), pk2(o[6], o[7])});
            }
        }
    } else {
#pragma unroll
        for (int q = 0; q < 2; ++q) {
            f32x4 r[4];
#pragma unroll
            for (int i = 0; i < 4; ++i) r[i] = ldsr<f32x4>(pr + (2 * q + (i >> 1)) * 128 + g * 32 + (i & 1) * 16);
#pragma unroll
            for (int n = 0; n < 2; ++n) {
                const unsigned col = (nt - 10) * 128 + cb * 32 + 16 * n + t, head = (col * 3277) >> 16, d = col - 20 * head;
                float o[8];
#pragma unroll
                for (int i = 0; i < 8; ++i) o[i] = acc[n][2 * q + (i >> 2)][i & 3] * r[i >> 1][2 * (i & 1)] + r[i >> 1][2 * (i & 1) + 1] * cs[n][0];
                st16(rws, head * 163840 + g * 320 + d * 16, WS_KV + g64 * 5120 + 2560 + (q ^ (cb >> 1)) * 1280, u32x4{pk2(o[0], o[1]), pk2(o[2], o[3]), pk2(o[4], o[5]), pk2(o[6], o[7])});
            }
        }
    }
}

DI f32x16 qk_tile(u32x4 km, u32x2 kt, u32x4 qm, u32x2 qt, f32x16 c) {
    c = mfma32(asbf(km), asbf(qm), c);
    return __builtin_amdgcn_mfma_f32_32x32x8bf16_1k(__builtin_bit_cast(s4v, kt), __builtin_bit_cast(s4v, qt), c, 0, 0, 0);
}
DI f32x16 lo16(const f32x32& s) { return __builtin_shufflevector(s, s, 0, 1, 2, 3, 4, 5, 6, 7, 8, 9, 10, 11, 12, 13, 14, 15); }
DI f32x16 hi16(const f32x32& s) { return __builtin_shufflevector(s, s, 16, 17, 18, 19, 20, 21, 22, 23, 24, 25, 26, 27, 28, 29, 30, 31); }
DI f32x32 qk_pair(unsigned a0, unsigned at, const u32x8& qm, u32x2 qt, f32x32 c) {
    const u32x2 k0 = ldsr<u32x2>(a0), k1 = ldsr<u32x2>(a0 + 512), kt = ldsr<u32x2>(at);
    const u32x4 km = {k0[0], k0[1], k1[0], k1[1]};
    c = __builtin_amdgcn_mfma_f32_32x32x4bf16_1k(__builtin_bit_cast(s4v, kt), __builtin_bit_cast(s4v, qt), c, 1, 0, 0);
    const f32x16 a = mfma32(asbf(km), asbf(__builtin_shufflevector(qm, qm, 0, 1, 2, 3)), lo16(c));
    const f32x16 b = mfma32(asbf(km), asbf(__builtin_shufflevector(qm, qm, 4, 5, 6, 7)), hi16(c));
    return __builtin_shufflevector(a, b, 0, 1, 2, 3, 4, 5, 6, 7, 8, 9, 10, 11, 12, 13, 14, 15, 16, 17, 18, 19, 20, 21, 22, 23, 24, 25, 26, 27, 28, 29, 30, 31);
}
DI float max16(const f32x16& s, float m) {
#pragma unroll
    for (int r = 0; r < 16; r += 2) m = __builtin_fmaxf(__builtin_fmaxf(s[r], s[r + 1]), m);
    return m;
}
DI f32x16 pv16(u32x4 v, f32x8 pr, f32x16 o) {
    return __builtin_amdgcn_smfmac_f32_32x32x32_bf16(asbf(v), __builtin_bit_cast(bf16v, pr), o, 0xDDDD, 0, 0);
}
#define EX4(P, S) "v_exp_f32 v[" #P "], v[" S "]\n\tv_exp_f32 v[" #P "+1], v[" S "+1]\n\tv_exp_f32 v[" #P "+2], v[" S "+2]\n\tv_exp_f32 v[" #P "+3], v[" S "+3]\n\t"
#define SMF(O, V, P) "v_smfmac_f32_32x32x32_bf16 v[" #O ":" #O "+15], v[" #V ":" #V "+3], v[" #P ":" #P "+7], %[idx]\n\t"
#define STEP(SC, SN, AB, VP, VC, KTRD, VRD, KRD, DOFS) \
    "v_mfma_f32_32x32x4_2b_bf16 v[" #SN ":" #SN "+31], v[188:189], v[184:185], v[32:63] cbsz:1 abid:" #AB "\n\t" KTRD \
    EX4(160, #SC) EX4(164, #SC "+4") \
    SMF(144, VP, 168) \
    EX4(168, #SC "+16") \
    "s_waitcnt lgkmcnt(0)\n\t" \
    SMF(128, 196, 160) \
    EX4(172, #SC "+20") \
    "v_mfma_f32_32x32x16_bf16 v[" #SN ":" #SN "+15], v[192:195], v[176:179], v[" #SN ":" #SN "+15]\n\t" \
    "buffer_load_dwordx4 %[dv], s[40:43], s45 offen offset:" #DOFS " sc1 lds\n\t" \
    EX4(160, #SC "+8") \
    SMF(144, 196, 168) VRD \
    EX4(164, #SC "+12") \
    "v_mfma_f32_32x32x16_bf16 v[" #SN "+16:" #SN "+31], v[192:195], v[180:183], v[" #SN "+16:" #SN "+31]\n\t" KRD \
    EX4(168, #SC "+24") \
    SMF(128, VC, 160) \
    EX4(172, #SC "+28")
#define VR(VH, OL, PRE) PRE "s_mov_b64 exec, s[56:57]\n\tds_read_b128 v[196:199], %[av] offset:" #OL "\n\tds_read_b128 v[" #VH ":" #VH "+3], %[av] offset:" #OL "+640\n\ts_mov_b64 exec, -1\n\t"
#define KR(O, PRE) PRE "ds_read_b64 v[192:193], %[a0] offset:" #O "\n\tds_read_b64 v[194:195], %[a0] offset:" #O "+512\n\t"
#define KT(O, PRE) PRE "ds_read_b64 v[188:189], %[at] offset:" #O "\n\t"
#define ADD(r) "v_add_u32 %[" #r "], s55, %[" #r "]\n\t"
#define CALL "s_call_b64 s[58:59], 8b\n\t"
#define ST16(O) "buffer_store_dwordx4 %[mk], %[l16], s[40:43], s54 offen offset:" #O " sc1\n\t"

DI void phase_attn(const Params& p, const Retile& rt, rsrc_t rws, unsigned T, int b, int w, int lane, int nt80, int mt64, int ntg,
                   unsigned rv0, unsigned rv1, unsigned rsb, u32x4& r0, u32x2& r1 PROFARG) {
    const int t = lane & 31, hh = lane >> 5, wl = w & 3, grp = w >> 2;
    const int head = (b & 7) * 4 + (b >> 6), qb = (b >> 3) & 7;
    const unsigned l16 = lane * 16, gb = grp * 61440, kvb = WS_KV + head * 163840, kvg = kvb + grp * 81920;
    u32x2 tag = {T, ~T};
    if (w < 2) {
        const int n0 = (head * 20 + ((lane & 16) ? 19 : 0)) >> 7;
        const unsigned fo = WS_FQ + (w == 0 ? (5 + 5 * hh + n0) * 16 + (lane & 15) : n0 * 16 + 2 * qb + (lane & 1)) * 128;
        u32x2 f; int s;
        asm volatile("s_movk_i32 %1, " SPINS "\n1:\n\tbuffer_load_dwordx2 %0, %2, %3, 0 offen sc1\n\ts_waitcnt vmcnt(0)\n\tv_cmp_ne_u64 vcc, %0, %4\n\t"
                     "s_cbranch_vccz 2f\n\ts_sub_u32 %1, %1, 1\n\ts_cbranch_scc0 1b\n2:" : "=&v"(f), "=&s"(s) : "v"(fo), "s"(rws), "v"(tag) : "memory", "vcc", "scc");
    }
    u32x8 qm; u32x2 qt;
    {
        unsigned qs = WS_Q + head * 81920 + (qb * 4 + wl) * 2560, va = hh * 1024 + grp * 256 + t * 8, vb = va ^ 256, vc = (lane * 8) ^ (grp * 256);
        unsigned dm = gb + wl * 1024, ds = kvg + wl * 1024;
#define KVP "buffer_load_dwordx4 %[l16], %[rs], %[ds] offen sc1 lds\n\ts_add_u32 m0, m0, 4096\n\ts_add_u32 %[ds], %[ds], 4096\n\t"
        asm volatile("s_mov_b32 m0, %[dm]\n\ts_barrier\n\t" TSA("4")
                     "buffer_load_dwordx2 v[176:177], %[va], %[rs], %[qs] offen sc1\n\tbuffer_load_dwordx2 v[178:179], %[va], %[rs], %[qs] offen offset:512 sc1\n\t"
                     "buffer_load_dwordx2 v[180:181], %[vb], %[rs], %[qs] offen sc1\n\tbuffer_load_dwordx2 v[182:183], %[vb], %[rs], %[qs] offen offset:512 sc1\n\t"
                     "buffer_load_dwordx2 v[184:185], %[vc], %[rs], %[qs] offen offset:2048 sc1\n\t"
                     KVP KVP KVP KVP "s_barrier\n\t" KVP KVP KVP
                     : "=&{v[176:183]}"(qm), "=&{v[184:185]}"(qt), [ds] "+&s"(ds) PFIO
                     : [va] "v"(va), [vb] "v"(vb), [vc] "v"(vc), [rs] "s"(rws), [qs] "s"(qs), [dm] "s"(dm), [l16] "v"(l16) : "memory", "scc" PFCL);
    }
    retile_store(rt, rws, w, lane);
    u32x4 mark = {~0u, ~0u, ~0u, ~0u};
    PIN(mark);
    st16(rws, l16, WS_O + b * 10240 + w * 1280, mark);
    st16(rws, lane < 16 ? l16 : OOB, WS_O + b * 10240 + w * 1280 + 1024, mark);
    unsigned a0 = hh * 1024 + t * 8, at = lane * 8 + 2048, av = gb + 2560 + hh * 320 + (t < 20 ? t : 0) * 16;
    const unsigned ones = t == 20 ? 0x3f803f80u : 0u;
    const unsigned wob = WS_WO + nt80 * 102400;
    u32x8 sa;
    sa[0] = gb + (28 + 4 * wl) * 1024; sa[1] = kvg + (28 + 4 * wl) * 1024;
    sa[2] = 122880 + (w ? w + 1 : 0) * 4096;
    sa[3] = wob;
    sa[4] = 122880 + (w + 1) * 4096; sa[5] = wob + (w + 1) * 4096;
    sa[6] = (w + 9) * 4096 + (w == 0 ? 122880 : -20480);
    sa[7] = (w + 17) * 4096 + (w < 3 ? -20480 : 0);
    u32x4 sb = {(unsigned)grp, rsb, WS_B + qb * QBS + head * 40960 + w * 5120, 10240};
    unsigned dv4 = w == 0 ? l16 : OOB;
    unsigned vfs = (w == 2 && lane == 0) ? WS_FR + b * 8 : OOB, vfe = (b == 0 && w == 0 && lane == 0) ? WS_E : OOB, vfp = WS_FR + w * 256 + t * 8;
    const unsigned fg = (b * 205) >> 10;
    unsigned vcs = (b < 135 && w < 2) ? WS_CS + ((b - fg * 5) * 3584 + fg * 128 + w * 64 + lane) * 4 : OOB;
    unsigned idx = 0xDDDD;
    rsrc_t rx = mkrs(p.x, 2048 * 1280);
    f32x32 S0, S1, O, NM; f32x16 P;
    u32x4 km, vlo, vh0, vh1 = {ones, ones, ones, ones}; u32x2 kt;
    asm volatile(Z8("128") Z8("136") Z8("144") Z8("152") Z8("168") "s_waitcnt vmcnt(9) lgkmcnt(0)\n\ts_barrier"
                 : "+{v[176:183]}"(qm), "+{v[184:185]}"(qt), "={v[96:127]}"(S1), "={v[128:159]}"(O), "={v[160:175]}"(P), "+{v[204:207]}"(vh1), "+{s[44:51]}"(sa), "+{s[52:55]}"(sb),
                   "+v"(dv4), "+v"(vfs), "+v"(vfe), "+v"(vfp), "+v"(vcs), "+v"(idx), "+v"(a0), "+v"(at), "+v"(av), "+v"(rv0), "+v"(rv1), "+s"(rx) :: "memory");
    TS(5);
    {
        const f32x32 r = qk_pair(a0, at, qm, qt, f32x32{});
        a0 += gb; at += gb;
        const f32x2 mm = swap32(max16(lo16(r), r[0]), max16(hi16(r), r[16]));
        const float m = __builtin_fmaxf(mm[0], mm[1]);
        const f32x2 m2 = swap32(m, m);
#pragma unroll
        for (int i = 0; i < 16; ++i) { NM[i] = -m2[0]; NM[16 + i] = -m2[1]; }
        S0 = qk_pair(a0, at, qm, qt, NM);
        kt = ldsr<u32x2>(at);
        const u32x2 n0 = ldsr<u32x2>(a0 + 256), n1 = ldsr<u32x2>(a0 + 768);
        km = u32x4{n0[0], n0[1], n1[0], n1[1]};
        const u32x4 v0 = ldsr<u32x4>(av), v1 = ldsr<u32x4>(av + 640);
#pragma unroll
        for (int i = 0; i < 4; ++i) { vlo[i] = t < 20 ? v0[i] : ones; vh0[i] = t < 20 ? v1[i] : ones; }
        float c = 0.f;
#pragma unroll
        for (int i = 0; i < 4; ++i) c += ldsr<float>(L_CP + (2 * i + (w & 1)) * 256 + lane * 4);
        asm volatile("buffer_store_dword %0, %1, %2, 0 offen sc1" :: "v"(c), "v"(vcs), "s"(rws) : "memory");
    }
    unsigned dv; u32x2 fr;
    asm volatile(
        "s_mov_b32 s56, 0xfffff\n\ts_mov_b32 s57, s56\n\tv_mov_b32 %[dv], %[l16]\n\ts_mov_b32 m0, s44\n\ts_nop 7\n\ts_branch 9f\n"
        "8:\n\ts_cmp_eq_u32 s52, 0\n\ts_cbranch_scc1 2f\n\ts_setprio 1\n2:\n\t"
        STEP(64, 96, 1, 204, 200, KT(5120, ""), VR(204, 1280, ""), KR(5120, ""), 0)
        STEP(96, 64, 0, 200, 204, "", VR(200, 5120, ""), KR(5376, ""), 1024)
        STEP(64, 96, 1, 204, 200, KT(0, ADD(at)), VR(204, 6400, ""), KR(0, ADD(a0)), 2048) "s_setprio 0\n\t"
        STEP(96, 64, 0, 200, 204, "", VR(200, 0, ADD(av)), KR(256, ""), 3072)
        TSI "s_setpc_b64 s[58:59]\n"
        "9:\n\ts_nop 7\n\t" TSS("17") TSF("17") CALL
        "s_add_u32 m0, m0, 16384\n\ts_add_u32 s45, s45, 16384\n\ts_waitcnt vmcnt(11)\n\ts_barrier\n\t" CALL
        "s_sub_u32 m0, m0, 45056\n\ts_add_u32 s45, s45, 16384\n\ts_waitcnt vmcnt(4)\n\ts_barrier\n\t"
        "buffer_store_dwordx2 %[tag], %[vfs], s[40:43], 0 offen sc1\n\t" CALL
        "s_add_u32 m0, m0, 4096\n\ts_add_u32 s45, s45, 4096\n\tbuffer_load_dwordx2 %[fr], %[vfp], s[40:43], 0 offen sc1\n\t" CALL
        "s_mov_b32 m0, s46\n\ts_mov_b32 s45, s47\n\tv_mov_b32 %[dv], %[dv4]\n\ts_movk_i32 s44, " SPINS "\n\ts_waitcnt vmcnt(4)\n"
        "3:\n\tv_cmp_ne_u64 vcc, %[fr], %[tag]\n\ts_cbranch_vccz 4f\n\t"
        "buffer_load_dwordx2 %[fr], %[vfp], s[40:43], 0 offen sc1\n\ts_waitcnt vmcnt(0)\n\ts_sub_u32 s44, s44, 1\n\ts_cbranch_scc0 3b\n"
        "4:\n\ts_barrier\n\t" TSA("6")
        "buffer_store_dwordx2 %[tag], %[vfe], s[40:43], 0 offen sc1\n\t" ST16(0) "s_add_u32 s54, s54, 1024\n\t" ST16(0) ST16(1024) ST16(2048) ST16(3072) CALL
        "s_mov_b32 m0, s48\n\ts_mov_b32 s45, s49\n\tv_mov_b32 %[dv], %[l16]\n\ts_mov_b32 s55, -51200\n\t" CALL
        "s_mov_b32 m0, s50\n\ts_add_u32 s45, s45, 32768\n\ts_movk_i32 s55, 10240\n\ts_waitcnt vmcnt(14)\n\ts_barrier\n\t"
        "buffer_load_dwordx4 %[r0], %[rv0], %[rx], s53 offen\n\tbuffer_load_dwordx2 %[r1], %[rv1], %[rx], s53 offen\n\t" CALL
        "s_mov_b32 m0, s51\n\ts_add_u32 s45, s45, 32768\n\t" CALL
        "s_nop 1\n\t" SMF(144, 204, 168) "s_waitcnt vmcnt(8) lgkmcnt(0)\n\ts_barrier\n\ts_nop 7\n\ts_nop 7\n\ts_nop 7"
        : "+{v[64:95]}"(S0), "+{v[96:127]}"(S1), "+{v[128:159]}"(O), "+{v[160:175]}"(P), "+{v[188:189]}"(kt), "+{v[192:195]}"(km),
          "+{v[196:199]}"(vlo), "+{v[200:203]}"(vh0), "+{v[204:207]}"(vh1), "+{s[44:51]}"(sa), "+{s[52:55]}"(sb),
          [a0] "+&v"(a0), [at] "+&v"(at), [av] "+&v"(av), [dv] "=&v"(dv), [fr] "=&v"(fr), [r0] "=&v"(r0), [r1] "=&v"(r1) PFIO
        : "{v[32:63]}"(NM), "{v[176:183]}"(qm), "{v[184:185]}"(qt), "{s[40:43]}"(rws), [idx] "v"(idx), [l16] "v"(l16), [dv4] "v"(dv4), [mk] "v"(mark), [tag] "v"(tag),
          [vfs] "v"(vfs), [vfe] "v"(vfe), [vfp] "v"(vfp), [rv0] "v"(rv0), [rv1] "v"(rv1), [rx] "s"(rx)
        : "memory", "scc", "vcc", "s56", "s57", "s58", "s59" PFCL);
    TS(7);
    float o[12];
    {
        const unsigned e0 = (w < 6 ? w * 3072 : w * 40960 - 184320) + l16, w2 = w ^ 4, e1 = (w2 < 6 ? w2 * 3072 : w2 * 40960 - 184320) + l16;
#pragma unroll
        for (int i = 0; i < 3; ++i) ldsw<f32x4>(e0 + i * 1024, f32x4{O[16 + 4 * i], O[17 + 4 * i], O[18 + 4 * i], O[19 + 4 * i]});
        LBAR();
#pragma unroll
        for (int i = 0; i < 3; ++i) {
            const f32x4 v = ldsr<f32x4>(e1 + i * 1024);
#pragma unroll
            for (int k = 0; k < 4; ++k) o[4 * i + k] = O[4 * i + k] + v[k];
        }
    }
    {
        float mx = 0.f;
#pragma unroll
        for (int r = 0; r < 12; ++r) mx += __builtin_fabsf(o[r]);
        const bool bad = hh ? !(o[8] < 1.8446744e19f) || !(mx < __builtin_inff()) : !(mx < __builtin_inff());
        if (__builtin_expect(anyl(bad), 0)) {
            float m = -__builtin_inff();
            const unsigned ko = hh * 1024 + t * 8, vo = 2560 + hh * 320 + (t < 20 ? t : 0) * 16;
            const u32x4 qa = {qm[0], qm[1], qm[2], qm[3]}; const u32x2 qe = {hh ? 0u : qt[0], hh ? 0u : qt[1]};
            f32x16 c = {}, A = {};
#pragma unroll 1
            for (int it = 0; it < 128; ++it) {
                const unsigned sb = kvb + ((it & 63) >> 1) * 5120;
                const u32x2 k0 = ld8c(rws, ko, sb + (it & 1) * 256), k1 = ld8c(rws, ko + 512, sb + (it & 1) * 256), k2 = ld8c(rws, t * 8 + 2048, sb + (it & 1) * 256);
                f32x16 s = qk_tile(u32x4{k0[0], k0[1], k1[0], k1[1]}, k2, qa, qe, c);
                if (it < 64) {
                    m = max16(s, m);
                    if (it == 63) {
                        const f32x2 mm = swap32(m, m);
                        m = __builtin_fmaxf(mm[0], mm[1]);
#pragma unroll
                        for (int r = 0; r < 16; ++r) c[r] = -m;
                    }
                } else {
                    u32x4 v0 = ld16c(rws, vo, sb + (it & 1) * 1280), v1 = ld16c(rws, vo + 640, sb + (it & 1) * 1280);
#pragma unroll
                    for (int i = 0; i < 4; ++i) { v0[i] = t < 20 ? v0[i] : ones; v1[i] = t < 20 ? v1[i] : ones; }
#pragma unroll
                    for (int r = 0; r < 16; ++r) s[r] = __builtin_amdgcn_exp2f(s[r]);
                    A = pv16(v0, __builtin_shufflevector(s, s, 0, 1, 2, 3, 4, 5, 6, 7), A);
                    A = pv16(v1, __builtin_shufflevector(s, s, 8, 9, 10, 11, 12, 13, 14, 15), A);
                }
            }
#pragma unroll
            for (int r = 0; r < 12; ++r) o[r] = A[r];
        }
    }
    const float inv = __builtin_amdgcn_rcpf(swap32(o[8], o[8])[1]);
    const unsigned ob = WS_O + (qb * 16 + wl * 4 + grp * 2) * 20480, ov = (t >> 4) * 20480 + (t & 15) * 16;
    st16(rws, ov + hh * 256, ob + head * 512, u32x4{pk2m(o[0] * inv, o[1] * inv), pk2m(o[2] * inv, o[3] * inv), pk2m(o[4] * inv, o[5] * inv), pk2m(o[6] * inv, o[7] * inv)});
    st8(rws, hh ? OOB : ov, ob + (64 + (head >> 1)) * 256 + (head & 1) * 8, u32x2{pk2m(o[8] * inv, o[9] * inv), pk2m(o[10] * inv, o[11] * inv)});
    const unsigned u = (grp ? 21 : 11) + wl;
    dma4c(20480 + u * 4096, l16, rws, WS_WGU + ntg * 143360 + u * 4096);
}

#define MF16(A, W, B) "v_mfma_f32_16x16x32_bf16 v[" A ":" A "+3], v[" W ":" W "+3], v[" B ":" B "+3], v[" A ":" A "+3]\n\t"
#define RD1(W, V, O) "ds_read_b128 v[" W ":" W "+3], " V " offset:" O "\n\t"
#define GUN(n, H, V, O) "s_waitcnt lgkmcnt(6)\n\t" MF16("140+8*" #n, "112+4*" #n, H) MF16("144+8*" #n, "112+4*" #n, H "+8") RD1("112+4*" #n, V, O "+1024*" #n)
#define GUK(H, V, O) GUN(0, H, V, O) GUN(1, H, V, O) GUN(2, H, V, O) GUN(3, H, V, O) GUN(4, H, V, O) GUN(5, H, V, O) GUN(6, H, V, O)
#define LDH(H, S, O) LD1(#H, "%[l16]", S, O) LD1(#H "+4", "%[l16]", S, O "+1024") LD1(#H "+8", "%[lb]", S, O) LD1(#H "+12", "%[lb]", S, O "+1024")
#define RETRY(H, TMP, L) "1:\n\t" CHK(H, TMP, "vcc") "s_cbranch_vccz 2f\n\t" L "s_waitcnt vmcnt(0)\n\ts_sub_u32 s59, s59, 1\n\ts_cbranch_scc0 1b\n\ts_mov_b32 s59, 0\n2:\n\t"
#define GUS(H, PRE, O1, V2, O2, TAIL) \
    "s_waitcnt vmcnt(16)\n" RETRY(H, 196, LDH(H, "s44", "0")) PRE \
    GUK(#H, "%[wa]", O1) GUK(#H "+4", V2, O2) \
    "s_add_u32 s45, s44, 10240\n\ts_add_u32 s44, s44, 2048\n\ts_cmp_lt_u32 s45, s47\n\ts_cbranch_scc0 3f\n\t" LDH(H, "s45", "0") "s_branch 4f\n3:\n\t" TAIL "4:\n\t" TSI
#define LDST LD1("32", "%[l16]", "s51", "0") LD1("36", "%[l16]", "s51", "1024") LD1("40", "%[l16]", "s51", "2048") LD1("44", "%[l16]", "s51", "3072")
DI void phase_gu(rsrc_t rws, int w, int lane, int qb, int nt80, int ntg PROFARG) {
    const int t = lane & 15, g = lane >> 4;
    const unsigned l16 = lane * 16, wd = WS_WD + nt80 * 276480;
    if (ntg == 31) {
#pragma unroll 1
        for (int i = 0; i < 5; ++i) dma3c(20480 + (i * 8 + w) * 3072, l16, rws, wd + (i * 8 + w) * 3072);
        return;
    }
#pragma unroll 1
    for (int u1 = w; u1 < 27; u1 += 8) {
        const unsigned u = u1 < 11 ? u1 : u1 < 17 ? u1 + 4 : u1 + 8;
        dma4c(20480 + u * 4096, l16, rws, WS_WGU + ntg * 143360 + u * 4096);
    }
    const unsigned qbb = WS_B + qb * QBS, hb = qbb + w * 40960, e = w * 64 + lane;
    u32x8 sa = {hb, WS_CS + ntg * 448, wd + w * 4096, hb + 20480, 20480u + w * 4096, wd + 65536 + w * 7168, 86016u + w * 7168, qbb + OFF_HS + w * 4096};
    unsigned wa = 20480 + l16;
    f32x32 A0; f32x16 A1, ST; f32x8 A2;
#define CSL(R) "buffer_load_dword v" #R ", %[ve], s[40:43], s45 offen sc1\n\ts_add_u32 s45, s45, 14336\n\t"
    asm volatile(
        TSQ("59", "63") CSL(112) CSL(113) CSL(114) CSL(115) CSL(116) "s_add_u32 s45, s44, 4096\n\t"
        POLL3(LDH(32, "s44", "0"), LDH(48, "s44", "2048"), LDH(64, "s45", "0"), 32, 48, 64, 196, Z8("140") Z8("148") Z8("156") Z8("164") Z8("172") Z8("180") Z8("188"))
        "s_add_u32 s59, s44, 8192\n\t" LDH(80, "s45", "2048") LDH(96, "s59", "0")
        "v_add_f32 v112, v112, v113\n\tv_add_f32 v114, v114, v115\n\tv_add_f32 v112, v112, v116\n\tv_add_f32 v196, v112, v114\n\ts_movk_i32 s59, " SPINS "\n\ts_barrier\n\t" TSA("11") TSS("35")
        "ds_write_b32 %[vl], v196\n\t"
        RD1("112", "%[wa]", "0") RD1("116", "%[wa]", "1024") RD1("120", "%[wa]", "2048") RD1("124", "%[wa]", "3072") RD1("128", "%[wa]", "4096") RD1("132", "%[wa]", "5120") RD1("136", "%[wa]", "6144")
        "6:\n\t"
        GUS(32, "", "7168", "%[wa]", "14336", LDST)
        GUS(48, "", "21504", "%[wa]", "28672", "s_mov_b32 m0, s48\n\ts_barrier\n\t" DM4("%[l16]", "s46"))
        GUS(64, "", "35840", "%[wa]", "43008", "s_add_u32 m0, s48, 32768\n\ts_add_u32 s46, s46, 32768\n\t" DM4("%[l16]", "s46"))
        GUS(80, "", "50176", "%[wa]", "57344", "s_mov_b32 m0, s50\n\ts_barrier\n\t" DM4("%[l16]", "s49") "s_add_u32 m0, s50, 4096\n\ts_add_u32 s49, s49, 4096\n\t" DM3("%[l16]", "s49"))
        GUS(96, "v_add_u32 %[wa], 0xfc00, %[wa]\n\t", "0", "%[wn]", "0", "s_branch 7f\n")
        "v_mov_b32 %[wa], %[wn]\n\ts_branch 6b\n"
        "7:\n\t" TSA("12") "s_waitcnt vmcnt(15)\n" RETRY(32, 196, LDST) "s_waitcnt lgkmcnt(0)\n\ts_nop 7"
        : "=&{v[140:171]}"(A0), "=&{v[172:187]}"(A1), "=&{v[188:195]}"(A2), "=&{v[32:47]}"(ST), "+{s[44:51]}"(sa), [wa] "+&v"(wa) PFIO
        : "{s[40:43]}"(rws), [l16] "v"(l16), [lb] "v"(l16 + 20480), [wn] "v"(l16 + 92160), [ve] "v"(e < 112 ? e * 4 : OOB), [vl] "v"(e * 4)
        : "memory", "scc", "vcc", "s59", "s60", "s61", "s62", "s63", "s64", "s65", VCL48, VCL64, VCL80, VCL96, VCL112, VCL128, "v196" PFCL);
    float ra, rb;
    {
        float s[2], q[2];
#pragma unroll
        for (int i = 0; i < 2; ++i) {
            s[i] = (ST[8 * i] + ST[8 * i + 2]) + (ST[8 * i + 4] + ST[8 * i + 6]);
            q[i] = (ST[8 * i + 1] + ST[8 * i + 3]) + (ST[8 * i + 5] + ST[8 * i + 7]);
        }
        const f32x2 ss = swap32(s[0], s[1]), qq = swap32(q[0], q[1]);
        const float mean = sum16(ss[0] + ss[1]) * (1.f / 640.f);
        const float var = __builtin_fmaxf(sum16(qq[0] + qq[1]) * (1.f / 640.f) - mean * mean, 0.f);
        ra = __builtin_amdgcn_rsqf(var + 1e-5f); rb = -mean * ra;
    }
    unsigned fo[14];
#pragma unroll
    for (int n = 0; n < 7; ++n) {
        const f32x4 cg = ldsr<f32x4>(n * 64 + (g & 1) * 16), cu = ldsr<f32x4>(n * 64 + 32 + (g & 1) * 16);
        float o[4];
#pragma unroll
        for (int i = 0; i < 4; ++i) {
            const f32x2 gu = n < 4 ? swap32(A0[8 * (n & 3) + i], A0[8 * (n & 3) + 4 + i]) : n < 6 ? swap32(A1[8 * (n & 1) + i], A1[8 * (n & 1) + 4 + i]) : swap32(A2[i], A2[4 + i]);
            const float gt = gu[0] * ra + rb * cg[i], up = gu[1] * ra + rb * cu[i];
            o[i] = (gt > 0.f ? gt : gt * 0.f) * up;
        }
        fo[2 * n] = pk2m(o[0], o[1]); fo[2 * n + 1] = pk2m(o[2], o[3]);
        const unsigned fv = (g >> 1) * 55296 + t * 16, fs = qbb + OFF_F + w * 110592 + ntg * 256;
        if (n & 1) st16(rws, fv + (g & 1) * 7936, fs + (n - 1) * 7936, u32x4{fo[2 * n - 2], fo[2 * n - 1], fo[2 * n], fo[2 * n + 1]});
        if (n == 6) st8(rws, ntg < 30 ? fv + (g & 1) * 8 : OOB, fs + 6 * 7936, u32x2{fo[12], fo[13]});
    }
}

#define G8N(n, W, B, V, O) "s_waitcnt lgkmcnt(9)\n\t" MF16("120+4*" #n, W "+4*" #n, B) RD1(W "+4*" #n, V, O "+1024*" #n)
#define G8K(W, B, V, O) G8N(0, W, B, V, O) G8N(1, W, B, V, O) G8N(2, W, B, V, O) G8N(3, W, B, V, O) G8N(4, W, B, V, O)
#define LDB(B, S, S2, S3) LD1(#B, "v141", S, "0") LD1(#B "+4", "v141", S, "1024") LD1(#B "+8", "v141", S2, "2048") LD1(#B "+12", "v141", S3, "3072")
#define DUP(N, S) "s_cmp_eq_u32 s45, " #N "\n\ts_cselect_b32 s51, 1024, 0\n\ts_sub_u32 s51, " S ", s51\n\t"
#define BRK(N) "s_cmp_eq_u32 s45, " #N "\n\ts_cbranch_scc1 9f\n\t"
#define G8S(SLOT, B, BN, LA, LN, BRK1, BRK2, LN4, LR4) \
    G8K("80", #B, LA, "20480") G8K("100", #B "+4", LA, "30720") BRK1 \
    "s_cmp_lt_u32 s45, s47\n\ts_cbranch_scc1 7f\n\ts_cmp_gt_u32 s45, 8\n\ts_cbranch_scc1 3f\n\ts_waitcnt vmcnt(0)\n3:\n\ts_waitcnt vmcnt(9)\n" \
    RETRY(BN, 140, LN4) \
    "s_waitcnt lgkmcnt(0)\n\ts_barrier\n\ts_cmp_gt_u32 s45, 12\n\ts_cbranch_scc0 7f\n\t" \
    "s_add_u32 m0, s48, " #SLOT "*40960\n\ts_add_u32 s50, s46, 4096\n\t" DM4("v141", "s46") "s_add_u32 m0, m0, 4096\n\ts_add_u32 s46, s46, 40960\n\t" DM1("v141", "s50", 0) \
    "7:\n\t" G8K("80", #B "+8", LN, "0") BRK2 G8K("100", #B "+12", LN, "10240") \
    "s_add_u32 s50, s49, 8192\n\ts_add_u32 s49, s49, 4096\n\ts_sub_u32 s45, s45, 4\n\ts_cmp_gt_u32 s45, 8\n\ts_cbranch_scc0 8f\n\t" LR4 "8:\n\t" TSI
#define RD5(W, V, O) RD1(W, V, O) RD1(W "+4", V, O "+1024") RD1(W "+8", V, O "+2048") RD1(W "+12", V, O "+3072") RD1(W "+16", V, O "+4096")
#define G80_BODY \
    ".Lg80:\n\ts_add_u32 s49, s44, 4096\n\ts_add_u32 s50, s44, 8192\n\ts_cmp_gt_u32 s45, 12\n\ts_cselect_b32 s51, 0, 2048\n\ts_sub_u32 s51, s50, s51\n\t" \
    POLL3(LDB(32, "s44", "s44", "s44"), LDB(48, "s49", "s49", "s49"), LDB(64, "s50", "s51", "s51"), 32, 48, 64, 140, Z8("120") Z8("128") Z4("136")) \
    "s_movk_i32 s59, " SPINS "\n\ts_barrier\n\t" TSM TSS("45") RD5("80", "v142", "0") RD5("100", "v142", "10240") \
    "6:\n\t" \
    G8S(0, 32, 48, "v142", "v143", "", BRK(3), LDB(48, "s49", "s49", "s49"), DUP(11, "s50") LDB(32, "s50", "s50", "s51")) \
    G8S(1, 48, 64, "v143", "v144", "", "", LDB(64, "s49", "s49", "s49"), LDB(48, "s50", "s50", "s50")) \
    G8S(2, 64, 32, "v144", "v142", BRK(2), "", DUP(7, "s49") LDB(32, "s49", "s49", "s51"), LDB(64, "s50", "s50", "s50")) \
    "s_branch 6b\n9:\n\ts_waitcnt lgkmcnt(0)\n\ts_nop 7\n\ts_setpc_b64 s[56:57]\n"
template <int PS>
DI void g80(rsrc_t rws, int w, int lane, int nt, unsigned ab, u32x4& r0, u32x2& r1 PROFARG) {
    const int tg = w & 3, kh = w >> 2;
    const unsigned l16 = lane * 16, la = kh * 5120 + l16;
    u32x4 sa = {ab, PS ? 27u : 10u, WS_WD + nt * 276480 + w * 5120 + 3 * 40960, PS ? 5u : 100u};
    f32x16 A; f32x4 a4;
    asm volatile(PFT TSQ("%[ti]*4/5+45", "%[ti]*4/5+49") "s_call_b64 s[56:57], .Lg80"
        : "=&{v[120:135]}"(A), "=&{v[136:139]}"(a4), "+{s[44:47]}"(sa) PFIO
        : "{s[40:43]}"(rws), "{s48}"(20480 + w * 5120), "{v141}"(l16), "{v142}"(la + (PS ? 20480 : 122880)), "{v143}"(la + (PS ? 61440 : 20480)), "{v144}"(la + (PS ? 102400 : 81920)) PFM(PS ? 13 : 8)
        : "memory", "scc", "vcc", "s49", "s50", "s51", "s56", "s57", "s59", "s60", "s61", "s62", "s63", "s64", "s65", VCL32, VCL48, VCL64, VCL80, VCL96, VCL112L, "v140" PFCL);
    if (PS) TS(14); else TS(9);
    const unsigned ex = tg * 5120 + l16;
    f32x4 a0 = {A[0], A[1], A[2], A[3]}, a1 = {A[4], A[5], A[6], A[7]};
    const f32x4 a2 = {A[8], A[9], A[10], A[11]}, a3 = {A[12], A[13], A[14], A[15]};
    if (kh == 0) { ldsw<f32x4>(ex + 2048, a2); ldsw<f32x4>(ex + 3072, a3); }
    else { ldsw<f32x4>(ex, a0); ldsw<f32x4>(ex + 1024, a1); ldsw<f32x4>(ex + 4096, a4); a0 = a2; a1 = a3; }
    LBAR();
    a0 += ldsr<f32x4>(ex + kh * 2048); a1 += ldsr<f32x4>(ex + kh * 2048 + 1024); a4 += ldsr<f32x4>(ex + 4096);
    r0 = u32x4{pk2(a0[0] + bflo(r0[0]), a0[1] + bfhi(r0[0])), pk2(a0[2] + bflo(r0[1]), a0[3] + bfhi(r0[1])),
               pk2(a1[0] + bflo(r0[2]), a1[1] + bfhi(r0[2])), pk2(a1[2] + bflo(r0[3]), a1[3] + bfhi(r0[3]))};
    r1 = u32x2{pk2(a4[0] + bflo(r1[0]), a4[1] + bfhi(r1[0])), pk2(a4[2] + bflo(r1[1]), a4[3] + bfhi(r1[1]))};
}

__global__ void __launch_bounds__(512, 2) block_kernel(Params p) {
    __shared__ __attribute__((aligned(16))) char lds[163840];
    asm volatile("" :: "v"((LAS char*)lds) : "memory");
    const int lane = threadIdx.x & 63, w = __builtin_amdgcn_readfirstlane(threadIdx.x >> 6), b = blockIdx.x;
    const rsrc_t rws = mkrs(p.ws, WS_END);
    const int xcd = b & 7, i = b >> 3;
    unsigned T;
    Retile rt;
    {
        const int mt = (xcd >> 1) * 4 + (i & 3), nt = (xcd & 1) * 8 + (i >> 2);
        QkvSrc c;
        qkv_start(c, p, mt, nt, w, lane PROFPASS);
        asm volatile("s_load_dword %0, %1, 0\n\ts_waitcnt lgkmcnt(0)\n\ts_add_u32 %0, %0, 1" : "=&s"(T) : "s"(p.ws + WS_E) : "memory", "scc");
        TS(42);
        retile_setup(rt, p, b, w, lane);
        TS(43);
        if (nt == 15) retile_issue(rt, w);
        else if (nt >= 10) qkv_body<true>(c, rt, rws, mt, nt, w, lane PROFPASS);
        else qkv_body<false>(c, rt, rws, mt, nt, w, lane PROFPASS);
        VBAR(0);
        if (w == 2) st8(rws, lane == 0 ? 0 : OOB, WS_FQ + (nt * 16 + mt) * 128, u32x2{T, ~T});
        TS(3);
    }
    const int mt = (xcd >> 1) * 8 + (i & 7), nt = (xcd & 1) * 4 + (i >> 3), ntg = (xcd & 1) * 16 + (i & 3) * 4 + (i >> 3);
    const int tg = w & 3, kh = w >> 2, t = lane & 15, g = lane >> 4, qb = mt >> 2, tk = (mt & 3) * 4 + tg;
    const unsigned rv0 = t * 1280 + (nt & 1) * 32 + kh * 64 + g * 16, rv1 = kh ? OOB : t * 1280 + ((nt & 1) ? 0 : 128) + g * 8;
    const unsigned rsb = (mt * 64 + tg * 16) * 1280 + nt * 160, qbb = WS_B + qb * QBS;
    u32x4 r0; u32x2 r1;
    phase_attn(p, rt, rws, T, b, w, lane, nt, mt, ntg, rv0, rv1, rsb, r0, r1 PROFPASS);
    g80<0>(rws, w, lane, nt, WS_O + (mt * 4 + tg) * 20480 + kh * 10240, r0, r1 PROFPASS);
    {
#pragma unroll
        for (int k = 0; k < 4; ++k) r0[k] = nomark(r0[k]);
        r1[0] = nomark(r1[0]); r1[1] = nomark(r1[1]);
        const unsigned hs = qbb + tk * 20480 + nt * 256;
        st16(rws, (2 * g + kh) * 2048 + t * 16, hs, r0);
        st8(rws, kh ? OOB : (8 + (g >> 1)) * 2048 + t * 16 + (g & 1) * 8, hs, r1);
        float s = 0.f, q = 0.f;
#pragma unroll
        for (int k = 0; k < 6; ++k) {
            const unsigned u = k < 4 ? r0[k] : kh ? 0u : r1[k - 4];
            const float a = bflo(u), c = bfhi(u);
            s += a + c; q += a * a + c * c;
        }
        const f32x2 y = swap32(s, q);
        const float z = sum16(y[0] + y[1]);
        const f32x2 v = swap32(z, z);
        st8(rws, g == 0 ? t * 16 + kh * 8 : OOB, qbb + OFF_HS + tk * 2048 + nt * 256, u32x2{nomark(__float_as_uint(v[0])), nomark(__float_as_uint(v[1]))});
    }
    TS(10);
    unsigned fb = qbb + OFF_F + tk * 55296 + kh * 27648;
    PINS(fb);
    phase_gu(rws, w, lane, qb, nt, ntg PROFPASS);
    g80<1>(rws, w, lane, nt, fb, r0, r1 PROFPASS);
    asm volatile("buffer_store_dwordx4 %0, %2, %4, %5 offen\n\tbuffer_store_dwordx2 %1, %3, %4, %5 offen" :: "v"(r0), "v"(r1), "v"(rv0), "v"(rv1), "s"(mkrs(p.out, 2048 * 1280)), "s"(rsb) : "memory");
    asm volatile("s_endpgm\n" G80_BODY ::: "memory");
}

extern "C" void kernel_launch(void* const* d_in, const int* in_sizes, int n_in, void* d_out, int out_size, void* d_ws, size_t ws_size, hipStream_t stream) {
    Params p;
    p.x = (const char*)d_in[0]; p.wqkv = (const char*)d_in[1]; p.wo = (const char*)d_in[2];
    p.wg = (const char*)d_in[4]; p.wu = (const char*)d_in[5]; p.wd = (const char*)d_in[6];
    p.out = (char*)d_out; p.ws = (char*)d_ws;
    block_kernel<<<256, 512, 0, stream>>>(p);
}
```

```cpp
#include <hip/hip_runtime.h>
#include <hip/hip_bf16.h>
#include <stdint.h>

#define DI __device__ __forceinline__
typedef __attribute__((ext_vector_type(2))) __bf16 bf2v;
typedef __attribute__((ext_vector_type(8))) __bf16 bf8v;
typedef __attribute__((ext_vector_type(16))) __bf16 bf16v;
typedef __attribute__((ext_vector_type(4))) short s4v;
typedef __attribute__((ext_vector_type(2))) float f32x2;
typedef __attribute__((ext_vector_type(4))) float f32x4;
typedef __attribute__((ext_vector_type(8))) float f32x8;
typedef __attribute__((ext_vector_type(16))) float f32x16;
typedef __attribute__((ext_vector_type(32))) float f32x32;
typedef __attribute__((ext_vector_type(2))) unsigned u32x2;
typedef __attribute__((ext_vector_type(4))) unsigned u32x4;
typedef __attribute__((ext_vector_type(8))) unsigned u32x8;
typedef __attribute__((ext_vector_type(16))) unsigned u32x16;
typedef __attribute__((ext_vector_type(32))) unsigned u32x32;
typedef __amdgpu_buffer_rsrc_t rsrc_t;
#define LAS __attribute__((address_space(3)))

constexpr unsigned WS_Q = 0;
constexpr unsigned WS_KV = WS_Q + 32 * 81920;
constexpr unsigned WS_O = WS_KV + 32 * 163840;
constexpr unsigned WS_B = WS_O + 128 * 20480;
constexpr unsigned QBS = 32 * 40960, OFF_HS = 16 * 20480, OFF_F = OFF_HS + 16 * 2048;
constexpr unsigned WS_WO = WS_B + 8 * QBS;
constexpr unsigned WS_WD = WS_WO + 8 * 102400;
constexpr unsigned WS_WGU = WS_WD + 8 * 276480;
constexpr unsigned WS_CS = WS_WGU + 32 * 143360;
constexpr unsigned WS_FQ = WS_CS + 5 * 3584 * 4;
constexpr unsigned WS_FR = WS_FQ + 256 * 128;
constexpr unsigned WS_XS = WS_FR + 2048;
constexpr unsigned WS_E = WS_XS + 2048 * 16;
constexpr unsigned WS_END = WS_E + 128;
constexpr unsigned OOB = 0x40000000u;
constexpr int SPIN = 1 << 14;

struct Params {
    const char* x; const char* wqkv; const char* wo; const char* wg; const char* wu; const char* wd;
    char* out; char* ws;
};

#define SBAR() __builtin_amdgcn_sched_barrier(0)
#define PIN(x) asm volatile("" : "+v"(x))
#define WAITBAR(N) asm volatile("s_waitcnt vmcnt(" #N ") lgkmcnt(0)\n\ts_barrier" ::: "memory")
#define VBAR(N) asm volatile("s_waitcnt vmcnt(" #N ")\n\ts_barrier" ::: "memory")
#define LBAR() asm volatile("s_waitcnt lgkmcnt(0)\n\ts_barrier" ::: "memory")
#define BAR() asm volatile("s_barrier" ::: "memory")

DI rsrc_t mkrs(const char* p, unsigned bytes) { return __builtin_amdgcn_make_buffer_rsrc((void*)p, (short)0, (int)bytes, 0x00020000); }
template <class T> DI T ldsr(unsigned a) { return *(const LAS T*)(uintptr_t)a; }
template <class T> DI void ldsw(unsigned a, T v) { *(LAS T*)(uintptr_t)a = v; }
DI s4v ldstr(unsigned a) { return __builtin_amdgcn_ds_read_tr16_b64_v4i16((LAS s4v*)(uintptr_t)a); }
DI bf8v ldstr2(unsigned a, unsigned d) {
    s4v lo = ldstr(a), hi = ldstr(a + d);
    return __builtin_bit_cast(bf8v, __builtin_shufflevector(lo, hi, 0, 1, 2, 3, 4, 5, 6, 7));
}
DI f32x16 mfma32(bf8v a, bf8v b, f32x16 c) { return __builtin_amdgcn_mfma_f32_32x32x16_bf16(a, b, c, 0, 0, 0); }
DI f32x4 mfma16(bf8v a, bf8v b, f32x4 c) { return __builtin_amdgcn_mfma_f32_16x16x32_bf16(a, b, c, 0, 0, 0); }
DI bf8v asbf(u32x4 v) { return __builtin_bit_cast(bf8v, v); }
DI unsigned pk2(float lo, float hi) { f32x2 v = {lo, hi}; return __builtin_bit_cast(unsigned, __builtin_convertvector(v, bf2v)); }
DI unsigned nomark(unsigned u) { return u < 0xfffffffeu ? u : 0xfffffffeu; }
DI unsigned pk2m(float lo, float hi) { return nomark(pk2(lo, hi)); }
DI float bflo(unsigned u) { return __uint_as_float(u << 16); }
DI float bfhi(unsigned u) { return __uint_as_float(u & 0xffff0000u); }
DI f32x2 swap32(float a, float b) { auto r = __builtin_amdgcn_permlane32_swap(__float_as_uint(a), __float_as_uint(b), false, false); return f32x2{__uint_as_float(r[0]), __uint_as_float(r[1])}; }
DI float sum16(float a) { auto r = __builtin_amdgcn_permlane16_swap(__float_as_uint(a), __float_as_uint(a), false, false); return __uint_as_float(r[0]) + __uint_as_float(r[1]); }
DI bool anyl(bool c) { return __builtin_amdgcn_ballot_w64(c) != 0; }
DI void st16(rsrc_t r, unsigned voff, unsigned soff, u32x4 v) { asm volatile("buffer_store_dwordx4 %0, %1, %2, %3 offen sc1\n\ts_nop 1" :: "v"(v), "v"(voff), "s"(r), "s"(soff) : "memory"); }
DI void st8(rsrc_t r, unsigned voff, unsigned soff, u32x2 v) { asm volatile("buffer_store_dwordx2 %0, %1, %2, %3 offen sc1" :: "v"(v), "v"(voff), "s"(r), "s"(soff) : "memory"); }
DI void st4(rsrc_t r, unsigned voff, unsigned soff, unsigned v) { asm volatile("buffer_store_dword %0, %1, %2, %3 offen sc1" :: "v"(v), "v"(voff), "s"(r), "s"(soff) : "memory"); }
DI u32x4 ld16c(rsrc_t r, unsigned voff, unsigned soff) { return __builtin_amdgcn_raw_buffer_load_b128(r, voff, soff, 16); }
DI u32x2 ld8c(rsrc_t r, unsigned voff, unsigned soff) { return __builtin_amdgcn_raw_buffer_load_b64(r, voff, soff, 16); }
DI void dma1(unsigned m0v, unsigned voff, rsrc_t r, unsigned soff) {
    asm volatile("s_mov_b32 m0, %0\n\ts_nop 0\n\tbuffer_load_dwordx4 %1, %2, %3 offen lds" :: "s"(m0v), "v"(voff), "s"(r), "s"(soff) : "memory"); }
DI void dma1c(unsigned m0v, unsigned voff, rsrc_t r, unsigned soff) {
    asm volatile("s_mov_b32 m0, %0\n\ts_nop 0\n\tbuffer_load_dwordx4 %1, %2, %3 offen sc1 lds" :: "s"(m0v), "v"(voff), "s"(r), "s"(soff) : "memory"); }
DI void dma3c(unsigned m0v, unsigned voff, rsrc_t r, unsigned soff) {
    asm volatile("s_mov_b32 m0, %0\n\ts_nop 0\n\tbuffer_load_dwordx4 %1, %2, %3 offen sc1 lds\n\tbuffer_load_dwordx4 %1, %2, %3 offen offset:1024 sc1 lds\n\t"
                 "buffer_load_dwordx4 %1, %2, %3 offen offset:2048 sc1 lds" :: "s"(m0v), "v"(voff), "s"(r), "s"(soff) : "memory"); }
DI void dma4c(unsigned m0v, unsigned voff, rsrc_t r, unsigned soff) {
    asm volatile("s_mov_b32 m0, %0\n\ts_nop 0\n\tbuffer_load_dwordx4 %1, %2, %3 offen sc1 lds\n\tbuffer_load_dwordx4 %1, %2, %3 offen offset:1024 sc1 lds\n\t"
                 "buffer_load_dwordx4 %1, %2, %3 offen offset:2048 sc1 lds\n\tbuffer_load_dwordx4 %1, %2, %3 offen offset:3072 sc1 lds" :: "s"(m0v), "v"(voff), "s"(r), "s"(soff) : "memory"); }
#define SPINS "0x4000"
#define PINS(x) asm volatile("" : "+s"(x))
#define CHK(B, TMP, DST) \
    "v_max3_u32 v" #TMP ", v[" #B "], v[" #B "+1], v[" #B "+2]\n\tv_max3_u32 v" #TMP ", v[" #B "+3], v[" #B "+4], v" #TMP "\n\t" \
    "v_max3_u32 v" #TMP ", v[" #B "+5], v[" #B "+6], v" #TMP "\n\tv_max3_u32 v" #TMP ", v[" #B "+7], v[" #B "+8], v" #TMP "\n\t" \
    "v_max3_u32 v" #TMP ", v[" #B "+9], v[" #B "+10], v" #TMP "\n\tv_max3_u32 v" #TMP ", v[" #B "+11], v[" #B "+12], v" #TMP "\n\t" \
    "v_max3_u32 v" #TMP ", v[" #B "+13], v[" #B "+14], v" #TMP "\n\tv_max_u32 v" #TMP ", v[" #B "+15], v" #TMP "\n\t" \
    "v_cmp_eq_u32 " DST ", -1, v" #TMP "\n\t"
#define LD1(R, V, S, O) "buffer_load_dwordx4 v[" R ":" R "+3], " V ", s[40:43], " S " offen offset:" O " sc1\n\t"
#define DM1(V, S, O) "buffer_load_dwordx4 " V ", s[40:43], " S " offen offset:" #O " sc1 lds\n\t"
#define DM3(V, S) DM1(V, S, 0) DM1(V, S, 1024) DM1(V, S, 2048)
#define DM4(V, S) DM3(V, S) DM1(V, S, 3072)
#define POLL3(L0, L1, L2, B0, B1, B2, TMP, FILL) \
    "s_mov_b64 s[60:61], -1\n\ts_mov_b64 s[62:63], -1\n\ts_mov_b64 s[64:65], -1\n\ts_movk_i32 s59, " SPINS "\n" \
    "1:\n\ts_mov_b64 exec, s[60:61]\n\ts_cbranch_execz 2f\n\t" L0 \
    "2:\n\ts_mov_b64 exec, s[62:63]\n\ts_cbranch_execz 3f\n\t" L1 \
    "3:\n\ts_mov_b64 exec, s[64:65]\n\ts_cbranch_execz 4f\n\t" L2 \
    "4:\n\ts_mov_b64 exec, -1\n\t" FILL "s_waitcnt vmcnt(0)\n\t" TSP \
    CHK(B0, TMP, "s[60:61]") CHK(B1, TMP, "s[62:63]") CHK(B2, TMP, "s[64:65]") \
    "s_or_b64 vcc, s[60:61], s[62:63]\n\ts_or_b64 vcc, vcc, s[64:65]\n\ts_cbranch_scc0 5f\n\t" \
    "s_sub_u32 s59, s59, 1\n\ts_cbranch_scc0 1b\n" \
    "5:\n\t"
#define Z2(R) "v_mov_b64 v[" R ":" R "+1], 0\n\t"
#define Z4(R) Z2(R) Z2(R "+2")
#define Z8(R) Z4(R) Z4(R "+4")

#define VCL32 "v32", "v33", "v34", "v35", "v36", "v37", "v38", "v39", "v40", "v41", "v42", "v43", "v44", "v45", "v46", "v47"
#define VCL48 "v48", "v49", "v50", "v51", "v52", "v53", "v54", "v55", "v56", "v57", "v58", "v59", "v60", "v61", "v62", "v63"
#define VCL64 "v64", "v65", "v66", "v67", "v68", "v69", "v70", "v71", "v72", "v73", "v74", "v75", "v76", "v77", "v78", "v79"
#define VCL80 "v80", "v81", "v82", "v83", "v84", "v85", "v86", "v87", "v88", "v89", "v90", "v91", "v92", "v93", "v94", "v95"
#define VCL96 "v96", "v97", "v98", "v99", "v100", "v101", "v102", "v103", "v104", "v105", "v106", "v107", "v108", "v109", "v110", "v111"
#define VCL112 "v112", "v113", "v114", "v115", "v116", "v117", "v118", "v119", "v120", "v121", "v122", "v123", "v124", "v125", "v126", "v127"
#define VCL128 "v128", "v129", "v130", "v131", "v132", "v133", "v134", "v135", "v136", "v137", "v138", "v139"
#define VCL160 "v160", "v161", "v162", "v163", "v164", "v165", "v166", "v167", "v168", "v169", "v170", "v171", "v172", "v173", "v174", "v175"
#define VCL112L "v112", "v113", "v114", "v115", "v116", "v117", "v118", "v119"

#define PROFARG
#define PROFPASS
#define TSA(i)
#define PFIO
#define PFCL
#define TSM
#define PFM(i)
#define PFT
#define TS(i)

#define TSF(i)
#define TSI
#define TSS(i)
#define TSW(i)
#define TSP
#define TSQ(a, b)
constexpr unsigned L_RT = 0, L_SM = 32768, L_CP = 49152;
struct RtTab { unsigned v[2048][16]; };
constexpr RtTab make_rt() {
    RtTab t{};
    for (unsigned b = 0; b < 256; ++b) for (unsigned w = 0; w < 8; ++w) for (unsigned f = 0; f < 4; ++f) {
        unsigned A = 0, M = 0, SO = 0, R0 = 0, R1 = 0;
        if (b < 135) {
            const unsigned fg = b / 5, ks = (b % 5) * 4 + (w >> 1), j = ks >> 1, o = fg * 8 + (w & 1) * 4 + f;
            A = (320 * (ks & 1) + (j < 8 ? (j & 1) * 32 + (j >> 1) * 8 : 8 * j) + (f & 1) * 160) * 3456 + fg * 128 + (w & 1) * 64;
            M = (j < 8 ? 96 : 16) * 3456;
            SO = WS_WGU + (o / 7) * 143360 + ks * 7168 + (o % 7) * 1024;
            R0 = 16 * f;
        } else {
            const unsigned v = (b - 135) * 8 + w;
            const bool isWo = v < 200;
            const unsigned vv = v < 740 ? (isWo ? v : v - 200) : 0, ks = vv / 10, cu = vv % 10, NK = isWo ? 10 : 27;
            const unsigned c = 4 * ks + f, j = c / 31, ng = c % 31;
            A = (isWo ? (ks >= 16 ? 40 * c - 2544 : 20 * (c >> 1) + 4 * (c & 1)) : 56 * ng + 16 * (j >> 1) + 4 * (j & 1)) * 1280 + cu * 128;
            M = (isWo ? (ks >= 16 ? 20 : 8) : (j < 6 ? 8 : 4)) * 1280;
            const unsigned seg = 2 * cu + (f >> 1), a = seg / 5, sm = seg % 5, fi = f & 1;
            const unsigned tile = 2 * a + (sm == 2 ? fi : sm > 2), blk = sm == 2 ? 4 : (sm == 1 || sm == 4) ? 2 + fi : fi;
            SO = (isWo ? WS_WO : WS_WD) + (tile * NK * 10 + (ks >= NK ? (ks - NK) * 10 + 5 : ks * 10) + blk) * 1024;
            R0 = (f >> 1) * 64 + (sm == 2 ? 32 : 8) * fi; R1 = sm == 2 ? 8 : 16;
        }
        unsigned* r = t.v[b * 8 + w];
        r[f] = A + 3072 - 1024 * f; r[4 + f] = M; r[8 + f] = SO; r[12 + f] = (L_RT + w * 4096 + R0) | R1 << 16;
    }
    return t;
}
static __device__ const __attribute__((aligned(64))) RtTab RT_TAB = make_rt();
struct Retile { rsrc_t ra, rb; u32x16 t; unsigned vlo, hi; };
DI void retile_setup(Retile& r, const Params& p, unsigned b, unsigned lane) {
    const bool gu = b < 135;
    const char *pa, *pb; unsigned n;
    asm("s_cmpk_lt_u32 %3, 160\n\ts_cselect_b64 %0, %4, %5\n\ts_cselect_b32 %2, %9, %8\n\ts_cmpk_lt_u32 %3, 135\n\ts_cselect_b64 %1, %7, %0\n\ts_cselect_b64 %0, %6, %0\n\ts_cselect_b32 %2, %8, %2"
        : "=&s"(pa), "=&s"(pb), "=&s"(n) : "s"(b), "s"(p.wo), "s"(p.wd), "s"(p.wg), "s"(p.wu), "s"(640 * 3456 + 3072), "n"(640 * 1280 + 3072) : "scc");
    r.ra = mkrs(pa - 3072, n); r.rb = mkrs(pb - 3072, n);
    r.vlo = gu ? ((lane >> 2) & 7) * 3456 + (lane & 3) * 16 : ((lane >> 3) & 3) * 1280 + (lane & 7) * 16;
    r.hi = lane >> 5;
    PIN(r.vlo); PIN(r.hi); PINS(r.ra); PINS(r.rb);
}
DI void retile_issue(const Retile& r, int w) {
    asm volatile("s_mov_b32 m0, %0\n\ts_nop 0\n\tbuffer_load_dwordx4 %1, %5, 0 offen lds\n\tbuffer_load_dwordx4 %2, %5, 0 offen offset:1024 lds\n\t"
                 "buffer_load_dwordx4 %3, %6, 0 offen offset:2048 lds\n\tbuffer_load_dwordx4 %4, %6, 0 offen offset:3072 lds"
                 :: "s"(L_RT + w * 4096), "v"(r.vlo + r.t[0] + r.hi * r.t[4]), "v"(r.vlo + r.t[1] + r.hi * r.t[5]), "v"(r.vlo + r.t[2] + r.hi * r.t[6]),
                    "v"(r.vlo + r.t[3] + r.hi * r.t[7]), "s"(r.ra), "s"(r.rb) : "memory");
}
DI u32x32 retile_read(const Retile& r, unsigned b, int w, unsigned lane) {
    const u32x4 one = {0x3f803f80u, 0x3f803f80u, 0x3f803f80u, 0x3f803f80u};
    const unsigned f = lane & 3, row = 8 * (lane >> 4) + ((lane >> 2) & 3), dl = b < 135 ? 256 : 512;
    const unsigned rb = b < 135 ? (f >> 1) * 2048 + row * 64 + (f & 1) * 8 : row * 128;
    u32x32 o; f32x4 cp[4];
#pragma unroll
    for (int i = 0; i < 4; ++i) {
        const bf8v fr = ldstr2(rb + (r.t[12 + i] & 0xffff) + (r.t[12 + i] >> 16) * f, dl);
        cp[i] = mfma16(fr, asbf(one), f32x4{});
#pragma unroll
        for (int k = 0; k < 4; ++k) o[4 * i + k] = __builtin_bit_cast(u32x4, fr)[k];
    }
    if ((lane & 15) == 0) {
#pragma unroll
        for (int i = 0; i < 4; ++i) ldsw<f32x4>(L_CP + w * 256 + i * 64 + lane, cp[i]);
    }
    return o;
}

struct QkvSrc { rsrc_t rx, rw; unsigned xo[2], wo[2], sx, sw; };
#define QDX(V, O) "buffer_load_dwordx4 %[" #V "], %[rx], %[sx] offen offset:" #O " lds\n\t"
#define QDW(V, O) "buffer_load_dwordx4 %[" #V "], %[rw], %[sw] offen offset:" #O " lds\n\t"
#define QADV "s_add_u32 %[sx], %[sx], 128\n\ts_cmp_eq_u32 %[sx], 1280\n\ts_cselect_b32 %[sx], 0, %[sx]\n\ts_mul_i32 %[sw], %[sx], 1920\n\t"
#define QD4 QDX(x0, 0) QDX(x1, 1024) QDW(w0, 2048) QDW(w1, 3072) QADV "s_add_u32 m0, m0, 32768\n\t"
DI void qkv_start(QkvSrc& c, u32x4& row, unsigned& rowt, Params& p, u32x16 ka, int mt, int nt, int rot, int w, int lane) {
    const unsigned rr = lane >> 3, isv = nt >= 10;
    const unsigned tok = mt * 128 + (w >> 1) * 32 + ((w & 1) << (2 * isv)) + (rr & 4) * 2 + ((rr & 3) << (1 - isv));
    c.xo[0] = tok * 1280 + (((lane & 7) ^ (rr >> 1)) << 4);
    c.xo[1] = (c.xo[0] ^ 64) + 16 * 1280 - 1024;
    c.wo[0] = (8 * w + ((lane >> 2) & 3)) * 3840 + nt * 256 + (lane >> 4) * 64 + (((lane & 3) ^ ((w & 1) << 1)) << 4) + 1024;
    c.wo[1] = c.wo[0] + 4 * 3840 - 1024;
    c.sx = rot * 128; c.sw = rot * 245760;
    unsigned ro = (mt * 128 + nt * 8 + w) * 1280;
    asm volatile("s_waitcnt lgkmcnt(0)" : "+s"(ka), "+v"(c.xo[1]), "+v"(c.wo[1]), "+s"(c.sw), "+s"(ro) :: "memory");
    p = __builtin_bit_cast(Params, ka);
    c.rx = mkrs(p.x, 2048 * 1280); c.rw = mkrs(p.wqkv - 3072, 640 * 3840 + 3072);
    asm volatile("s_mov_b32 m0, %[dm]\n\tbuffer_load_dwordx4 %[r4], %[l16], %[rx], %[ro] offen\n\tbuffer_load_dword %[r1], %[l4], %[rx], %[ro] offen offset:1024\n\t"
                 "s_cmp_eq_u32 %[nt], 15\n\ts_cbranch_scc1 1f\n\t" QD4 "s_barrier\n\t" QD4 "s_branch 2f\n1:\n\ts_waitcnt vmcnt(0)\n2:"
                 : [r4] "=&v"(row), [r1] "=&v"(rowt), [sx] "+&s"(c.sx), [sw] "+&s"(c.sw)
                 : [x0] "v"(c.xo[0]), [x1] "v"(c.xo[1]), [w0] "v"(c.wo[0]), [w1] "v"(c.wo[1]), [rx] "s"(c.rx), [rw] "s"(c.rw), [dm] "s"(w * 4096),
                   [l16] "v"(lane * 16), [l4] "v"(lane * 4), [ro] "s"(ro), [nt] "s"(nt) : "memory", "scc");
}
template <int C> DI float rorsum(float v) { return v + __uint_as_float(__builtin_amdgcn_update_dpp(0u, __float_as_uint(v), 0x120 + C, 0xf, 0xf, true)); }
DI u32x4 row_stats(u32x4 row, unsigned rowt, unsigned T) {
    float s = 0.f, q = 0.f;
#pragma unroll
    for (int i = 0; i < 5; ++i) {
        const bf2v v = __builtin_bit_cast(bf2v, i < 4 ? row[i & 3] : rowt);
        s = __builtin_amdgcn_fdot2_f32_bf16(v, __builtin_bit_cast(bf2v, 0x3f803f80u), s, false);
        q = __builtin_amdgcn_fdot2_f32_bf16(v, v, q, false);
    }
    const f32x2 y = swap32(s, q);
    const float z = rorsum<1>(rorsum<2>(rorsum<4>(rorsum<8>(sum16(y[0] + y[1]))))) * (1.f / 640.f);
    const f32x2 v = swap32(z, z);
    const float ra = __builtin_amdgcn_rsqf(__builtin_fmaxf(v[1] - v[0] * v[0], 0.f) + 1e-5f);
    return u32x4{__float_as_uint(ra), __float_as_uint(-v[0] * ra), T, ~T};
}
#define MFQ(A, W, X) "v_mfma_f32_16x16x32_bf16 v[" A ":" A "+3], v[" W ":" W "+3], v[" X ":" X "+3], v[" A ":" A "+3]\n\t"
#define MFV(A, W, X) MFQ(A, X, W)
#define QTR(R, V, O) "ds_read_b64_tr_b16 v[" R ":" R "+1], %[" #V "] offset:" O "\n\t"
#define QRX(R, V, O) "ds_read_b128 v[" R ":" R "+3], %[" #V "] offset:" O "\n\t"
#define LGK(N) "s_waitcnt lgkmcnt(" #N ")\n\t"
#define QSEC(MF, P, Q, XA, WO, D1, D2, E1, E2) \
    "s_cmp_lt_u32 %[n], 8\n\t" LGK(4) MF("64", P, "68") QTR(Q, wa0, WO) MF("32", P, P "+8") QTR(Q "+2", wa0, WO "+1024") \
    "s_cbranch_scc0 1f\n\t" D1 "1:\n\t" MF("36", P, P "+12") QRX(Q "+8", XA, "0") LGK(5) MF("48", P "+4", P "+8") QRX(Q "+12", XA, "4096") \
    MF("52", P "+4", P "+12") QTR(Q "+4", wa1, WO) LGK(5) MF("40", P, P "+16") QTR(Q "+6", wa1, WO "+1024") \
    "s_cbranch_scc0 1f\n\t" D2 "1:\n\t" MF("56", P "+4", P "+16") QRX(Q "+16", XA, "8192") E1 MF("44", P, P "+20") QRX(Q "+20", XA, "12288") E2 MF("60", P "+4", P "+20")
#define QLDG "buffer_load_dwordx4 v[120:123], %[ga], %[rs], 0 offen sc1\n\t"
#define QADD(r) "v_add_u32 %[" #r "], %[d], %[" #r "]\n\t"
#define QRT(r, A, M) "v_mad_u32_u24 %[" #r "], %[hi], s" #M ", %[vlo]\n\tv_add_u32 %[" #r "], s" #A ", %[" #r "]\n\t"
#define QKV_ASM(MF, STRD) asm volatile( \
    "s_add_u32 m0, %[dm], 65536\n\ts_mov_b32 %[n], 0\n\t" QD4 "buffer_store_dwordx4 %[gr], %[go], %[rs], 0 offen sc1\n\t" \
    Z8("32") Z8("40") Z8("48") Z8("56") Z4("64") "v_mov_b32 v68, 0x3f803f80\n\tv_mov_b32 v69, v68\n\tv_mov_b64 v[70:71], v[68:69]\n\t" \
    "s_waitcnt vmcnt(9)\n\ts_barrier\n\t" TSA("1") TSS("25") \
    QTR("72", wa0, "0") QTR("74", wa0, "1024") QRX("80", xa0, "0") QRX("84", xa0, "4096") QTR("76", wa1, "0") QTR("78", wa1, "1024") QRX("88", xa0, "8192") QRX("92", xa0, "12288") \
    QDX(x0, 0) QDX(x1, 1024) "s_branch 6f\n" \
    "3:\n\ts_cmp_gt_u32 %[n], 2\n\ts_cbranch_scc1 2f\n\ts_waitcnt vmcnt(9)\n\ts_branch 5f\n" \
    "2:\n\ts_cmp_eq_u32 %[n], 10\n\ts_cbranch_scc1 7f\n\ts_cmp_eq_u32 %[n], 7\n\ts_cbranch_scc1 2f\n\ts_waitcnt vmcnt(5)\n\ts_branch 5f\n" \
    "2:\n\t" QLDG QRT(x0, 60, 64) QRT(x1, 61, 65) QRT(w0, 62, 66) QRT(w1, 63, 67) \
    "s_mov_b64 s[44:45], s[52:53]\n\ts_mov_b32 s46, s54\n\ts_mov_b64 s[48:49], s[56:57]\n\ts_mov_b32 s50, s58\n\ts_mov_b32 %[sx], 0\n\ts_mov_b32 %[sw], 0\n" \
    "4:\n\ts_waitcnt vmcnt(8)\n5:\n\ts_barrier\n\t" \
    QSEC(MF, "96", "72", xa0, "0", QDX(x0, 0), QDX(x1, 1024), "", "") \
    "6:\n\t" \
    QSEC(MF, "72", "96", xa1, "16384", QDW(w0, 2048), QDW(w1, 3072), \
         QADV "s_bitcmp1_b32 0x42, %[n]\n\ts_cselect_b32 %[d], %[wr], %[up]\n\ts_add_u32 m0, m0, %[d]\n\ts_cmp_eq_u32 %[n], 4\n\ts_cselect_b32 %[d], %[wr], %[up]\n\ts_add_u32 %[n], %[n], 1\n\t", \
         QADD(xa0) QADD(xa1) QADD(wa0) QADD(wa1)) TSI \
    "s_bitcmp1_b32 0x686, %[n]\n\ts_cbranch_scc0 4b\n\ts_branch 3b\n" \
    "7:\n\ts_movk_i32 %[n], " SPINS "\n\t" LGK(4) MF("64", "96", "68") MF("32", "96", "104") "s_waitcnt vmcnt(4)\n" \
    "1:\n\tv_cmp_ne_u64 vcc, v[122:123], %[tag]\n\ts_cbranch_vccz 2f\n\t" QLDG "s_waitcnt vmcnt(0)\n\ts_sub_u32 %[n], %[n], 1\n\ts_cbranch_scc0 1b\n" \
    "2:\n\t" MF("36", "96", "108") "v_mul_f32 v120, %[sc], v120\n\tv_mul_f32 v121, %[sc], v121\n\t" LGK(2) MF("48", "100", "104") \
    "ds_write2_b32 %[pa], v120, v121 offset1:64\n\t" MF("52", "100", "108") "s_nop 1\n\tds_write_b128 %[ca], v[64:67]\n\t" LGK(2) \
    MF("40", "96", "112") STRD MF("56", "100", "112") MF("44", "96", "116") MF("60", "100", "116") \
    "s_waitcnt lgkmcnt(0)\n\ts_barrier\n\ts_nop 7" \
    : "=&{v[32:63]}"(A), "=&{v[64:67]}"(cs0), "=&{v[124:155]}"(S), [rx] "+{s[44:47]}"(c.rx), [rw] "+{s[48:51]}"(c.rw), [sx] "+&s"(c.sx), [sw] "+&s"(c.sw), \
      [x0] "+&v"(c.xo[0]), [x1] "+&v"(c.xo[1]), [w0] "+&v"(c.wo[0]), [w1] "+&v"(c.wo[1]), [xa0] "+&v"(e.xa0), [xa1] "+&v"(e.xa1), [wa0] "+&v"(e.wa0), [wa1] "+&v"(e.wa1), \
      [n] "=&s"(n), [d] "=&s"(d) PFIO \
    : "{s[52:55]}"(rt.ra), "{s[56:59]}"(rt.rb), [rs] "s"(rws), [dm] "s"(w * 4096), [wr] "s"(-131072), [up] "s"(32768), [sc] "s"(e.sc), [gr] "v"(gr), [go] "v"(e.go), [ga] "v"(e.ga), \
      "{s[60:75]}"(rt.t), [vlo] "v"(rt.vlo), [hi] "v"(rt.hi), [tag] "v"(tag), [pa] "v"(e.pa), [pr] "v"(e.pr), [ca] "v"(e.ca) \
    : "memory", "scc", "vcc", "v68", "v69", "v70", "v71", "v72", "v73", "v74", "v75", "v76", "v77", "v78", "v79", VCL80, VCL96, VCL112L, "v120", "v121", "v122", "v123" PFCL)
#define QS2(R, O) "ds_read2_b32 v[" #R ":" #R "+1], %[pr] offset0:" #O " offset1:" #O "+64\n\t"
struct QkvAdr { unsigned xa0, xa1, wa0, wa1, go, ga, pa, pr, ca, so[2], sb, sc; };
DI void qkv_setup(QkvAdr& e, int mt, int nt, int w, int lane) {
    const unsigned t = lane & 15, g = lane >> 4, T = w & 1, cb = w >> 1, g64 = 2 * mt + T;
    const bool isv = nt >= 10, isq = nt < 5;
    e.xa0 = 4 * T * 4096 + t * 128 + ((g ^ (t >> 1)) << 4); e.xa1 = e.xa0 ^ 64;
    e.wa0 = g * 4096 + 2048 + cb * 256 + ((lane >> 2) & 3) * 64 + ((g ^ T) & 1) * 32 + (lane & 3) * 8; e.wa1 = e.wa0 ^ 32;
    e.go = lane == 0 ? WS_XS + (mt * 128 + nt * 8 + w) * 16 : OOB;
    e.ga = WS_XS + (g64 * 64 + (g >> 1) * 32 + (isv ? 8 * (t >> 2) + 4 * (g & 1) + (t & 3) : 2 * t + (g & 1))) * 16;
    e.pa = L_SM + w * 2048 + lane * 4; e.pr = L_SM + w * 2048 + (isv ? g * 16 : t * 4); e.ca = L_SM + w * 2048 + 1024 + lane * 16;
    e.sc = isq ? __builtin_bit_cast(unsigned, 0.22360679774997896f * 1.4426950408889634f) : 0x3f800000u;
    e.sb = isq ? WS_Q + g64 * 2560 : WS_KV + g64 * 5120 + (isv ? 2560 : 0);
#pragma unroll
    for (int n = 0; n < 2; ++n) {
        if (isv) {
            const unsigned col = (nt - 10) * 128 + cb * 32 + 16 * (n ^ T) + t, head = (col * 3277) >> 16;
            e.so[n] = head * (163840 - 320) + g * 320 + col * 16;
        } else {
            const unsigned G = (isq ? nt : nt - 5) * 32 + cb * 8 + 4 * (n ^ T) + g, head = (G * 205) >> 10;
            e.so[n] = head * (isq ? 81920 - 2560 : 163840 - 2560) + G * 512 + t * 16;
        }
        PIN(e.so[n]);
    }
    PIN(e.xa0); PIN(e.xa1); PIN(e.wa0); PIN(e.wa1); PIN(e.go); PIN(e.ga); PIN(e.pa); PIN(e.pr); PIN(e.ca); PINS(e.sb); PINS(e.sc);
}
template <bool ISV>
DI void qkv_body(QkvSrc& c, QkvAdr& e, const Retile& rt, rsrc_t rws, u32x4 gr, u32x2 tag, int w PROFARG) {
    f32x32 A, S; f32x4 cs0; unsigned n, d;
    if (ISV) QKV_ASM(MFV, QRX("124", pr, "0") QRX("128", pr, "256") QRX("132", pr, "64") QRX("136", pr, "320") QRX("140", pr, "128") QRX("144", pr, "384") QRX("148", pr, "192") QRX("152", pr, "448"));
    else QKV_ASM(MFQ, QS2(124, 0) QS2(126, 16) QS2(128, 32) QS2(130, 48));
    TS(2);
    const f32x4 cs1 = ldsr<f32x4>(e.ca ^ 2048);
#pragma unroll
    for (int n = 0; n < 2; ++n) {
        const f32x4 cs = n ? cs1 : cs0;
#pragma unroll
        for (int q = 0; q < 2; ++q) {
            u32x4 o;
#pragma unroll
            for (int i = 0; i < 4; ++i) {
                const int k = 2 * q + (i >> 1), j = 2 * (i & 1), a = 16 * n + 4 * k + j;
                o[i] = ISV ? pk2(A[a] * S[8 * k + j] + S[8 * k + 4 + j] * cs[0], A[a + 1] * S[8 * k + j + 1] + S[8 * k + 5 + j] * cs[0])
                           : pk2(A[a] * S[2 * k] + S[2 * k + 1] * cs[j], A[a + 1] * S[2 * k] + S[2 * k + 1] * cs[j + 1]);
            }
            asm volatile("buffer_store_dwordx4 %0, %1, %2, %3 offen offset:%4 sc1\n\ts_nop 1" :: "v"(o), "v"(e.so[n]), "s"(rws), "s"(e.sb), "n"(q * (ISV ? 1280 : 256)) : "memory");
        }
    }
    asm volatile("s_waitcnt vmcnt(4)" ::: "memory");
}

DI f32x16 qk_tile(u32x4 km, u32x2 kt, u32x4 qm, u32x2 qt, f32x16 c) {
    c = mfma32(asbf(km), asbf(qm), c);
    return __builtin_amdgcn_mfma_f32_32x32x8bf16_1k(__builtin_bit_cast(s4v, kt), __builtin_bit_cast(s4v, qt), c, 0, 0, 0);
}
DI float max16(const f32x16& s, float m) {
#pragma unroll
    for (int r = 0; r < 16; r += 2) m = __builtin_fmaxf(__builtin_fmaxf(s[r], s[r + 1]), m);
    return m;
}
DI f32x16 pv16(u32x4 v, f32x8 pr, f32x16 o) {
    return __builtin_amdgcn_smfmac_f32_32x32x32_bf16(asbf(v), __builtin_bit_cast(bf16v, pr), o, 0xDDDD, 0, 0);
}
#define EX4(P, S) "v_exp_f32 v[" #P "], v[" S "]\n\tv_exp_f32 v[" #P "+1], v[" S "+1]\n\tv_exp_f32 v[" #P "+2], v[" S "+2]\n\tv_exp_f32 v[" #P "+3], v[" S "+3]\n\t"
#define SMF(O, V, P) "v_smfmac_f32_32x32x32_bf16 v[" #O ":" #O "+15], v[" #V ":" #V "+3], v[" #P ":" #P "+7], %[idx]\n\t"
#define QKT(SN, AB) "v_mfma_f32_32x32x4_2b_bf16 v[" #SN ":" #SN "+31], v[156:157], v[152:153], 0 cbsz:1 abid:" #AB "\n\t"
#define QKA(SN) "v_mfma_f32_32x32x16_bf16 v[" #SN ":" #SN "+15], v[160:163], v[144:147], v[" #SN ":" #SN "+15]\n\t"
#define QKB(SN) "v_mfma_f32_32x32x16_bf16 v[" #SN "+16:" #SN "+31], v[160:163], v[148:151], v[" #SN "+16:" #SN "+31]\n\t"
#define STEP_(SC, VP, VC, L0, L1, L2, L3, L4) \
    L0 EX4(128, #SC) EX4(132, #SC "+4") SMF(112, VP, 136) EX4(136, #SC "+16") "s_waitcnt lgkmcnt(0)\n\t" SMF(96, 164, 128) EX4(140, #SC "+20") \
    L1 EX4(128, #SC "+8") SMF(112, 164, 136) L2 EX4(132, #SC "+12") L3 EX4(136, #SC "+24") SMF(96, VC, 128) L4 EX4(140, #SC "+28")
#define STEP(SC, SN, AB, VP, VC, KTRD, VRD, KRD, M1, M2) STEP_(SC, VP, VC, QKT(SN, AB) KTRD, QKA(SN) M1, VRD, QKB(SN) KRD, M2)
#define VR(VH, O) "s_mov_b64 exec, s[56:57]\n\tds_read_b128 v[164:167], %[av] offset:" O "\n\tds_read_b128 v[" #VH ":" #VH "+3], %[av] offset:" O "+640\n\ts_mov_b64 exec, -1\n\t"
#define KR(O) "ds_read_b64 v[160:161], %[a0] offset:" O "\n\tds_read_b64 v[162:163], %[a0] offset:" O "+512\n\t"
#define KT(O) "ds_read_b64 v[156:157], %[at] offset:" O "\n\t"
#define PRIO "s_cmp_eq_u32 %[grp], 0\n\ts_cbranch_scc1 1f\n\ts_setprio 1\n1:\n\t"
#define QUAD3(X, N, A0, B0, A1, B1, A2, B2) PRIO \
    STEP(32, 64, 1, 172, 168, KT(#X "+5120"), VR(172, #X "+1280"), KR(#X "+5120"), A0, B0) \
    STEP(64, 32, 0, 168, 172, "", VR(168, #X "+5120"), KR(#X "+5376"), A1, B1) \
    STEP(32, 64, 1, 172, 168, KT(#N), VR(172, #X "+6400"), KR(#N), A2, B2) "s_setprio 0\n\t"
#define QUAD(X, N, A0, B0, A1, B1, A2, B2, A3, B3) QUAD3(X, N, A0, B0, A1, B1, A2, B2) STEP(64, 32, 0, 168, 172, "", VR(168, #N), KR(#N "+256"), A3, B3) TSI
#define KVP_(D) "buffer_load_dwordx4 %[l16], s[40:43], s44 offen sc1 lds\n\ts_add_u32 m0, m0, " #D "\n\ts_add_u32 s44, s44, 4096\n\t"
#define KVP KVP_(4096)
#define LDQ(R, V, O) "buffer_load_dwordx2 v[" #R ":" #R "+1], %[" #V "], s[40:43], s45 offen offset:" #O " sc1\n\t"
#define STW(R, S) "buffer_store_dwordx4 v[" #R ":" #R "+3], %[vs], s[40:43], s" #S " offen sc1\n\t"
#define STM(V, S, O) "buffer_store_dwordx4 v[192:195], %[" #V "], s[40:43], s" #S " offen offset:" #O " sc1\n\t"
#define WOP(O) DM1("%[l16]", "s48", O)
#define WO5 "s_add_u32 m0, m0, 1024\n\ts_add_u32 s48, s48, 1024\n\t"
#define PK(R, O) LD1(#R, "%[l16]", "s52", #O)
#define PKA "s_add_u32 s52, s52, 4096\n\t"
struct Park { u32x32 a, b; u32x8 c; };

DI void phase_attn(const Params& p, const Retile& rt, rsrc_t rws, u32x2 tag, unsigned fq, int b, int w, int lane, int nt80, int ntg,
                   unsigned rv0, unsigned rv1, unsigned rsb, u32x4& r0, u32x2& r1, Park& pk PROFARG) {
    const int t = lane & 31, hh = lane >> 5, wl = w & 3, grp = w >> 2;
    const int head = (b & 7) * 4 + (b >> 6), qb = (b >> 3) & 7;
    const unsigned gb = grp * 61440, kvb = WS_KV + head * 163840;
    pk.a = retile_read(rt, b, w, lane);
    asm volatile("s_cmp_eq_u32 %[w], 2\n\ts_waitcnt vmcnt(0) lgkmcnt(0)\n\ts_barrier\n\ts_cbranch_scc0 1f\n\tbuffer_store_dwordx2 %[tag], %[fq], %[rs], 0 offen sc1\n1:"
                 : "+{v[176:207]}"(pk.a) : [w] "s"(w), [tag] "v"(tag), [fq] "v"(fq), [rs] "s"(rws) : "memory", "scc");
    TS(3);
    SBAR();
    const unsigned l16 = lane * 16, a0 = gb + hh * 1024 + t * 8, at = gb + lane * 8 + 2048, av = gb + 2560 + hh * 320 + (t < 20 ? t : 0) * 16;
    const unsigned ones = t == 20 ? 0x3f803f80u : 0u;
    const unsigned wob = WS_WO + nt80 * 102400;
    u32x8 sa;
    sa[0] = kvb + grp * 81920 + wl * 1024;
    sa[1] = WS_Q + head * 81920 + (qb * 4 + wl) * 2560;
    sa[2] = WS_O + b * 10240 + w * 1280;
    sa[3] = WS_B + qb * QBS + head * 40960 + w * 5120;
    sa[4] = wob + w * 5120; sa[5] = 122880 + w * 5120;
    sa[6] = wob + (grp + 1) * 40960 + wl * 5120; sa[7] = gb + 20480 + wl * 5120;
    u32x4 sb = {WS_WGU + ntg * 143360 + w * 17920, rsb, wob + 61440 + w * 2560, 40960u + w * 2560};
    const unsigned vfs = (w == 2 && lane == 0) ? WS_FR + b * 8 : OOB, vfe = (b == 0 && w == 0 && lane == 0) ? WS_E : OOB, vfp = WS_FR + w * 256 + t * 8;
    const unsigned fg = (b * 205) >> 10;
    const unsigned vcs = (b < 135 && w < 2) ? WS_CS + ((b - fg * 5) * 3584 + fg * 128 + w * 64 + lane) * 4 : OOB;
    const int n0 = (head * 20 + ((lane & 16) ? 19 : 0)) >> 7;
    const unsigned fo = WS_FQ + (w == 0 ? (5 + 5 * hh + n0) * 16 + (lane & 15) : n0 * 16 + 2 * qb + (lane & 1)) * 128;
    const unsigned va = hh * 1024 + grp * 256 + t * 8;
    unsigned e0 = gb + wl * 3072 + l16, e1 = 61440 - gb + wl * 3072 + l16, ov = (t >> 4) * 20480 + (t & 15) * 16, ow = hh ? OOB : ov;
    unsigned ob = WS_O + (qb * 16 + wl * 4 + grp * 2) * 20480, oc = ob + (64 + (head >> 1)) * 256 + (head & 1) * 8;
    ov += hh * 256; ob += head * 512;
    f32x32 O; u32x8 qm; u32x2 qt;
    asm volatile(
        Z8("96") Z8("104") Z8("112") Z8("120") Z8("136") "v_mov_b32 v164, %[one]\n\tv_mov_b32 v165, %[one]\n\tv_mov_b64 v[166:167], v[164:165]\n\tv_mov_b64 v[168:169], v[164:165]\n\t"
        "v_mov_b64 v[170:171], v[164:165]\n\tv_mov_b64 v[172:173], v[164:165]\n\tv_mov_b64 v[174:175], v[164:165]\n\tv_mov_b64 v[192:193], -1\n\tv_mov_b64 v[194:195], -1\n\t"
        "s_mov_b32 s56, 0xfffff\n\ts_mov_b32 s57, s56\n\ts_movk_i32 s58, " SPINS "\n\ts_cmp_gt_u32 %[w], 1\n\ts_cbranch_scc1 2f\n\t"
        "ds_read2st64_b32 v[196:197], %[cp] offset1:2\n\tds_read2st64_b32 v[198:199], %[cp] offset0:4 offset1:6\n"
        "1:\n\tbuffer_load_dwordx2 v[32:33], %[fo], s[40:43], 0 offen sc1\n\ts_waitcnt vmcnt(0) lgkmcnt(0)\n\tv_cmp_ne_u64 vcc, v[32:33], %[tag]\n\t"
        "s_cbranch_vccz 2f\n\ts_sub_u32 s58, s58, 1\n\ts_cbranch_scc0 1b\n"
        "2:\n\ts_mov_b32 m0, %[dm]\n\ts_barrier\n\t" TSA("4")
        KVP LDQ(144, va, 0) LDQ(146, va, 512) LDQ(148, vb, 0) LDQ(150, vb, 512) LDQ(152, vc, 2048) KVP "s_barrier\n\t" KVP KVP
        "v_add_f32 v196, v196, v197\n\tv_add_f32 v198, v198, v199\n\tv_add_f32 v196, v196, v198\n\ts_movk_i32 s58, " SPINS "\n\t"
        "s_waitcnt vmcnt(2)\n\ts_barrier\n\t" TSA("5")
        KT("0") KR("0") VR(168, "0") "s_waitcnt lgkmcnt(4)\n\t" QKT(32, 0) "s_waitcnt lgkmcnt(2)\n\ts_nop 7\n\ts_nop 7\n\ts_nop 2\n\t" QKA(32) QKB(32) KR("256")
        "s_waitcnt vmcnt(0)\n\ts_barrier\n\ts_nop 7\n\t" TSS("17") TSF("17")
        QUAD(0, 10240, KVP, KVP STW(176, 68), KVP, STW(180, 69) "buffer_store_dword v196, %[vcs], s[40:43], 0 offen sc1\n\t", KVP, STW(184, 70), KVP, STW(188, 71))
        "s_waitcnt vmcnt(8)\n\ts_barrier\n\t"
        QUAD(10240, 20480, KVP, STM(l16, 46, 0) STM(mv, 46, 1024), KVP, STM(l16, 47, 0) "s_add_u32 s47, s47, 1024\n\t", KVP, STM(l16, 47, 0), KVP, STM(l16, 47, 1024))
        "s_waitcnt vmcnt(5)\n\ts_barrier\n\tbuffer_store_dwordx2 %[tag], %[vfs], s[40:43], 0 offen sc1\n\t"
        QUAD(20480, 30720, KVP, STM(l16, 47, 2048), KVP_(-57344), STM(l16, 47, 3072), KVP, KVP, KVP, KVP)
        QUAD(30720, 40960, KVP, "", "buffer_load_dwordx2 v[198:199], %[vfp], s[40:43], 0 offen sc1\n\t", "", "", "", "", "")
        "s_mov_b32 m0, s49\n"
        "3:\n\ts_waitcnt vmcnt(0)\n\tv_cmp_ne_u64 vcc, v[198:199], %[tag]\n\ts_cbranch_vccz 4f\n\t"
        "buffer_load_dwordx2 v[198:199], %[vfp], s[40:43], 0 offen sc1\n\ts_sub_u32 s58, s58, 1\n\ts_cbranch_scc0 3b\n"
        "4:\n\ts_barrier\n\t" TSA("6") "buffer_store_dwordx2 %[tag], %[vfe], s[40:43], 0 offen sc1\n\t"
        QUAD(40960, 51200, WOP(0) WO5, PK(176, 0), WOP(0), PK(180, 1024), WOP(1024), PK(184, 2048), WOP(2048), PK(188, 3072) PKA)
        QUAD(51200, 0, WOP(3072) "s_mov_b32 m0, s51\n\ts_mov_b32 s48, s50\n\t", PK(192, 0), WOP(0) WO5, PK(196, 1024), WOP(0), PK(200, 2048), WOP(1024), PK(204, 3072) PKA)
        QUAD(0, 10240, WOP(2048), PK(208, 0), WOP(3072), PK(212, 1024), PK(216, 2048), PK(220, 3072) PKA, PK(224, 0), PK(228, 1024))
        QUAD3(10240, 0, PK(232, 2048), PK(236, 3072) PKA, PK(240, 0), PK(244, 1024),
              "buffer_load_dwordx4 v[248:251], %[rv0], %[rx], s53 offen\n\t", "buffer_load_dwordx2 v[252:253], %[rv1], %[rx], s53 offen\n\t")
        STEP_(64, 168, 172, "", "", "", "", "") TSI
        "s_mov_b32 m0, s55\n\ts_nop 0\n\t" SMF(112, 172, 136) "s_barrier\n\t"
        DM1("%[l16]", "s54", 0) DM1("%[l16]", "s54", 1024) "s_mov_b32 exec_hi, 0\n\t" DM1("%[l16]", "s54", 2048) "s_mov_b32 exec_hi, -1\n\ts_nop 7\n\ts_nop 7"
        : "=&{v[96:127]}"(O), "=&{v[144:151]}"(qm), "=&{v[152:153]}"(qt), "+{v[176:207]}"(pk.a), "=&{v[208:239]}"(pk.b), "=&{v[240:247]}"(pk.c), "=&{v[248:251]}"(r0), "=&{v[252:253]}"(r1),
          "+{s[44:51]}"(sa), "+{s[52:55]}"(sb), "+v"(e0), "+v"(e1), "+v"(ov), "+v"(ow), "+s"(ob), "+s"(oc) PFIO
        : "{s[40:43]}"(rws), "{s[60:75]}"(rt.t), [a0] "v"(a0), [at] "v"(at), [av] "v"(av), [l16] "v"(l16), [tag] "v"(tag), [vcs] "v"(vcs), [fo] "v"(fo),
          [cp] "v"(L_CP + (w & 1) * 256 + lane * 4), [mv] "v"(lane < 16 ? l16 : OOB), [vs] "v"(b * 8 + w < 1820 ? l16 : OOB), [one] "v"(ones), [idx] "v"(0xDDDD),
          [va] "v"(va), [vb] "v"(va ^ 256), [vc] "v"((lane * 8) ^ (grp * 256)), [vfs] "v"(vfs), [vfe] "v"(vfe), [vfp] "v"(vfp), [rv0] "v"(rv0), [rv1] "v"(rv1),
          [dm] "s"(gb + wl * 1024), [w] "s"(w), [grp] "s"(grp), [rx] "s"(mkrs(p.x, 2048 * 1280))
        : "memory", "scc", "vcc", "s56", "s57", "s58", VCL32, VCL48, VCL64, VCL80, VCL128, "v140", "v141", "v142", "v143", "v156", "v157", VCL160 PFCL);
    TS(7);
    float o[12];
#pragma unroll
    for (int i = 0; i < 3; ++i) ldsw<f32x4>(e0 + i * 1024, f32x4{O[16 + 4 * i], O[17 + 4 * i], O[18 + 4 * i], O[19 + 4 * i]});
    LBAR();
#pragma unroll
    for (int i = 0; i < 3; ++i) {
        const f32x4 v = ldsr<f32x4>(e1 + i * 1024);
#pragma unroll
        for (int k = 0; k < 4; ++k) o[4 * i + k] = O[4 * i + k] + v[k];
    }
    {
        f32x2 z = {0.f, 0.f};
#pragma unroll
        for (int r = 0; r < 12; r += 2) z += f32x2{o[r], o[r + 1]} * 0.f;
        const float l = (hh ? o[8] : 1.f) + (z[0] + z[1]);
        const bool bad = !(l < 1.8446744e19f && l > 8.6736174e-19f);
        if (__builtin_expect(anyl(bad), 0)) {
            float m = -__builtin_inff();
            const unsigned ko = hh * 1024 + t * 8, vo = 2560 + hh * 320 + (t < 20 ? t : 0) * 16;
            const u32x4 qa = {qm[0], qm[1], qm[2], qm[3]}; const u32x2 qe = {hh ? 0u : qt[0], hh ? 0u : qt[1]};
            f32x16 c = {}, A = {};
#pragma unroll 1
            for (int it = 0; it < 128; ++it) {
                const unsigned sb = kvb + ((it & 63) >> 1) * 5120;
                const u32x2 k0 = ld8c(rws, ko, sb + (it & 1) * 256), k1 = ld8c(rws, ko + 512, sb + (it & 1) * 256), k2 = ld8c(rws, t * 8 + 2048, sb + (it & 1) * 256);
                f32x16 s = qk_tile(u32x4{k0[0], k0[1], k1[0], k1[1]}, k2, qa, qe, c);
                if (it < 64) {
                    m = max16(s, m);
                    if (it == 63) {
                        const f32x2 mm = swap32(m, m);
                        m = __builtin_fmaxf(mm[0], mm[1]);
#pragma unroll
                        for (int r = 0; r < 16; ++r) c[r] = -m;
                    }
                } else {
                    u32x4 v0 = ld16c(rws, vo, sb + (it & 1) * 1280), v1 = ld16c(rws, vo + 640, sb + (it & 1) * 1280);
#pragma unroll
                    for (int i = 0; i < 4; ++i) { v0[i] = t < 20 ? v0[i] : ones; v1[i] = t < 20 ? v1[i] : ones; }
#pragma unroll
                    for (int r = 0; r < 16; ++r) s[r] = __builtin_amdgcn_exp2f(s[r]);
                    A = pv16(v0, __builtin_shufflevector(s, s, 0, 1, 2, 3, 4, 5, 6, 7), A);
                    A = pv16(v1, __builtin_shufflevector(s, s, 8, 9, 10, 11, 12, 13, 14, 15), A);
                }
            }
#pragma unroll
            for (int r = 0; r < 12; ++r) o[r] = A[r];
        }
    }
    const float inv = __builtin_amdgcn_rcpf(swap32(o[8], o[8])[1]);
    st16(rws, ov, ob, u32x4{pk2m(o[0] * inv, o[1] * inv), pk2m(o[2] * inv, o[3] * inv), pk2m(o[4] * inv, o[5] * inv), pk2m(o[6] * inv, o[7] * inv)});
    st8(rws, ow, oc, u32x2{pk2m(o[8] * inv, o[9] * inv), pk2m(o[10] * inv, o[11] * inv)});
}

#define MF16(A, W, B) "v_mfma_f32_16x16x32_bf16 v[" A ":" A "+3], v[" W ":" W "+3], v[" B ":" B "+3], v[" A ":" A "+3]\n\t"
#define RD1(W, V, O) "ds_read_b128 v[" W ":" W "+3], " V " offset:" O "\n\t"
#define GUN(n, H, V, O) "s_waitcnt lgkmcnt(6)\n\t" MF16("140+8*" #n, "112+4*" #n, H) MF16("144+8*" #n, "112+4*" #n, H "+8") RD1("112+4*" #n, V, O "+1024*" #n)
#define GUK(H, V, O) GUN(0, H, V, O) GUN(1, H, V, O) GUN(2, H, V, O) GUN(3, H, V, O) GUN(4, H, V, O) GUN(5, H, V, O) GUN(6, H, V, O)
#define LDH(H, S, O) LD1(#H, "%[l16]", S, O) LD1(#H "+4", "%[l16]", S, O "+1024") LD1(#H "+8", "%[lb]", S, O) LD1(#H "+12", "%[lb]", S, O "+1024")
#define RETRY(H, TMP, L) "1:\n\t" CHK(H, TMP, "vcc") "s_cbranch_vccz 2f\n\t" L "s_waitcnt vmcnt(0)\n\ts_sub_u32 s59, s59, 1\n\ts_cbranch_scc0 1b\n\ts_mov_b32 s59, 0\n2:\n\t"
#define GUS(H, PRE, O1, V2, O2, TAIL) \
    "s_waitcnt vmcnt(16)\n" RETRY(H, 196, LDH(H, "s44", "0")) PRE \
    GUK(#H, "%[wa]", O1) GUK(#H "+4", V2, O2) \
    "s_add_u32 s45, s44, 10240\n\ts_add_u32 s44, s44, 2048\n\ts_cmp_lt_u32 s45, s47\n\ts_cbranch_scc0 3f\n\t" LDH(H, "s45", "0") "s_branch 4f\n3:\n\t" TAIL "4:\n\t" TSI
#define LDST LD1("32", "%[l16]", "s51", "0") LD1("36", "%[l16]", "s51", "1024") LD1("40", "%[l16]", "s51", "2048") LD1("44", "%[l16]", "s51", "3072")
#define PW(R, O) "ds_write_b128 %[pw], v[" #R ":" #R "+3] offset:" #O "\n\t"
#define PW4(R, O) PW(R, O) PW(R+4, O+1024) PW(R+8, O+2048) PW(R+12, O+3072)
#define PARK "s_cmpk_lg_u32 s59, " SPINS "\n\ts_cbranch_scc1 8f\n\t" PW4(176, 0) PW4(192, 4096) PW4(208, 8192) PW4(224, 12288) PW(240, 16384) \
    "s_mov_b32 exec_hi, 0\n\t" PW(244, 17408) "s_mov_b32 exec_hi, -1\n\t" Z8("140") Z8("148") Z8("156") Z8("164") Z8("172") Z8("180") Z8("188") "8:\n\t"
DI void phase_gu(rsrc_t rws, int w, int lane, int qb, int nt80, int ntg, const Park& pk PROFARG) {
    const int t = lane & 15, g = lane >> 4;
    const unsigned l16 = lane * 16, wd = WS_WD + nt80 * 276480;
    if (ntg == 31) {
#pragma unroll 1
        for (int i = 0; i < 5; ++i) dma3c(20480 + (i * 8 + w) * 3072, l16, rws, wd + (i * 8 + w) * 3072);
        return;
    }
    const unsigned qbb = WS_B + qb * QBS, hb = qbb + w * 40960, e = w * 64 + lane;
    u32x8 sa = {hb, WS_CS + ntg * 448, wd + w * 4096, hb + 20480, 20480u + w * 4096, wd + 65536 + w * 7168, 86016u + w * 7168, qbb + OFF_HS + w * 4096};
    unsigned wa = 20480 + l16;
    f32x32 A0; f32x16 A1, A2, ST; u32x4 any;
    asm volatile("" : "={v[172:175]}"(any));
#define CSL(R) "buffer_load_dword v" #R ", %[ve], s[40:43], s45 offen sc1\n\ts_add_u32 s45, s45, 14336\n\t"
    asm volatile(
        TSQ("59", "63") CSL(112) CSL(113) CSL(114) CSL(115) CSL(116) "s_add_u32 s45, s44, 4096\n\t"
        POLL3(LDH(32, "s44", "0"), LDH(48, "s44", "2048"), LDH(64, "s45", "0"), 32, 48, 64, 196, PARK)
        "s_add_u32 s59, s44, 8192\n\t" LDH(80, "s45", "2048") LDH(96, "s59", "0")
        "v_add_f32 v112, v112, v113\n\tv_add_f32 v114, v114, v115\n\tv_add_f32 v112, v112, v116\n\tv_add_f32 v196, v112, v114\n\ts_movk_i32 s59, " SPINS "\n\ts_waitcnt lgkmcnt(0)\n\ts_barrier\n\t" TSA("11") TSS("35")
        "ds_write_b32 %[vl], v196\n\t"
        RD1("112", "%[wa]", "0") RD1("116", "%[wa]", "1024") RD1("120", "%[wa]", "2048") RD1("124", "%[wa]", "3072") RD1("128", "%[wa]", "4096") RD1("132", "%[wa]", "5120") RD1("136", "%[wa]", "6144")
        "6:\n\t"
        GUS(32, "", "7168", "%[wa]", "14336", LDST)
        GUS(48, "", "21504", "%[wa]", "28672", "s_mov_b32 m0, s48\n\ts_barrier\n\t" DM4("%[l16]", "s46"))
        GUS(64, "", "35840", "%[wa]", "43008", "s_add_u32 m0, s48, 32768\n\ts_add_u32 s46, s46, 32768\n\t" DM4("%[l16]", "s46"))
        GUS(80, "", "50176", "%[wa]", "57344", "s_mov_b32 m0, s50\n\ts_barrier\n\t" DM4("%[l16]", "s49") "s_add_u32 m0, s50, 4096\n\ts_add_u32 s49, s49, 4096\n\t" DM3("%[l16]", "s49"))
        GUS(96, "v_add_u32 %[wa], 0xfc00, %[wa]\n\t", "0", "%[wn]", "0", "s_branch 7f\n")
        "v_mov_b32 %[wa], %[wn]\n\ts_branch 6b\n"
        "7:\n\t" TSA("12") "s_waitcnt vmcnt(15)\n" RETRY(32, 196, LDST) "s_waitcnt lgkmcnt(0)\n\ts_nop 7"
        : "=&{v[140:171]}"(A0), "={v[172:187]}"(A1), "={v[188:203]}"(A2), "=&{v[32:47]}"(ST), "+{s[44:51]}"(sa), [wa] "+&v"(wa) PFIO
        : "{s[40:43]}"(rws), "{v[176:207]}"(pk.a), "{v[208:239]}"(pk.b), "{v[240:247]}"(pk.c), "{v[172:175]}"(any), [pw] "v"(20480 + w * 17920 + l16), [l16] "v"(l16), [lb] "v"(l16 + 20480), [wn] "v"(l16 + 92160), [ve] "v"(e < 112 ? e * 4 : OOB), [vl] "v"(e * 4)
        : "memory", "scc", "vcc", "s59", "s60", "s61", "s62", "s63", "s64", "s65", VCL48, VCL64, VCL80, VCL96, VCL112, VCL128 PFCL);
    float ra, rb;
    {
        float s[2], q[2];
#pragma unroll
        for (int i = 0; i < 2; ++i) {
            s[i] = (ST[8 * i] + ST[8 * i + 2]) + (ST[8 * i + 4] + ST[8 * i + 6]);
            q[i] = (ST[8 * i + 1] + ST[8 * i + 3]) + (ST[8 * i + 5] + ST[8 * i + 7]);
        }
        const f32x2 ss = swap32(s[0], s[1]), qq = swap32(q[0], q[1]);
        const float mean = sum16(ss[0] + ss[1]) * (1.f / 640.f);
        const float var = __builtin_fmaxf(sum16(qq[0] + qq[1]) * (1.f / 640.f) - mean * mean, 0.f);
        ra = __builtin_amdgcn_rsqf(var + 1e-5f); rb = -mean * ra;
    }
    unsigned fo[14];
#pragma unroll
    for (int n = 0; n < 7; ++n) {
        const f32x4 cg = ldsr<f32x4>(n * 64 + (g & 1) * 16), cu = ldsr<f32x4>(n * 64 + 32 + (g & 1) * 16);
        float o[4];
#pragma unroll
        for (int i = 0; i < 4; ++i) {
            const f32x2 gu = n < 4 ? swap32(A0[8 * (n & 3) + i], A0[8 * (n & 3) + 4 + i]) : n < 6 ? swap32(A1[8 * (n & 1) + i], A1[8 * (n & 1) + 4 + i]) : swap32(A2[i], A2[4 + i]);
            const float gt = gu[0] * ra + rb * cg[i], up = gu[1] * ra + rb * cu[i];
            o[i] = (gt > 0.f ? gt : gt * 0.f) * up;
        }
        fo[2 * n] = pk2m(o[0], o[1]); fo[2 * n + 1] = pk2m(o[2], o[3]);
        const unsigned fv = (g >> 1) * 55296 + t * 16, fs = qbb + OFF_F + w * 110592 + ntg * 256;
        if (n & 1) st16(rws, fv + (g & 1) * 7936, fs + (n - 1) * 7936, u32x4{fo[2 * n - 2], fo[2 * n - 1], fo[2 * n], fo[2 * n + 1]});
        if (n == 6) st8(rws, ntg < 30 ? fv + (g & 1) * 8 : OOB, fs + 6 * 7936, u32x2{fo[12], fo[13]});
    }
}

#define G8N(n, W, B, V, O) "s_waitcnt lgkmcnt(9)\n\t" MF16("120+4*" #n, W "+4*" #n, B) RD1(W "+4*" #n, V, O "+1024*" #n)
#define G8K(W, B, V, O) G8N(0, W, B, V, O) G8N(1, W, B, V, O) G8N(2, W, B, V, O) G8N(3, W, B, V, O) G8N(4, W, B, V, O)
#define LDB(B, S, S2, S3) LD1(#B, "v141", S, "0") LD1(#B "+4", "v141", S, "1024") LD1(#B "+8", "v141", S2, "2048") LD1(#B "+12", "v141", S3, "3072")
#define DUP(N, S) "s_cmp_eq_u32 s45, " #N "\n\ts_cselect_b32 s51, 1024, 0\n\ts_sub_u32 s51, " S ", s51\n\t"
#define BRK(N) "s_cmp_eq_u32 s45, " #N "\n\ts_cbranch_scc1 9f\n\t"
#define G8S(SLOT, B, BN, LA, LN, BRK1, BRK2, LN4, LR4) \
    G8K("80", #B, LA, "20480") G8K("100", #B "+4", LA, "30720") BRK1 \
    "s_cmp_lt_u32 s45, s47\n\ts_cbranch_scc1 7f\n\ts_cmp_gt_u32 s45, 8\n\ts_cbranch_scc1 3f\n\ts_waitcnt vmcnt(0)\n3:\n\ts_waitcnt vmcnt(9)\n" \
    RETRY(BN, 140, LN4) \
    "s_waitcnt lgkmcnt(0)\n\ts_barrier\n\ts_cmp_gt_u32 s45, 12\n\ts_cbranch_scc0 7f\n\t" \
    "s_add_u32 m0, s48, " #SLOT "*40960\n\ts_add_u32 s50, s46, 4096\n\t" DM4("v141", "s46") "s_add_u32 m0, m0, 4096\n\ts_add_u32 s46, s46, 40960\n\t" DM1("v141", "s50", 0) \
    "7:\n\t" G8K("80", #B "+8", LN, "0") BRK2 G8K("100", #B "+12", LN, "10240") \
    "s_add_u32 s50, s49, 8192\n\ts_add_u32 s49, s49, 4096\n\ts_sub_u32 s45, s45, 4\n\ts_cmp_gt_u32 s45, 8\n\ts_cbranch_scc0 8f\n\t" LR4 "8:\n\t" TSI
#define RD5(W, V, O) RD1(W, V, O) RD1(W "+4", V, O "+1024") RD1(W "+8", V, O "+2048") RD1(W "+12", V, O "+3072") RD1(W "+16", V, O "+4096")
#define G80_BODY \
    ".Lg80:\n\ts_add_u32 s49, s44, 4096\n\ts_add_u32 s50, s44, 8192\n\ts_cmp_gt_u32 s45, 12\n\ts_cselect_b32 s51, 0, 2048\n\ts_sub_u32 s51, s50, s51\n\t" \
    POLL3(LDB(32, "s44", "s44", "s44"), LDB(48, "s49", "s49", "s49"), LDB(64, "s50", "s51", "s51"), 32, 48, 64, 140, Z8("120") Z8("128") Z4("136")) \
    "s_movk_i32 s59, " SPINS "\n\ts_barrier\n\t" TSM TSS("45") RD5("80", "v142", "0") RD5("100", "v142", "10240") \
    "6:\n\t" \
    G8S(0, 32, 48, "v142", "v143", "", BRK(3), LDB(48, "s49", "s49", "s49"), DUP(11, "s50") LDB(32, "s50", "s50", "s51")) \
    G8S(1, 48, 64, "v143", "v144", "", "", LDB(64, "s49", "s49", "s49"), LDB(48, "s50", "s50", "s50")) \
    G8S(2, 64, 32, "v144", "v142", BRK(2), "", DUP(7, "s49") LDB(32, "s49", "s49", "s51"), LDB(64, "s50", "s50", "s50")) \
    "s_branch 6b\n9:\n\ts_waitcnt lgkmcnt(0)\n\ts_nop 7\n\ts_setpc_b64 s[56:57]\n"
template <int PS>
DI void g80(rsrc_t rws, int w, int lane, int nt, unsigned ab, u32x4& r0, u32x2& r1, Park& pk PROFARG) {
    const int tg = w & 3, kh = w >> 2;
    const unsigned l16 = lane * 16, la = kh * 5120 + l16;
    u32x4 sa = {ab, PS ? 27u : 10u, WS_WD + nt * 276480 + w * 5120 + 3 * 40960, PS ? 5u : 100u};
    f32x16 A; f32x4 a4;
    asm volatile(PFT TSQ("%[ti]*4/5+45", "%[ti]*4/5+49") "s_call_b64 s[56:57], .Lg80"
        : "=&{v[120:135]}"(A), "=&{v[136:139]}"(a4), "+{s[44:47]}"(sa) PFIO
        : "{s[40:43]}"(rws), "{s48}"(20480 + w * 5120), "{v141}"(l16), "{v142}"(la + (PS ? 20480 : 122880)), "{v143}"(la + (PS ? 61440 : 20480)), "{v144}"(la + (PS ? 102400 : 81920)) PFM(PS ? 13 : 8)
        : "memory", "scc", "vcc", "s49", "s50", "s51", "s56", "s57", "s59", "s60", "s61", "s62", "s63", "s64", "s65", VCL32, VCL48, VCL64, VCL80, VCL96, VCL112L, "v140" PFCL);
    if (!PS) asm volatile("" : "+{v[176:207]}"(pk.a), "+{v[208:239]}"(pk.b), "+{v[240:247]}"(pk.c), "+{v[248:251]}"(r0), "+{v[252:253]}"(r1));
    if (PS) TS(14); else TS(9);
    const unsigned ex = tg * 5120 + l16;
    f32x4 a0 = {A[0], A[1], A[2], A[3]}, a1 = {A[4], A[5], A[6], A[7]};
    const f32x4 a2 = {A[8], A[9], A[10], A[11]}, a3 = {A[12], A[13], A[14], A[15]};
    if (kh == 0) { ldsw<f32x4>(ex + 2048, a2); ldsw<f32x4>(ex + 3072, a3); }
    else { ldsw<f32x4>(ex, a0); ldsw<f32x4>(ex + 1024, a1); ldsw<f32x4>(ex + 4096, a4); a0 = a2; a1 = a3; }
    LBAR();
    a0 += ldsr<f32x4>(ex + kh * 2048); a1 += ldsr<f32x4>(ex + kh * 2048 + 1024); a4 += ldsr<f32x4>(ex + 4096);
    r0 = u32x4{pk2(a0[0] + bflo(r0[0]), a0[1] + bfhi(r0[0])), pk2(a0[2] + bflo(r0[1]), a0[3] + bfhi(r0[1])),
               pk2(a1[0] + bflo(r0[2]), a1[1] + bfhi(r0[2])), pk2(a1[2] + bflo(r0[3]), a1[3] + bfhi(r0[3]))};
    r1 = u32x2{pk2(a4[0] + bflo(r1[0]), a4[1] + bfhi(r1[0])), pk2(a4[2] + bflo(r1[1]), a4[3] + bfhi(r1[1]))};
}

__global__ void __launch_bounds__(512, 2) block_kernel(Params) {
    u32x16 ka;
    asm volatile("s_load_dwordx16 %0, %1, 0" : "=s"(ka) : "s"(__builtin_amdgcn_kernarg_segment_ptr()) : "memory");
    __shared__ __attribute__((aligned(16))) char lds[163840];
    asm volatile("" :: "v"((LAS char*)lds) : "memory");
    const int lane = threadIdx.x & 63, w = __builtin_amdgcn_readfirstlane(threadIdx.x >> 6), b = blockIdx.x;
    Params p;
    rsrc_t rws;
    const int xcd = b & 7, i = b >> 3;
    Retile rt;
    u32x2 tag;
    unsigned fq;
    {
        const int mt = (xcd >> 1) * 4 + (i & 3), nt = (xcd & 1) * 8 + (i >> 2), rot = (i >> 2) + (((i & 3) * 5 + 1) >> 1);
        QkvSrc c; QkvAdr e; u32x4 row; unsigned rowt, T;
        qkv_start(c, row, rowt, p, ka, mt, nt, rot < 10 ? rot : rot - 10, w, lane);
        rws = mkrs(p.ws, WS_END);
        asm volatile("s_load_dword %0, %2, %4\n\ts_load_dwordx16 %1, %3, %5" : "=&s"(T), "=&{s[60:75]}"(rt.t) : "s"(p.ws), "s"(&RT_TAB), "s"(WS_E), "s"((b * 8 + w) * 64) : "memory");
        TSW(40);
        retile_setup(rt, p, b, lane);
        qkv_setup(e, mt, nt, w, lane);
        fq = lane == 0 ? WS_FQ + (nt * 16 + mt) * 128 : OOB;
        TSW(41);
        asm volatile("s_waitcnt vmcnt(8) lgkmcnt(0)\n\ts_add_u32 %0, %0, 1" : "+s"(T), "+{s[60:75]}"(rt.t), "+v"(row), "+v"(rowt), "+v"(fq) :: "memory", "scc");
        TSW(42);
        const u32x4 gr = row_stats(row, rowt, T);
        tag = u32x2{gr[2], gr[3]};
        TSW(43);
        if (nt == 15) {
            st16(rws, e.go, 0, gr);
            retile_issue(rt, w);
            asm volatile("s_waitcnt vmcnt(0)" ::: "memory");
        } else if (nt >= 10) qkv_body<true>(c, e, rt, rws, gr, tag, w PROFPASS);
        else qkv_body<false>(c, e, rt, rws, gr, tag, w PROFPASS);
    }
    const int mt = (xcd >> 1) * 8 + (i & 7), nt = (xcd & 1) * 4 + (i >> 3), ntg = (xcd & 1) * 16 + (i & 3) * 4 + (i >> 3);
    const int tg = w & 3, kh = w >> 2, t = lane & 15, g = lane >> 4, qb = mt >> 2, tk = (mt & 3) * 4 + tg;
    const unsigned rv0 = t * 1280 + (nt & 1) * 32 + kh * 64 + g * 16, rv1 = kh ? OOB : t * 1280 + ((nt & 1) ? 0 : 128) + g * 8;
    const unsigned rsb = (mt * 64 + tg * 16) * 1280 + nt * 160, qbb = WS_B + qb * QBS;
    u32x4 r0; u32x2 r1; Park pk;
    phase_attn(p, rt, rws, tag, fq, b, w, lane, nt, ntg, rv0, rv1, rsb, r0, r1, pk PROFPASS);
    g80<0>(rws, w, lane, nt, WS_O + (mt * 4 + tg) * 20480 + kh * 10240, r0, r1, pk PROFPASS);
    {
#pragma unroll
        for (int k = 0; k < 4; ++k) r0[k] = nomark(r0[k]);
        r1[0] = nomark(r1[0]); r1[1] = nomark(r1[1]);
        const unsigned hs = qbb + tk * 20480 + nt * 256;
        st16(rws, (2 * g + kh) * 2048 + t * 16, hs, r0);
        st8(rws, kh ? OOB : (8 + (g >> 1)) * 2048 + t * 16 + (g & 1) * 8, hs, r1);
        float s = 0.f, q = 0.f;
#pragma unroll
        for (int k = 0; k < 6; ++k) {
            const unsigned u = k < 4 ? r0[k] : kh ? 0u : r1[k - 4];
            const float a = bflo(u), c = bfhi(u);
            s += a + c; q += a * a + c * c;
        }
        const f32x2 y = swap32(s, q);
        const float z = sum16(y[0] + y[1]);
        const f32x2 v = swap32(z, z);
        st8(rws, g == 0 ? t * 16 + kh * 8 : OOB, qbb + OFF_HS + tk * 2048 + nt * 256, u32x2{nomark(__float_as_uint(v[0])), nomark(__float_as_uint(v[1]))});
    }
    TS(10);
    unsigned fb = qbb + OFF_F + tk * 55296 + kh * 27648;
    PINS(fb);
    phase_gu(rws, w, lane, qb, nt, ntg, pk PROFPASS);
    g80<1>(rws, w, lane, nt, fb, r0, r1, pk PROFPASS);
    asm volatile("buffer_store_dwordx4 %0, %2, %4, %5 offen\n\tbuffer_store_dwordx2 %1, %3, %4, %5 offen" :: "v"(r0), "v"(r1), "v"(rv0), "v"(rv1), "s"(mkrs(p.out, 2048 * 1280)), "s"(rsb) : "memory");
    asm volatile("s_endpgm\n" G80_BODY ::: "memory");
}

extern "C" void kernel_launch(void* const* d_in, const int* in_sizes, int n_in, void* d_out, int out_size, void* d_ws, size_t ws_size, hipStream_t stream) {
    Params p;
    p.x = (const char*)d_in[0]; p.wqkv = (const char*)d_in[1]; p.wo = (const char*)d_in[2];
    p.wg = (const char*)d_in[4]; p.wu = (const char*)d_in[5]; p.wd = (const char*)d_in[6];
    p.out = (char*)d_out; p.ws = (char*)d_ws;
    block_kernel<<<256, 512, 0, stream>>>(p);
}
```

```cpp
#include <hip/hip_runtime.h>
#include <hip/hip_bf16.h>
#include <stdint.h>

#define DI __device__ __forceinline__
typedef __attribute__((ext_vector_type(2))) __bf16 bf2v;
typedef __attribute__((ext_vector_type(8))) __bf16 bf8v;
typedef __attribute__((ext_vector_type(16))) __bf16 bf16v;
typedef __attribute__((ext_vector_type(4))) short s4v;
typedef __attribute__((ext_vector_type(2))) float f32x2;
typedef __attribute__((ext_vector_type(4))) float f32x4;
typedef __attribute__((ext_vector_type(8))) float f32x8;
typedef __attribute__((ext_vector_type(16))) float f32x16;
typedef __attribute__((ext_vector_type(32))) float f32x32;
typedef __attribute__((ext_vector_type(2))) unsigned u32x2;
typedef __attribute__((ext_vector_type(4))) unsigned u32x4;
typedef __attribute__((ext_vector_type(8))) unsigned u32x8;
typedef __attribute__((ext_vector_type(16))) unsigned u32x16;
typedef __attribute__((ext_vector_type(32))) unsigned u32x32;
typedef __amdgpu_buffer_rsrc_t rsrc_t;
#define LAS __attribute__((address_space(3)))

constexpr unsigned WS_Q = 0;
constexpr unsigned WS_KV = WS_Q + 32 * 81920;
constexpr unsigned WS_O = WS_KV + 32 * 163840;
constexpr unsigned WS_B = WS_O + 128 * 20480;
constexpr unsigned QBS = 32 * 40960, OFF_HS = 16 * 20480, OFF_F = OFF_HS + 16 * 2048;
constexpr unsigned WS_WO = WS_B + 8 * QBS;
constexpr unsigned WS_WD = WS_WO + 8 * 102400;
constexpr unsigned WS_WGU = WS_WD + 8 * 276480;
constexpr unsigned WS_CS = WS_WGU + 32 * 143360;
constexpr unsigned WS_FQ = WS_CS + 5 * 3584 * 4;
constexpr unsigned WS_FR = WS_FQ + 256 * 128;
constexpr unsigned WS_XS = WS_FR + 2048;
constexpr unsigned WS_E = WS_XS + 2048 * 16;
constexpr unsigned WS_END = WS_E + 128;
constexpr unsigned OOB = 0x40000000u;
constexpr int SPIN = 1 << 14;

struct Params {
    const char* x; const char* wqkv; const char* wo; const char* wg; const char* wu; const char* wd;
    char* out; char* ws;
};

#define SBAR() __builtin_amdgcn_sched_barrier(0)
#define PIN(x) asm volatile("" : "+v"(x))
#define WAITBAR(N) asm volatile("s_waitcnt vmcnt(" #N ") lgkmcnt(0)\n\ts_barrier" ::: "memory")
#define VBAR(N) asm volatile("s_waitcnt vmcnt(" #N ")\n\ts_barrier" ::: "memory")
#define LBAR() asm volatile("s_waitcnt lgkmcnt(0)\n\ts_barrier" ::: "memory")
#define BAR() asm volatile("s_barrier" ::: "memory")

DI rsrc_t mkrs(const char* p, unsigned bytes) { return __builtin_amdgcn_make_buffer_rsrc((void*)p, (short)0, (int)bytes, 0x00020000); }
template <class T> DI T ldsr(unsigned a) { return *(const LAS T*)(uintptr_t)a; }
template <class T> DI void ldsw(unsigned a, T v) { *(LAS T*)(uintptr_t)a = v; }
DI s4v ldstr(unsigned a) { return __builtin_amdgcn_ds_read_tr16_b64_v4i16((LAS s4v*)(uintptr_t)a); }
DI bf8v ldstr2(unsigned a, unsigned d) {
    s4v lo = ldstr(a), hi = ldstr(a + d);
    return __builtin_bit_cast(bf8v, __builtin_shufflevector(lo, hi, 0, 1, 2, 3, 4, 5, 6, 7));
}
DI f32x16 mfma32(bf8v a, bf8v b, f32x16 c) { return __builtin_amdgcn_mfma_f32_32x32x16_bf16(a, b, c, 0, 0, 0); }
DI f32x4 mfma16(bf8v a, bf8v b, f32x4 c) { return __builtin_amdgcn_mfma_f32_16x16x32_bf16(a, b, c, 0, 0, 0); }
DI bf8v asbf(u32x4 v) { return __builtin_bit_cast(bf8v, v); }
DI unsigned pk2(float lo, float hi) { f32x2 v = {lo, hi}; return __builtin_bit_cast(unsigned, __builtin_convertvector(v, bf2v)); }
DI unsigned nomark(unsigned u) { return u < 0xfffffffeu ? u : 0xfffffffeu; }
DI unsigned pk2m(float lo, float hi) { return nomark(pk2(lo, hi)); }
DI float bflo(unsigned u) { return __uint_as_float(u << 16); }
DI float bfhi(unsigned u) { return __uint_as_float(u & 0xffff0000u); }
DI f32x2 swap32(float a, float b) { auto r = __builtin_amdgcn_permlane32_swap(__float_as_uint(a), __float_as_uint(b), false, false); return f32x2{__uint_as_float(r[0]), __uint_as_float(r[1])}; }
DI float sum16(float a) { auto r = __builtin_amdgcn_permlane16_swap(__float_as_uint(a), __float_as_uint(a), false, false); return __uint_as_float(r[0]) + __uint_as_float(r[1]); }
DI bool anyl(bool c) { return __builtin_amdgcn_ballot_w64(c) != 0; }
DI void st16(rsrc_t r, unsigned voff, unsigned soff, u32x4 v) { asm volatile("buffer_store_dwordx4 %0, %1, %2, %3 offen sc1\n\ts_nop 1" :: "v"(v), "v"(voff), "s"(r), "s"(soff) : "memory"); }
DI void st8(rsrc_t r, unsigned voff, unsigned soff, u32x2 v) { asm volatile("buffer_store_dwordx2 %0, %1, %2, %3 offen sc1" :: "v"(v), "v"(voff), "s"(r), "s"(soff) : "memory"); }
DI void st4(rsrc_t r, unsigned voff, unsigned soff, unsigned v) { asm volatile("buffer_store_dword %0, %1, %2, %3 offen sc1" :: "v"(v), "v"(voff), "s"(r), "s"(soff) : "memory"); }
DI u32x4 ld16c(rsrc_t r, unsigned voff, unsigned soff) { return __builtin_amdgcn_raw_buffer_load_b128(r, voff, soff, 16); }
DI u32x2 ld8c(rsrc_t r, unsigned voff, unsigned soff) { return __builtin_amdgcn_raw_buffer_load_b64(r, voff, soff, 16); }
DI void dma1(unsigned m0v, unsigned voff, rsrc_t r, unsigned soff) {
    asm volatile("s_mov_b32 m0, %0\n\ts_nop 0\n\tbuffer_load_dwordx4 %1, %2, %3 offen lds" :: "s"(m0v), "v"(voff), "s"(r), "s"(soff) : "memory"); }
DI void dma1c(unsigned m0v, unsigned voff, rsrc_t r, unsigned soff) {
    asm volatile("s_mov_b32 m0, %0\n\ts_nop 0\n\tbuffer_load_dwordx4 %1, %2, %3 offen sc1 lds" :: "s"(m0v), "v"(voff), "s"(r), "s"(soff) : "memory"); }
DI void dma3c(unsigned m0v, unsigned voff, rsrc_t r, unsigned soff) {
    asm volatile("s_mov_b32 m0, %0\n\ts_nop 0\n\tbuffer_load_dwordx4 %1, %2, %3 offen sc1 lds\n\tbuffer_load_dwordx4 %1, %2, %3 offen offset:1024 sc1 lds\n\t"
                 "buffer_load_dwordx4 %1, %2, %3 offen offset:2048 sc1 lds" :: "s"(m0v), "v"(voff), "s"(r), "s"(soff) : "memory"); }
DI void dma4c(unsigned m0v, unsigned voff, rsrc_t r, unsigned soff) {
    asm volatile("s_mov_b32 m0, %0\n\ts_nop 0\n\tbuffer_load_dwordx4 %1, %2, %3 offen sc1 lds\n\tbuffer_load_dwordx4 %1, %2, %3 offen offset:1024 sc1 lds\n\t"
                 "buffer_load_dwordx4 %1, %2, %3 offen offset:2048 sc1 lds\n\tbuffer_load_dwordx4 %1, %2, %3 offen offset:3072 sc1 lds" :: "s"(m0v), "v"(voff), "s"(r), "s"(soff) : "memory"); }
#define SPINS "0x4000"
#define PINS(x) asm volatile("" : "+s"(x))
#define CHK(B, TMP, DST) \
    "v_max3_u32 v" #TMP ", v[" #B "], v[" #B "+1], v[" #B "+2]\n\tv_max3_u32 v" #TMP ", v[" #B "+3], v[" #B "+4], v" #TMP "\n\t" \
    "v_max3_u32 v" #TMP ", v[" #B "+5], v[" #B "+6], v" #TMP "\n\tv_max3_u32 v" #TMP ", v[" #B "+7], v[" #B "+8], v" #TMP "\n\t" \
    "v_max3_u32 v" #TMP ", v[" #B "+9], v[" #B "+10], v" #TMP "\n\tv_max3_u32 v" #TMP ", v[" #B "+11], v[" #B "+12], v" #TMP "\n\t" \
    "v_max3_u32 v" #TMP ", v[" #B "+13], v[" #B "+14], v" #TMP "\n\tv_max_u32 v" #TMP ", v[" #B "+15], v" #TMP "\n\t" \
    "v_cmp_eq_u32 " DST ", -1, v" #TMP "\n\t"
#define LD1(R, V, S, O) "buffer_load_dwordx4 v[" R ":" R "+3], " V ", s[40:43], " S " offen offset:" O " sc1\n\t"
#define DM1(V, S, O) "buffer_load_dwordx4 " V ", s[40:43], " S " offen offset:" #O " sc1 lds\n\t"
#define DM3(V, S) DM1(V, S, 0) DM1(V, S, 1024) DM1(V, S, 2048)
#define DM4(V, S) DM3(V, S) DM1(V, S, 3072)
#define POLL3(L0, L1, L2, B0, B1, B2, TMP, FILL) \
    "s_mov_b64 s[60:61], -1\n\ts_mov_b64 s[62:63], -1\n\ts_mov_b64 s[64:65], -1\n\ts_movk_i32 s59, " SPINS "\n" \
    "1:\n\ts_mov_b64 exec, s[60:61]\n\ts_cbranch_execz 2f\n\t" L0 \
    "2:\n\ts_mov_b64 exec, s[62:63]\n\ts_cbranch_execz 3f\n\t" L1 \
    "3:\n\ts_mov_b64 exec, s[64:65]\n\ts_cbranch_execz 4f\n\t" L2 \
    "4:\n\ts_mov_b64 exec, -1\n\t" FILL "s_waitcnt vmcnt(0)\n\t" TSP \
    CHK(B0, TMP, "s[60:61]") CHK(B1, TMP, "s[62:63]") CHK(B2, TMP, "s[64:65]") \
    "s_or_b64 vcc, s[60:61], s[62:63]\n\ts_or_b64 vcc, vcc, s[64:65]\n\ts_cbranch_scc0 5f\n\t" \
    "s_sub_u32 s59, s59, 1\n\ts_cbranch_scc0 1b\n" \
    "5:\n\t"
#define Z2(R) "v_mov_b64 v[" R ":" R "+1], 0\n\t"
#define Z4(R) Z2(R) Z2(R "+2")
#define Z8(R) Z4(R) Z4(R "+4")

#define VCL32 "v32", "v33", "v34", "v35", "v36", "v37", "v38", "v39", "v40", "v41", "v42", "v43", "v44", "v45", "v46", "v47"
#define VCL48 "v48", "v49", "v50", "v51", "v52", "v53", "v54", "v55", "v56", "v57", "v58", "v59", "v60", "v61", "v62", "v63"
#define VCL64 "v64", "v65", "v66", "v67", "v68", "v69", "v70", "v71", "v72", "v73", "v74", "v75", "v76", "v77", "v78", "v79"
#define VCL80 "v80", "v81", "v82", "v83", "v84", "v85", "v86", "v87", "v88", "v89", "v90", "v91", "v92", "v93", "v94", "v95"
#define VCL96 "v96", "v97", "v98", "v99", "v100", "v101", "v102", "v103", "v104", "v105", "v106", "v107", "v108", "v109", "v110", "v111"
#define VCL112 "v112", "v113", "v114", "v115", "v116", "v117", "v118", "v119", "v120", "v121", "v122", "v123", "v124", "v125", "v126", "v127"
#define VCL128 "v128", "v129", "v130", "v131", "v132", "v133", "v134", "v135", "v136", "v137", "v138", "v139"
#define VCL160 "v160", "v161", "v162", "v163", "v164", "v165", "v166", "v167", "v168", "v169", "v170", "v171", "v172", "v173", "v174", "v175"
#define VCL112L "v112", "v113", "v114", "v115", "v116", "v117", "v118", "v119"

#define PROFARG
#define PROFPASS
#define TSA(i)
#define PFIO
#define PFCL
#define TSM
#define PFM(i)
#define PFT
#define TS(i)

#define TSF(i)
#define TSI
#define TSS(i)
#define TSW(i)
#define TSP
#define TSQ(a, b)
constexpr unsigned L_RT = 0, L_SM = 32768, L_CP = 49152;
struct RtTab { unsigned v[2048][16]; };
constexpr RtTab make_rt() {
    RtTab t{};
    for (unsigned b = 0; b < 256; ++b) for (unsigned w = 0; w < 8; ++w) for (unsigned f = 0; f < 4; ++f) {
        unsigned A = 0, M = 0, SO = 0, R0 = 0, R1 = 0;
        if (b < 135) {
            const unsigned fg = b / 5, ks = (b % 5) * 4 + (w >> 1), j = ks >> 1, o = fg * 8 + (w & 1) * 4 + f;
            A = (320 * (ks & 1) + (j < 8 ? (j & 1) * 32 + (j >> 1) * 8 : 8 * j) + (f & 1) * 160) * 3456 + fg * 128 + (w & 1) * 64;
            M = (j < 8 ? 96 : 16) * 3456;
            SO = WS_WGU + (o / 7) * 143360 + ks * 7168 + (o % 7) * 1024;
            R0 = 16 * f;
        } else {
            const unsigned v = (b - 135) * 8 + w;
            const bool isWo = v < 200;
            const unsigned vv = v < 740 ? (isWo ? v : v - 200) : 0, ks = vv / 10, cu = vv % 10, NK = isWo ? 10 : 27;
            const unsigned c = 4 * ks + f, j = c / 31, ng = c % 31;
            A = (isWo ? (ks >= 16 ? 40 * c - 2544 : 20 * (c >> 1) + 4 * (c & 1)) : 56 * ng + 16 * (j >> 1) + 4 * (j & 1)) * 1280 + cu * 128;
            M = (isWo ? (ks >= 16 ? 20 : 8) : (j < 6 ? 8 : 4)) * 1280;
            const unsigned seg = 2 * cu + (f >> 1), a = seg / 5, sm = seg % 5, fi = f & 1;
            const unsigned tile = 2 * a + (sm == 2 ? fi : sm > 2), blk = sm == 2 ? 4 : (sm == 1 || sm == 4) ? 2 + fi : fi;
            SO = (isWo ? WS_WO : WS_WD) + (tile * NK * 10 + (ks >= NK ? (ks - NK) * 10 + 5 : ks * 10) + blk) * 1024;
            R0 = (f >> 1) * 64 + (sm == 2 ? 32 : 8) * fi; R1 = sm == 2 ? 8 : 16;
        }
        unsigned* r = t.v[b * 8 + w];
        r[f] = A + 3072 - 1024 * f; r[4 + f] = M; r[8 + f] = SO; r[12 + f] = (L_RT + w * 4096 + R0) | R1 << 16;
    }
    return t;
}
static __device__ const __attribute__((aligned(64))) RtTab RT_TAB = make_rt();
struct Retile { rsrc_t ra, rb; u32x16 t; unsigned vlo, hi; };
DI void retile_setup(Retile& r, const Params& p, unsigned b, unsigned lane) {
    const bool gu = b < 135;
    const char *pa, *pb; unsigned n;
    asm("s_cmpk_lt_u32 %3, 160\n\ts_cselect_b64 %0, %4, %5\n\ts_cselect_b32 %2, %9, %8\n\ts_cmpk_lt_u32 %3, 135\n\ts_cselect_b64 %1, %7, %0\n\ts_cselect_b64 %0, %6, %0\n\ts_cselect_b32 %2, %8, %2"
        : "=&s"(pa), "=&s"(pb), "=&s"(n) : "s"(b), "s"(p.wo), "s"(p.wd), "s"(p.wg), "s"(p.wu), "s"(640 * 3456 + 3072), "n"(640 * 1280 + 3072) : "scc");
    r.ra = mkrs(pa - 3072, n); r.rb = mkrs(pb - 3072, n);
    r.vlo = gu ? ((lane >> 2) & 7) * 3456 + (lane & 3) * 16 : ((lane >> 3) & 3) * 1280 + (lane & 7) * 16;
    r.hi = lane >> 5;
    PIN(r.vlo); PIN(r.hi); PINS(r.ra); PINS(r.rb);
}
DI void retile_issue(const Retile& r, int w) {
    asm volatile("s_mov_b32 m0, %0\n\ts_nop 0\n\tbuffer_load_dwordx4 %1, %5, 0 offen lds\n\tbuffer_load_dwordx4 %2, %5, 0 offen offset:1024 lds\n\t"
                 "buffer_load_dwordx4 %3, %6, 0 offen offset:2048 lds\n\tbuffer_load_dwordx4 %4, %6, 0 offen offset:3072 lds"
                 :: "s"(L_RT + w * 4096), "v"(r.vlo + r.t[0] + r.hi * r.t[4]), "v"(r.vlo + r.t[1] + r.hi * r.t[5]), "v"(r.vlo + r.t[2] + r.hi * r.t[6]),
                    "v"(r.vlo + r.t[3] + r.hi * r.t[7]), "s"(r.ra), "s"(r.rb) : "memory");
}
DI u32x32 retile_read(const Retile& r, unsigned b, int w, unsigned lane) {
    const u32x4 one = {0x3f803f80u, 0x3f803f80u, 0x3f803f80u, 0x3f803f80u};
    const unsigned f = lane & 3, row = 8 * (lane >> 4) + ((lane >> 2) & 3), dl = b < 135 ? 256 : 512;
    const unsigned rb = b < 135 ? (f >> 1) * 2048 + row * 64 + (f & 1) * 8 : row * 128;
    u32x32 o; f32x4 cp[4];
#pragma unroll
    for (int i = 0; i < 4; ++i) {
        const bf8v fr = ldstr2(rb + (r.t[12 + i] & 0xffff) + (r.t[12 + i] >> 16) * f, dl);
        cp[i] = mfma16(fr, asbf(one), f32x4{});
#pragma unroll
        for (int k = 0; k < 4; ++k) o[4 * i + k] = __builtin_bit_cast(u32x4, fr)[k];
    }
    if ((lane & 15) == 0) {
#pragma unroll
        for (int i = 0; i < 4; ++i) ldsw<f32x4>(L_CP + w * 256 + i * 64 + lane, cp[i]);
    }
    return o;
}

struct QkvSrc { rsrc_t rx, rw; unsigned xo[2], wo[2], sx, sw; };
#define QDX(V, O) "buffer_load_dwordx4 %[" #V "], %[rx], %[sx] offen offset:" #O " lds\n\t"
#define QDW(V, O) "buffer_load_dwordx4 %[" #V "], %[rw], %[sw] offen offset:" #O " lds\n\t"
#define QADV "s_add_u32 %[sx], %[sx], 128\n\ts_cmp_eq_u32 %[sx], 1280\n\ts_cselect_b32 %[sx], 0, %[sx]\n\ts_mul_i32 %[sw], %[sx], 1920\n\t"
#define QD4 QDX(x0, 0) QDX(x1, 1024) QDW(w0, 2048) QDW(w1, 3072) QADV "s_add_u32 m0, m0, 32768\n\t"
DI void qkv_start(QkvSrc& c, u32x4& row, unsigned& rowt, Params& p, u32x16 ka, int mt, int nt, int rot, int w, int lane) {
    const unsigned rr = lane >> 3, isv = nt >= 10;
    const unsigned tok = mt * 128 + (w >> 1) * 32 + ((w & 1) << (2 * isv)) + (rr & 4) * 2 + ((rr & 3) << (1 - isv));
    c.xo[0] = tok * 1280 + (((lane & 7) ^ (rr >> 1)) << 4);
    c.xo[1] = (c.xo[0] ^ 64) + 16 * 1280 - 1024;
    c.wo[0] = (8 * w + ((lane >> 2) & 3)) * 3840 + nt * 256 + (lane >> 4) * 64 + (((lane & 3) ^ ((w & 1) << 1)) << 4) + 1024;
    c.wo[1] = c.wo[0] + 4 * 3840 - 1024;
    c.sx = rot * 128; c.sw = rot * 245760;
    unsigned ro = (mt * 128 + nt * 8 + w) * 1280;
    asm volatile("s_waitcnt lgkmcnt(0)" : "+s"(ka), "+v"(c.xo[1]), "+v"(c.wo[1]), "+s"(c.sw), "+s"(ro) :: "memory");
    p = __builtin_bit_cast(Params, ka);
    c.rx = mkrs(p.x, 2048 * 1280); c.rw = mkrs(p.wqkv - 3072, 640 * 3840 + 3072);
    asm volatile("s_mov_b32 m0, %[dm]\n\tbuffer_load_dwordx4 %[r4], %[l16], %[rx], %[ro] offen\n\tbuffer_load_dword %[r1], %[l4], %[rx], %[ro] offen offset:1024\n\t"
                 "s_cmp_eq_u32 %[nt], 15\n\ts_cbranch_scc1 1f\n\t" QD4 "s_barrier\n\t" QD4 "s_branch 2f\n1:\n\ts_waitcnt vmcnt(0)\n2:"
                 : [r4] "=&v"(row), [r1] "=&v"(rowt), [sx] "+&s"(c.sx), [sw] "+&s"(c.sw)
                 : [x0] "v"(c.xo[0]), [x1] "v"(c.xo[1]), [w0] "v"(c.wo[0]), [w1] "v"(c.wo[1]), [rx] "s"(c.rx), [rw] "s"(c.rw), [dm] "s"(w * 4096),
                   [l16] "v"(lane * 16), [l4] "v"(lane * 4), [ro] "s"(ro), [nt] "s"(nt) : "memory", "scc");
}
template <int C> DI float rorsum(float v) { return v + __uint_as_float(__builtin_amdgcn_update_dpp(0u, __float_as_uint(v), 0x120 + C, 0xf, 0xf, true)); }
DI u32x4 row_stats(u32x4 row, unsigned rowt, unsigned T) {
    float s = 0.f, q = 0.f;
#pragma unroll
    for (int i = 0; i < 5; ++i) {
        const bf2v v = __builtin_bit_cast(bf2v, i < 4 ? row[i & 3] : rowt);
        s = __builtin_amdgcn_fdot2_f32_bf16(v, __builtin_bit_cast(bf2v, 0x3f803f80u), s, false);
        q = __builtin_amdgcn_fdot2_f32_bf16(v, v, q, false);
    }
    const f32x2 y = swap32(s, q);
    const float z = rorsum<1>(rorsum<2>(rorsum<4>(rorsum<8>(sum16(y[0] + y[1]))))) * (1.f / 640.f);
    const f32x2 v = swap32(z, z);
    const float ra = __builtin_amdgcn_rsqf(__builtin_fmaxf(v[1] - v[0] * v[0], 0.f) + 1e-5f);
    return u32x4{__float_as_uint(ra), __float_as_uint(-v[0] * ra), T, ~T};
}
#define MFQ(A, W, X) "v_mfma_f32_16x16x32_bf16 v[" A ":" A "+3], v[" W ":" W "+3], v[" X ":" X "+3], v[" A ":" A "+3]\n\t"
#define MFV(A, W, X) MFQ(A, X, W)
#define QTR(R, V, O) "ds_read_b64_tr_b16 v[" R ":" R "+1], %[" #V "] offset:" O "\n\t"
#define QRX(R, V, O) "ds_read_b128 v[" R ":" R "+3], %[" #V "] offset:" O "\n\t"
#define LGK(N) "s_waitcnt lgkmcnt(" #N ")\n\t"
#define QSEC(MF, P, Q, XA, WO, D1, D2, E1, E2) \
    "s_cmp_lt_u32 %[n], 8\n\t" LGK(4) MF("64", P, "68") QTR(Q, wa0, WO) MF("32", P, P "+8") QTR(Q "+2", wa0, WO "+1024") \
    "s_cbranch_scc0 1f\n\t" D1 "1:\n\t" MF("36", P, P "+12") QRX(Q "+8", XA, "0") LGK(5) MF("48", P "+4", P "+8") QRX(Q "+12", XA, "4096") \
    MF("52", P "+4", P "+12") QTR(Q "+4", wa1, WO) LGK(5) MF("40", P, P "+16") QTR(Q "+6", wa1, WO "+1024") \
    "s_cbranch_scc0 1f\n\t" D2 "1:\n\t" MF("56", P "+4", P "+16") QRX(Q "+16", XA, "8192") E1 MF("44", P, P "+20") QRX(Q "+20", XA, "12288") E2 MF("60", P "+4", P "+20")
#define QLDG "buffer_load_dwordx4 v[120:123], %[ga], %[rs], 0 offen sc1\n\t"
#define QADD(r) "v_add_u32 %[" #r "], %[d], %[" #r "]\n\t"
#define QRT(r, A, M) "v_mad_u32_u24 %[" #r "], %[hi], s" #M ", %[vlo]\n\tv_add_u32 %[" #r "], s" #A ", %[" #r "]\n\t"
#define QKV_ASM(MF, STRD) asm volatile( \
    "s_add_u32 m0, %[dm], 65536\n\ts_mov_b32 %[n], 0\n\t" QD4 "buffer_store_dwordx4 %[gr], %[go], %[rs], 0 offen sc1\n\t" \
    Z8("32") Z8("40") Z8("48") Z8("56") Z4("64") "v_mov_b32 v68, 0x3f803f80\n\tv_mov_b32 v69, v68\n\tv_mov_b64 v[70:71], v[68:69]\n\t" \
    "s_waitcnt vmcnt(9)\n\ts_barrier\n\t" TSA("1") TSS("25") \
    QTR("72", wa0, "0") QTR("74", wa0, "1024") QRX("80", xa0, "0") QRX("84", xa0, "4096") QTR("76", wa1, "0") QTR("78", wa1, "1024") QRX("88", xa0, "8192") QRX("92", xa0, "12288") \
    QDX(x0, 0) QDX(x1, 1024) "s_branch 6f\n" \
    "3:\n\ts_cmp_gt_u32 %[n], 2\n\ts_cbranch_scc1 2f\n\ts_waitcnt vmcnt(9)\n\ts_branch 5f\n" \
    "2:\n\ts_cmp_eq_u32 %[n], 10\n\ts_cbranch_scc1 7f\n\ts_cmp_eq_u32 %[n], 7\n\ts_cbranch_scc1 2f\n\ts_waitcnt vmcnt(5)\n\ts_branch 5f\n" \
    "2:\n\t" QLDG QRT(x0, 60, 64) QRT(x1, 61, 65) QRT(w0, 62, 66) QRT(w1, 63, 67) \
    "s_mov_b64 s[44:45], s[52:53]\n\ts_mov_b32 s46, s54\n\ts_mov_b64 s[48:49], s[56:57]\n\ts_mov_b32 s50, s58\n\ts_mov_b32 %[sx], 0\n\ts_mov_b32 %[sw], 0\n" \
    "4:\n\ts_waitcnt vmcnt(8)\n5:\n\ts_barrier\n\t" \
    QSEC(MF, "96", "72", xa0, "0", QDX(x0, 0), QDX(x1, 1024), "", "") \
    "6:\n\t" \
    QSEC(MF, "72", "96", xa1, "16384", QDW(w0, 2048), QDW(w1, 3072), \
         QADV "s_bitcmp1_b32 0x42, %[n]\n\ts_cselect_b32 %[d], %[wr], %[up]\n\ts_add_u32 m0, m0, %[d]\n\ts_cmp_eq_u32 %[n], 4\n\ts_cselect_b32 %[d], %[wr], %[up]\n\ts_add_u32 %[n], %[n], 1\n\t", \
         QADD(xa0) QADD(xa1) QADD(wa0) QADD(wa1)) TSI \
    "s_bitcmp1_b32 0x686, %[n]\n\ts_cbranch_scc0 4b\n\ts_branch 3b\n" \
    "7:\n\ts_movk_i32 %[n], " SPINS "\n\t" LGK(4) MF("64", "96", "68") MF("32", "96", "104") "s_waitcnt vmcnt(4)\n" \
    "1:\n\tv_cmp_ne_u64 vcc, v[122:123], %[tag]\n\ts_cbranch_vccz 2f\n\t" QLDG "s_waitcnt vmcnt(0)\n\ts_sub_u32 %[n], %[n], 1\n\ts_cbranch_scc0 1b\n" \
    "2:\n\t" MF("36", "96", "108") "v_mul_f32 v120, %[sc], v120\n\tv_mul_f32 v121, %[sc], v121\n\t" LGK(2) MF("48", "100", "104") \
    "ds_write2_b32 %[pa], v120, v121 offset1:64\n\t" MF("52", "100", "108") "s_nop 1\n\tds_write_b128 %[ca], v[64:67]\n\t" LGK(2) \
    MF("40", "96", "112") STRD MF("56", "100", "112") MF("44", "96", "116") MF("60", "100", "116") \
    "s_waitcnt lgkmcnt(0)\n\ts_barrier\n\ts_nop 7" \
    : "=&{v[32:63]}"(A), "=&{v[64:67]}"(cs0), "=&{v[124:155]}"(S), [rx] "+{s[44:47]}"(c.rx), [rw] "+{s[48:51]}"(c.rw), [sx] "+&s"(c.sx), [sw] "+&s"(c.sw), \
      [x0] "+&v"(c.xo[0]), [x1] "+&v"(c.xo[1]), [w0] "+&v"(c.wo[0]), [w1] "+&v"(c.wo[1]), [xa0] "+&v"(e.xa0), [xa1] "+&v"(e.xa1), [wa0] "+&v"(e.wa0), [wa1] "+&v"(e.wa1), \
      [n] "=&s"(n), [d] "=&s"(d) PFIO \
    : "{s[52:55]}"(rt.ra), "{s[56:59]}"(rt.rb), [rs] "s"(rws), [dm] "s"(w * 4096), [wr] "s"(-131072), [up] "s"(32768), [sc] "s"(e.sc), [gr] "v"(gr), [go] "v"(e.go), [ga] "v"(e.ga), \
      "{s[60:75]}"(rt.t), [vlo] "v"(rt.vlo), [hi] "v"(rt.hi), [tag] "v"(tag), [pa] "v"(e.pa), [pr] "v"(e.pr), [ca] "v"(e.ca) \
    : "memory", "scc", "vcc", "v68", "v69", "v70", "v71", "v72", "v73", "v74", "v75", "v76", "v77", "v78", "v79", VCL80, VCL96, VCL112L, "v120", "v121", "v122", "v123" PFCL)
#define QS2(R, O) "ds_read2_b32 v[" #R ":" #R "+1], %[pr] offset0:" #O " offset1:" #O "+64\n\t"
struct QkvAdr { unsigned xa0, xa1, wa0, wa1, go, ga, pa, pr, ca, so[2], sb, sc; };
DI void qkv_setup(QkvAdr& e, int mt, int nt, int w, int lane) {
    const unsigned t = lane & 15, g = lane >> 4, T = w & 1, cb = w >> 1, g64 = 2 * mt + T;
    const bool isv = nt >= 10, isq = nt < 5;
    e.xa0 = 4 * T * 4096 + t * 128 + ((g ^ (t >> 1)) << 4); e.xa1 = e.xa0 ^ 64;
    e.wa0 = g * 4096 + 2048 + cb * 256 + ((lane >> 2) & 3) * 64 + ((g ^ T) & 1) * 32 + (lane & 3) * 8; e.wa1 = e.wa0 ^ 32;
    e.go = lane == 0 ? WS_XS + (mt * 128 + nt * 8 + w) * 16 : OOB;
    e.ga = WS_XS + (g64 * 64 + (g >> 1) * 32 + (isv ? 8 * (t >> 2) + 4 * (g & 1) + (t & 3) : 2 * t + (g & 1))) * 16;
    e.pa = L_SM + w * 2048 + lane * 4; e.pr = L_SM + w * 2048 + (isv ? g * 16 : t * 4); e.ca = L_SM + w * 2048 + 1024 + lane * 16;
    e.sc = isq ? __builtin_bit_cast(unsigned, 0.22360679774997896f * 1.4426950408889634f) : 0x3f800000u;
    e.sb = isq ? WS_Q + g64 * 2560 : WS_KV + g64 * 5120 + (isv ? 2560 : 0);
#pragma unroll
    for (int n = 0; n < 2; ++n) {
        if (isv) {
            const unsigned col = (nt - 10) * 128 + cb * 32 + 16 * (n ^ T) + t, head = (col * 3277) >> 16;
            e.so[n] = head * (163840 - 320) + g * 320 + col * 16;
        } else {
            const unsigned G = (isq ? nt : nt - 5) * 32 + cb * 8 + 4 * (n ^ T) + g, head = (G * 205) >> 10;
            e.so[n] = head * (isq ? 81920 - 2560 : 163840 - 2560) + G * 512 + t * 16;
        }
        PIN(e.so[n]);
    }
    PIN(e.xa0); PIN(e.xa1); PIN(e.wa0); PIN(e.wa1); PIN(e.go); PIN(e.ga); PIN(e.pa); PIN(e.pr); PIN(e.ca); PINS(e.sb); PINS(e.sc);
}
template <bool ISV>
DI void qkv_body(QkvSrc& c, QkvAdr& e, const Retile& rt, rsrc_t rws, u32x4 gr, u32x2 tag, int w PROFARG) {
    f32x32 A, S; f32x4 cs0; unsigned n, d;
    if (ISV) QKV_ASM(MFV, QRX("124", pr, "0") QRX("128", pr, "256") QRX("132", pr, "64") QRX("136", pr, "320") QRX("140", pr, "128") QRX("144", pr, "384") QRX("148", pr, "192") QRX("152", pr, "448"));
    else QKV_ASM(MFQ, QS2(124, 0) QS2(126, 16) QS2(128, 32) QS2(130, 48));
    TS(2);
    const f32x4 cs1 = ldsr<f32x4>(e.ca ^ 2048);
#pragma unroll
    for (int n = 0; n < 2; ++n) {
        const f32x4 cs = n ? cs1 : cs0;
#pragma unroll
        for (int q = 0; q < 2; ++q) {
            u32x4 o;
#pragma unroll
            for (int i = 0; i < 4; ++i) {
                const int k = 2 * q + (i >> 1), j = 2 * (i & 1), a = 16 * n + 4 * k + j;
                o[i] = ISV ? pk2(A[a] * S[8 * k + j] + S[8 * k + 4 + j] * cs[0], A[a + 1] * S[8 * k + j + 1] + S[8 * k + 5 + j] * cs[0])
                           : pk2(A[a] * S[2 * k] + S[2 * k + 1] * cs[j], A[a + 1] * S[2 * k] + S[2 * k + 1] * cs[j + 1]);
            }
            asm volatile("buffer_store_dwordx4 %0, %1, %2, %3 offen offset:%4 sc1\n\ts_nop 1" :: "v"(o), "v"(e.so[n]), "s"(rws), "s"(e.sb), "n"(q * (ISV ? 1280 : 256)) : "memory");
        }
    }
    asm volatile("s_waitcnt vmcnt(4)" ::: "memory");
}

DI f32x16 qk_tile(u32x4 km, u32x2 kt, u32x4 qm, u32x2 qt, f32x16 c) {
    c = mfma32(asbf(km), asbf(qm), c);
    return __builtin_amdgcn_mfma_f32_32x32x8bf16_1k(__builtin_bit_cast(s4v, kt), __builtin_bit_cast(s4v, qt), c, 0, 0, 0);
}
DI float max16(const f32x16& s, float m) {
#pragma unroll
    for (int r = 0; r < 16; r += 2) m = __builtin_fmaxf(__builtin_fmaxf(s[r], s[r + 1]), m);
    return m;
}
DI f32x16 pv16(u32x4 v, f32x8 pr, f32x16 o) {
    return __builtin_amdgcn_smfmac_f32_32x32x32_bf16(asbf(v), __builtin_bit_cast(bf16v, pr), o, 0xDDDD, 0, 0);
}
#define EX4(P, S) "v_exp_f32 v[" #P "], v[" S "]\n\tv_exp_f32 v[" #P "+1], v[" S "+1]\n\tv_exp_f32 v[" #P "+2], v[" S "+2]\n\tv_exp_f32 v[" #P "+3], v[" S "+3]\n\t"
#define SMF(O, V, P) "v_smfmac_f32_32x32x32_bf16 v[" #O ":" #O "+15], v[" #V ":" #V "+3], v[" #P ":" #P "+7], %[idx]\n\t"
#define QKT(SN, AB) "v_mfma_f32_32x32x4_2b_bf16 v[" #SN ":" #SN "+31], v[156:157], v[152:153], 0 cbsz:1 abid:" #AB "\n\t"
#define QKA(SN) "v_mfma_f32_32x32x16_bf16 v[" #SN ":" #SN "+15], v[160:163], v[144:147], v[" #SN ":" #SN "+15]\n\t"
#define QKB(SN) "v_mfma_f32_32x32x16_bf16 v[" #SN "+16:" #SN "+31], v[160:163], v[148:151], v[" #SN "+16:" #SN "+31]\n\t"
#define STEP_(SC, VP, VC, L0, L1, L2, L3, L4) \
    L0 EX4(128, #SC) EX4(132, #SC "+4") SMF(112, VP, 136) EX4(136, #SC "+16") "s_waitcnt lgkmcnt(0)\n\t" SMF(96, 164, 128) EX4(140, #SC "+20") \
    L1 EX4(128, #SC "+8") SMF(112, 164, 136) L2 EX4(132, #SC "+12") L3 EX4(136, #SC "+24") SMF(96, VC, 128) L4 EX4(140, #SC "+28")
#define STEP(SC, SN, AB, VP, VC, KTRD, VRD, KRD, M1, M2) STEP_(SC, VP, VC, QKT(SN, AB) KTRD, QKA(SN) M1, VRD, QKB(SN) KRD, M2)
#define VR(VH, O) "s_mov_b64 exec, s[56:57]\n\tds_read_b128 v[164:167], %[av] offset:" O "\n\tds_read_b128 v[" #VH ":" #VH "+3], %[av] offset:" O "+640\n\ts_mov_b64 exec, -1\n\t"
#define KR(O) "ds_read_b64 v[160:161], %[a0] offset:" O "\n\tds_read_b64 v[162:163], %[a0] offset:" O "+512\n\t"
#define KT(O) "ds_read_b64 v[156:157], %[at] offset:" O "\n\t"
#define PRIO "s_cmp_eq_u32 %[grp], 0\n\ts_cbranch_scc1 1f\n\ts_setprio 1\n1:\n\t"
#define QUAD3(X, N, A0, B0, A1, B1, A2, B2) PRIO \
    STEP(32, 64, 1, 172, 168, KT(#X "+5120"), VR(172, #X "+1280"), KR(#X "+5120"), A0, B0) \
    STEP(64, 32, 0, 168, 172, "", VR(168, #X "+5120"), KR(#X "+5376"), A1, B1) \
    STEP(32, 64, 1, 172, 168, KT(#N), VR(172, #X "+6400"), KR(#N), A2, B2) "s_setprio 0\n\t"
#define QUAD(X, N, A0, B0, A1, B1, A2, B2, A3, B3) QUAD3(X, N, A0, B0, A1, B1, A2, B2) STEP(64, 32, 0, 168, 172, "", VR(168, #N), KR(#N "+256"), A3, B3) TSI
#define KVP_(D) "buffer_load_dwordx4 %[l16], s[40:43], s44 offen sc1 lds\n\ts_add_u32 m0, m0, " #D "\n\ts_add_u32 s44, s44, 4096\n\t"
#define KVP KVP_(4096)
#define LDQ(R, V, O) "buffer_load_dwordx2 v[" #R ":" #R "+1], %[" #V "], s[40:43], s45 offen offset:" #O " sc1\n\t"
#define STW(R, S) "buffer_store_dwordx4 v[" #R ":" #R "+3], %[vs], s[40:43], s" #S " offen sc1\n\t"
#define STM(V, S, O) "buffer_store_dwordx4 v[192:195], %[" #V "], s[40:43], s" #S " offen offset:" #O " sc1\n\t"
#define WOP(O) DM1("%[l16]", "s48", O)
#define WO5 "s_add_u32 m0, m0, 1024\n\ts_add_u32 s48, s48, 1024\n\t"
#define PK(R, O) LD1(#R, "%[l16]", "s52", #O)
#define PKA "s_add_u32 s52, s52, 4096\n\t"
struct Park { u32x32 a, b; u32x8 c; };

DI void phase_attn(const Params& p, const Retile& rt, rsrc_t rws, u32x2 tag, unsigned fq, int b, int w, int lane, int nt80, int ntg,
                   unsigned rv0, unsigned rv1, unsigned rsb, u32x4& r0, u32x2& r1, Park& pk PROFARG) {
    const int t = lane & 31, hh = lane >> 5, wl = w & 3, grp = w >> 2;
    const int head = (b & 7) * 4 + (b >> 6), qb = (b >> 3) & 7;
    const unsigned gb = grp * 61440, kvb = WS_KV + head * 163840;
    pk.a = retile_read(rt, b, w, lane);
    asm volatile("s_cmp_eq_u32 %[w], 2\n\ts_waitcnt vmcnt(0) lgkmcnt(0)\n\ts_barrier\n\ts_cbranch_scc0 1f\n\tbuffer_store_dwordx2 %[tag], %[fq], %[rs], 0 offen sc1\n1:"
                 : "+{v[176:207]}"(pk.a) : [w] "s"(w), [tag] "v"(tag), [fq] "v"(fq), [rs] "s"(rws) : "memory", "scc");
    TS(3);
    SBAR();
    const unsigned l16 = lane * 16, a0 = gb + hh * 1024 + t * 8, at = gb + lane * 8 + 2048, av = gb + 2560 + hh * 320 + (t < 20 ? t : 0) * 16;
    const unsigned ones = t == 20 ? 0x3f803f80u : 0u;
    const unsigned wob = WS_WO + nt80 * 102400;
    u32x8 sa;
    sa[0] = kvb + grp * 81920 + wl * 1024;
    sa[1] = WS_Q + head * 81920 + (qb * 4 + wl) * 2560;
    sa[2] = WS_O + b * 10240 + w * 1280;
    sa[3] = WS_B + qb * QBS + head * 40960 + w * 5120;
    sa[4] = wob + w * 5120; sa[5] = 122880 + w * 5120;
    sa[6] = wob + (grp + 1) * 40960 + wl * 5120; sa[7] = gb + 20480 + wl * 5120;
    u32x4 sb = {WS_WGU + ntg * 143360 + w * 17920, rsb, wob + 61440 + w * 2560, 40960u + w * 2560};
    const unsigned vfs = (w == 2 && lane == 0) ? WS_FR + b * 8 : OOB, vfe = (b == 0 && w == 0 && lane == 0) ? WS_E : OOB, vfp = WS_FR + w * 256 + t * 8;
    const unsigned fg = (b * 205) >> 10;
    const unsigned vcs = (b < 135 && w < 2) ? WS_CS + ((b - fg * 5) * 3584 + fg * 128 + w * 64 + lane) * 4 : OOB;
    const int n0 = (head * 20 + ((lane & 16) ? 19 : 0)) >> 7;
    const unsigned fo = WS_FQ + (w == 0 ? (5 + 5 * hh + n0) * 16 + (lane & 15) : n0 * 16 + 2 * qb + (lane & 1)) * 128;
    const unsigned va = hh * 1024 + grp * 256 + t * 8;
    unsigned e0 = gb + wl * 3072 + l16, e1 = 61440 - gb + wl * 3072 + l16, ov = (t >> 4) * 20480 + (t & 15) * 16, ow = hh ? OOB : ov;
    unsigned ob = WS_O + (qb * 16 + wl * 4 + grp * 2) * 20480, oc = ob + (64 + (head >> 1)) * 256 + (head & 1) * 8;
    ov += hh * 256; ob += head * 512;
    f32x32 O; u32x8 qm; u32x2 qt;
    asm volatile(
        Z8("96") Z8("104") Z8("112") Z8("120") Z8("136") "v_mov_b32 v164, %[one]\n\tv_mov_b32 v165, %[one]\n\tv_mov_b64 v[166:167], v[164:165]\n\tv_mov_b64 v[168:169], v[164:165]\n\t"
        "v_mov_b64 v[170:171], v[164:165]\n\tv_mov_b64 v[172:173], v[164:165]\n\tv_mov_b64 v[174:175], v[164:165]\n\tv_mov_b64 v[192:193], -1\n\tv_mov_b64 v[194:195], -1\n\t"
        "s_mov_b32 s56, 0xfffff\n\ts_mov_b32 s57, s56\n\ts_movk_i32 s58, " SPINS "\n\ts_cmp_gt_u32 %[w], 1\n\ts_cbranch_scc1 2f\n\t"
        "ds_read2st64_b32 v[196:197], %[cp] offset1:2\n\tds_read2st64_b32 v[198:199], %[cp] offset0:4 offset1:6\n"
        "1:\n\tbuffer_load_dwordx2 v[32:33], %[fo], s[40:43], 0 offen sc1\n\ts_waitcnt vmcnt(0) lgkmcnt(0)\n\tv_cmp_ne_u64 vcc, v[32:33], %[tag]\n\t"
        "s_cbranch_vccz 2f\n\ts_sub_u32 s58, s58, 1\n\ts_cbranch_scc0 1b\n"
        "2:\n\ts_mov_b32 m0, %[dm]\n\ts_barrier\n\t" TSA("4")
        KVP LDQ(144, va, 0) LDQ(146, va, 512) LDQ(148, vb, 0) LDQ(150, vb, 512) LDQ(152, vc, 2048) KVP "s_barrier\n\t" KVP KVP
        "v_add_f32 v196, v196, v197\n\tv_add_f32 v198, v198, v199\n\tv_add_f32 v196, v196, v198\n\ts_movk_i32 s58, " SPINS "\n\t"
        "s_waitcnt vmcnt(2)\n\ts_barrier\n\t" TSA("5")
        KT("0") KR("0") VR(168, "0") "s_waitcnt lgkmcnt(4)\n\t" QKT(32, 0) "s_waitcnt lgkmcnt(2)\n\ts_nop 7\n\ts_nop 7\n\ts_nop 2\n\t" QKA(32) QKB(32) KR("256")
        "s_waitcnt vmcnt(0)\n\ts_barrier\n\ts_nop 7\n\t" TSS("17") TSF("17")
        QUAD(0, 10240, KVP, KVP STW(176, 68), KVP, STW(180, 69) "buffer_store_dword v196, %[vcs], s[40:43], 0 offen sc1\n\t", KVP, STW(184, 70), KVP, STW(188, 71))
        "s_waitcnt vmcnt(8)\n\ts_barrier\n\t"
        QUAD(10240, 20480, KVP, STM(l16, 46, 0) STM(mv, 46, 1024), KVP, STM(l16, 47, 0) "s_add_u32 s47, s47, 1024\n\t", KVP, STM(l16, 47, 0), KVP, STM(l16, 47, 1024))
        "s_waitcnt vmcnt(5)\n\ts_barrier\n\tbuffer_store_dwordx2 %[tag], %[vfs], s[40:43], 0 offen sc1\n\t"
        QUAD(20480, 30720, KVP, STM(l16, 47, 2048), KVP_(-57344), STM(l16, 47, 3072), KVP, KVP, KVP, KVP)
        QUAD(30720, 40960, KVP, "", "buffer_load_dwordx2 v[198:199], %[vfp], s[40:43], 0 offen sc1\n\t", "", "", "", "", "")
        "s_mov_b32 m0, s49\n"
        "3:\n\ts_waitcnt vmcnt(0)\n\tv_cmp_ne_u64 vcc, v[198:199], %[tag]\n\ts_cbranch_vccz 4f\n\t"
        "buffer_load_dwordx2 v[198:199], %[vfp], s[40:43], 0 offen sc1\n\ts_sub_u32 s58, s58, 1\n\ts_cbranch_scc0 3b\n"
        "4:\n\ts_barrier\n\t" TSA("6") "buffer_store_dwordx2 %[tag], %[vfe], s[40:43], 0 offen sc1\n\t"
        QUAD(40960, 51200, WOP(0) WO5, PK(176, 0), WOP(0), PK(180, 1024), WOP(1024), PK(184, 2048), WOP(2048), PK(188, 3072) PKA)
        QUAD(51200, 0, WOP(3072) "s_mov_b32 m0, s51\n\ts_mov_b32 s48, s50\n\t", PK(192, 0), WOP(0) WO5, PK(196, 1024), WOP(0), PK(200, 2048), WOP(1024), PK(204, 3072) PKA)
        QUAD(0, 10240, WOP(2048), PK(208, 0), WOP(3072), PK(212, 1024), PK(216, 2048), PK(220, 3072) PKA, PK(224, 0), PK(228, 1024))
        QUAD3(10240, 0, PK(232, 2048), PK(236, 3072) PKA, PK(240, 0), PK(244, 1024),
              "buffer_load_dwordx4 v[248:251], %[rv0], %[rx], s53 offen\n\t", "buffer_load_dwordx2 v[252:253], %[rv1], %[rx], s53 offen\n\t")
        STEP_(64, 168, 172, "", "", "", "", "") TSI
        "s_mov_b32 m0, s55\n\ts_nop 0\n\t" SMF(112, 172, 136) "s_barrier\n\t"
        DM1("%[l16]", "s54", 0) DM1("%[l16]", "s54", 1024) "s_mov_b32 exec_hi, 0\n\t" DM1("%[l16]", "s54", 2048) "s_mov_b32 exec_hi, -1\n\ts_nop 7\n\ts_nop 7"
        : "=&{v[96:127]}"(O), "=&{v[144:151]}"(qm), "=&{v[152:153]}"(qt), "+{v[176:207]}"(pk.a), "=&{v[208:239]}"(pk.b), "=&{v[240:247]}"(pk.c), "=&{v[248:251]}"(r0), "=&{v[252:253]}"(r1),
          "+{s[44:51]}"(sa), "+{s[52:55]}"(sb), "+v"(e0), "+v"(e1), "+v"(ov), "+v"(ow), "+s"(ob), "+s"(oc) PFIO
        : "{s[40:43]}"(rws), "{s[60:75]}"(rt.t), [a0] "v"(a0), [at] "v"(at), [av] "v"(av), [l16] "v"(l16), [tag] "v"(tag), [vcs] "v"(vcs), [fo] "v"(fo),
          [cp] "v"(L_CP + (w & 1) * 256 + lane * 4), [mv] "v"(lane < 16 ? l16 : OOB), [vs] "v"(b * 8 + w < 1820 ? l16 : OOB), [one] "v"(ones), [idx] "v"(0xDDDD),
          [va] "v"(va), [vb] "v"(va ^ 256), [vc] "v"((lane * 8) ^ (grp * 256)), [vfs] "v"(vfs), [vfe] "v"(vfe), [vfp] "v"(vfp), [rv0] "v"(rv0), [rv1] "v"(rv1),
          [dm] "s"(gb + wl * 1024), [w] "s"(w), [grp] "s"(grp), [rx] "s"(mkrs(p.x, 2048 * 1280))
        : "memory", "scc", "vcc", "s56", "s57", "s58", VCL32, VCL48, VCL64, VCL80, VCL128, "v140", "v141", "v142", "v143", "v156", "v157", VCL160 PFCL);
    TS(7);
    float o[12];
#pragma unroll
    for (int i = 0; i < 3; ++i) ldsw<f32x4>(e0 + i * 1024, f32x4{O[16 + 4 * i], O[17 + 4 * i], O[18 + 4 * i], O[19 + 4 * i]});
    LBAR();
#pragma unroll
    for (int i = 0; i < 3; ++i) {
        const f32x4 v = ldsr<f32x4>(e1 + i * 1024);
#pragma unroll
        for (int k = 0; k < 4; ++k) o[4 * i + k] = O[4 * i + k] + v[k];
    }
    {
        f32x2 z = {0.f, 0.f};
#pragma unroll
        for (int r = 0; r < 12; r += 2) z += f32x2{o[r], o[r + 1]} * 0.f;
        const float l = (hh ? o[8] : 1.f) + (z[0] + z[1]);
        const bool bad = !(l < 1.8446744e19f && l > 8.6736174e-19f);
        if (__builtin_expect(anyl(bad), 0)) {
            float m = -__builtin_inff();
            const unsigned ko = hh * 1024 + t * 8, vo = 2560 + hh * 320 + (t < 20 ? t : 0) * 16;
            const u32x4 qa = {qm[0], qm[1], qm[2], qm[3]}; const u32x2 qe = {hh ? 0u : qt[0], hh ? 0u : qt[1]};
            f32x16 c = {}, A = {};
#pragma unroll 1
            for (int it = 0; it < 128; ++it) {
                const unsigned sb = kvb + ((it & 63) >> 1) * 5120;
                const u32x2 k0 = ld8c(rws, ko, sb + (it & 1) * 256), k1 = ld8c(rws, ko + 512, sb + (it & 1) * 256), k2 = ld8c(rws, t * 8 + 2048, sb + (it & 1) * 256);
                f32x16 s = qk_tile(u32x4{k0[0], k0[1], k1[0], k1[1]}, k2, qa, qe, c);
                if (it < 64) {
                    m = max16(s, m);
                    if (it == 63) {
                        const f32x2 mm = swap32(m, m);
                        m = __builtin_fmaxf(mm[0], mm[1]);
#pragma unroll
                        for (int r = 0; r < 16; ++r) c[r] = -m;
                    }
                } else {
                    u32x4 v0 = ld16c(rws, vo, sb + (it & 1) * 1280), v1 = ld16c(rws, vo + 640, sb + (it & 1) * 1280);
#pragma unroll
                    for (int i = 0; i < 4; ++i) { v0[i] = t < 20 ? v0[i] : ones; v1[i] = t < 20 ? v1[i] : ones; }
#pragma unroll
                    for (int r = 0; r < 16; ++r) s[r] = __builtin_amdgcn_exp2f(s[r]);
                    A = pv16(v0, __builtin_shufflevector(s, s, 0, 1, 2, 3, 4, 5, 6, 7), A);
                    A = pv16(v1, __builtin_shufflevector(s, s, 8, 9, 10, 11, 12, 13, 14, 15), A);
                }
            }
#pragma unroll
            for (int r = 0; r < 12; ++r) o[r] = A[r];
        }
    }
    const float inv = __builtin_amdgcn_rcpf(swap32(o[8], o[8])[1]);
    st16(rws, ov, ob, u32x4{pk2m(o[0] * inv, o[1] * inv), pk2m(o[2] * inv, o[3] * inv), pk2m(o[4] * inv, o[5] * inv), pk2m(o[6] * inv, o[7] * inv)});
    st8(rws, ow, oc, u32x2{pk2m(o[8] * inv, o[9] * inv), pk2m(o[10] * inv, o[11] * inv)});
    asm volatile("s_waitcnt vmcnt(0)" ::: "memory");
    TS(20);
}

#define MF16(A, W, B) "v_mfma_f32_16x16x32_bf16 v[" A ":" A "+3], v[" W ":" W "+3], v[" B ":" B "+3], v[" A ":" A "+3]\n\t"
#define RD1(W, V, O) "ds_read_b128 v[" W ":" W "+3], " V " offset:" O "\n\t"
#define GUN(n, H, V, O) "s_waitcnt lgkmcnt(6)\n\t" MF16("140+8*" #n, "112+4*" #n, H) MF16("144+8*" #n, "112+4*" #n, H "+8") RD1("112+4*" #n, V, O "+1024*" #n)
#define GUK(H, V, O) GUN(0, H, V, O) GUN(1, H, V, O) GUN(2, H, V, O) GUN(3, H, V, O) GUN(4, H, V, O) GUN(5, H, V, O) GUN(6, H, V, O)
#define LDH(H, S, O) LD1(#H, "%[l16]", S, O) LD1(#H "+4", "%[l16]", S, O "+1024") LD1(#H "+8", "%[lb]", S, O) LD1(#H "+12", "%[lb]", S, O "+1024")
#define RETRY(H, TMP, L) "1:\n\t" CHK(H, TMP, "vcc") "s_cbranch_vccz 2f\n\t" L "s_waitcnt vmcnt(0)\n\ts_sub_u32 s59, s59, 1\n\ts_cbranch_scc0 1b\n\ts_mov_b32 s59, 0\n2:\n\t"
#define GUS(H, PRE, O1, V2, O2, TAIL) \
    "s_waitcnt vmcnt(16)\n" RETRY(H, 196, LDH(H, "s44", "0")) PRE \
    GUK(#H, "%[wa]", O1) GUK(#H "+4", V2, O2) \
    "s_add_u32 s45, s44, 10240\n\ts_add_u32 s44, s44, 2048\n\ts_cmp_lt_u32 s45, s47\n\ts_cbranch_scc0 3f\n\t" LDH(H, "s45", "0") "s_branch 4f\n3:\n\t" TAIL "4:\n\t" TSI
#define LDST LD1("32", "%[l16]", "s51", "0") LD1("36", "%[l16]", "s51", "1024") LD1("40", "%[l16]", "s51", "2048") LD1("44", "%[l16]", "s51", "3072")
#define PW(R, O) "ds_write_b128 %[pw], v[" #R ":" #R "+3] offset:" #O "\n\t"
#define PW4(R, O) PW(R, O) PW(R+4, O+1024) PW(R+8, O+2048) PW(R+12, O+3072)
#define PARK "s_cmpk_lg_u32 s59, " SPINS "\n\ts_cbranch_scc1 8f\n\t" PW4(176, 0) PW4(192, 4096) PW4(208, 8192) PW4(224, 12288) PW(240, 16384) \
    "s_mov_b32 exec_hi, 0\n\t" PW(244, 17408) "s_mov_b32 exec_hi, -1\n\t" Z8("140") Z8("148") Z8("156") Z8("164") Z8("172") Z8("180") Z8("188") "8:\n\t"
DI void phase_gu(rsrc_t rws, int w, int lane, int qb, int nt80, int ntg, const Park& pk PROFARG) {
    const int t = lane & 15, g = lane >> 4;
    const unsigned l16 = lane * 16, wd = WS_WD + nt80 * 276480;
    if (ntg == 31) {
#pragma unroll 1
        for (int i = 0; i < 5; ++i) dma3c(20480 + (i * 8 + w) * 3072, l16, rws, wd + (i * 8 + w) * 3072);
        return;
    }
    const unsigned qbb = WS_B + qb * QBS, hb = qbb + w * 40960, e = w * 64 + lane;
    u32x8 sa = {hb, WS_CS + ntg * 448, wd + w * 4096, hb + 20480, 20480u + w * 4096, wd + 65536 + w * 7168, 86016u + w * 7168, qbb + OFF_HS + w * 4096};
    unsigned wa = 20480 + l16;
    f32x32 A0; f32x16 A1, A2, ST; u32x4 any;
    asm volatile("" : "={v[172:175]}"(any));
#define CSL(R) "buffer_load_dword v" #R ", %[ve], s[40:43], s45 offen sc1\n\ts_add_u32 s45, s45, 14336\n\t"
    asm volatile(
        TSQ("59", "63") CSL(112) CSL(113) CSL(114) CSL(115) CSL(116) "s_add_u32 s45, s44, 4096\n\t"
        POLL3(LDH(32, "s44", "0"), LDH(48, "s44", "2048"), LDH(64, "s45", "0"), 32, 48, 64, 196, PARK)
        "s_add_u32 s59, s44, 8192\n\t" LDH(80, "s45", "2048") LDH(96, "s59", "0")
        "v_add_f32 v112, v112, v113\n\tv_add_f32 v114, v114, v115\n\tv_add_f32 v112, v112, v116\n\tv_add_f32 v196, v112, v114\n\ts_movk_i32 s59, " SPINS "\n\ts_waitcnt lgkmcnt(0)\n\ts_barrier\n\t" TSA("11") TSS("35")
        "ds_write_b32 %[vl], v196\n\t"
        RD1("112", "%[wa]", "0") RD1("116", "%[wa]", "1024") RD1("120", "%[wa]", "2048") RD1("124", "%[wa]", "3072") RD1("128", "%[wa]", "4096") RD1("132", "%[wa]", "5120") RD1("136", "%[wa]", "6144")
        "6:\n\t"
        GUS(32, "", "7168", "%[wa]", "14336", LDST)
        GUS(48, "", "21504", "%[wa]", "28672", "s_mov_b32 m0, s48\n\ts_barrier\n\t" DM4("%[l16]", "s46"))
        GUS(64, "", "35840", "%[wa]", "43008", "s_add_u32 m0, s48, 32768\n\ts_add_u32 s46, s46, 32768\n\t" DM4("%[l16]", "s46"))
        GUS(80, "", "50176", "%[wa]", "57344", "s_mov_b32 m0, s50\n\ts_barrier\n\t" DM4("%[l16]", "s49") "s_add_u32 m0, s50, 4096\n\ts_add_u32 s49, s49, 4096\n\t" DM3("%[l16]", "s49"))
        GUS(96, "v_add_u32 %[wa], 0xfc00, %[wa]\n\t", "0", "%[wn]", "0", "s_branch 7f\n")
        "v_mov_b32 %[wa], %[wn]\n\ts_branch 6b\n"
        "7:\n\t" TSA("12") "s_waitcnt vmcnt(15)\n" RETRY(32, 196, LDST) "s_waitcnt lgkmcnt(0)\n\ts_nop 7"
        : "=&{v[140:171]}"(A0), "={v[172:187]}"(A1), "={v[188:203]}"(A2), "=&{v[32:47]}"(ST), "+{s[44:51]}"(sa), [wa] "+&v"(wa) PFIO
        : "{s[40:43]}"(rws), "{v[176:207]}"(pk.a), "{v[208:239]}"(pk.b), "{v[240:247]}"(pk.c), "{v[172:175]}"(any), [pw] "v"(20480 + w * 17920 + l16), [l16] "v"(l16), [lb] "v"(l16 + 20480), [wn] "v"(l16 + 92160), [ve] "v"(e < 112 ? e * 4 : OOB), [vl] "v"(e * 4)
        : "memory", "scc", "vcc", "s59", "s60", "s61", "s62", "s63", "s64", "s65", VCL48, VCL64, VCL80, VCL96, VCL112, VCL128 PFCL);
    float ra, rb;
    {
        float s[2], q[2];
#pragma unroll
        for (int i = 0; i < 2; ++i) {
            s[i] = (ST[8 * i] + ST[8 * i + 2]) + (ST[8 * i + 4] + ST[8 * i + 6]);
            q[i] = (ST[8 * i + 1] + ST[8 * i + 3]) + (ST[8 * i + 5] + ST[8 * i + 7]);
        }
        const f32x2 ss = swap32(s[0], s[1]), qq = swap32(q[0], q[1]);
        const float mean = sum16(ss[0] + ss[1]) * (1.f / 640.f);
        const float var = __builtin_fmaxf(sum16(qq[0] + qq[1]) * (1.f / 640.f) - mean * mean, 0.f);
        ra = __builtin_amdgcn_rsqf(var + 1e-5f); rb = -mean * ra;
    }
    unsigned fo[14];
#pragma unroll
    for (int n = 0; n < 7; ++n) {
        const f32x4 cg = ldsr<f32x4>(n * 64 + (g & 1) * 16), cu = ldsr<f32x4>(n * 64 + 32 + (g & 1) * 16);
        float o[4];
#pragma unroll
        for (int i = 0; i < 4; ++i) {
            const f32x2 gu = n < 4 ? swap32(A0[8 * (n & 3) + i], A0[8 * (n & 3) + 4 + i]) : n < 6 ? swap32(A1[8 * (n & 1) + i], A1[8 * (n & 1) + 4 + i]) : swap32(A2[i], A2[4 + i]);
            const float gt = gu[0] * ra + rb * cg[i], up = gu[1] * ra + rb * cu[i];
            o[i] = (gt > 0.f ? gt : gt * 0.f) * up;
        }
        fo[2 * n] = pk2m(o[0], o[1]); fo[2 * n + 1] = pk2m(o[2], o[3]);
        const unsigned fv = (g >> 1) * 55296 + t * 16, fs = qbb + OFF_F + w * 110592 + ntg * 256;
        if (n & 1) st16(rws, fv + (g & 1) * 7936, fs + (n - 1) * 7936, u32x4{fo[2 * n - 2], fo[2 * n - 1], fo[2 * n], fo[2 * n + 1]});
        if (n == 6) st8(rws, ntg < 30 ? fv + (g & 1) * 8 : OOB, fs + 6 * 7936, u32x2{fo[12], fo[13]});
    }
}

#define G8N(n, W, B, V, O) "s_waitcnt lgkmcnt(9)\n\t" MF16("120+4*" #n, W "+4*" #n, B) RD1(W "+4*" #n, V, O "+1024*" #n)
#define G8K(W, B, V, O) G8N(0, W, B, V, O) G8N(1, W, B, V, O) G8N(2, W, B, V, O) G8N(3, W, B, V, O) G8N(4, W, B, V, O)
#define LDB(B, S, S2, S3) LD1(#B, "v141", S, "0") LD1(#B "+4", "v141", S, "1024") LD1(#B "+8", "v141", S2, "2048") LD1(#B "+12", "v141", S3, "3072")
#define DUP(N, S) "s_cmp_eq_u32 s45, " #N "\n\ts_cselect_b32 s51, 1024, 0\n\ts_sub_u32 s51, " S ", s51\n\t"
#define BRK(N) "s_cmp_eq_u32 s45, " #N "\n\ts_cbranch_scc1 9f\n\t"
#define G8S(SLOT, B, BN, LA, LN, BRK1, BRK2, LN4, LR4) \
    G8K("80", #B, LA, "20480") G8K("100", #B "+4", LA, "30720") BRK1 \
    "s_cmp_lt_u32 s45, s47\n\ts_cbranch_scc1 7f\n\ts_cmp_gt_u32 s45, 8\n\ts_cbranch_scc1 3f\n\ts_waitcnt vmcnt(0)\n3:\n\ts_waitcnt vmcnt(9)\n" \
    RETRY(BN, 140, LN4) \
    "s_waitcnt lgkmcnt(0)\n\ts_barrier\n\ts_cmp_gt_u32 s45, 12\n\ts_cbranch_scc0 7f\n\t" \
    "s_add_u32 m0, s48, " #SLOT "*40960\n\ts_add_u32 s50, s46, 4096\n\t" DM4("v141", "s46") "s_add_u32 m0, m0, 4096\n\ts_add_u32 s46, s46, 40960\n\t" DM1("v141", "s50", 0) \
    "7:\n\t" G8K("80", #B "+8", LN, "0") BRK2 G8K("100", #B "+12", LN, "10240") \
    "s_add_u32 s50, s49, 8192\n\ts_add_u32 s49, s49, 4096\n\ts_sub_u32 s45, s45, 4\n\ts_cmp_gt_u32 s45, 8\n\ts_cbranch_scc0 8f\n\t" LR4 "8:\n\t" TSI
#define RD5(W, V, O) RD1(W, V, O) RD1(W "+4", V, O "+1024") RD1(W "+8", V, O "+2048") RD1(W "+12", V, O "+3072") RD1(W "+16", V, O "+4096")
#define G80_BODY \
    ".Lg80:\n\ts_add_u32 s49, s44, 4096\n\ts_add_u32 s50, s44, 8192\n\ts_cmp_gt_u32 s45, 12\n\ts_cselect_b32 s51, 0, 2048\n\ts_sub_u32 s51, s50, s51\n\t" \
    POLL3(LDB(32, "s44", "s44", "s44"), LDB(48, "s49", "s49", "s49"), LDB(64, "s50", "s51", "s51"), 32, 48, 64, 140, Z8("120") Z8("128") Z4("136")) \
    "s_movk_i32 s59, " SPINS "\n\ts_barrier\n\t" TSM TSS("45") RD5("80", "v142", "0") RD5("100", "v142", "10240") \
    "6:\n\t" \
    G8S(0, 32, 48, "v142", "v143", "", BRK(3), LDB(48, "s49", "s49", "s49"), DUP(11, "s50") LDB(32, "s50", "s50", "s51")) \
    G8S(1, 48, 64, "v143", "v144", "", "", LDB(64, "s49", "s49", "s49"), LDB(48, "s50", "s50", "s50")) \
    G8S(2, 64, 32, "v144", "v142", BRK(2), "", DUP(7, "s49") LDB(32, "s49", "s49", "s51"), LDB(64, "s50", "s50", "s50")) \
    "s_branch 6b\n9:\n\ts_waitcnt lgkmcnt(0)\n\ts_nop 7\n\ts_setpc_b64 s[56:57]\n"
template <int PS>
DI void g80(rsrc_t rws, int w, int lane, int nt, unsigned ab, u32x4& r0, u32x2& r1, Park& pk PROFARG) {
    const int tg = w & 3, kh = w >> 2;
    const unsigned l16 = lane * 16, la = kh * 5120 + l16;
    u32x4 sa = {ab, PS ? 27u : 10u, WS_WD + nt * 276480 + w * 5120 + 3 * 40960, PS ? 5u : 100u};
    f32x16 A; f32x4 a4;
    asm volatile(PFT TSQ("%[ti]*4/5+45", "%[ti]*4/5+49") "s_call_b64 s[56:57], .Lg80"
        : "=&{v[120:135]}"(A), "=&{v[136:139]}"(a4), "+{s[44:47]}"(sa) PFIO
        : "{s[40:43]}"(rws), "{s48}"(20480 + w * 5120), "{v141}"(l16), "{v142}"(la + (PS ? 20480 : 122880)), "{v143}"(la + (PS ? 61440 : 20480)), "{v144}"(la + (PS ? 102400 : 81920)) PFM(PS ? 13 : 8)
        : "memory", "scc", "vcc", "s49", "s50", "s51", "s56", "s57", "s59", "s60", "s61", "s62", "s63", "s64", "s65", VCL32, VCL48, VCL64, VCL80, VCL96, VCL112L, "v140" PFCL);
    if (!PS) asm volatile("" : "+{v[176:207]}"(pk.a), "+{v[208:239]}"(pk.b), "+{v[240:247]}"(pk.c), "+{v[248:251]}"(r0), "+{v[252:253]}"(r1));
    if (PS) TS(14); else TS(9);
    const unsigned ex = tg * 5120 + l16;
    f32x4 a0 = {A[0], A[1], A[2], A[3]}, a1 = {A[4], A[5], A[6], A[7]};
    const f32x4 a2 = {A[8], A[9], A[10], A[11]}, a3 = {A[12], A[13], A[14], A[15]};
    if (kh == 0) { ldsw<f32x4>(ex + 2048, a2); ldsw<f32x4>(ex + 3072, a3); }
    else { ldsw<f32x4>(ex, a0); ldsw<f32x4>(ex + 1024, a1); ldsw<f32x4>(ex + 4096, a4); a0 = a2; a1 = a3; }
    LBAR();
    a0 += ldsr<f32x4>(ex + kh * 2048); a1 += ldsr<f32x4>(ex + kh * 2048 + 1024); a4 += ldsr<f32x4>(ex + 4096);
    r0 = u32x4{pk2(a0[0] + bflo(r0[0]), a0[1] + bfhi(r0[0])), pk2(a0[2] + bflo(r0[1]), a0[3] + bfhi(r0[1])),
               pk2(a1[0] + bflo(r0[2]), a1[1] + bfhi(r0[2])), pk2(a1[2] + bflo(r0[3]), a1[3] + bfhi(r0[3]))};
    r1 = u32x2{pk2(a4[0] + bflo(r1[0]), a4[1] + bfhi(r1[0])), pk2(a4[2] + bflo(r1[1]), a4[3] + bfhi(r1[1]))};
}

__global__ void __launch_bounds__(512, 2) block_kernel(Params) {
    u32x16 ka;
    asm volatile("s_load_dwordx16 %0, %1, 0" : "=s"(ka) : "s"(__builtin_amdgcn_kernarg_segment_ptr()) : "memory");
    __shared__ __attribute__((aligned(16))) char lds[163840];
    asm volatile("" :: "v"((LAS char*)lds) : "memory");
    const int lane = threadIdx.x & 63, w = __builtin_amdgcn_readfirstlane(threadIdx.x >> 6), b = blockIdx.x;
    unsigned pre;
    asm volatile("s_cmp_lg_u32 %1, %2\n\ts_cbranch_scc1 1f\n\ts_getpc_b64 s[4:5]\n.Lpc:\n\tv_lshlrev_b32 %0, 7, %3\n\tv_min_u32 %0, .Lend-.Lpc-4, %0\n\t"
                 "global_load_dword %0, %0, s[4:5]\n1:" : "=&{v200}"(pre) : "s"(w), "s"((b >> 3) & 3), "v"(threadIdx.x) : "memory", "scc", "s4", "s5");
    Params p;
    rsrc_t rws;
    const int xcd = b & 7, i = b >> 3;
    Retile rt;
    u32x2 tag;
    unsigned fq;
    {
        const int mt = (xcd >> 1) * 4 + (i & 3), nt = (xcd & 1) * 8 + (i >> 2), rot = (i >> 2) + (((i & 3) * 5 + 1) >> 1);
        QkvSrc c; QkvAdr e; u32x4 row; unsigned rowt, T;
        qkv_start(c, row, rowt, p, ka, mt, nt, rot < 10 ? rot : rot - 10, w, lane);
        rws = mkrs(p.ws, WS_END);
        asm volatile("s_load_dword %0, %2, %4\n\ts_load_dwordx16 %1, %3, %5" : "=&s"(T), "=&{s[60:75]}"(rt.t) : "s"(p.ws), "s"(&RT_TAB), "s"(WS_E), "s"((b * 8 + w) * 64) : "memory");
        TSW(40);
        retile_setup(rt, p, b, lane);
        qkv_setup(e, mt, nt, w, lane);
        fq = lane == 0 ? WS_FQ + (nt * 16 + mt) * 128 : OOB;
        TSW(41);
        asm volatile("s_waitcnt vmcnt(8) lgkmcnt(0)\n\ts_add_u32 %0, %0, 1" : "+s"(T), "+{s[60:75]}"(rt.t), "+v"(row), "+v"(rowt), "+v"(fq), "+{v200}"(pre) :: "memory", "scc");
        TSW(42);
        const u32x4 gr = row_stats(row, rowt, T);
        tag = u32x2{gr[2], gr[3]};
        TSW(43);
        if (nt == 15) {
            st16(rws, e.go, 0, gr);
            retile_issue(rt, w);
            asm volatile("s_waitcnt vmcnt(0)" ::: "memory");
        } else if (nt >= 10) qkv_body<true>(c, e, rt, rws, gr, tag, w PROFPASS);
        else qkv_body<false>(c, e, rt, rws, gr, tag, w PROFPASS);
    }
    const int mt = (xcd >> 1) * 8 + (i & 7), nt = (xcd & 1) * 4 + (i >> 3), ntg = (xcd & 1) * 16 + (i & 3) * 4 + (i >> 3);
    const int tg = w & 3, kh = w >> 2, t = lane & 15, g = lane >> 4, qb = mt >> 2, tk = (mt & 3) * 4 + tg;
    const unsigned rv0 = t * 1280 + (nt & 1) * 32 + kh * 64 + g * 16, rv1 = kh ? OOB : t * 1280 + ((nt & 1) ? 0 : 128) + g * 8;
    const unsigned rsb = (mt * 64 + tg * 16) * 1280 + nt * 160, qbb = WS_B + qb * QBS;
    u32x4 r0; u32x2 r1; Park pk;
    phase_attn(p, rt, rws, tag, fq, b, w, lane, nt, ntg, rv0, rv1, rsb, r0, r1, pk PROFPASS);
    g80<0>(rws, w, lane, nt, WS_O + (mt * 4 + tg) * 20480 + kh * 10240, r0, r1, pk PROFPASS);
    {
#pragma unroll
        for (int k = 0; k < 4; ++k) r0[k] = nomark(r0[k]);
        r1[0] = nomark(r1[0]); r1[1] = nomark(r1[1]);
        const unsigned hs = qbb + tk * 20480 + nt * 256;
        st16(rws, (2 * g + kh) * 2048 + t * 16, hs, r0);
        st8(rws, kh ? OOB : (8 + (g >> 1)) * 2048 + t * 16 + (g & 1) * 8, hs, r1);
        float s = 0.f, q = 0.f;
#pragma unroll
        for (int k = 0; k < 6; ++k) {
            const unsigned u = k < 4 ? r0[k] : kh ? 0u : r1[k - 4];
            const float a = bflo(u), c = bfhi(u);
            s += a + c; q += a * a + c * c;
        }
        const f32x2 y = swap32(s, q);
        const float z = sum16(y[0] + y[1]);
        const f32x2 v = swap32(z, z);
        st8(rws, g == 0 ? t * 16 + kh * 8 : OOB, qbb + OFF_HS + tk * 2048 + nt * 256, u32x2{nomark(__float_as_uint(v[0])), nomark(__float_as_uint(v[1]))});
    }
    TS(10);
    asm volatile("s_waitcnt vmcnt(0)" ::: "memory");
    TS(21);
    unsigned fb = qbb + OFF_F + tk * 55296 + kh * 27648;
    PINS(fb);
    phase_gu(rws, w, lane, qb, nt, ntg, pk PROFPASS);
    TS(22);
    asm volatile("s_waitcnt vmcnt(0)" ::: "memory");
    TS(23);
    g80<1>(rws, w, lane, nt, fb, r0, r1, pk PROFPASS);
    asm volatile("buffer_store_dwordx4 %0, %2, %4, %5 offen sc1\n\tbuffer_store_dwordx2 %1, %3, %4, %5 offen sc1" :: "v"(r0), "v"(r1), "v"(rv0), "v"(rv1), "s"(mkrs(p.out, 2048 * 1280)), "s"(rsb) : "memory");
    asm volatile("s_endpgm\n" G80_BODY ".Lend:\n" ::: "memory");
}

extern "C" void kernel_launch(void* const* d_in, const int* in_sizes, int n_in, void* d_out, int out_size, void* d_ws, size_t ws_size, hipStream_t stream) {
    Params p;
    p.x = (const char*)d_in[0]; p.wqkv = (const char*)d_in[1]; p.wo = (const char*)d_in[2];
    p.wg = (const char*)d_in[4]; p.wu = (const char*)d_in[5]; p.wd = (const char*)d_in[6];
    p.out = (char*)d_out; p.ws = (char*)d_ws;
    block_kernel<<<256, 512, 0, stream>>>(p);
}
```

```cpp
#include <hip/hip_runtime.h>
#include <hip/hip_bf16.h>
#include <stdint.h>

#define DI __device__ __forceinline__
typedef __attribute__((ext_vector_type(2))) __bf16 bf2v;
typedef __attribute__((ext_vector_type(8))) __bf16 bf8v;
typedef __attribute__((ext_vector_type(16))) __bf16 bf16v;
typedef __attribute__((ext_vector_type(4))) short s4v;
typedef __attribute__((ext_vector_type(2))) float f32x2;
typedef __attribute__((ext_vector_type(4))) float f32x4;
typedef __attribute__((ext_vector_type(8))) float f32x8;
typedef __attribute__((ext_vector_type(16))) float f32x16;
typedef __attribute__((ext_vector_type(32))) float f32x32;
typedef __attribute__((ext_vector_type(2))) unsigned u32x2;
typedef __attribute__((ext_vector_type(4))) unsigned u32x4;
typedef __attribute__((ext_vector_type(8))) unsigned u32x8;
typedef __attribute__((ext_vector_type(16))) unsigned u32x16;
typedef __attribute__((ext_vector_type(32))) unsigned u32x32;
typedef __amdgpu_buffer_rsrc_t rsrc_t;
#define LAS __attribute__((address_space(3)))

constexpr unsigned WS_Q = 0;
constexpr unsigned WS_KV = WS_Q + 32 * 81920;
constexpr unsigned WS_O = WS_KV + 32 * 163840;
constexpr unsigned WS_B = WS_O + 128 * 20480;
constexpr unsigned QBS = 32 * 40960, OFF_HS = 16 * 20480, OFF_F = OFF_HS + 16 * 2048;
constexpr unsigned WS_WO = WS_B + 8 * QBS;
constexpr unsigned WS_WD = WS_WO + 8 * 102400;
constexpr unsigned WS_WGU = WS_WD + 8 * 276480;
constexpr unsigned WS_CS = WS_WGU + 32 * 143360;
constexpr unsigned WS_FQ = WS_CS + 5 * 3584 * 4;
constexpr unsigned WS_FR = WS_FQ + 256 * 128;
constexpr unsigned WS_XS = WS_FR + 2048;
constexpr unsigned WS_E = WS_XS + 2048 * 16;
constexpr unsigned WS_END = WS_E + 128;
constexpr unsigned OOB = 0x40000000u;
constexpr int SPIN = 1 << 14;

struct Params {
    const char* x; const char* wqkv; const char* wo; const char* wg; const char* wu; const char* wd;
    char* out; char* ws;
};

#define SBAR() __builtin_amdgcn_sched_barrier(0)
#define PIN(x) asm volatile("" : "+v"(x))
#define WAITBAR(N) asm volatile("s_waitcnt vmcnt(" #N ") lgkmcnt(0)\n\ts_barrier" ::: "memory")
#define VBAR(N) asm volatile("s_waitcnt vmcnt(" #N ")\n\ts_barrier" ::: "memory")
#define LBAR() asm volatile("s_waitcnt lgkmcnt(0)\n\ts_barrier" ::: "memory")
#define BAR() asm volatile("s_barrier" ::: "memory")

DI rsrc_t mkrs(const char* p, unsigned bytes) { return __builtin_amdgcn_make_buffer_rsrc((void*)p, (short)0, (int)bytes, 0x00020000); }
template <class T> DI T ldsr(unsigned a) { return *(const LAS T*)(uintptr_t)a; }
template <class T> DI void ldsw(unsigned a, T v) { *(LAS T*)(uintptr_t)a = v; }
DI s4v ldstr(unsigned a) { return __builtin_amdgcn_ds_read_tr16_b64_v4i16((LAS s4v*)(uintptr_t)a); }
DI bf8v ldstr2(unsigned a, unsigned d) {
    s4v lo = ldstr(a), hi = ldstr(a + d);
    return __builtin_bit_cast(bf8v, __builtin_shufflevector(lo, hi, 0, 1, 2, 3, 4, 5, 6, 7));
}
DI f32x16 mfma32(bf8v a, bf8v b, f32x16 c) { return __builtin_amdgcn_mfma_f32_32x32x16_bf16(a, b, c, 0, 0, 0); }
DI f32x4 mfma16(bf8v a, bf8v b, f32x4 c) { return __builtin_amdgcn_mfma_f32_16x16x32_bf16(a, b, c, 0, 0, 0); }
DI bf8v asbf(u32x4 v) { return __builtin_bit_cast(bf8v, v); }
DI unsigned pk2(float lo, float hi) { f32x2 v = {lo, hi}; return __builtin_bit_cast(unsigned, __builtin_convertvector(v, bf2v)); }
DI float bflo(unsigned u) { return __uint_as_float(u << 16); }
DI float bfhi(unsigned u) { return __uint_as_float(u & 0xffff0000u); }
DI f32x2 swap32(float a, float b) { auto r = __builtin_amdgcn_permlane32_swap(__float_as_uint(a), __float_as_uint(b), false, false); return f32x2{__uint_as_float(r[0]), __uint_as_float(r[1])}; }
DI float sum16(float a) { auto r = __builtin_amdgcn_permlane16_swap(__float_as_uint(a), __float_as_uint(a), false, false); return __uint_as_float(r[0]) + __uint_as_float(r[1]); }
DI bool anyl(bool c) { return __builtin_amdgcn_ballot_w64(c) != 0; }
DI void st16(rsrc_t r, unsigned voff, unsigned soff, u32x4 v) { asm volatile("buffer_store_dwordx4 %0, %1, %2, %3 offen sc1\n\ts_nop 1" :: "v"(v), "v"(voff), "s"(r), "s"(soff) : "memory"); }
DI void st8(rsrc_t r, unsigned voff, unsigned soff, u32x2 v) { asm volatile("buffer_store_dwordx2 %0, %1, %2, %3 offen sc1" :: "v"(v), "v"(voff), "s"(r), "s"(soff) : "memory"); }
DI void st4(rsrc_t r, unsigned voff, unsigned soff, unsigned v) { asm volatile("buffer_store_dword %0, %1, %2, %3 offen sc1" :: "v"(v), "v"(voff), "s"(r), "s"(soff) : "memory"); }
DI u32x4 ld16c(rsrc_t r, unsigned voff, unsigned soff) { return __builtin_amdgcn_raw_buffer_load_b128(r, voff, soff, 16); }
DI u32x2 ld8c(rsrc_t r, unsigned voff, unsigned soff) { return __builtin_amdgcn_raw_buffer_load_b64(r, voff, soff, 16); }
DI void dma1(unsigned m0v, unsigned voff, rsrc_t r, unsigned soff) {
    asm volatile("s_mov_b32 m0, %0\n\ts_nop 0\n\tbuffer_load_dwordx4 %1, %2, %3 offen lds" :: "s"(m0v), "v"(voff), "s"(r), "s"(soff) : "memory"); }
DI void dma1c(unsigned m0v, unsigned voff, rsrc_t r, unsigned soff) {
    asm volatile("s_mov_b32 m0, %0\n\ts_nop 0\n\tbuffer_load_dwordx4 %1, %2, %3 offen sc1 lds" :: "s"(m0v), "v"(voff), "s"(r), "s"(soff) : "memory"); }
DI void dma3c(unsigned m0v, unsigned voff, rsrc_t r, unsigned soff) {
    asm volatile("s_mov_b32 m0, %0\n\ts_nop 0\n\tbuffer_load_dwordx4 %1, %2, %3 offen sc1 lds\n\tbuffer_load_dwordx4 %1, %2, %3 offen offset:1024 sc1 lds\n\t"
                 "buffer_load_dwordx4 %1, %2, %3 offen offset:2048 sc1 lds" :: "s"(m0v), "v"(voff), "s"(r), "s"(soff) : "memory"); }
DI void dma4c(unsigned m0v, unsigned voff, rsrc_t r, unsigned soff) {
    asm volatile("s_mov_b32 m0, %0\n\ts_nop 0\n\tbuffer_load_dwordx4 %1, %2, %3 offen sc1 lds\n\tbuffer_load_dwordx4 %1, %2, %3 offen offset:1024 sc1 lds\n\t"
                 "buffer_load_dwordx4 %1, %2, %3 offen offset:2048 sc1 lds\n\tbuffer_load_dwordx4 %1, %2, %3 offen offset:3072 sc1 lds" :: "s"(m0v), "v"(voff), "s"(r), "s"(soff) : "memory"); }
#define SPINS "0x4000"
#define PINS(x) asm volatile("" : "+s"(x))
#define CHK(B, TMP, DST) \
    "v_max3_u32 v" #TMP ", v[" #B "], v[" #B "+1], v[" #B "+2]\n\tv_max3_u32 v" #TMP ", v[" #B "+3], v[" #B "+4], v" #TMP "\n\t" \
    "v_max3_u32 v" #TMP ", v[" #B "+5], v[" #B "+6], v" #TMP "\n\tv_max3_u32 v" #TMP ", v[" #B "+7], v[" #B "+8], v" #TMP "\n\t" \
    "v_max3_u32 v" #TMP ", v[" #B "+9], v[" #B "+10], v" #TMP "\n\tv_max3_u32 v" #TMP ", v[" #B "+11], v[" #B "+12], v" #TMP "\n\t" \
    "v_max3_u32 v" #TMP ", v[" #B "+13], v[" #B "+14], v" #TMP "\n\tv_max_u32 v" #TMP ", v[" #B "+15], v" #TMP "\n\t" \
    "v_cmp_eq_u32 " DST ", -1, v" #TMP "\n\t"
#define LD1(R, V, S, O) "buffer_load_dwordx4 v[" R ":" R "+3], " V ", s[40:43], " S " offen offset:" O " sc1\n\t"
#define DM1(V, S, O) "buffer_load_dwordx4 " V ", s[40:43], " S " offen offset:" #O " sc1 lds\n\t"
#define DM3(V, S) DM1(V, S, 0) DM1(V, S, 1024) DM1(V, S, 2048)
#define DM4(V, S) DM3(V, S) DM1(V, S, 3072)
#define Z2(R) "v_mov_b64 v[" R ":" R "+1], 0\n\t"
#define Z4(R) Z2(R) Z2(R "+2")
#define Z8(R) Z4(R) Z4(R "+4")

#define VCL32 "v32", "v33", "v34", "v35", "v36", "v37", "v38", "v39", "v40", "v41", "v42", "v43", "v44", "v45", "v46", "v47"
#define VCL48 "v48", "v49", "v50", "v51", "v52", "v53", "v54", "v55", "v56", "v57", "v58", "v59", "v60", "v61", "v62", "v63"
#define VCL64 "v64", "v65", "v66", "v67", "v68", "v69", "v70", "v71", "v72", "v73", "v74", "v75", "v76", "v77", "v78", "v79"
#define VCL80 "v80", "v81", "v82", "v83", "v84", "v85", "v86", "v87", "v88", "v89", "v90", "v91", "v92", "v93", "v94", "v95"
#define VCL96 "v96", "v97", "v98", "v99", "v100", "v101", "v102", "v103", "v104", "v105", "v106", "v107", "v108", "v109", "v110", "v111"
#define VCL112 "v112", "v113", "v114", "v115", "v116", "v117", "v118", "v119", "v120", "v121", "v122", "v123", "v124", "v125", "v126", "v127"
#define VCL128 "v128", "v129", "v130", "v131", "v132", "v133", "v134", "v135", "v136", "v137", "v138", "v139"
#define VCL140 "v140", "v141", "v142", "v143", "v144", "v145", "v146", "v147", "v148", "v149", "v150", "v151", "v152", "v153", "v154", "v155", "v156", "v157", "v158", "v159"
#define VCL160 "v160", "v161", "v162", "v163", "v164", "v165", "v166", "v167", "v168", "v169", "v170", "v171", "v172", "v173", "v174", "v175"
#define VCL112L "v112", "v113", "v114", "v115", "v116", "v117", "v118", "v119"

#define PROFARG
#define PROFPASS
#define TSA(i)
#define PFIO
#define PFCL
#define TSM
#define PFM(i)
#define PFT
#define TS(i)

#define TSF(i)
#define TSI
#define TSS(i)
#define TSW(i)
#define TSP
#define TSQ(a, b)
constexpr unsigned L_RT = 0, L_SM = 32768, L_CP = 49152;
struct RtTab { unsigned v[2048][16]; };
constexpr RtTab make_rt() {
    RtTab t{};
    for (unsigned b = 0; b < 256; ++b) for (unsigned w = 0; w < 8; ++w) for (unsigned f = 0; f < 4; ++f) {
        unsigned A = 0, M = 0, SO = 0, R0 = 0, R1 = 0;
        if (b < 135) {
            const unsigned fg = b / 5, ks = (b % 5) * 4 + (w >> 1), j = ks >> 1, o = fg * 8 + (w & 1) * 4 + f;
            A = (320 * (ks & 1) + (j < 8 ? (j & 1) * 32 + (j >> 1) * 8 : 8 * j) + (f & 1) * 160) * 3456 + fg * 128 + (w & 1) * 64;
            M = (j < 8 ? 96 : 16) * 3456;
            SO = WS_WGU + (o / 7) * 143360 + ks * 7168 + (o % 7) * 1024;
            R0 = 16 * f;
        } else {
            const unsigned v = (b - 135) * 8 + w;
            const bool isWo = v < 200;
            const unsigned vv = v < 740 ? (isWo ? v : v - 200) : 0, ks = vv / 10, cu = vv % 10;
            const unsigned c = 4 * ks + f, j = c / 31, ng = c % 31;
            A = (isWo ? (ks >= 16 ? 40 * c - 2544 : 20 * (c >> 1) + 4 * (c & 1)) : 56 * ng + 16 * (j >> 1) + 4 * (j & 1)) * 1280 + cu * 128;
            M = (isWo ? (ks >= 16 ? 20 : 8) : (j < 6 ? 8 : 4)) * 1280;
            const unsigned seg = 2 * cu + (f >> 1), a = seg / 5, sm = seg % 5, fi = f & 1;
            const unsigned tile = 2 * a + (sm == 2 ? fi : sm > 2), blk = sm == 2 ? 4 : (sm == 1 || sm == 4) ? 2 + fi : fi;
            SO = isWo ? WS_WO + (tile * 100 + (ks >= 10 ? (ks - 10) * 10 + 5 + (blk < 4 ? blk ^ 2 : 4) : ks * 10 + blk)) * 1024 : WS_WD + (tile * 270 + ks * 5 + blk) * 1024;
            R0 = (f >> 1) * 64 + (sm == 2 ? 32 : 8) * fi; R1 = sm == 2 ? 8 : 16;
        }
        unsigned* r = t.v[b * 8 + w];
        r[f] = A + 3072 - 1024 * f; r[4 + f] = M; r[8 + f] = SO; r[12 + f] = (L_RT + w * 4096 + R0) | R1 << 16;
    }
    return t;
}
static __device__ const __attribute__((aligned(64))) RtTab RT_TAB = make_rt();
struct Retile { rsrc_t ra, rb; u32x16 t; unsigned vlo, hi; };
DI void retile_setup(Retile& r, const Params& p, unsigned b, unsigned lane) {
    const bool gu = b < 135;
    const char *pa, *pb; unsigned n;
    asm("s_cmpk_lt_u32 %3, 160\n\ts_cselect_b64 %0, %4, %5\n\ts_cselect_b32 %2, %9, %8\n\ts_cmpk_lt_u32 %3, 135\n\ts_cselect_b64 %1, %7, %0\n\ts_cselect_b64 %0, %6, %0\n\ts_cselect_b32 %2, %8, %2"
        : "=&s"(pa), "=&s"(pb), "=&s"(n) : "s"(b), "s"(p.wo), "s"(p.wd), "s"(p.wg), "s"(p.wu), "s"(640 * 3456 + 3072), "n"(640 * 1280 + 3072) : "scc");
    r.ra = mkrs(pa - 3072, n); r.rb = mkrs(pb - 3072, n);
    r.vlo = gu ? ((lane >> 2) & 7) * 3456 + (lane & 3) * 16 : ((lane >> 3) & 3) * 1280 + (lane & 7) * 16;
    r.hi = lane >> 5;
    PIN(r.vlo); PIN(r.hi); PINS(r.ra); PINS(r.rb);
}
DI void retile_issue(const Retile& r, int w) {
    asm volatile("s_mov_b32 m0, %0\n\ts_nop 0\n\tbuffer_load_dwordx4 %1, %5, 0 offen lds\n\tbuffer_load_dwordx4 %2, %5, 0 offen offset:1024 lds\n\t"
                 "buffer_load_dwordx4 %3, %6, 0 offen offset:2048 lds\n\tbuffer_load_dwordx4 %4, %6, 0 offen offset:3072 lds"
                 :: "s"(L_RT + w * 4096), "v"(r.vlo + r.t[0] + r.hi * r.t[4]), "v"(r.vlo + r.t[1] + r.hi * r.t[5]), "v"(r.vlo + r.t[2] + r.hi * r.t[6]),
                    "v"(r.vlo + r.t[3] + r.hi * r.t[7]), "s"(r.ra), "s"(r.rb) : "memory");
}
DI u32x32 retile_read(const Retile& r, unsigned b, int w, unsigned lane) {
    const u32x4 one = {0x3f803f80u, 0x3f803f80u, 0x3f803f80u, 0x3f803f80u};
    const unsigned f = lane & 3, row = 8 * (lane >> 4) + ((lane >> 2) & 3), dl = b < 135 ? 256 : 512;
    const unsigned rb = b < 135 ? (f >> 1) * 2048 + row * 64 + (f & 1) * 8 : row * 128;
    u32x32 o; f32x4 cp[4];
#pragma unroll
    for (int i = 0; i < 4; ++i) {
        const bf8v fr = ldstr2(rb + (r.t[12 + i] & 0xffff) + (r.t[12 + i] >> 16) * f, dl);
        cp[i] = mfma16(fr, asbf(one), f32x4{});
#pragma unroll
        for (int k = 0; k < 4; ++k) o[4 * i + k] = __builtin_bit_cast(u32x4, fr)[k];
    }
    if ((lane & 15) == 0) {
#pragma unroll
        for (int i = 0; i < 4; ++i) ldsw<f32x4>(L_CP + w * 256 + i * 64 + lane, cp[i]);
    }
    return o;
}

struct QkvSrc { rsrc_t rx, rw; unsigned xo[2], wo[2], sx, sw; };
#define QDX(V, O) "buffer_load_dwordx4 %[" #V "], %[rx], %[sx] offen offset:" #O " lds\n\t"
#define QDW(V, O) "buffer_load_dwordx4 %[" #V "], %[rw], %[sw] offen offset:" #O " lds\n\t"
#define QADV "s_add_u32 %[sx], %[sx], 128\n\ts_cmp_eq_u32 %[sx], 1280\n\ts_cselect_b32 %[sx], 0, %[sx]\n\ts_mul_i32 %[sw], %[sx], 1920\n\t"
#define QD4 QDX(x0, 0) QDX(x1, 1024) QDW(w0, 2048) QDW(w1, 3072) QADV "s_add_u32 m0, m0, 32768\n\t"
DI void qkv_start(QkvSrc& c, u32x4& row, unsigned& rowt, Params& p, u32x16 ka, int mt, int nt, int rot, int w, int lane) {
    const unsigned rr = lane >> 3, isv = nt >= 10;
    const unsigned tok = mt * 128 + (w >> 1) * 32 + ((w & 1) << (2 * isv)) + (rr & 4) * 2 + ((rr & 3) << (1 - isv));
    c.xo[0] = tok * 1280 + (((lane & 7) ^ (rr >> 1)) << 4);
    c.xo[1] = (c.xo[0] ^ 64) + 16 * 1280 - 1024;
    c.wo[0] = (8 * w + ((lane >> 2) & 3)) * 3840 + nt * 256 + (lane >> 4) * 64 + (((lane & 3) ^ ((w & 1) << 1)) << 4) + 1024;
    c.wo[1] = c.wo[0] + 4 * 3840 - 1024;
    c.sx = rot * 128; c.sw = rot * 245760;
    unsigned ro = (mt * 128 + nt * 8 + w) * 1280;
    asm volatile("s_waitcnt lgkmcnt(0)" : "+s"(ka), "+v"(c.xo[1]), "+v"(c.wo[1]), "+s"(c.sw), "+s"(ro) :: "memory");
    p = __builtin_bit_cast(Params, ka);
    c.rx = mkrs(p.x, 2048 * 1280); c.rw = mkrs(p.wqkv - 3072, 640 * 3840 + 3072);
    asm volatile("s_mov_b32 m0, %[dm]\n\tbuffer_load_dwordx4 %[r4], %[l16], %[rx], %[ro] offen\n\tbuffer_load_dword %[r1], %[l4], %[rx], %[ro] offen offset:1024\n\t"
                 "s_cmp_eq_u32 %[nt], 15\n\ts_cbranch_scc1 1f\n\t" QD4 "s_barrier\n\t" QD4 "s_branch 2f\n1:\n\ts_waitcnt vmcnt(0)\n2:"
                 : [r4] "=&v"(row), [r1] "=&v"(rowt), [sx] "+&s"(c.sx), [sw] "+&s"(c.sw)
                 : [x0] "v"(c.xo[0]), [x1] "v"(c.xo[1]), [w0] "v"(c.wo[0]), [w1] "v"(c.wo[1]), [rx] "s"(c.rx), [rw] "s"(c.rw), [dm] "s"(w * 4096),
                   [l16] "v"(lane * 16), [l4] "v"(lane * 4), [ro] "s"(ro), [nt] "s"(nt) : "memory", "scc");
}
template <int C> DI float rorsum(float v) { return v + __uint_as_float(__builtin_amdgcn_update_dpp(0u, __float_as_uint(v), 0x120 + C, 0xf, 0xf, true)); }
DI u32x4 row_stats(u32x4 row, unsigned rowt, unsigned T) {
    float s = 0.f, q = 0.f;
#pragma unroll
    for (int i = 0; i < 5; ++i) {
        const bf2v v = __builtin_bit_cast(bf2v, i < 4 ? row[i & 3] : rowt);
        s = __builtin_amdgcn_fdot2_f32_bf16(v, __builtin_bit_cast(bf2v, 0x3f803f80u), s, false);
        q = __builtin_amdgcn_fdot2_f32_bf16(v, v, q, false);
    }
    const f32x2 y = swap32(s, q);
    const float z = rorsum<1>(rorsum<2>(rorsum<4>(rorsum<8>(sum16(y[0] + y[1]))))) * (1.f / 640.f);
    const f32x2 v = swap32(z, z);
    const float ra = __builtin_amdgcn_rsqf(__builtin_fmaxf(v[1] - v[0] * v[0], 0.f) + 1e-5f);
    return u32x4{__float_as_uint(ra), __float_as_uint(-v[0] * ra), T, ~T};
}
#define MFQ(A, W, X) "v_mfma_f32_16x16x32_bf16 v[" A ":" A "+3], v[" W ":" W "+3], v[" X ":" X "+3], v[" A ":" A "+3]\n\t"
#define MFV(A, W, X) MFQ(A, X, W)
#define QTR(R, V, O) "ds_read_b64_tr_b16 v[" R ":" R "+1], %[" #V "] offset:" O "\n\t"
#define QRX(R, V, O) "ds_read_b128 v[" R ":" R "+3], %[" #V "] offset:" O "\n\t"
#define LGK(N) "s_waitcnt lgkmcnt(" #N ")\n\t"
#define QSEC(MF, P, Q, XA, WO, D1, D2, E1, E2) \
    "s_cmp_lt_u32 %[n], 8\n\t" LGK(4) MF("64", P, "68") QTR(Q, wa0, WO) MF("32", P, P "+8") QTR(Q "+2", wa0, WO "+1024") \
    "s_cbranch_scc0 1f\n\t" D1 "1:\n\t" MF("36", P, P "+12") QRX(Q "+8", XA, "0") LGK(5) MF("48", P "+4", P "+8") QRX(Q "+12", XA, "4096") \
    MF("52", P "+4", P "+12") QTR(Q "+4", wa1, WO) LGK(5) MF("40", P, P "+16") QTR(Q "+6", wa1, WO "+1024") \
    "s_cbranch_scc0 1f\n\t" D2 "1:\n\t" MF("56", P "+4", P "+16") QRX(Q "+16", XA, "8192") E1 MF("44", P, P "+20") QRX(Q "+20", XA, "12288") E2 MF("60", P "+4", P "+20")
#define QLDG "buffer_load_dwordx4 v[120:123], %[ga], %[rs], 0 offen sc1\n\t"
#define QADD(r) "v_add_u32 %[" #r "], %[d], %[" #r "]\n\t"
#define QRT(r, A, M) "v_mad_u32_u24 %[" #r "], %[hi], s" #M ", %[vlo]\n\tv_add_u32 %[" #r "], s" #A ", %[" #r "]\n\t"
#define QKV_ASM(MF, STRD) asm volatile( \
    "s_add_u32 m0, %[dm], 65536\n\ts_mov_b32 %[n], 0\n\t" QD4 "buffer_store_dwordx4 %[gr], %[go], %[rs], 0 offen sc1\n\t" \
    Z8("32") Z8("40") Z8("48") Z8("56") Z4("64") "v_mov_b32 v68, 0x3f803f80\n\tv_mov_b32 v69, v68\n\tv_mov_b64 v[70:71], v[68:69]\n\t" \
    "s_waitcnt vmcnt(9)\n\ts_barrier\n\t" TSA("1") TSS("25") \
    QTR("72", wa0, "0") QTR("74", wa0, "1024") QRX("80", xa0, "0") QRX("84", xa0, "4096") QTR("76", wa1, "0") QTR("78", wa1, "1024") QRX("88", xa0, "8192") QRX("92", xa0, "12288") \
    QDX(x0, 0) QDX(x1, 1024) "s_branch 6f\n" \
    "3:\n\ts_cmp_gt_u32 %[n], 2\n\ts_cbranch_scc1 2f\n\ts_waitcnt vmcnt(9)\n\ts_branch 5f\n" \
    "2:\n\ts_cmp_eq_u32 %[n], 10\n\ts_cbranch_scc1 7f\n\ts_cmp_eq_u32 %[n], 7\n\ts_cbranch_scc1 2f\n\ts_waitcnt vmcnt(5)\n\ts_branch 5f\n" \
    "2:\n\t" QLDG QRT(x0, 60, 64) QRT(x1, 61, 65) QRT(w0, 62, 66) QRT(w1, 63, 67) \
    "s_mov_b64 s[44:45], s[52:53]\n\ts_mov_b32 s46, s54\n\ts_mov_b64 s[48:49], s[56:57]\n\ts_mov_b32 s50, s58\n\ts_mov_b32 %[sx], 0\n\ts_mov_b32 %[sw], 0\n" \
    "4:\n\ts_waitcnt vmcnt(8)\n5:\n\ts_barrier\n\t" \
    QSEC(MF, "96", "72", xa0, "0", QDX(x0, 0), QDX(x1, 1024), "", "") \
    "6:\n\t" \
    QSEC(MF, "72", "96", xa1, "16384", QDW(w0, 2048), QDW(w1, 3072), \
         QADV "s_bitcmp1_b32 0x42, %[n]\n\ts_cselect_b32 %[d], %[wr], %[up]\n\ts_add_u32 m0, m0, %[d]\n\ts_cmp_eq_u32 %[n], 4\n\ts_cselect_b32 %[d], %[wr], %[up]\n\ts_add_u32 %[n], %[n], 1\n\t", \
         QADD(xa0) QADD(xa1) QADD(wa0) QADD(wa1)) TSI \
    "s_bitcmp1_b32 0x686, %[n]\n\ts_cbranch_scc0 4b\n\ts_branch 3b\n" \
    "7:\n\ts_movk_i32 %[n], " SPINS "\n\t" LGK(4) MF("64", "96", "68") MF("32", "96", "104") "s_waitcnt vmcnt(4)\n" \
    "1:\n\tv_cmp_ne_u64 vcc, v[122:123], %[tag]\n\ts_cbranch_vccz 2f\n\t" QLDG "s_waitcnt vmcnt(0)\n\ts_sub_u32 %[n], %[n], 1\n\ts_cbranch_scc0 1b\n" \
    "2:\n\t" MF("36", "96", "108") "v_mul_f32 v120, %[sc], v120\n\tv_mul_f32 v121, %[sc], v121\n\t" LGK(2) MF("48", "100", "104") \
    "ds_write2_b32 %[pa], v120, v121 offset1:64\n\t" MF("52", "100", "108") "s_nop 1\n\tds_write_b128 %[ca], v[64:67]\n\t" LGK(2) \
    MF("40", "96", "112") STRD MF("56", "100", "112") MF("44", "96", "116") MF("60", "100", "116") \
    "s_waitcnt lgkmcnt(0)\n\ts_barrier\n\ts_nop 7" \
    : "=&{v[32:63]}"(A), "=&{v[64:67]}"(cs0), "=&{v[124:155]}"(S), [rx] "+{s[44:47]}"(c.rx), [rw] "+{s[48:51]}"(c.rw), [sx] "+&s"(c.sx), [sw] "+&s"(c.sw), \
      [x0] "+&v"(c.xo[0]), [x1] "+&v"(c.xo[1]), [w0] "+&v"(c.wo[0]), [w1] "+&v"(c.wo[1]), [xa0] "+&v"(e.xa0), [xa1] "+&v"(e.xa1), [wa0] "+&v"(e.wa0), [wa1] "+&v"(e.wa1), \
      [n] "=&s"(n), [d] "=&s"(d) PFIO \
    : "{s[52:55]}"(rt.ra), "{s[56:59]}"(rt.rb), [rs] "s"(rws), [dm] "s"(w * 4096), [wr] "s"(-131072), [up] "s"(32768), [sc] "s"(e.sc), [gr] "v"(gr), [go] "v"(e.go), [ga] "v"(e.ga), \
      "{s[60:75]}"(rt.t), [vlo] "v"(rt.vlo), [hi] "v"(rt.hi), [tag] "v"(tag), [pa] "v"(e.pa), [pr] "v"(e.pr), [ca] "v"(e.ca) \
    : "memory", "scc", "vcc", "v68", "v69", "v70", "v71", "v72", "v73", "v74", "v75", "v76", "v77", "v78", "v79", VCL80, VCL96, VCL112L, "v120", "v121", "v122", "v123" PFCL)
#define QS2(R, O) "ds_read2_b32 v[" #R ":" #R "+1], %[pr] offset0:" #O " offset1:" #O "\n\tds_read2_b32 v[" #R "+2:" #R "+3], %[pr] offset0:" #O "+64 offset1:" #O "+64\n\t"
struct QkvAdr { unsigned xa0, xa1, wa0, wa1, go, ga, pa, pr, ca, so[2], sb, sc; };
DI void qkv_setup(QkvAdr& e, int mt, int nt, int w, int lane) {
    const unsigned t = lane & 15, g = lane >> 4, T = w & 1, cb = w >> 1, g64 = 2 * mt + T;
    const bool isv = nt >= 10, isq = nt < 5;
    e.xa0 = 4 * T * 4096 + t * 128 + ((g ^ (t >> 1)) << 4); e.xa1 = e.xa0 ^ 64;
    e.wa0 = g * 4096 + 2048 + cb * 256 + ((lane >> 2) & 3) * 64 + ((g ^ T) & 1) * 32 + (lane & 3) * 8; e.wa1 = e.wa0 ^ 32;
    e.go = lane == 0 ? WS_XS + (mt * 128 + nt * 8 + w) * 16 : OOB;
    e.ga = WS_XS + (g64 * 64 + (g >> 1) * 32 + (isv ? 8 * (t >> 2) + 4 * (g & 1) + (t & 3) : 2 * t + (g & 1))) * 16;
    e.pa = L_SM + w * 2048 + lane * 4; e.pr = L_SM + w * 2048 + (isv ? g * 16 : t * 4); e.ca = L_SM + w * 2048 + 1024 + lane * 16;
    e.sc = isq ? __builtin_bit_cast(unsigned, 0.22360679774997896f * 1.4426950408889634f) : 0x3f800000u;
    e.sb = isq ? WS_Q + g64 * 2560 : WS_KV + g64 * 5120 + (isv ? 2560 : 0);
#pragma unroll
    for (int n = 0; n < 2; ++n) {
        if (isv) {
            const unsigned col = (nt - 10) * 128 + cb * 32 + 16 * (n ^ T) + t, head = (col * 3277) >> 16;
            e.so[n] = head * (163840 - 320) + g * 320 + col * 16;
        } else {
            const unsigned G = (isq ? nt : nt - 5) * 32 + cb * 8 + 4 * (n ^ T) + g, head = (G * 205) >> 10;
            e.so[n] = head * (isq ? 81920 - 2560 : 163840 - 2560) + G * 512 + t * 16;
        }
        PIN(e.so[n]);
    }
    PIN(e.xa0); PIN(e.xa1); PIN(e.wa0); PIN(e.wa1); PIN(e.go); PIN(e.ga); PIN(e.pa); PIN(e.pr); PIN(e.ca); PINS(e.sb); PINS(e.sc);
}
template <bool ISV>
DI void qkv_body(QkvSrc& c, QkvAdr& e, const Retile& rt, rsrc_t rws, u32x4 gr, u32x2 tag, int w PROFARG) {
    f32x32 A, S; f32x4 cs0; unsigned n, d;
    if (ISV) QKV_ASM(MFV, QRX("124", pr, "0") QRX("128", pr, "256") QRX("132", pr, "64") QRX("136", pr, "320") QRX("140", pr, "128") QRX("144", pr, "384") QRX("148", pr, "192") QRX("152", pr, "448"));
    else QKV_ASM(MFQ, QS2(124, 0) QS2(128, 16) QS2(132, 32) QS2(136, 48));
    TS(2);
    const f32x4 cs1 = ldsr<f32x4>(e.ca ^ 2048);
#pragma unroll
    for (int n = 0; n < 2; ++n) {
        const f32x4 cs = n ? cs1 : cs0;
#pragma unroll
        for (int q = 0; q < 2; ++q) {
            u32x4 o;
#pragma unroll
            for (int i = 0; i < 4; ++i) {
                const int k = 2 * q + (i >> 1), j = 2 * (i & 1), a = 16 * n + 4 * k + j;
                o[i] = ISV ? pk2(A[a] * S[8 * k + j] + S[8 * k + 4 + j] * cs[0], A[a + 1] * S[8 * k + j + 1] + S[8 * k + 5 + j] * cs[0])
                           : pk2(A[a] * S[4 * k] + S[4 * k + 2] * cs[j], A[a + 1] * S[4 * k + 1] + S[4 * k + 3] * cs[j + 1]);
            }
            asm volatile("buffer_store_dwordx4 %0, %1, %2, %3 offen offset:%4 sc1\n\ts_nop 1" :: "v"(o), "v"(e.so[n]), "s"(rws), "s"(e.sb), "n"(q * (ISV ? 1280 : 256)) : "memory");
        }
    }
    asm volatile("s_waitcnt vmcnt(4)" ::: "memory");
}

DI f32x16 qk_tile(u32x4 km, u32x2 kt, u32x4 qm, u32x2 qt, f32x16 c) {
    c = mfma32(asbf(km), asbf(qm), c);
    return __builtin_amdgcn_mfma_f32_32x32x8bf16_1k(__builtin_bit_cast(s4v, kt), __builtin_bit_cast(s4v, qt), c, 0, 0, 0);
}
DI float max16(const f32x16& s, float m) {
#pragma unroll
    for (int r = 0; r < 16; r += 2) m = __builtin_fmaxf(__builtin_fmaxf(s[r], s[r + 1]), m);
    return m;
}
DI f32x16 pv16(u32x4 v, f32x8 pr, f32x16 o) {
    return __builtin_amdgcn_smfmac_f32_32x32x32_bf16(asbf(v), __builtin_bit_cast(bf16v, pr), o, 0xDDDD, 0, 0);
}
#define EX4(P, S) "v_exp_f32 v[" #P "], v[" S "]\n\tv_exp_f32 v[" #P "+1], v[" S "+1]\n\tv_exp_f32 v[" #P "+2], v[" S "+2]\n\tv_exp_f32 v[" #P "+3], v[" S "+3]\n\t"
#define SMF(O, V, P) "v_smfmac_f32_32x32x32_bf16 v[" #O ":" #O "+15], v[" #V ":" #V "+3], v[" #P ":" #P "+7], %[idx]\n\t"
#define QKT(SN, AB) "v_mfma_f32_32x32x4_2b_bf16 v[" #SN ":" #SN "+31], v[156:157], v[152:153], 0 cbsz:1 abid:" #AB "\n\t"
#define QKA(SN) "v_mfma_f32_32x32x16_bf16 v[" #SN ":" #SN "+15], v[160:163], v[144:147], v[" #SN ":" #SN "+15]\n\t"
#define QKB(SN) "v_mfma_f32_32x32x16_bf16 v[" #SN "+16:" #SN "+31], v[160:163], v[148:151], v[" #SN "+16:" #SN "+31]\n\t"
#define STEP_(SC, VP, VC, L0, L1, L2, L3, L4) \
    L0 EX4(128, #SC) EX4(132, #SC "+4") SMF(112, VP, 136) EX4(136, #SC "+16") "s_waitcnt lgkmcnt(0)\n\t" SMF(96, 164, 128) EX4(140, #SC "+20") \
    L1 EX4(128, #SC "+8") SMF(112, 164, 136) L2 EX4(132, #SC "+12") L3 EX4(136, #SC "+24") SMF(96, VC, 128) L4 EX4(140, #SC "+28")
#define STEP(SC, SN, AB, VP, VC, KTRD, VRD, KRD, M1, M2) STEP_(SC, VP, VC, QKT(SN, AB) KTRD, QKA(SN) M1, VRD, QKB(SN) KRD, M2)
#define VR(VH, O) "s_mov_b64 exec, s[56:57]\n\tds_read_b128 v[164:167], %[av] offset:" O "\n\tds_read_b128 v[" #VH ":" #VH "+3], %[av] offset:" O "+640\n\ts_mov_b64 exec, -1\n\t"
#define KR(O) "ds_read_b64 v[160:161], %[a0] offset:" O "\n\tds_read_b64 v[162:163], %[a0] offset:" O "+512\n\t"
#define KT(O) "ds_read_b64 v[156:157], %[at] offset:" O "\n\t"
#define PRIO "s_cmp_eq_u32 %[grp], 0\n\ts_cbranch_scc1 1f\n\ts_setprio 1\n1:\n\t"
#define QUAD3(X, N, A0, B0, A1, B1, A2, B2) PRIO \
    STEP(32, 64, 1, 172, 168, KT(#X "+5120"), VR(172, #X "+1280"), KR(#X "+5120"), A0, B0) \
    STEP(64, 32, 0, 168, 172, "", VR(168, #X "+5120"), KR(#X "+5376"), A1, B1) \
    STEP(32, 64, 1, 172, 168, KT(#N), VR(172, #X "+6400"), KR(#N), A2, B2) "s_setprio 0\n\t"
#define QUAD(X, N, A0, B0, A1, B1, A2, B2, A3, B3) QUAD3(X, N, A0, B0, A1, B1, A2, B2) STEP(64, 32, 0, 168, 172, "", VR(168, #N), KR(#N "+256"), A3, B3) TSI
#define KVP_(D) "buffer_load_dwordx4 %[l16], s[40:43], s44 offen sc1 lds\n\ts_add_u32 m0, m0, " #D "\n\ts_add_u32 s44, s44, 4096\n\t"
#define KVP KVP_(4096)
#define LDQ(R, V, O) "buffer_load_dwordx2 v[" #R ":" #R "+1], %[" #V "], s[40:43], s45 offen offset:" #O " sc1\n\t"
#define STW(R, S) "buffer_store_dwordx4 v[" #R ":" #R "+3], %[vs], s[40:43], s" #S " offen sc1\n\t"
#define STM(V, S, O) "buffer_store_dwordx4 v[192:195], %[" #V "], s[40:43], s" #S " offen offset:" #O " sc1\n\t"
#define WOP(O) DM1("%[l16]", "s48", O)
#define WO5 "s_add_u32 m0, m0, 1024\n\ts_add_u32 s48, s48, 1024\n\t"
#define PK(R, O) LD1(#R, "%[l16]", "s52", #O)
#define PKA "s_add_u32 s52, s52, 2048\n\ts_cmp_eq_u32 s52, %[pe]\n\ts_cselect_b32 s52, %[pb], s52\n\t"
#define TCH(O) "buffer_load_dword v154, %[vt], s[40:43], 0 offen offset:" #O " sc1\n\t"
DI unsigned mod5(unsigned v) { return (0x21043210u >> (4 * v)) & 7; }
DI unsigned park0(int w) { return ((w * 35 + 2) >> 2) * 2048; }
struct Park { u32x32 a, b; u32x8 c; unsigned d; };

DI void phase_attn(const Params& p, const Retile& rt, rsrc_t rws, u32x2 tag, unsigned fq, int b, int w, int lane, int nt80, int ntg,
                   unsigned rv0, unsigned rv1, unsigned rsb, u32x4& r0, u32x2& r1, Park& pk PROFARG) {
    const int t = lane & 31, hh = lane >> 5, wl = w & 3, grp = w >> 2;
    const int head = (b & 7) * 4 + (b >> 6), qb = (b >> 3) & 7;
    const unsigned gb = grp * 61440, kvb = WS_KV + head * 163840;
    pk.a = retile_read(rt, b, w, lane);
    asm volatile("s_cmp_eq_u32 %[w], 2\n\ts_waitcnt vmcnt(0) lgkmcnt(0)\n\ts_barrier\n\ts_cbranch_scc0 1f\n\tbuffer_store_dwordx2 %[tag], %[fq], %[rs], 0 offen sc1\n1:"
                 : "+{v[176:207]}"(pk.a) : [w] "s"(w), [tag] "v"(tag), [fq] "v"(fq), [rs] "s"(rws) : "memory", "scc");
    TS(3);
    SBAR();
    const unsigned l16 = lane * 16, a0 = gb + hh * 1024 + t * 8, at = gb + lane * 8 + 2048, av = gb + 2560 + hh * 320 + (t < 20 ? t : 0) * 16;
    const unsigned ones = t == 20 ? 0x3f803f80u : 0u;
    const unsigned wob = WS_WO + nt80 * 102400;
    u32x8 sa;
    sa[0] = kvb + grp * 81920 + wl * 1024;
    sa[1] = WS_Q + head * 81920 + (qb * 4 + wl) * 2560;
    sa[2] = WS_O + b * 10240 + w * 1280;
    sa[3] = WS_B + qb * QBS + head * 40960 + w * 5120;
    const unsigned ro = nt80 & 3, pb = WS_WGU + ntg * 143360, p0 = park0(w) + (((ntg & 15) * 5) >> 3) * 14336;
    sa[4] = wob + (grp + ro) * 20480 + wl * 5120; sa[5] = 122880 + w * 5120;
    sa[6] = wob + mod5(2 + 2 * grp + ro) * 20480 + wl * 5120; sa[7] = gb + 20480 + wl * 5120;
    u32x4 sb = {pb + p0 - ((int)p0 < 143360 ? 0 : 143360), rsb, wob + mod5(3 + ro) * 20480 + w * 2560, 40960u + w * 2560};
    const unsigned vt = lane < 34 ? WS_WD + nt80 * 276480 + qb * 34560 + (w * 34 + lane) * 128 : OOB;
    const unsigned vs = b < 135 ? l16 ^ (((lane >> 1) ^ (lane >> 3)) & 1) * 0xa0 : b * 8 + w < 1820 ? l16 : OOB;
    const unsigned vfs = (w == 2 && lane == 0) ? WS_FR + b * 8 : OOB, vfe = (b == 0 && w == 0 && lane == 0) ? WS_E : OOB, vfp = WS_FR + w * 256 + t * 8;
    const unsigned fg = (b * 205) >> 10;
    const unsigned vcs = (b < 135 && w < 2) ? WS_CS + ((b - fg * 5) * 3584 + fg * 128 + w * 64 + lane) * 4 : OOB;
    const int n0 = (head * 20 + ((lane & 16) ? 19 : 0)) >> 7;
    const unsigned fo = WS_FQ + (w == 0 ? (5 + 5 * hh + n0) * 16 + (lane & 15) : n0 * 16 + 2 * qb + (lane & 1)) * 128;
    const unsigned va = hh * 1024 + grp * 256 + t * 8;
    unsigned e0 = gb + wl * 3072 + l16, e1 = 61440 - gb + wl * 3072 + l16, ov = (t >> 4) * 20480 + (t & 15) * 16, ow = hh ? OOB : ov;
    unsigned ob = WS_O + (qb * 16 + wl * 4 + grp * 2) * 20480, oc = ob + (64 + (head >> 1)) * 256 + (head & 1) * 8;
    ov += hh * 256; ob += head * 512;
    f32x32 O; u32x8 qm; u32x2 qt;
    asm volatile(
        Z8("96") Z8("104") Z8("112") Z8("120") Z8("136") "v_mov_b32 v164, %[one]\n\tv_mov_b32 v165, %[one]\n\tv_mov_b64 v[166:167], v[164:165]\n\tv_mov_b64 v[168:169], v[164:165]\n\t"
        "v_mov_b64 v[170:171], v[164:165]\n\tv_mov_b64 v[172:173], v[164:165]\n\tv_mov_b64 v[174:175], v[164:165]\n\tv_mov_b64 v[192:193], -1\n\tv_mov_b64 v[194:195], -1\n\t"
        "s_mov_b32 s56, 0xfffff\n\ts_mov_b32 s57, s56\n\ts_movk_i32 s58, " SPINS "\n\ts_cmp_gt_u32 %[w], 1\n\ts_cbranch_scc1 2f\n\t"
        "ds_read2st64_b32 v[196:197], %[cp] offset1:2\n\tds_read2st64_b32 v[198:199], %[cp] offset0:4 offset1:6\n"
        "1:\n\tbuffer_load_dwordx2 v[32:33], %[fo], s[40:43], 0 offen sc1\n\ts_waitcnt vmcnt(0) lgkmcnt(0)\n\tv_cmp_ne_u64 vcc, v[32:33], %[tag]\n\t"
        "s_cbranch_vccz 2f\n\ts_sub_u32 s58, s58, 1\n\ts_cbranch_scc0 1b\n"
        "2:\n\ts_mov_b32 m0, %[dm]\n\ts_barrier\n\t" TSA("4")
        KVP LDQ(144, va, 0) LDQ(146, va, 512) LDQ(148, vb, 0) LDQ(150, vb, 512) LDQ(152, vc, 2048) KVP "s_barrier\n\t" KVP KVP
        "v_add_f32 v196, v196, v197\n\tv_add_f32 v198, v198, v199\n\tv_add_f32 v196, v196, v198\n\ts_movk_i32 s58, " SPINS "\n\t"
        "s_waitcnt vmcnt(2)\n\ts_barrier\n\t" TSA("5")
        KT("0") KR("0") VR(168, "0") "s_waitcnt lgkmcnt(4)\n\t" QKT(32, 0) "s_waitcnt lgkmcnt(2)\n\ts_nop 7\n\ts_nop 7\n\ts_nop 2\n\t" QKA(32) QKB(32) KR("256")
        "s_waitcnt vmcnt(0)\n\ts_barrier\n\ts_nop 7\n\t" TSS("17") TSF("17")
        QUAD(0, 10240, KVP, KVP STW(176, 68), KVP, STW(180, 69) "buffer_store_dword v196, %[vcs], s[40:43], 0 offen sc1\n\t", KVP, STW(184, 70), KVP, STW(188, 71))
        "s_waitcnt vmcnt(8)\n\ts_barrier\n\t"
        QUAD(10240, 20480, KVP, STM(l16, 46, 0) STM(mv, 46, 1024), KVP, STM(l16, 47, 0) "s_add_u32 s47, s47, 1024\n\t", KVP, STM(l16, 47, 0), KVP, STM(l16, 47, 1024))
        "s_waitcnt vmcnt(5)\n\ts_barrier\n\tbuffer_store_dwordx2 %[tag], %[vfs], s[40:43], 0 offen sc1\n\t"
        QUAD(20480, 30720, KVP, STM(l16, 47, 2048), KVP_(-57344), STM(l16, 47, 3072), KVP, KVP, KVP, KVP)
        QUAD(30720, 40960, KVP, "", "buffer_load_dwordx2 v[198:199], %[vfp], s[40:43], 0 offen sc1\n\t", "", "", "", "", "")
        "s_mov_b32 m0, s49\n"
        "3:\n\ts_waitcnt vmcnt(0)\n\tv_cmp_ne_u64 vcc, v[198:199], %[tag]\n\ts_cbranch_vccz 4f\n\t"
        "buffer_load_dwordx2 v[198:199], %[vfp], s[40:43], 0 offen sc1\n\ts_sub_u32 s58, s58, 1\n\ts_cbranch_scc0 3b\n"
        "4:\n\ts_barrier\n\t" TSA("6") "buffer_store_dwordx2 %[tag], %[vfe], s[40:43], 0 offen sc1\n\t"
        QUAD(40960, 51200, WOP(0) WO5, PK(176, 0), WOP(0), PK(180, 1024) PKA, WOP(1024), PK(184, 0), WOP(2048), PK(188, 1024) PKA)
        QUAD(51200, 0, WOP(3072) "s_mov_b32 m0, s51\n\ts_mov_b32 s48, s50\n\t", PK(192, 0), WOP(0) WO5, PK(196, 1024) PKA, WOP(0), PK(200, 0), WOP(1024), PK(204, 1024) PKA)
        QUAD(0, 10240, WOP(2048), PK(208, 0), WOP(3072), PK(212, 1024) PKA, PK(216, 0), PK(220, 1024) PKA, PK(224, 0), PK(228, 1024) PKA)
        QUAD3(10240, 0, PK(232, 0) TCH(0), PK(236, 1024) PKA, PK(240, 0) TCH(64), PK(244, 1024),
              "buffer_load_dwordx4 v[248:251], %[rv0], %[rx], s53 offen\n\t", "buffer_load_dwordx2 v[252:253], %[rv1], %[rx], s53 offen\n\t")
        STEP_(64, 168, 172, "", "", "", "", "") TSI
        "s_mov_b32 m0, s55\n\ts_nop 0\n\t" SMF(112, 172, 136) "s_barrier\n\t"
        DM1("%[l16]", "s54", 0) DM1("%[l16]", "s54", 1024) "s_mov_b32 exec_hi, 0\n\t" DM1("%[l16]", "s54", 2048) "s_mov_b32 exec_hi, -1\n\ts_nop 7\n\ts_nop 7"
        : "=&{v[96:127]}"(O), "=&{v[144:151]}"(qm), "=&{v[152:153]}"(qt), "+{v[176:207]}"(pk.a), "=&{v[208:239]}"(pk.b), "=&{v[240:247]}"(pk.c), "=&{v[248:251]}"(r0), "=&{v[252:253]}"(r1), "=&{v154}"(pk.d),
          "+{s[44:51]}"(sa), "+{s[52:55]}"(sb), "+v"(e0), "+v"(e1), "+v"(ov), "+v"(ow), "+s"(ob), "+s"(oc) PFIO
        : "{s[40:43]}"(rws), "{s[60:75]}"(rt.t), [a0] "v"(a0), [at] "v"(at), [av] "v"(av), [l16] "v"(l16), [tag] "v"(tag), [vcs] "v"(vcs), [fo] "v"(fo),
          [cp] "v"(L_CP + (w & 1) * 256 + lane * 4), [mv] "v"(lane < 16 ? l16 : OOB), [vs] "v"(vs), [vt] "v"(vt), [pb] "s"(pb), [pe] "s"(pb + 143360), [one] "v"(ones), [idx] "v"(0xDDDD),
          [va] "v"(va), [vb] "v"(va ^ 256), [vc] "v"((lane * 8) ^ (grp * 256)), [vfs] "v"(vfs), [vfe] "v"(vfe), [vfp] "v"(vfp), [rv0] "v"(rv0), [rv1] "v"(rv1),
          [dm] "s"(gb + wl * 1024), [w] "s"(w), [grp] "s"(grp), [rx] "s"(mkrs(p.x, 2048 * 1280))
        : "memory", "scc", "vcc", "s56", "s57", "s58", VCL32, VCL48, VCL64, VCL80, VCL128, "v140", "v141", "v142", "v143", "v156", "v157", VCL160 PFCL);
    TS(7);
    float o[12];
#pragma unroll
    for (int i = 0; i < 3; ++i) ldsw<f32x4>(e0 + i * 1024, f32x4{O[16 + 4 * i], O[17 + 4 * i], O[18 + 4 * i], O[19 + 4 * i]});
    LBAR();
#pragma unroll
    for (int i = 0; i < 3; ++i) {
        const f32x4 v = ldsr<f32x4>(e1 + i * 1024);
#pragma unroll
        for (int k = 0; k < 4; ++k) o[4 * i + k] = O[4 * i + k] + v[k];
    }
    {
        f32x2 z = {0.f, 0.f};
#pragma unroll
        for (int r = 0; r < 12; r += 2) z += f32x2{o[r], o[r + 1]} * 0.f;
        const float l = (hh ? o[8] : 1.f) + (z[0] + z[1]);
        const bool bad = !(l < 1.8446744e19f && l > 8.6736174e-19f);
        if (__builtin_expect(anyl(bad), 0)) {
            float m = -__builtin_inff();
            const unsigned ko = hh * 1024 + t * 8, vo = 2560 + hh * 320 + (t < 20 ? t : 0) * 16;
            const u32x4 qa = {qm[0], qm[1], qm[2], qm[3]}; const u32x2 qe = {hh ? 0u : qt[0], hh ? 0u : qt[1]};
            f32x16 c = {}, A = {};
#pragma unroll 1
            for (int it = 0; it < 128; ++it) {
                const unsigned sb = kvb + ((it & 63) >> 1) * 5120;
                const u32x2 k0 = ld8c(rws, ko, sb + (it & 1) * 256), k1 = ld8c(rws, ko + 512, sb + (it & 1) * 256), k2 = ld8c(rws, t * 8 + 2048, sb + (it & 1) * 256);
                f32x16 s = qk_tile(u32x4{k0[0], k0[1], k1[0], k1[1]}, k2, qa, qe, c);
                if (it < 64) {
                    m = max16(s, m);
                    if (it == 63) {
                        const f32x2 mm = swap32(m, m);
                        m = __builtin_fmaxf(mm[0], mm[1]);
#pragma unroll
                        for (int r = 0; r < 16; ++r) c[r] = -m;
                    }
                } else {
                    u32x4 v0 = ld16c(rws, vo, sb + (it & 1) * 1280), v1 = ld16c(rws, vo + 640, sb + (it & 1) * 1280);
#pragma unroll
                    for (int i = 0; i < 4; ++i) { v0[i] = t < 20 ? v0[i] : ones; v1[i] = t < 20 ? v1[i] : ones; }
#pragma unroll
                    for (int r = 0; r < 16; ++r) s[r] = __builtin_amdgcn_exp2f(s[r]);
                    A = pv16(v0, __builtin_shufflevector(s, s, 0, 1, 2, 3, 4, 5, 6, 7), A);
                    A = pv16(v1, __builtin_shufflevector(s, s, 8, 9, 10, 11, 12, 13, 14, 15), A);
                }
            }
#pragma unroll
            for (int r = 0; r < 12; ++r) o[r] = A[r];
        }
    }
    const float inv = __builtin_amdgcn_rcpf(swap32(o[8], o[8])[1]);
    st16(rws, ov, ob, u32x4{pk2(o[0] * inv, o[1] * inv), pk2(o[2] * inv, o[3] * inv), pk2(o[4] * inv, o[5] * inv), pk2(o[6] * inv, o[7] * inv)});
    st8(rws, ow, oc, u32x2{pk2(o[8] * inv, o[9] * inv), pk2(o[10] * inv, o[11] * inv)});
}

constexpr unsigned L_CS = 14336;
#define MF16(A, W, B) "v_mfma_f32_16x16x32_bf16 v[" A ":" A "+3], v[" W ":" W "+3], v[" B ":" B "+3], v[" A ":" A "+3]\n\t"
#define MF0(A, W, B) "v_mfma_f32_16x16x32_bf16 v[" A ":" A "+3], v[" W ":" W "+3], v[" B ":" B "+3], 0\n\t"
#define RD1(W, V, O) "ds_read_b128 v[" W ":" W "+3], " V " offset:" O "\n\t"
#define RD5(W, V, O) RD1(W, V, O) RD1(W "+4", V, O "+1024") RD1(W "+8", V, O "+2048") RD1(W "+12", V, O "+3072") RD1(W "+16", V, O "+4096")
#define WR1(V, R, O) "ds_write_b128 " V ", v[" R ":" R "+3] offset:" O "\n\t"
#define VM(N) "s_waitcnt vmcnt(" #N ")\n\t"
#define MX3(B) "v_max3_u32 %[t], v[" B "], v[" B "+1], v[" B "+2]\n\t"
#define MX2(B) "v_max3_u32 %[t], v[" B "], v[" B "+1], %[t]\n\t"
#define MXE(B) "v_max_u32 %[t], v[" B "], %[t]\n\tv_cmp_eq_u32 vcc, -1, %[t]\n\ts_and_b64 vcc, vcc, s[60:61]\n\t"
#define CK4(B) MX3(B) MXE(B "+3")
#define CK8(B) MX3(B) MX2(B "+3") MX2(B "+5") MXE(B "+7")
#define CK16(B) MX3(B) MX2(B "+3") MX2(B "+5") MX2(B "+7") MX2(B "+9") MX2(B "+11") MX2(B "+13") MXE(B "+15")
#define CK28(B) MX3(B) MX2(B "+3") MX2(B "+5") MX2(B "+7") MX2(B "+9") MX2(B "+11") MX2(B "+13") MX2(B "+15") MX2(B "+17") MX2(B "+19") MX2(B "+21") MX2(B "+23") MX2(B "+25") MXE(B "+27")
#define SPIN0 "s_movk_i32 s59, " SPINS "\n\ts_mov_b64 s[60:61], -1\n\t"
#define SPIN1 "s_sub_u32 s59, s59, 1\n\ts_cselect_b64 s[60:61], 0, s[60:61]\n\t"
#define UNP(D, S) "v_lshlrev_b32 v[" #D "], 16, v" #S "\n\tv_and_b32 v[" #D "+1], 0xffff0000, v" #S "\n\t"
#define PKADD(D, S) "v_pk_add_f32 v[" D ":" D "+1], v[" D ":" D "+1], v[" S ":" S "+1]\n\t"
#define CVT(D, S) "v_cvt_pk_bf16_f32 v[" D "], v[" S "], v[" S "+1]\n\t"
#define WON(n, W, B, R) MF16("72+4*" #n, W "+4*" #n, B) R
#define WOK(W, B, V, O) WON(0, W, B, RD1(W, V, O)) WON(1, W, B, RD1(W "+4", V, O "+1024")) WON(2, W, B, RD1(W "+8", V, O "+2048")) WON(3, W, B, RD1(W "+12", V, O "+3072")) WON(4, W, B, RD1(W "+16", V, O "+4096"))
#define WOE(W, B) WON(0, W, B, "") WON(1, W, B, "") WON(2, W, B, "") WON(3, W, B, "") WON(4, W, B, "")
#define WOL(B, S) LD1(#B, "%[l16]", #S, "0") LD1(#B "+4", "%[l16]", #S, "1024")
#define WOC(p, N, B) "1" #p ":\n\t" VM(N) CK8(#B) "s_cbranch_scc1 2" #p "f\n\t" TSI
#define WOT(p) "2" #p ":\n\ts_call_b64 s[56:57], 8f\n\ts_branch 1" #p "b\n"
#define WOR(B, S) CK8(#B) "s_cbranch_scc0 1f\n\ts_mov_b64 exec, vcc\n\t" WOL(B, S) "s_mov_b64 exec, -1\n1:\n\t"
#define PW(R, O) WR1("%[pw]", #R, #O)
#define PW4(R, O) PW(R, O) PW(R+4, O+1024) PW(R+8, O+2048) PW(R+12, O+3072)
#define PX4(R, O) WR1("%[t]", #R, #O) WR1("%[t]", #R "+4", #O "+1024") WR1("%[t]", #R "+8", #O "+2048") WR1("%[t]", #R "+12", #O "+3072")
#define CSL(R) "buffer_load_dword " R ", %[ve], s[40:43], s49 offen sc1\n\ts_add_u32 s49, s49, 14336\n\t"
DI void phase_wo(rsrc_t rws, int w, unsigned lane, int mt, int nt, int ntg, u32x4& r0, u32x2& r1, Park& pk PROFARG) {
    const unsigned tg = w & 3, kh = w >> 2, t = lane & 15, g = lane >> 4, l16 = lane * 16, la = kh * 5120 + l16, e = w * 64 + lane, ro = nt & 3;
    const unsigned ab = WS_O + (mt * 4 + tg) * 20480 + kh * 10240, hb = WS_B + (mt >> 2) * QBS + nt * 256, tk = (mt & 3) * 4 + tg;
    u32x8 sa = {ab + ro * 2048, ab + (ro + 1) * 2048, ab + mod5(ro + 2) * 2048, ab + mod5(ro + 3) * 2048, ab + mod5(ro + 4) * 2048, WS_CS + ntg * 448, hb + tk * 20480, 0};
    const unsigned ex = tg * 5120 + kh * 2048 + l16;
    unsigned tmp;
    asm volatile(
        CSL("v92") CSL("v93") CSL("v94") CSL("v95") CSL("%[t]") SPIN0 VM(7) "s_barrier\n\t"
        RD5("96", "%[wa]", "0") RD5("116", "%[wa]", "10240")
        UNP(72, 248) UNP(74, 249) UNP(76, 250) UNP(78, 251) Z8("80") UNP(88, 252) UNP(90, 253)
        VM(5) TSA("20") TSS("45") WOL(32, s44) WOL(40, s45) WOL(48, s46) WOL(56, s47) WOL(64, s48) RD5("136", "%[wa]", "20480") RD5("156", "%[wa]", "30720")
        "s_waitcnt vmcnt(10) lgkmcnt(0)\n\tv_add_f32 v92, v92, v93\n\tv_add_f32 v94, v94, v95\n\tv_add_f32 v92, v92, %[t]\n\tv_add_f32 v92, v92, v94\n\tds_write_b32 %[vl], v92\n"
        WOC(0, 8, 32) TSA("8") WOK("96", "32", "%[wb]", "0") WOK("116", "36", "%[wb]", "10240")
        WOC(1, 6, 40) WOK("136", "40", "%[wb]", "20480") WOK("156", "44", "%[wb]", "30720")
        WOC(2, 4, 48) LGK(10) WOK("96", "48", "%[wc]", "0") WOK("116", "52", "%[wc]", "10240")
        WOC(3, 2, 56) LGK(10) WOE("136", "56") WOE("156", "60")
        WOC(4, 0, 64) LGK(0) WOE("96", "64") WOE("116", "68") TSA("9")
        "s_cmp_eq_u32 %[kh], 0\n\ts_nop 5\n\t" WR1("%[xw]", "80", "0") WR1("%[xw]", "84", "1024") "s_cbranch_scc1 1f\n\t" WR1("%[x4]", "88", "0")
        "1:\n\ts_waitcnt lgkmcnt(0)\n\ts_barrier\n\t" RD1("80", "%[xr]", "0") RD1("84", "%[xr]", "1024") RD1("92", "%[x4]", "0")
        LGK(2) PKADD("72", "80") PKADD("74", "82") LGK(1) PKADD("76", "84") PKADD("78", "86") CVT("248", "72") CVT("249", "74") CVT("250", "76") CVT("251", "78")
        "buffer_store_dwordx4 v[248:251], %[h0], s[40:43], s50 offen sc1\n\t" LGK(0) "s_cbranch_scc0 9f\n\t"
        PKADD("88", "92") PKADD("90", "94") CVT("252", "88") CVT("253", "90") "buffer_store_dwordx2 v[252:253], %[h1], s[40:43], s50 offen sc1\n\ts_branch 9f\n"
        WOT(0) WOT(1) WOT(2) WOT(3) WOT(4)
        "8:\n\t" VM(0) WOR(32, s44) WOR(40, s45) WOR(48, s46) WOR(56, s47) WOR(64, s48) VM(0) SPIN1 "s_setpc_b64 s[56:57]\n9:\n\t"
        PW4(176, 0) PW4(192, 4096) PW(208, 8192) PW(212, 9216)
        : "+{v[248:251]}"(r0), "+{v[252:253]}"(r1), "+{v[176:207]}"(pk.a), "+{v[208:239]}"(pk.b), "+{v[240:247]}"(pk.c), "+{s[44:51]}"(sa), [t] "=&v"(tmp) PFIO
        : "{s[40:43]}"(rws), [l16] "v"(l16), [wa] "v"(la + 122880), [wb] "v"(la + 20480), [wc] "v"(la + 81920), [kh] "s"(kh), [pw] "v"(20480 + park0(w) + l16),
          [ve] "v"(e < 112 ? (e ^ (((lane >> 1) ^ (lane >> 3)) & 1) * 10) * 4 : OOB), [vl] "v"(L_CS + (e < 112 ? e : 112) * 4),
          [xw] "v"(tg * 5120 + 2048 - kh * 2048 + l16), [xr] "v"(ex), [x4] "v"((tg == 2 ? 61440 : tg * 5120 + 4096) + l16),
          [h0] "v"((2 * g + kh) * 2048 + t * 16), [h1] "v"((8 + (g >> 1)) * 2048 + t * 16 + (g & 1) * 8)
        : "memory", "scc", "vcc", "s56", "s57", "s59", "s60", "s61", VCL32, VCL48, VCL64, VCL80, VCL96, VCL112, VCL128, VCL140, VCL160 PFCL);
    TS(10);
    float s = 0.f, q = 0.f;
#pragma unroll
    for (int k = 0; k < 6; ++k) {
        const bf2v v = __builtin_bit_cast(bf2v, k < 4 ? r0[k & 3] : r1[k & 1]);
        s = __builtin_amdgcn_fdot2_f32_bf16(v, __builtin_bit_cast(bf2v, 0x3f803f80u), s, false);
        q = __builtin_amdgcn_fdot2_f32_bf16(v, v, q, false);
    }
    const f32x2 y = swap32(s, q);
    const float z = sum16(y[0] + y[1]);
    const f32x2 v = swap32(z, z);
    st8(rws, g == 0 ? t * 16 + kh * 8 : OOB, hb + OFF_HS + tk * 2048, u32x2{__float_as_uint(v[0]), __float_as_uint(v[1])});
}

#define GUN(MF, n, H, R, X) LGK(6) MF("140+8*" #n, "112+4*" #n, H) X MF("144+8*" #n, "112+4*" #n, H "+8") R
#define GUK(MF, H, V, O, D, X0, X1, X2, X3, X4, X5, X6) \
    GUN(MF, 0, H, RD1("112", V, O), X0) GUN(MF, 1, H, RD1("116", V, O "+" D), X1) GUN(MF, 2, H, RD1("120", V, O "+2*" D), X2) GUN(MF, 3, H, RD1("124", V, O "+3*" D), X3) \
    GUN(MF, 4, H, RD1("128", V, O "+4*" D), X4) GUN(MF, 5, H, RD1("132", V, O "+5*" D), X5) GUN(MF, 6, H, RD1("136", V, O "+6*" D), X6)
#define LH0(H, S) LD1(#H, "%[l16]", #S, "0")
#define LH1(H, S) LD1(#H "+4", "%[l16]", #S, "1024")
#define LH2(H, S) LD1(#H "+8", "%[lb]", #S, "0")
#define LH3(H, S) LD1(#H "+12", "%[lb]", #S, "1024")
#define LDH(H, S) LH0(H, S) LH1(H, S) LH2(H, S) LH3(H, S)
#define LH(H, S) LH0(H, S), LH1(H, S), LH2(H, S), LH3(H, S)
#define LS(R, O) LD1(#R, "%[l16]", "s54", #O)
#define LDST LS(32, 0) LS(36, 1024) LS(40, 2048) LS(44, 3072)
#define WL(R, O) LD1(#R, "%[l16]", "s52", #O)
#define WA "s_add_u32 s52, s52, 4096\n\t"
#define WEND "v_add_u32 %[lb], 4096, %[l16]\n\t" WL(32, 3072) LD1("36", "%[lb]", "s52", "0") LD1("40", "%[lb]", "s52", "1024") LD1("44", "%[z4]", "s52", "2048") LD1("96", "%[z4]", "s52", "3072") WA \
    LD1("100", "%[z4]", "s52", "0") LD1("104", "%[z4]", "s52", "1024") LD1("108", "%[z4]", "s52", "2048")
#define CKS(N, B, s) VM(N) MX3(B), MX2(B "+3"), MX2(B "+5"), MX2(B "+7"), MX2(B "+9"), MX2(B "+11"), MX2(B "+13") MXE(B "+15") "s_cbranch_scc1 4" #s "f\n3" #s ":\n\t"
#define GUK_(...) GUK(__VA_ARGS__)
#define GUS(MF, s, H, O1, PRE, O2, HN, N, ...) GUK_(MF, #H, "%[wa]", O1, "1024", __VA_ARGS__) PRE GUK_(MF16, #H "+4", "%[wa]", O2, "1024", CKS(N, #HN, s)) TSI
#define REDO(s, L, CK) "4" #s ":\n\ts_mov_b64 exec, vcc\n\t" L "s_mov_b64 exec, -1\n\t" VM(0) SPIN1 CK "s_cbranch_scc1 4" #s "b\n\ts_branch 3" #s "b\n"
#define WADV "v_add_u32 %[wa], 0xfc00, %[wa]\n\t"
#define HNX(A, B) "s_add_u32 s" #B ", s" #A ", 2048\n\ts_cmp_eq_u32 s" #B ", s55\n\ts_cselect_b32 s" #B ", %[hb], s" #B "\n\t"
#define SW32(A, B) "v_permlane32_swap_b32 v[" A "], v[" B "]\n\t"
#define SW16(A, B) "v_permlane16_swap_b32 v[" A "], v[" B "]\n\t"
#define EPF(A, C, T) "v_pk_fma_f32 v[" A ":" A "+1], v[" C ":" C "+1], v[16+4*" #T ":17+4*" #T "], v[" A ":" A "+1]\n\t"
#define EPT(A, C, T, D) EPF(A, C, T) EPF(A "+2", C "+2", T) "v_max_f32 v[" A "], 0, v[" A "]\n\tv_max_f32 v[" A "+1], 0, v[" A "+1]\n\t" \
    "v_pk_mul_f32 v[" A "+2:" A "+3], v[" A "+2:" A "+3], v[18+4*" #T ":19+4*" #T "]\n\t" \
    "v_pk_mul_f32 v[" A ":" A "+1], v[" A ":" A "+1], v[" A "+2:" A "+3]\n\t" CVT(D, A)
#define EPN(n, D) EPT("140+8*" #n, "112+4*" #n, 0, D) EPT("144+8*" #n, "112+4*" #n, 1, D "+1")
#define EP2(n, m, D, N) LGK(N) EPN(n, D) EPN(m, D "+2") SW32(D, D "+1") "s_nop 0\n\t" SW32(D "+2", D "+3") \
    "buffer_store_dwordx4 v[" D ":" D "+3], %[f0], s[40:43], s53 offen sc1\n\ts_add_u32 s53, s53, 15872\n\t"
#define LF0(F, S) LD1(F, "%[l16]", S, "0")
#define LF1(F, S) LD1(F "+4", "%[l16]", S, "1024")
#define LF2(F, S) LD1(F "+8", "%[l16]", S, "2048")
#define LF3(F, S) LD1(F "+12", "%[l16]", S, "3072")
#define LF4(F, S) LD1(F "+16", "%[lb]", S, "0")
#define LF5(F, S) LD1(F "+20", "%[lb]", S, "1024")
#define LF6(F, S) LD1(F "+24", "%[z4]", S, "2048")
#define LDF(F, S) LF0(F, S) LF1(F, S) LF2(F, S) LF3(F, S) LF4(F, S) LF5(F, S) LF6(F, S)
#define DK(MF, F, a, b, c, d, e) MF("168", #a, F) MF("172", #b, F) MF("176", #c, F) MF("180", #d, F) MF("184", #e, F)
#define DT(F, X0, X1, X2, X3, X4, X5, X6) DK(MF0, F, 196, 200, 204, 208, 212) X0 DK(MF16, F "+4", 216, 220, 224, 228, 232) X1 DK(MF16, F "+8", 236, 240, 244, 24, 28) X2 \
    DK(MF16, F "+12", 48, 52, 56, 60, 64) X3 DK(MF16, F "+16", 68, 72, 76, 80, 84) X4 DK(MF16, F "+20", 88, 92, 32, 36, 40) X5 DK(MF16, F "+24", 44, 96, 100, 104, 108) X6 TSI
#define DTL(F, S) DT(F, LF0(F, S), LF1(F, S), LF2(F, S), LF3(F, S), LF4(F, S), LF5(F, S), LF6(F, S))
#define DTE(F) DT(F, "", "", "", "", "", "", "")
#define DW(S) "v_add_u32 %[wa], " S ", %[l16]\n\t" WR1("%[wa]", "168", "0") WR1("%[wa]", "172", "1024") WR1("%[wa]", "176", "2048") WR1("%[wa]", "180", "3072") WR1("%[wa]", "184", "4096")
#define DC(N, F, s) VM(N) CK28(F) "s_cbranch_scc1 4" #s "f\n3" #s ":\n\t"
DI void phase_ffn(rsrc_t rws, int w, unsigned lane, int mt, int nt, int ntg, const Park& pk PROFARG) {
    const unsigned t = lane & 15, g = lane >> 4, l16 = lane * 16, qbb = WS_B + (mt >> 2) * QBS, hb = qbb + w * 40960, k0 = 7 * w - (w == 7);
    const unsigned fb = qbb + OFF_F + (mt & 3) * 4 * 55296 + k0 * 1024, fv = (g >> 1) * 55296 + t * 16;
    u32x8 sa; u32x4 sb = {WS_WD + nt * 276480 + k0 * 5120, qbb + OFF_F + w * 110592 + ntg * 256, qbb + OFF_HS + w * 4096, hb + 20480};
#pragma unroll
    for (int i = 0; i < 4; ++i) { sa[i] = fb + ((i + nt) & 3) * 55296; sa[4 + i] = ((i + nt) & 3) * 40960 + w * 5120; }
    unsigned wa = 20480 + l16, h0 = hb + (((ntg & 15) * 5) >> 3) * 2048, tmp = 20480 + park0(w) + l16, lb = l16 + 20480;
    u32x32 da = pk.a, db = pk.b; u32x8 dc = pk.c;
    asm volatile(
        SPIN0 "s_cmp_eq_u32 %[ng], 31\n\ts_cbranch_scc1 7f\n\t" HNX(64, 65) VM(1) TSA("21") LDH(32, s64) LDH(48, s65) PX4(216, 10240) PX4(232, 14336)
        HNX(65, 66) HNX(66, 67) HNX(67, 68) HNX(68, 69) HNX(69, 70) HNX(70, 71) HNX(71, 72) HNX(72, 73) "s_waitcnt lgkmcnt(0)\n\ts_barrier\n\t"
        RD5("112", "%[wa]", "0") RD1("132", "%[wa]", "5120") RD1("136", "%[wa]", "6144")
        "1:\n\t" VM(4) CK16("32") "s_cbranch_scc1 2f\n\t" TSA("11") TSS("35") LDH(64, s66) LDH(80, s67) LDH(96, s68)
        GUS(MF0, 0, 32, "7168", "", "14336", 48, 12, "", "", "", "", "", "", "")
        GUS(MF16, 1, 48, "21504", "", "28672", 64, 12, LH(32, s69), "", "", "")
        GUS(MF16, 2, 64, "35840", "", "43008", 80, 13, LH(48, s70), WL(196, 0), "", "")
        GUS(MF16, 3, 80, "50176", "", "57344", 96, 15, LH(64, s71), WL(200, 1024), WL(204, 2048), "")
        WADV
        GUS(MF16, 4, 96, "0", "", "7168", 32, 18, LH(80, s72), WL(208, 3072) WA, WL(212, 0), WL(216, 1024))
        GUS(MF16, 5, 32, "14336", "", "21504", 48, 21, LH(96, s73), WL(220, 2048), WL(224, 3072) WA, WL(228, 0))
        GUS(MF16, 6, 48, "28672", "", "35840", 64, 23, LS(32, 0), LS(36, 1024), LS(40, 2048), LS(44, 3072), WL(232, 1024), WL(236, 2048), WL(240, 3072) WA)
        GUS(MF16, 7, 64, "43008", "", "50176", 80, 24, WL(244, 0), WL(24, 1024), WL(28, 2048), WL(48, 3072) WA, WL(52, 0), WL(56, 1024), WL(60, 2048))
        GUS(MF16, 8, 80, "57344", WADV, "0", 96, 21, WL(64, 3072) WA, WL(68, 0), WL(72, 1024), WL(76, 2048), "", "", "")
        GUK_(MF16, "96", "%[wa]", "7168", "1024", CKS(14, "32", 9))
        "v_lshrrev_b32 %[wa], 4, %[l16]\n\tv_and_b32 %[wa], 48, %[wa]\n\t"
        GUK(MF16, "100", "%[wa]", "14336", "64",
            WL(80, 3072) WA PKADD("32", "34") PKADD("36", "38") PKADD("40", "42") PKADD("44", "46"),
            WL(84, 0) PKADD("32", "36") PKADD("40", "44"),
            WL(88, 1024) SW32("32", "40") SW32("33", "41") PKADD("32", "40") "v_mov_b64 v[34:35], v[32:33]\n\t",
            WL(92, 2048) SW16("32", "34") SW16("33", "35") PKADD("32", "34") "v_mul_f32 v16, 0xbacccccd, v32\n\t",
            "v_mul_f32 v33, 0x3acccccd, v33\n\tv_fma_f32 v33, -v16, v16, v33\n\tv_max_f32 v33, 0, v33\n\tv_add_f32 v33, 0x3727c5ac, v33\n\t",
            "v_rcp_f32 v18, v33\n\tv_mov_b32 v20, v16\n\tv_mov_b32 v22, v18\n\t",
            SW32("16", "20") SW32("18", "22") "v_mov_b32 v17, v16\n\tv_mov_b32 v21, v20\n\tv_mov_b32 v19, v18\n\tv_mov_b32 v23, v22\n\t")
        TSA("12") WEND EP2(0, 1, "140", 5) EP2(2, 3, "156", 3) EP2(4, 5, "172", 1) LGK(0) EPN(6, "188") "s_nop 1\n\t" SW32("188", "189")
        "buffer_store_dwordx2 v[188:189], %[f1], s[40:43], s53 offen sc1\n"
        "6:\n\t" TSA("22") VM(0) "s_barrier\n\t" TSA("23") LDF("112", "s44") LDF("140", "s45")
        "5:\n\t" VM(7) CK28("112") "s_cbranch_scc1 8f\n\t" TSA("13") TSS("50")
        DTL("112", "s46") DC(7, "140", 10) DW("s48") DTL("140", "s47") DC(7, "112", 11) DW("s49") DTE("112") DC(0, "140", 12) DW("s50") DTE("140") "s_nop 6\n\t" DW("s51")
        "s_waitcnt lgkmcnt(0)\n\ts_barrier\n\ts_branch 9f\n"
        "2:\n\ts_mov_b64 exec, vcc\n\t" LDH(32, s64) LDH(48, s65) "s_mov_b64 exec, -1\n\t" SPIN1 "s_branch 1b\n"
        "8:\n\ts_mov_b64 exec, vcc\n\t" LDF("112", "s44") LDF("140", "s45") "s_mov_b64 exec, -1\n\t" SPIN1 "s_branch 5b\n"
        REDO(0, LDH(48, s65), CK16("48")) REDO(1, LDH(64, s66), CK16("64")) REDO(2, LDH(80, s67), CK16("80")) REDO(3, LDH(96, s68), CK16("96")) REDO(4, LDH(32, s69), CK16("32"))
        REDO(5, LDH(48, s70), CK16("48")) REDO(6, LDH(64, s71), CK16("64")) REDO(7, LDH(80, s72), CK16("80")) REDO(8, LDH(96, s73), CK16("96")) REDO(9, LDST, CK16("32"))
        REDO(10, LDF("140", "s45"), CK28("140")) REDO(11, LDF("112", "s46"), CK28("112")) REDO(12, LDF("140", "s47"), CK28("140"))
        "7:\n\t" LGK(0) WL(196, 0) WL(200, 1024) WL(204, 2048) WL(208, 3072) WA WL(212, 0) WL(216, 1024) WL(220, 2048) WL(224, 3072) WA WL(228, 0) WL(232, 1024) WL(236, 2048) WL(240, 3072) WA
        WL(244, 0) WL(24, 1024) WL(28, 2048) WL(48, 3072) WA WL(52, 0) WL(56, 1024) WL(60, 2048) WL(64, 3072) WA WL(68, 0) WL(72, 1024) WL(76, 2048) WL(80, 3072) WA WL(84, 0) WL(88, 1024) WL(92, 2048) WEND
        "1:\n\ts_sleep 4\n\t" LF0("112", "s44") SPIN1 VM(0) CK4("112") "s_cbranch_scc1 1b\n\ts_branch 6b\n9:"
        : "+{v[176:207]}"(da), "+{v[208:239]}"(db), "+{v[240:247]}"(dc), "+{s[52:55]}"(sb), "+{s64}"(h0), [wa] "+&v"(wa), [t] "+&v"(tmp), [lb] "+&v"(lb) PFIO
        : "{s[40:43]}"(rws), "{s[44:51]}"(sa), [l16] "v"(l16), [z4] "v"(w < 6 ? l16 + 4096 : OOB),
          [f0] "v"(fv + (g & 1) * 7936), [f1] "v"(ntg < 30 ? fv + (g & 1) * 8 : OOB), [ng] "s"(ntg), [hb] "s"(hb)
        : "memory", "scc", "vcc", "s59", "s60", "s61", "s65", "s66", "s67", "s68", "s69", "s70", "s71", "s72", "s73", "v16", "v17", "v18", "v19", "v20", "v21", "v22", "v23",
          "v24", "v25", "v26", "v27", "v28", "v29", "v30", "v31", VCL32, VCL48, VCL64, VCL80, VCL96, VCL112, VCL128, VCL140, VCL160 PFCL);
}
DI f32x4 sum8(unsigned a) {
    f32x4 v[8];
#pragma unroll
    for (int i = 0; i < 8; ++i) v[i] = ldsr<f32x4>(a + i * 5120);
    return ((v[0] + v[1]) + (v[2] + v[3])) + ((v[4] + v[5]) + (v[6] + v[7]));
}
DI u32x2 addres(f32x4 a, unsigned lo, unsigned hi) { return u32x2{pk2(a[0] + bflo(lo), a[1] + bfhi(lo)), pk2(a[2] + bflo(hi), a[3] + bfhi(hi))}; }

__global__ void __launch_bounds__(512, 2) block_kernel(Params) {
    u32x16 ka;
    asm volatile("s_load_dwordx16 %0, %1, 0" : "=s"(ka) : "s"(__builtin_amdgcn_kernarg_segment_ptr()) : "memory");
    __shared__ __attribute__((aligned(16))) char lds[163840];
    asm volatile("" :: "v"((LAS char*)lds) : "memory");
    const int lane = threadIdx.x & 63, w = __builtin_amdgcn_readfirstlane(threadIdx.x >> 6), b = blockIdx.x;
    unsigned pre;
    asm volatile("s_cmp_lg_u32 %1, %2\n\ts_cbranch_scc1 1f\n\ts_getpc_b64 s[4:5]\n.Lpc:\n\tv_lshlrev_b32 %0, 7, %3\n\tv_min_u32 %0, .Lend-.Lpc-4, %0\n\t"
                 "global_load_dword %0, %0, s[4:5]\n1:" : "=&{v200}"(pre) : "s"(w), "s"((b >> 3) & 3), "v"(threadIdx.x) : "memory", "scc", "s4", "s5");
    Params p;
    rsrc_t rws;
    const int xcd = b & 7, i = b >> 3;
    Retile rt;
    u32x2 tag;
    unsigned fq;
    {
        const int mt = (xcd >> 1) * 4 + (i & 3), nt = (xcd & 1) * 8 + (i >> 2), rot = (i >> 2) + (((i & 3) * 5 + 1) >> 1);
        QkvSrc c; QkvAdr e; u32x4 row; unsigned rowt, T;
        qkv_start(c, row, rowt, p, ka, mt, nt, rot < 10 ? rot : rot - 10, w, lane);
        rws = mkrs(p.ws, WS_END);
        asm volatile("s_load_dword %0, %2, %4\n\ts_load_dwordx16 %1, %3, %5" : "=&s"(T), "=&{s[60:75]}"(rt.t) : "s"(p.ws), "s"(&RT_TAB), "s"(WS_E), "s"((b * 8 + w) * 64) : "memory");
        TSW(40);
        retile_setup(rt, p, b, lane);
        qkv_setup(e, mt, nt, w, lane);
        fq = lane == 0 ? WS_FQ + (nt * 16 + mt) * 128 : OOB;
        TSW(41);
        asm volatile("s_waitcnt vmcnt(8) lgkmcnt(0)\n\ts_add_u32 %0, %0, 1" : "+s"(T), "+{s[60:75]}"(rt.t), "+v"(row), "+v"(rowt), "+v"(fq), "+{v200}"(pre) :: "memory", "scc");
        TSW(42);
        const u32x4 gr = row_stats(row, rowt, T);
        tag = u32x2{gr[2], gr[3]};
        TSW(43);
        if (nt == 15) {
            st16(rws, e.go, 0, gr);
            retile_issue(rt, w);
            asm volatile("s_waitcnt vmcnt(0)" ::: "memory");
        } else if (nt >= 10) qkv_body<true>(c, e, rt, rws, gr, tag, w PROFPASS);
        else qkv_body<false>(c, e, rt, rws, gr, tag, w PROFPASS);
    }
    const int mt = (xcd >> 1) * 8 + (i & 7), nt = (xcd & 1) * 4 + (i >> 3), ntg = (xcd & 1) * 16 + (i & 3) * 4 + (i >> 3);
    const int tg = w & 3, kh = w >> 2;
    auto rv0 = [&](unsigned l) { return (l & 15) * 1280 + (nt & 1) * 32 + kh * 64 + (l >> 4) * 16; };
    auto rv1 = [&](unsigned l) { return kh ? OOB : (l & 15) * 1280 + ((nt & 1) ? 0 : 128) + (l >> 4) * 8; };
    const unsigned rsb = (mt * 64 + tg * 16) * 1280 + nt * 160;
    u32x4 r0; u32x2 r1; Park pk;
    phase_attn(p, rt, rws, tag, fq, b, w, lane, nt, ntg, rv0(lane), rv1(lane), rsb, r0, r1, pk PROFPASS);
    unsigned ln = lane;
    asm volatile("" : "+v"(ln) : "{v154}"(pk.d));
    phase_wo(rws, w, ln, mt, nt, ntg, r0, r1, pk PROFPASS);
    PIN(ln);
    unsigned o0 = rv0(ln), o1 = rv1(ln), a = tg * 40960 + ln * 16;
    PIN(o0); PIN(o1); PIN(a);
    phase_ffn(rws, w, ln, mt, nt, ntg, pk PROFPASS);
    TS(14);
    {
        const rsrc_t ro = mkrs(p.out, 2048 * 1280);
        const u32x2 u = addres(sum8(a + kh * 2048), r0[0], r0[1]), v = addres(sum8(a + kh * 2048 + 1024), r0[2], r0[3]);
        st16(ro, o0, rsb, u32x4{u[0], u[1], v[0], v[1]});
        if (kh == 0) st8(ro, o1, rsb, addres(sum8(a + 4096), r1[0], r1[1]));
    }
    asm volatile(".Lend:" ::: "memory");
}

extern "C" void kernel_launch(void* const* d_in, const int* in_sizes, int n_in, void* d_out, int out_size, void* d_ws, size_t ws_size, hipStream_t stream) {
    Params p;
    p.x = (const char*)d_in[0]; p.wqkv = (const char*)d_in[1]; p.wo = (const char*)d_in[2];
    p.wg = (const char*)d_in[4]; p.wu = (const char*)d_in[5]; p.wd = (const char*)d_in[6];
    p.out = (char*)d_out; p.ws = (char*)d_ws;
    block_kernel<<<256, 512, 0, stream>>>(p);
}
```

```cpp
#include <hip/hip_runtime.h>
#include <hip/hip_bf16.h>
#include <stdint.h>

#define DI __device__ __forceinline__
typedef __attribute__((ext_vector_type(2))) __bf16 bf2v;
typedef __attribute__((ext_vector_type(8))) __bf16 bf8v;
typedef __attribute__((ext_vector_type(16))) __bf16 bf16v;
typedef __attribute__((ext_vector_type(4))) short s4v;
typedef __attribute__((ext_vector_type(2))) float f32x2;
typedef __attribute__((ext_vector_type(4))) float f32x4;
typedef __attribute__((ext_vector_type(8))) float f32x8;
typedef __attribute__((ext_vector_type(16))) float f32x16;
typedef __attribute__((ext_vector_type(32))) float f32x32;
typedef __attribute__((ext_vector_type(2))) unsigned u32x2;
typedef __attribute__((ext_vector_type(4))) unsigned u32x4;
typedef __attribute__((ext_vector_type(8))) unsigned u32x8;
typedef __attribute__((ext_vector_type(16))) unsigned u32x16;
typedef __attribute__((ext_vector_type(32))) unsigned u32x32;
typedef __amdgpu_buffer_rsrc_t rsrc_t;
#define LAS __attribute__((address_space(3)))

constexpr unsigned WS_Q = 0;
constexpr unsigned WS_KV = WS_Q + 32 * 81920;
constexpr unsigned WS_O = WS_KV + 32 * 163840;
constexpr unsigned WS_B = WS_O + 128 * 20480;
constexpr unsigned QBS = 32 * 40960, OFF_HS = 16 * 20480, OFF_F = OFF_HS + 16 * 2048;
constexpr unsigned WS_WO = WS_B + 8 * QBS;
constexpr unsigned WS_WD = WS_WO + 8 * 102400;
constexpr unsigned WS_WGU = WS_WD + 8 * 276480;
constexpr unsigned WS_CS = WS_WGU + 32 * 143360;
constexpr unsigned WS_FQ = WS_CS + 5 * 3584 * 4;
constexpr unsigned WS_FR = WS_FQ + 256 * 128;
constexpr unsigned WS_XS = WS_FR + 2048;
constexpr unsigned WS_E = WS_XS + 2048 * 16;
constexpr unsigned WS_END = WS_E + 128;
constexpr unsigned OOB = 0x40000000u;
constexpr int SPIN = 1 << 14;

struct Params {
    const char* x; const char* wqkv; const char* wo; const char* wg; const char* wu; const char* wd;
    char* out; char* ws;
};

#define SBAR() __builtin_amdgcn_sched_barrier(0)
#define PIN(x) asm volatile("" : "+v"(x))
#define WAITBAR(N) asm volatile("s_waitcnt vmcnt(" #N ") lgkmcnt(0)\n\ts_barrier" ::: "memory")
#define VBAR(N) asm volatile("s_waitcnt vmcnt(" #N ")\n\ts_barrier" ::: "memory")
#define LBAR() asm volatile("s_waitcnt lgkmcnt(0)\n\ts_barrier" ::: "memory")
#define BAR() asm volatile("s_barrier" ::: "memory")

DI rsrc_t mkrs(const char* p, unsigned bytes) { return __builtin_amdgcn_make_buffer_rsrc((void*)p, (short)0, (int)bytes, 0x00020000); }
template <class T> DI T ldsr(unsigned a) { return *(const LAS T*)(uintptr_t)a; }
template <class T> DI void ldsw(unsigned a, T v) { *(LAS T*)(uintptr_t)a = v; }
DI s4v ldstr(unsigned a) { return __builtin_amdgcn_ds_read_tr16_b64_v4i16((LAS s4v*)(uintptr_t)a); }
DI bf8v ldstr2(unsigned a, unsigned d) {
    s4v lo = ldstr(a), hi = ldstr(a + d);
    return __builtin_bit_cast(bf8v, __builtin_shufflevector(lo, hi, 0, 1, 2, 3, 4, 5, 6, 7));
}
DI f32x16 mfma32(bf8v a, bf8v b, f32x16 c) { return __builtin_amdgcn_mfma_f32_32x32x16_bf16(a, b, c, 0, 0, 0); }
DI f32x4 mfma16(bf8v a, bf8v b, f32x4 c) { return __builtin_amdgcn_mfma_f32_16x16x32_bf16(a, b, c, 0, 0, 0); }
DI bf8v asbf(u32x4 v) { return __builtin_bit_cast(bf8v, v); }
DI unsigned pk2(float lo, float hi) { f32x2 v = {lo, hi}; return __builtin_bit_cast(unsigned, __builtin_convertvector(v, bf2v)); }
DI float bflo(unsigned u) { return __uint_as_float(u << 16); }
DI float bfhi(unsigned u) { return __uint_as_float(u & 0xffff0000u); }
DI f32x2 swap32(float a, float b) { auto r = __builtin_amdgcn_permlane32_swap(__float_as_uint(a), __float_as_uint(b), false, false); return f32x2{__uint_as_float(r[0]), __uint_as_float(r[1])}; }
DI float sum16(float a) { auto r = __builtin_amdgcn_permlane16_swap(__float_as_uint(a), __float_as_uint(a), false, false); return __uint_as_float(r[0]) + __uint_as_float(r[1]); }
DI bool anyl(bool c) { return __builtin_amdgcn_ballot_w64(c) != 0; }
DI void st16(rsrc_t r, unsigned voff, unsigned soff, u32x4 v) { asm volatile("buffer_store_dwordx4 %0, %1, %2, %3 offen sc1\n\ts_nop 1" :: "v"(v), "v"(voff), "s"(r), "s"(soff) : "memory"); }
DI void st8(rsrc_t r, unsigned voff, unsigned soff, u32x2 v) { asm volatile("buffer_store_dwordx2 %0, %1, %2, %3 offen sc1" :: "v"(v), "v"(voff), "s"(r), "s"(soff) : "memory"); }
DI void st4(rsrc_t r, unsigned voff, unsigned soff, unsigned v) { asm volatile("buffer_store_dword %0, %1, %2, %3 offen sc1" :: "v"(v), "v"(voff), "s"(r), "s"(soff) : "memory"); }
DI u32x4 ld16c(rsrc_t r, unsigned voff, unsigned soff) { return __builtin_amdgcn_raw_buffer_load_b128(r, voff, soff, 16); }
DI u32x2 ld8c(rsrc_t r, unsigned voff, unsigned soff) { return __builtin_amdgcn_raw_buffer_load_b64(r, voff, soff, 16); }
DI void dma1(unsigned m0v, unsigned voff, rsrc_t r, unsigned soff) {
    asm volatile("s_mov_b32 m0, %0\n\ts_nop 0\n\tbuffer_load_dwordx4 %1, %2, %3 offen lds" :: "s"(m0v), "v"(voff), "s"(r), "s"(soff) : "memory"); }
DI void dma1c(unsigned m0v, unsigned voff, rsrc_t r, unsigned soff) {
    asm volatile("s_mov_b32 m0, %0\n\ts_nop 0\n\tbuffer_load_dwordx4 %1, %2, %3 offen sc1 lds" :: "s"(m0v), "v"(voff), "s"(r), "s"(soff) : "memory"); }
DI void dma3c(unsigned m0v, unsigned voff, rsrc_t r, unsigned soff) {
    asm volatile("s_mov_b32 m0, %0\n\ts_nop 0\n\tbuffer_load_dwordx4 %1, %2, %3 offen sc1 lds\n\tbuffer_load_dwordx4 %1, %2, %3 offen offset:1024 sc1 lds\n\t"
                 "buffer_load_dwordx4 %1, %2, %3 offen offset:2048 sc1 lds" :: "s"(m0v), "v"(voff), "s"(r), "s"(soff) : "memory"); }
DI void dma4c(unsigned m0v, unsigned voff, rsrc_t r, unsigned soff) {
    asm volatile("s_mov_b32 m0, %0\n\ts_nop 0\n\tbuffer_load_dwordx4 %1, %2, %3 offen sc1 lds\n\tbuffer_load_dwordx4 %1, %2, %3 offen offset:1024 sc1 lds\n\t"
                 "buffer_load_dwordx4 %1, %2, %3 offen offset:2048 sc1 lds\n\tbuffer_load_dwordx4 %1, %2, %3 offen offset:3072 sc1 lds" :: "s"(m0v), "v"(voff), "s"(r), "s"(soff) : "memory"); }
#define SPINS "0x4000"
#define PINS(x) asm volatile("" : "+s"(x))
#define CHK(B, TMP, DST) \
    "v_max3_u32 v" #TMP ", v[" #B "], v[" #B "+1], v[" #B "+2]\n\tv_max3_u32 v" #TMP ", v[" #B "+3], v[" #B "+4], v" #TMP "\n\t" \
    "v_max3_u32 v" #TMP ", v[" #B "+5], v[" #B "+6], v" #TMP "\n\tv_max3_u32 v" #TMP ", v[" #B "+7], v[" #B "+8], v" #TMP "\n\t" \
    "v_max3_u32 v" #TMP ", v[" #B "+9], v[" #B "+10], v" #TMP "\n\tv_max3_u32 v" #TMP ", v[" #B "+11], v[" #B "+12], v" #TMP "\n\t" \
    "v_max3_u32 v" #TMP ", v[" #B "+13], v[" #B "+14], v" #TMP "\n\tv_max_u32 v" #TMP ", v[" #B "+15], v" #TMP "\n\t" \
    "v_cmp_eq_u32 " DST ", -1, v" #TMP "\n\t"
#define LD1(R, V, S, O) "buffer_load_dwordx4 v[" R ":" R "+3], " V ", s[40:43], " S " offen offset:" O " sc1\n\t"
#define DM1(V, S, O) "buffer_load_dwordx4 " V ", s[40:43], " S " offen offset:" #O " sc1 lds\n\t"
#define DM3(V, S) DM1(V, S, 0) DM1(V, S, 1024) DM1(V, S, 2048)
#define DM4(V, S) DM3(V, S) DM1(V, S, 3072)
#define Z2(R) "v_mov_b64 v[" R ":" R "+1], 0\n\t"
#define Z4(R) Z2(R) Z2(R "+2")
#define Z8(R) Z4(R) Z4(R "+4")

#define VCL32 "v32", "v33", "v34", "v35", "v36", "v37", "v38", "v39", "v40", "v41", "v42", "v43", "v44", "v45", "v46", "v47"
#define VCL48 "v48", "v49", "v50", "v51", "v52", "v53", "v54", "v55", "v56", "v57", "v58", "v59", "v60", "v61", "v62", "v63"
#define VCL64 "v64", "v65", "v66", "v67", "v68", "v69", "v70", "v71", "v72", "v73", "v74", "v75", "v76", "v77", "v78", "v79"
#define VCL80 "v80", "v81", "v82", "v83", "v84", "v85", "v86", "v87", "v88", "v89", "v90", "v91", "v92", "v93", "v94", "v95"
#define VCL96 "v96", "v97", "v98", "v99", "v100", "v101", "v102", "v103", "v104", "v105", "v106", "v107", "v108", "v109", "v110", "v111"
#define VCL112 "v112", "v113", "v114", "v115", "v116", "v117", "v118", "v119", "v120", "v121", "v122", "v123", "v124", "v125", "v126", "v127"
#define VCL128 "v128", "v129", "v130", "v131", "v132", "v133", "v134", "v135", "v136", "v137", "v138", "v139"
#define VCL140 "v140", "v141", "v142", "v143", "v144", "v145", "v146", "v147", "v148", "v149", "v150", "v151", "v152", "v153", "v154", "v155", "v156", "v157", "v158", "v159"
#define VCL160 "v160", "v161", "v162", "v163", "v164", "v165", "v166", "v167", "v168", "v169", "v170", "v171", "v172", "v173", "v174", "v175"
#define VCL112L "v112", "v113", "v114", "v115", "v116", "v117", "v118", "v119"

#define PROFARG
#define PROFPASS
#define TSA(i)
#define PFIO
#define PFCL
#define TSM
#define PFM(i)
#define PFT
#define TS(i)

#define TSF(i)
#define TSI
#define TSS(i)
#define TSW(i)
#define TSP
#define TSQ(a, b)
constexpr unsigned L_RT = 0, L_SM = 32768, L_CP = 49152;
struct RtTab { unsigned v[2048][16]; };
constexpr RtTab make_rt() {
    RtTab t{};
    for (unsigned b = 0; b < 256; ++b) for (unsigned w = 0; w < 8; ++w) for (unsigned f = 0; f < 4; ++f) {
        unsigned A = 0, M = 0, SO = 0, R0 = 0, R1 = 0;
        if (b < 135) {
            const unsigned fg = b / 5, ks = (b % 5) * 4 + (w >> 1), j = ks >> 1, o = fg * 8 + (w & 1) * 4 + f;
            A = (320 * (ks & 1) + (j < 8 ? (j & 1) * 32 + (j >> 1) * 8 : 8 * j) + (f & 1) * 160) * 3456 + fg * 128 + (w & 1) * 64;
            M = (j < 8 ? 96 : 16) * 3456;
            SO = WS_WGU + (o / 7) * 143360 + ks * 7168 + (o % 7) * 1024;
            R0 = 16 * f;
        } else {
            const unsigned v = (b - 135) * 8 + w;
            const bool isWo = v < 200;
            const unsigned vv = v < 740 ? (isWo ? v : v - 200) : 0, ks = vv / 10, cu = vv % 10;
            const unsigned c = 4 * ks + f, j = c / 31, ng = c % 31;
            A = (isWo ? (ks >= 16 ? 40 * c - 2544 : 20 * (c >> 1) + 4 * (c & 1)) : 56 * ng + 16 * (j >> 1) + 4 * (j & 1)) * 1280 + cu * 128;
            M = (isWo ? (ks >= 16 ? 20 : 8) : (j < 6 ? 8 : 4)) * 1280;
            const unsigned seg = 2 * cu + (f >> 1), a = seg / 5, sm = seg % 5, fi = f & 1;
            const unsigned tile = 2 * a + (sm == 2 ? fi : sm > 2), blk = sm == 2 ? 4 : (sm == 1 || sm == 4) ? 2 + fi : fi;
            SO = isWo ? WS_WO + (tile * 100 + (ks >= 10 ? (ks - 10) * 10 + 5 + (blk < 4 ? blk ^ 2 : 4) : ks * 10 + blk)) * 1024 : WS_WD + (tile * 270 + ks * 5 + blk) * 1024;
            R0 = (f >> 1) * 64 + (sm == 2 ? 32 : 8) * fi; R1 = sm == 2 ? 8 : 16;
        }
        unsigned* r = t.v[b * 8 + w];
        r[f] = A + 3072 - 1024 * f; r[4 + f] = M; r[8 + f] = SO; r[12 + f] = (L_RT + w * 4096 + R0) | R1 << 16;
    }
    return t;
}
static __device__ const __attribute__((aligned(64))) RtTab RT_TAB = make_rt();
struct Retile { rsrc_t ra, rb; u32x16 t; unsigned vlo, hi; };
DI void retile_setup(Retile& r, const Params& p, unsigned b, unsigned lane) {
    const bool gu = b < 135;
    const char *pa, *pb; unsigned n;
    asm("s_cmpk_lt_u32 %3, 160\n\ts_cselect_b64 %0, %4, %5\n\ts_cselect_b32 %2, %9, %8\n\ts_cmpk_lt_u32 %3, 135\n\ts_cselect_b64 %1, %7, %0\n\ts_cselect_b64 %0, %6, %0\n\ts_cselect_b32 %2, %8, %2"
        : "=&s"(pa), "=&s"(pb), "=&s"(n) : "s"(b), "s"(p.wo), "s"(p.wd), "s"(p.wg), "s"(p.wu), "s"(640 * 3456 + 3072), "n"(640 * 1280 + 3072) : "scc");
    r.ra = mkrs(pa - 3072, n); r.rb = mkrs(pb - 3072, n);
    r.vlo = gu ? ((lane >> 2) & 7) * 3456 + (lane & 3) * 16 : ((lane >> 3) & 3) * 1280 + (lane & 7) * 16;
    r.hi = lane >> 5;
    PIN(r.vlo); PIN(r.hi); PINS(r.ra); PINS(r.rb);
}
DI void retile_issue(const Retile& r, int w) {
    asm volatile("s_mov_b32 m0, %0\n\ts_nop 0\n\tbuffer_load_dwordx4 %1, %5, 0 offen lds\n\tbuffer_load_dwordx4 %2, %5, 0 offen offset:1024 lds\n\t"
                 "buffer_load_dwordx4 %3, %6, 0 offen offset:2048 lds\n\tbuffer_load_dwordx4 %4, %6, 0 offen offset:3072 lds"
                 :: "s"(L_RT + w * 4096), "v"(r.vlo + r.t[0] + r.hi * r.t[4]), "v"(r.vlo + r.t[1] + r.hi * r.t[5]), "v"(r.vlo + r.t[2] + r.hi * r.t[6]),
                    "v"(r.vlo + r.t[3] + r.hi * r.t[7]), "s"(r.ra), "s"(r.rb) : "memory");
}
DI u32x32 retile_read(const Retile& r, unsigned b, int w, unsigned lane) {
    const u32x4 one = {0x3f803f80u, 0x3f803f80u, 0x3f803f80u, 0x3f803f80u};
    const unsigned f = lane & 3, row = 8 * (lane >> 4) + ((lane >> 2) & 3), dl = b < 135 ? 256 : 512;
    const unsigned rb = b < 135 ? (f >> 1) * 2048 + row * 64 + (f & 1) * 8 : row * 128;
    u32x32 o; f32x4 cp[4];
#pragma unroll
    for (int i = 0; i < 4; ++i) {
        const bf8v fr = ldstr2(rb + (r.t[12 + i] & 0xffff) + (r.t[12 + i] >> 16) * f, dl);
        cp[i] = mfma16(fr, asbf(one), f32x4{});
#pragma unroll
        for (int k = 0; k < 4; ++k) o[4 * i + k] = __builtin_bit_cast(u32x4, fr)[k];
    }
    if ((lane & 15) == 0) {
#pragma unroll
        for (int i = 0; i < 4; ++i) ldsw<f32x4>(L_CP + w * 256 + i * 64 + lane, cp[i]);
    }
    return o;
}

struct QkvSrc { rsrc_t rx, rw; unsigned xo[2], wo[2], sx, sw; };
#define QDX(V, O) "buffer_load_dwordx4 %[" #V "], %[rx], %[sx] offen offset:" #O " lds\n\t"
#define QDW(V, O) "buffer_load_dwordx4 %[" #V "], %[rw], %[sw] offen offset:" #O " lds\n\t"
#define QADV "s_add_u32 %[sx], %[sx], 128\n\ts_cmp_eq_u32 %[sx], 1280\n\ts_cselect_b32 %[sx], 0, %[sx]\n\ts_mul_i32 %[sw], %[sx], 1920\n\t"
#define QD4 QDX(x0, 0) QDX(x1, 1024) QDW(w0, 2048) QDW(w1, 3072) QADV "s_add_u32 m0, m0, 32768\n\t"
DI void qkv_start(QkvSrc& c, u32x4& row, unsigned& rowt, Params& p, u32x16 ka, int mt, int nt, int rot, int w, int lane) {
    const unsigned rr = lane >> 3, isv = nt >= 10;
    const unsigned tok = mt * 128 + (w >> 1) * 32 + ((w & 1) << (2 * isv)) + (rr & 4) * 2 + ((rr & 3) << (1 - isv));
    c.xo[0] = tok * 1280 + (((lane & 7) ^ (rr >> 1)) << 4);
    c.xo[1] = (c.xo[0] ^ 64) + 16 * 1280 - 1024;
    c.wo[0] = (8 * w + ((lane >> 2) & 3)) * 3840 + nt * 256 + (lane >> 4) * 64 + (((lane & 3) ^ ((w & 1) << 1)) << 4) + 1024;
    c.wo[1] = c.wo[0] + 4 * 3840 - 1024;
    c.sx = rot * 128; c.sw = rot * 245760;
    unsigned ro = (mt * 128 + nt * 8 + w) * 1280;
    asm volatile("s_waitcnt lgkmcnt(0)" : "+s"(ka), "+v"(c.xo[1]), "+v"(c.wo[1]), "+s"(c.sw), "+s"(ro) :: "memory");
    p = __builtin_bit_cast(Params, ka);
    c.rx = mkrs(p.x, 2048 * 1280); c.rw = mkrs(p.wqkv - 3072, 640 * 3840 + 3072);
    asm volatile("s_mov_b32 m0, %[dm]\n\tbuffer_load_dwordx4 %[r4], %[l16], %[rx], %[ro] offen\n\tbuffer_load_dword %[r1], %[l4], %[rx], %[ro] offen offset:1024\n\t"
                 "s_cmp_eq_u32 %[nt], 15\n\ts_cbranch_scc1 1f\n\t" QD4 "s_barrier\n\t" QD4 "s_branch 2f\n1:\n\ts_waitcnt vmcnt(0)\n2:"
                 : [r4] "=&v"(row), [r1] "=&v"(rowt), [sx] "+&s"(c.sx), [sw] "+&s"(c.sw)
                 : [x0] "v"(c.xo[0]), [x1] "v"(c.xo[1]), [w0] "v"(c.wo[0]), [w1] "v"(c.wo[1]), [rx] "s"(c.rx), [rw] "s"(c.rw), [dm] "s"(w * 4096),
                   [l16] "v"(lane * 16), [l4] "v"(lane * 4), [ro] "s"(ro), [nt] "s"(nt) : "memory", "scc");
}
template <int C> DI float rorsum(float v) { return v + __uint_as_float(__builtin_amdgcn_update_dpp(0u, __float_as_uint(v), 0x120 + C, 0xf, 0xf, true)); }
DI u32x4 row_stats(u32x4 row, unsigned rowt, unsigned T) {
    float s = 0.f, q = 0.f;
#pragma unroll
    for (int i = 0; i < 5; ++i) {
        const bf2v v = __builtin_bit_cast(bf2v, i < 4 ? row[i & 3] : rowt);
        s = __builtin_amdgcn_fdot2_f32_bf16(v, __builtin_bit_cast(bf2v, 0x3f803f80u), s, false);
        q = __builtin_amdgcn_fdot2_f32_bf16(v, v, q, false);
    }
    const f32x2 y = swap32(s, q);
    const float z = rorsum<1>(rorsum<2>(rorsum<4>(rorsum<8>(sum16(y[0] + y[1]))))) * (1.f / 640.f);
    const f32x2 v = swap32(z, z);
    const float ra = __builtin_amdgcn_rsqf(__builtin_fmaxf(v[1] - v[0] * v[0], 0.f) + 1e-5f);
    return u32x4{__float_as_uint(ra), __float_as_uint(-v[0] * ra), T, ~T};
}
#define MFQ(A, W, X) "v_mfma_f32_16x16x32_bf16 v[" A ":" A "+3], v[" W ":" W "+3], v[" X ":" X "+3], v[" A ":" A "+3]\n\t"
#define MFV(A, W, X) MFQ(A, X, W)
#define QTR(R, V, O) "ds_read_b64_tr_b16 v[" R ":" R "+1], %[" #V "] offset:" O "\n\t"
#define QRX(R, V, O) "ds_read_b128 v[" R ":" R "+3], %[" #V "] offset:" O "\n\t"
#define LGK(N) "s_waitcnt lgkmcnt(" #N ")\n\t"
#define QSEC(MF, P, Q, XA, WO, D1, D2, E1, E2) \
    "s_cmp_lt_u32 %[n], 8\n\t" LGK(4) MF("64", P, "68") QTR(Q, wa0, WO) MF("32", P, P "+8") QTR(Q "+2", wa0, WO "+1024") \
    "s_cbranch_scc0 1f\n\t" D1 "1:\n\t" MF("36", P, P "+12") QRX(Q "+8", XA, "0") LGK(5) MF("48", P "+4", P "+8") QRX(Q "+12", XA, "4096") \
    MF("52", P "+4", P "+12") QTR(Q "+4", wa1, WO) LGK(5) MF("40", P, P "+16") QTR(Q "+6", wa1, WO "+1024") \
    "s_cbranch_scc0 1f\n\t" D2 "1:\n\t" MF("56", P "+4", P "+16") QRX(Q "+16", XA, "8192") E1 MF("44", P, P "+20") QRX(Q "+20", XA, "12288") E2 MF("60", P "+4", P "+20")
#define QLDG "buffer_load_dwordx4 v[120:123], %[ga], %[rs], 0 offen sc1\n\t"
#define QADD(r) "v_add_u32 %[" #r "], %[d], %[" #r "]\n\t"
#define QRT(r, A, M) "v_mad_u32_u24 %[" #r "], %[hi], s" #M ", %[vlo]\n\tv_add_u32 %[" #r "], s" #A ", %[" #r "]\n\t"
#define QKV_ASM(MF, STRD) asm volatile( \
    "s_add_u32 m0, %[dm], 65536\n\ts_mov_b32 %[n], 0\n\t" QD4 "buffer_store_dwordx4 %[gr], %[go], %[rs], 0 offen sc1\n\t" \
    Z8("32") Z8("40") Z8("48") Z8("56") Z4("64") "v_mov_b32 v68, 0x3f803f80\n\tv_mov_b32 v69, v68\n\tv_mov_b64 v[70:71], v[68:69]\n\t" \
    "s_waitcnt vmcnt(9)\n\ts_barrier\n\t" TSA("1") TSS("25") \
    QTR("72", wa0, "0") QTR("74", wa0, "1024") QRX("80", xa0, "0") QRX("84", xa0, "4096") QTR("76", wa1, "0") QTR("78", wa1, "1024") QRX("88", xa0, "8192") QRX("92", xa0, "12288") \
    QDX(x0, 0) QDX(x1, 1024) "s_branch 6f\n" \
    "3:\n\ts_cmp_gt_u32 %[n], 2\n\ts_cbranch_scc1 2f\n\ts_waitcnt vmcnt(9)\n\ts_branch 5f\n" \
    "2:\n\ts_cmp_eq_u32 %[n], 10\n\ts_cbranch_scc1 7f\n\ts_cmp_eq_u32 %[n], 7\n\ts_cbranch_scc1 2f\n\ts_waitcnt vmcnt(5)\n\ts_branch 5f\n" \
    "2:\n\t" QLDG QRT(x0, 60, 64) QRT(x1, 61, 65) QRT(w0, 62, 66) QRT(w1, 63, 67) \
    "s_mov_b64 s[44:45], s[52:53]\n\ts_mov_b32 s46, s54\n\ts_mov_b64 s[48:49], s[56:57]\n\ts_mov_b32 s50, s58\n\ts_mov_b32 %[sx], 0\n\ts_mov_b32 %[sw], 0\n" \
    "4:\n\ts_waitcnt vmcnt(8)\n5:\n\ts_barrier\n\t" \
    QSEC(MF, "96", "72", xa0, "0", QDX(x0, 0), QDX(x1, 1024), "", "") \
    "6:\n\t" \
    QSEC(MF, "72", "96", xa1, "16384", QDW(w0, 2048), QDW(w1, 3072), \
         QADV "s_bitcmp1_b32 0x42, %[n]\n\ts_cselect_b32 %[d], %[wr], %[up]\n\ts_add_u32 m0, m0, %[d]\n\ts_cmp_eq_u32 %[n], 4\n\ts_cselect_b32 %[d], %[wr], %[up]\n\ts_add_u32 %[n], %[n], 1\n\t", \
         QADD(xa0) QADD(xa1) QADD(wa0) QADD(wa1)) TSI \
    "s_bitcmp1_b32 0x686, %[n]\n\ts_cbranch_scc0 4b\n\ts_branch 3b\n" \
    "7:\n\ts_movk_i32 %[n], " SPINS "\n\t" LGK(4) MF("64", "96", "68") MF("32", "96", "104") "s_waitcnt vmcnt(4)\n" \
    "1:\n\tv_cmp_ne_u64 vcc, v[122:123], %[tag]\n\ts_cbranch_vccz 2f\n\t" QLDG "s_waitcnt vmcnt(0)\n\ts_sub_u32 %[n], %[n], 1\n\ts_cbranch_scc0 1b\n" \
    "2:\n\t" MF("36", "96", "108") "v_mul_f32 v120, %[sc], v120\n\tv_mul_f32 v121, %[sc], v121\n\t" LGK(2) MF("48", "100", "104") \
    "ds_write2_b32 %[pa], v120, v121 offset1:64\n\t" MF("52", "100", "108") "s_nop 1\n\tds_write_b128 %[ca], v[64:67]\n\t" LGK(2) \
    MF("40", "96", "112") STRD MF("56", "100", "112") MF("44", "96", "116") MF("60", "100", "116") \
    "s_waitcnt lgkmcnt(0)\n\ts_barrier\n\ts_nop 7" \
    : "=&{v[32:63]}"(A), "=&{v[64:67]}"(cs0), "=&{v[124:155]}"(S), [rx] "+{s[44:47]}"(c.rx), [rw] "+{s[48:51]}"(c.rw), [sx] "+&s"(c.sx), [sw] "+&s"(c.sw), \
      [x0] "+&v"(c.xo[0]), [x1] "+&v"(c.xo[1]), [w0] "+&v"(c.wo[0]), [w1] "+&v"(c.wo[1]), [xa0] "+&v"(e.xa0), [xa1] "+&v"(e.xa1), [wa0] "+&v"(e.wa0), [wa1] "+&v"(e.wa1), \
      [n] "=&s"(n), [d] "=&s"(d) PFIO \
    : "{s[52:55]}"(rt.ra), "{s[56:59]}"(rt.rb), [rs] "s"(rws), [dm] "s"(w * 4096), [wr] "s"(-131072), [up] "s"(32768), [sc] "s"(e.sc), [gr] "v"(gr), [go] "v"(e.go), [ga] "v"(e.ga), \
      "{s[60:75]}"(rt.t), [vlo] "v"(rt.vlo), [hi] "v"(rt.hi), [tag] "v"(tag), [pa] "v"(e.pa), [pr] "v"(e.pr), [ca] "v"(e.ca) \
    : "memory", "scc", "vcc", "v68", "v69", "v70", "v71", "v72", "v73", "v74", "v75", "v76", "v77", "v78", "v79", VCL80, VCL96, VCL112L, "v120", "v121", "v122", "v123" PFCL)
#define QS2(R, O) "ds_read2_b32 v[" #R ":" #R "+1], %[pr] offset0:" #O " offset1:" #O "\n\tds_read2_b32 v[" #R "+2:" #R "+3], %[pr] offset0:" #O "+64 offset1:" #O "+64\n\t"
struct QkvAdr { unsigned xa0, xa1, wa0, wa1, go, ga, pa, pr, ca, so[2], sb, sc; };
DI void qkv_setup(QkvAdr& e, int mt, int nt, int w, int lane) {
    const unsigned t = lane & 15, g = lane >> 4, T = w & 1, cb = w >> 1, g64 = 2 * mt + T;
    const bool isv = nt >= 10, isq = nt < 5;
    e.xa0 = 4 * T * 4096 + t * 128 + ((g ^ (t >> 1)) << 4); e.xa1 = e.xa0 ^ 64;
    e.wa0 = g * 4096 + 2048 + cb * 256 + ((lane >> 2) & 3) * 64 + ((g ^ T) & 1) * 32 + (lane & 3) * 8; e.wa1 = e.wa0 ^ 32;
    e.go = lane == 0 ? WS_XS + (mt * 128 + nt * 8 + w) * 16 : OOB;
    e.ga = WS_XS + (g64 * 64 + (g >> 1) * 32 + (isv ? 8 * (t >> 2) + 4 * (g & 1) + (t & 3) : 2 * t + (g & 1))) * 16;
    e.pa = L_SM + w * 2048 + lane * 4; e.pr = L_SM + w * 2048 + (isv ? g * 16 : t * 4); e.ca = L_SM + w * 2048 + 1024 + lane * 16;
    e.sc = isq ? __builtin_bit_cast(unsigned, 0.22360679774997896f * 1.4426950408889634f) : 0x3f800000u;
    e.sb = isq ? WS_Q + g64 * 2560 : WS_KV + g64 * 5120 + (isv ? 2560 : 0);
#pragma unroll
    for (int n = 0; n < 2; ++n) {
        if (isv) {
            const unsigned col = (nt - 10) * 128 + cb * 32 + 16 * (n ^ T) + t, head = (col * 3277) >> 16;
            e.so[n] = head * (163840 - 320) + g * 320 + col * 16;
        } else {
            const unsigned G = (isq ? nt : nt - 5) * 32 + cb * 8 + 4 * (n ^ T) + g, head = (G * 205) >> 10;
            e.so[n] = head * (isq ? 81920 - 2560 : 163840 - 2560) + G * 512 + t * 16;
        }
        PIN(e.so[n]);
    }
    PIN(e.xa0); PIN(e.xa1); PIN(e.wa0); PIN(e.wa1); PIN(e.go); PIN(e.ga); PIN(e.pa); PIN(e.pr); PIN(e.ca); PINS(e.sb); PINS(e.sc);
}
template <bool ISV>
DI void qkv_body(QkvSrc& c, QkvAdr& e, const Retile& rt, rsrc_t rws, u32x4 gr, u32x2 tag, int w PROFARG) {
    f32x32 A, S; f32x4 cs0; unsigned n, d;
    if (ISV) QKV_ASM(MFV, QRX("124", pr, "0") QRX("128", pr, "256") QRX("132", pr, "64") QRX("136", pr, "320") QRX("140", pr, "128") QRX("144", pr, "384") QRX("148", pr, "192") QRX("152", pr, "448"));
    else QKV_ASM(MFQ, QS2(124, 0) QS2(128, 16) QS2(132, 32) QS2(136, 48));
    TS(2);
    const f32x4 cs1 = ldsr<f32x4>(e.ca ^ 2048);
#pragma unroll
    for (int n = 0; n < 2; ++n) {
        const f32x4 cs = n ? cs1 : cs0;
#pragma unroll
        for (int q = 0; q < 2; ++q) {
            u32x4 o;
#pragma unroll
            for (int i = 0; i < 4; ++i) {
                const int k = 2 * q + (i >> 1), j = 2 * (i & 1), a = 16 * n + 4 * k + j;
                o[i] = ISV ? pk2(A[a] * S[8 * k + j] + S[8 * k + 4 + j] * cs[0], A[a + 1] * S[8 * k + j + 1] + S[8 * k + 5 + j] * cs[0])
                           : pk2(A[a] * S[4 * k] + S[4 * k + 2] * cs[j], A[a + 1] * S[4 * k + 1] + S[4 * k + 3] * cs[j + 1]);
            }
            asm volatile("buffer_store_dwordx4 %0, %1, %2, %3 offen offset:%4 sc1\n\ts_nop 1" :: "v"(o), "v"(e.so[n]), "s"(rws), "s"(e.sb), "n"(q * (ISV ? 1280 : 256)) : "memory");
        }
    }
    asm volatile("s_waitcnt vmcnt(4)" ::: "memory");
}

DI f32x16 qk_tile(u32x4 km, u32x2 kt, u32x4 qm, u32x2 qt, f32x16 c) {
    c = mfma32(asbf(km), asbf(qm), c);
    return __builtin_amdgcn_mfma_f32_32x32x8bf16_1k(__builtin_bit_cast(s4v, kt), __builtin_bit_cast(s4v, qt), c, 0, 0, 0);
}
DI float max16(const f32x16& s, float m) {
#pragma unroll
    for (int r = 0; r < 16; r += 2) m = __builtin_fmaxf(__builtin_fmaxf(s[r], s[r + 1]), m);
    return m;
}
DI f32x16 pv16(u32x4 v, f32x8 pr, f32x16 o) {
    return __builtin_amdgcn_smfmac_f32_32x32x32_bf16(asbf(v), __builtin_bit_cast(bf16v, pr), o, 0xDDDD, 0, 0);
}
#define EX4(P, S) "v_exp_f32 v[" #P "], v[" S "]\n\tv_exp_f32 v[" #P "+1], v[" S "+1]\n\tv_exp_f32 v[" #P "+2], v[" S "+2]\n\tv_exp_f32 v[" #P "+3], v[" S "+3]\n\t"
#define SMF(O, V, P) "v_smfmac_f32_32x32x32_bf16 v[" #O ":" #O "+15], v[" #V ":" #V "+3], v[" #P ":" #P "+7], %[idx]\n\t"
#define QKT(SN, AB) "v_mfma_f32_32x32x4_2b_bf16 v[" #SN ":" #SN "+31], v[156:157], v[152:153], 0 cbsz:1 abid:" #AB "\n\t"
#define QKA(SN) "v_mfma_f32_32x32x16_bf16 v[" #SN ":" #SN "+15], v[160:163], v[144:147], v[" #SN ":" #SN "+15]\n\t"
#define QKB(SN) "v_mfma_f32_32x32x16_bf16 v[" #SN "+16:" #SN "+31], v[160:163], v[148:151], v[" #SN "+16:" #SN "+31]\n\t"
#define STEP_(SC, VP, VC, L0, L1, L2, L3, L4) \
    L0 EX4(128, #SC) EX4(132, #SC "+4") SMF(112, VP, 136) EX4(136, #SC "+16") "s_waitcnt lgkmcnt(0)\n\t" SMF(96, 164, 128) EX4(140, #SC "+20") \
    L1 EX4(128, #SC "+8") SMF(112, 164, 136) L2 EX4(132, #SC "+12") L3 EX4(136, #SC "+24") SMF(96, VC, 128) L4 EX4(140, #SC "+28")
#define STEP(SC, SN, AB, VP, VC, KTRD, VRD, KRD, M1, M2) STEP_(SC, VP, VC, QKT(SN, AB) KTRD, QKA(SN) M1, VRD, QKB(SN) KRD, M2)
#define VR(VH, O) "s_mov_b64 exec, s[56:57]\n\tds_read_b128 v[164:167], %[av] offset:" O "\n\tds_read_b128 v[" #VH ":" #VH "+3], %[av] offset:" O "+640\n\ts_mov_b64 exec, -1\n\t"
#define KR(O) "ds_read_b64 v[160:161], %[a0] offset:" O "\n\tds_read_b64 v[162:163], %[a0] offset:" O "+512\n\t"
#define KT(O) "ds_read_b64 v[156:157], %[at] offset:" O "\n\t"
#define PRIO "s_cmp_eq_u32 %[grp], 0\n\ts_cbranch_scc1 1f\n\ts_setprio 1\n1:\n\t"
#define QUAD3(X, N, A0, B0, A1, B1, A2, B2) PRIO \
    STEP(32, 64, 1, 172, 168, KT(#X "+5120"), VR(172, #X "+1280"), KR(#X "+5120"), A0, B0) \
    STEP(64, 32, 0, 168, 172, "", VR(168, #X "+5120"), KR(#X "+5376"), A1, B1) \
    STEP(32, 64, 1, 172, 168, KT(#N), VR(172, #X "+6400"), KR(#N), A2, B2) "s_setprio 0\n\t"
#define QUAD(X, N, A0, B0, A1, B1, A2, B2, A3, B3) QUAD3(X, N, A0, B0, A1, B1, A2, B2) STEP(64, 32, 0, 168, 172, "", VR(168, #N), KR(#N "+256"), A3, B3) TSI
#define KVP_(D) "buffer_load_dwordx4 %[l16], s[40:43], s44 offen sc1 lds\n\ts_add_u32 m0, m0, " #D "\n\ts_add_u32 s44, s44, 4096\n\t"
#define KVP KVP_(4096)
#define LDQ(R, V, O) "buffer_load_dwordx2 v[" #R ":" #R "+1], %[" #V "], s[40:43], s45 offen offset:" #O " sc1\n\t"
#define STW(R, S) "buffer_store_dwordx4 v[" #R ":" #R "+3], %[vs], s[40:43], s" #S " offen sc1\n\t"
#define STM(V, S, O) "buffer_store_dwordx4 v[192:195], %[" #V "], s[40:43], " S " offen offset:" #O " sc1\n\t"
#define WOP(O) DM1("%[l16]", "s48", O)
#define WO5 "s_add_u32 m0, m0, 1024\n\ts_add_u32 s48, s48, 1024\n\t"
#define PK(R, O) LD1(#R, "%[l16]", "s52", #O)
#define PKA "s_add_u32 s52, s52, 2048\n\ts_cmp_eq_u32 s52, %[pe]\n\ts_cselect_b32 s52, %[pb], s52\n\t"
#define TCH(O) "buffer_load_dword v154, %[vt], s[40:43], 0 offen offset:" #O " sc1\n\t"
DI unsigned mod5(unsigned v) { return (0x21043210u >> (4 * v)) & 7; }
DI unsigned park0(int w) { return ((w * 35 + 2) >> 2) * 2048; }
struct Park { u32x32 a, b; u32x8 c; unsigned d; };

DI void phase_attn(const Params& p, const Retile& rt, rsrc_t rws, u32x2 tag, unsigned fq, int b, int w, int lane, int nt80, int ntg,
                   unsigned rv0, unsigned rv1, unsigned rsb, u32x4& r0, u32x2& r1, Park& pk PROFARG) {
    const int t = lane & 31, hh = lane >> 5, wl = w & 3, grp = w >> 2;
    const int head = (b & 7) * 4 + (b >> 6), qb = (b >> 3) & 7;
    const unsigned gb = grp * 61440, kvb = WS_KV + head * 163840;
    pk.a = retile_read(rt, b, w, lane);
    asm volatile("s_cmp_eq_u32 %[w], 2\n\ts_waitcnt vmcnt(0) lgkmcnt(0)\n\ts_barrier\n\ts_cbranch_scc0 1f\n\tbuffer_store_dwordx2 %[tag], %[fq], %[rs], 0 offen sc1\n1:"
                 : "+{v[176:207]}"(pk.a) : [w] "s"(w), [tag] "v"(tag), [fq] "v"(fq), [rs] "s"(rws) : "memory", "scc");
    TS(3);
    SBAR();
    const unsigned l16 = lane * 16, a0 = gb + hh * 1024 + t * 8, at = gb + lane * 8 + 2048, av = gb + 2560 + hh * 320 + (t < 20 ? t : 0) * 16;
    const unsigned ones = t == 20 ? 0x3f803f80u : 0u;
    const unsigned wob = WS_WO + nt80 * 102400;
    u32x8 sa;
    sa[0] = kvb + grp * 81920 + wl * 1024;
    sa[1] = WS_Q + head * 81920 + (qb * 4 + wl) * 2560;
    sa[2] = WS_O + b * 10240 + w * 1280;
    const unsigned mh = WS_B + qb * QBS + (head * 8 + w) * 1408;
    sa[3] = WS_B + qb * QBS + OFF_F + (head * 8 + w) * 3456;
    const unsigned mv = l16 + (lane < 16 ? sa[2] + 1024 : lane < 40 ? mh + 768 : sa[3] + 2432);
    const unsigned ro = nt80 & 3, pb = WS_WGU + ntg * 143360, p0 = park0(w) + (((ntg & 15) * 5) >> 3) * 14336;
    sa[4] = wob + (grp + ro) * 20480 + wl * 5120; sa[5] = 122880 + w * 5120;
    sa[6] = wob + mod5(2 + 2 * grp + ro) * 20480 + wl * 5120; sa[7] = gb + 20480 + wl * 5120;
    u32x4 sb = {pb + p0 - ((int)p0 < 143360 ? 0 : 143360), rsb, wob + mod5(3 + ro) * 20480 + w * 2560, 40960u + w * 2560};
    const unsigned vt = lane < 17 ? WS_WD + nt80 * 276480 + qb * 34560 + (w * 17 + lane) * 256 : OOB;
    const unsigned vs = b < 135 ? l16 ^ (((lane >> 1) ^ (lane >> 3)) & 1) * 0xa0 : b * 8 + w < 1820 ? l16 : OOB;
    const unsigned vfs = (w == 2 && lane == 0) ? WS_FR + b * 8 : OOB, vfe = (b == 0 && w == 0 && lane == 0) ? WS_E : OOB, vfp = WS_FR + w * 256 + t * 8;
    const unsigned fg = (b * 205) >> 10;
    const unsigned vcs = (b < 135 && w < 2) ? WS_CS + ((b - fg * 5) * 3584 + fg * 128 + w * 64 + lane) * 4 : OOB;
    const int n0 = (head * 20 + ((lane & 16) ? 19 : 0)) >> 7;
    const unsigned fo = WS_FQ + (w == 0 ? (5 + 5 * hh + n0) * 16 + (lane & 15) : n0 * 16 + 2 * qb + (lane & 1)) * 128;
    const unsigned va = hh * 1024 + grp * 256 + t * 8;
    unsigned e0 = gb + wl * 3072 + l16, e1 = 61440 - gb + wl * 3072 + l16, ov = (t >> 4) * 20480 + (t & 15) * 16, ow = hh ? OOB : ov;
    unsigned ob = WS_O + (qb * 16 + wl * 4 + grp * 2) * 20480, oc = ob + (64 + (head >> 1)) * 256 + (head & 1) * 8;
    ov += hh * 256; ob += head * 512;
    f32x32 O; u32x8 qm; u32x2 qt;
    asm volatile(
        Z8("96") Z8("104") Z8("112") Z8("120") Z8("136") "v_mov_b32 v164, %[one]\n\tv_mov_b32 v165, %[one]\n\tv_mov_b64 v[166:167], v[164:165]\n\tv_mov_b64 v[168:169], v[164:165]\n\t"
        "v_mov_b64 v[170:171], v[164:165]\n\tv_mov_b64 v[172:173], v[164:165]\n\tv_mov_b64 v[174:175], v[164:165]\n\tv_mov_b64 v[192:193], -1\n\tv_mov_b64 v[194:195], -1\n\t"
        "s_mov_b32 s56, 0xfffff\n\ts_mov_b32 s57, s56\n\ts_movk_i32 s58, " SPINS "\n\ts_cmp_gt_u32 %[w], 1\n\ts_cbranch_scc1 2f\n\t"
        "ds_read2st64_b32 v[196:197], %[cp] offset1:2\n\tds_read2st64_b32 v[198:199], %[cp] offset0:4 offset1:6\n"
        "1:\n\tbuffer_load_dwordx2 v[32:33], %[fo], s[40:43], 0 offen sc1\n\ts_waitcnt vmcnt(0) lgkmcnt(0)\n\tv_cmp_ne_u64 vcc, v[32:33], %[tag]\n\t"
        "s_cbranch_vccz 2f\n\ts_sub_u32 s58, s58, 1\n\ts_cbranch_scc0 1b\n"
        "2:\n\ts_mov_b32 m0, %[dm]\n\ts_barrier\n\t" TSA("4")
        KVP LDQ(144, va, 0) LDQ(146, va, 512) LDQ(148, vb, 0) LDQ(150, vb, 512) LDQ(152, vc, 2048) KVP "s_barrier\n\t" KVP KVP
        "v_add_f32 v196, v196, v197\n\tv_add_f32 v198, v198, v199\n\tv_add_f32 v196, v196, v198\n\ts_movk_i32 s58, " SPINS "\n\t"
        "s_waitcnt vmcnt(2)\n\ts_barrier\n\t" TSA("5")
        KT("0") KR("0") VR(168, "0") "s_waitcnt lgkmcnt(4)\n\t" QKT(32, 0) "s_waitcnt lgkmcnt(2)\n\ts_nop 7\n\ts_nop 7\n\ts_nop 2\n\t" QKA(32) QKB(32) KR("256")
        "s_waitcnt vmcnt(0)\n\ts_barrier\n\ts_nop 7\n\t" TSS("17") TSF("17")
        QUAD(0, 10240, KVP, KVP STW(176, 68), KVP, STW(180, 69) "buffer_store_dword v196, %[vcs], s[40:43], 0 offen sc1\n\t", KVP, STW(184, 70), KVP, STW(188, 71))
        "s_waitcnt vmcnt(8)\n\ts_barrier\n\t"
        QUAD(10240, 20480, KVP, STM(l16, "s46", 0), KVP, STM(mv, "0", 0), KVP, "", KVP, "")
        "s_waitcnt vmcnt(2)\n\ts_barrier\n\tbuffer_store_dwordx2 %[tag], %[vfs], s[40:43], 0 offen sc1\n\t"
        QUAD(20480, 30720, KVP, STM(l16, "%[mh]", 0), KVP_(-57344), KVP, KVP, STM(l16, "s47", 0), KVP, KVP)
        QUAD(30720, 40960, KVP, "", "buffer_load_dwordx2 v[198:199], %[vfp], s[40:43], 0 offen sc1\n\t", STM(l16, "s47", 1024), "", STM(l16, "s47", 2048), "", "")
        "s_mov_b32 m0, s49\n\ts_waitcnt vmcnt(2)\n"
        "3:\n\tv_cmp_ne_u64 vcc, v[198:199], %[tag]\n\ts_cbranch_vccz 4f\n\t"
        "buffer_load_dwordx2 v[198:199], %[vfp], s[40:43], 0 offen sc1\n\ts_sub_u32 s58, s58, 1\n\ts_waitcnt vmcnt(0)\n\ts_cbranch_scc0 3b\n"
        "4:\n\ts_barrier\n\t" TSA("6") "buffer_store_dwordx2 %[tag], %[vfe], s[40:43], 0 offen sc1\n\t"
        QUAD(40960, 51200, PK(176, 0), PK(180, 1024) PKA, PK(184, 0), PK(188, 1024) PKA, PK(192, 0), PK(196, 1024) PKA, PK(200, 0), PK(204, 1024) PKA)
        QUAD(51200, 0, PK(208, 0), PK(212, 1024) PKA, PK(216, 0), PK(220, 1024) PKA, PK(224, 0), PK(228, 1024) PKA, PK(232, 0), PK(236, 1024) PKA)
        QUAD(0, 10240, PK(240, 0), PK(244, 1024), WOP(0) WO5, WOP(0) TCH(0), WOP(1024), WOP(2048), WOP(3072) "s_mov_b32 m0, s51\n\ts_mov_b32 s48, s50\n\t", WOP(0) WO5)
        QUAD3(10240, 0, WOP(0) TCH(128), WOP(1024), WOP(2048), WOP(3072),
              "buffer_load_dwordx4 v[248:251], %[rv0], %[rx], s53 offen\n\t", "buffer_load_dwordx2 v[252:253], %[rv1], %[rx], s53 offen\n\t")
        STEP_(64, 168, 172, "", "", "", "", "") TSI
        "s_mov_b32 m0, s55\n\ts_waitcnt vmcnt(24)\n\ts_nop 0\n\t" SMF(112, 172, 136) "s_barrier\n\t"
        DM1("%[l16]", "s54", 0) DM1("%[l16]", "s54", 1024) "s_mov_b32 exec_hi, 0\n\t" DM1("%[l16]", "s54", 2048) "s_mov_b32 exec_hi, -1\n\ts_nop 7\n\ts_nop 7"
        : "=&{v[96:127]}"(O), "=&{v[144:151]}"(qm), "=&{v[152:153]}"(qt), "+{v[176:207]}"(pk.a), "=&{v[208:239]}"(pk.b), "=&{v[240:247]}"(pk.c), "=&{v[248:251]}"(r0), "=&{v[252:253]}"(r1), "=&{v154}"(pk.d),
          "+{s[44:51]}"(sa), "+{s[52:55]}"(sb), "+v"(e0), "+v"(e1), "+v"(ov), "+v"(ow), "+s"(ob), "+s"(oc) PFIO
        : "{s[40:43]}"(rws), "{s[60:75]}"(rt.t), [a0] "v"(a0), [at] "v"(at), [av] "v"(av), [l16] "v"(l16), [tag] "v"(tag), [vcs] "v"(vcs), [fo] "v"(fo),
          [cp] "v"(L_CP + (w & 1) * 256 + lane * 4), [mv] "v"(mv), [mh] "s"(mh), [vs] "v"(vs), [vt] "v"(vt), [pb] "s"(pb), [pe] "s"(pb + 143360), [one] "v"(ones), [idx] "v"(0xDDDD),
          [va] "v"(va), [vb] "v"(va ^ 256), [vc] "v"((lane * 8) ^ (grp * 256)), [vfs] "v"(vfs), [vfe] "v"(vfe), [vfp] "v"(vfp), [rv0] "v"(rv0), [rv1] "v"(rv1),
          [dm] "s"(gb + wl * 1024), [w] "s"(w), [grp] "s"(grp), [rx] "s"(mkrs(p.x, 2048 * 1280))
        : "memory", "scc", "vcc", "s56", "s57", "s58", VCL32, VCL48, VCL64, VCL80, VCL128, "v140", "v141", "v142", "v143", "v156", "v157", VCL160 PFCL);
    TS(7);
    float o[12];
#pragma unroll
    for (int i = 0; i < 3; ++i) ldsw<f32x4>(e0 + i * 1024, f32x4{O[16 + 4 * i], O[17 + 4 * i], O[18 + 4 * i], O[19 + 4 * i]});
    LBAR();
#pragma unroll
    for (int i = 0; i < 3; ++i) {
        const f32x4 v = ldsr<f32x4>(e1 + i * 1024);
#pragma unroll
        for (int k = 0; k < 4; ++k) o[4 * i + k] = O[4 * i + k] + v[k];
    }
    {
        f32x2 z = {0.f, 0.f};
#pragma unroll
        for (int r = 0; r < 12; r += 2) z += f32x2{o[r], o[r + 1]} * 0.f;
        const float l = (hh ? o[8] : 1.f) + (z[0] + z[1]);
        const bool bad = !(l < 1.8446744e19f && l > 8.6736174e-19f);
        if (__builtin_expect(anyl(bad), 0)) {
            float m = -__builtin_inff();
            const unsigned ko = hh * 1024 + t * 8, vo = 2560 + hh * 320 + (t < 20 ? t : 0) * 16;
            const u32x4 qa = {qm[0], qm[1], qm[2], qm[3]}; const u32x2 qe = {hh ? 0u : qt[0], hh ? 0u : qt[1]};
            f32x16 c = {}, A = {};
#pragma unroll 1
            for (int it = 0; it < 128; ++it) {
                const unsigned sb = kvb + ((it & 63) >> 1) * 5120;
                const u32x2 k0 = ld8c(rws, ko, sb + (it & 1) * 256), k1 = ld8c(rws, ko + 512, sb + (it & 1) * 256), k2 = ld8c(rws, t * 8 + 2048, sb + (it & 1) * 256);
                f32x16 s = qk_tile(u32x4{k0[0], k0[1], k1[0], k1[1]}, k2, qa, qe, c);
                if (it < 64) {
                    m = max16(s, m);
                    if (it == 63) {
                        const f32x2 mm = swap32(m, m);
                        m = __builtin_fmaxf(mm[0], mm[1]);
#pragma unroll
                        for (int r = 0; r < 16; ++r) c[r] = -m;
                    }
                } else {
                    u32x4 v0 = ld16c(rws, vo, sb + (it & 1) * 1280), v1 = ld16c(rws, vo + 640, sb + (it & 1) * 1280);
#pragma unroll
                    for (int i = 0; i < 4; ++i) { v0[i] = t < 20 ? v0[i] : ones; v1[i] = t < 20 ? v1[i] : ones; }
#pragma unroll
                    for (int r = 0; r < 16; ++r) s[r] = __builtin_amdgcn_exp2f(s[r]);
                    A = pv16(v0, __builtin_shufflevector(s, s, 0, 1, 2, 3, 4, 5, 6, 7), A);
                    A = pv16(v1, __builtin_shufflevector(s, s, 8, 9, 10, 11, 12, 13, 14, 15), A);
                }
            }
#pragma unroll
            for (int r = 0; r < 12; ++r) o[r] = A[r];
        }
    }
    const float inv = __builtin_amdgcn_rcpf(swap32(o[8], o[8])[1]);
    st16(rws, ov, ob, u32x4{pk2(o[0] * inv, o[1] * inv), pk2(o[2] * inv, o[3] * inv), pk2(o[4] * inv, o[5] * inv), pk2(o[6] * inv, o[7] * inv)});
    st8(rws, ow, oc, u32x2{pk2(o[8] * inv, o[9] * inv), pk2(o[10] * inv, o[11] * inv)});
}

constexpr unsigned L_CS = 14336;
#define MF16(A, W, B) "v_mfma_f32_16x16x32_bf16 v[" A ":" A "+3], v[" W ":" W "+3], v[" B ":" B "+3], v[" A ":" A "+3]\n\t"
#define MF0(A, W, B) "v_mfma_f32_16x16x32_bf16 v[" A ":" A "+3], v[" W ":" W "+3], v[" B ":" B "+3], 0\n\t"
#define RD1(W, V, O) "ds_read_b128 v[" W ":" W "+3], " V " offset:" O "\n\t"
#define RD5(W, V, O) RD1(W, V, O) RD1(W "+4", V, O "+1024") RD1(W "+8", V, O "+2048") RD1(W "+12", V, O "+3072") RD1(W "+16", V, O "+4096")
#define WR1(V, R, O) "ds_write_b128 " V ", v[" R ":" R "+3] offset:" O "\n\t"
#define VM_(N) "s_waitcnt vmcnt(" #N ")\n\t"
#define VM(N) VM_(N)
#define MX3(B) "v_max3_u32 %[t], v[" B "], v[" B "+1], v[" B "+2]\n\t"
#define MX2(B) "v_max3_u32 %[t], v[" B "], v[" B "+1], %[t]\n\t"
#define MXE(B) "v_max_u32 %[t], v[" B "], %[t]\n\tv_cmp_eq_u32 vcc, -1, %[t]\n\ts_and_b64 vcc, vcc, s[60:61]\n\t"
#define CK4(B) MX3(B) MXE(B "+3")
#define CK8(B) MX3(B) MX2(B "+3") MX2(B "+5") MXE(B "+7")
#define CK16(B) MX3(B) MX2(B "+3") MX2(B "+5") MX2(B "+7") MX2(B "+9") MX2(B "+11") MX2(B "+13") MXE(B "+15")
#define CK28(B) MX3(B) MX2(B "+3") MX2(B "+5") MX2(B "+7") MX2(B "+9") MX2(B "+11") MX2(B "+13") MX2(B "+15") MX2(B "+17") MX2(B "+19") MX2(B "+21") MX2(B "+23") MX2(B "+25") MXE(B "+27")
#define SPIN0 "s_movk_i32 s59, " SPINS "\n\ts_mov_b64 s[60:61], -1\n\t"
#define SPIN1 "s_sub_u32 s59, s59, 1\n\ts_cselect_b64 s[60:61], 0, s[60:61]\n\t"
#define UNP(D, S) "v_lshlrev_b32 v[" #D "], 16, v" #S "\n\tv_and_b32 v[" #D "+1], 0xffff0000, v" #S "\n\t"
#define PKADD(D, S) "v_pk_add_f32 v[" D ":" D "+1], v[" D ":" D "+1], v[" S ":" S "+1]\n\t"
#define CVT(D, S) "v_cvt_pk_bf16_f32 v[" D "], v[" S "], v[" S "+1]\n\t"
#define WON(n, W, B, R) MF16("72+4*" #n, W "+4*" #n, B) R
#define WOK(W, B, V, O) WON(0, W, B, RD1(W, V, O)) WON(1, W, B, RD1(W "+4", V, O "+1024")) WON(2, W, B, RD1(W "+8", V, O "+2048")) WON(3, W, B, RD1(W "+12", V, O "+3072")) WON(4, W, B, RD1(W "+16", V, O "+4096"))
#define WOE(W, B) WON(0, W, B, "") WON(1, W, B, "") WON(2, W, B, "") WON(3, W, B, "") WON(4, W, B, "")
#define WOL(B, S) LD1(#B, "%[l16]", #S, "0") LD1(#B "+4", "%[l16]", #S, "1024")
#define WOC(p, N, B) "1" #p ":\n\t" VM(N) CK8(#B) "s_cbranch_scc1 2" #p "f\n\t" TSI
#define WOT(p) "2" #p ":\n\ts_call_b64 s[56:57], 8f\n\ts_branch 1" #p "b\n"
#define WOR(B, S) CK8(#B) "s_cbranch_scc0 1f\n\ts_mov_b64 exec, vcc\n\t" WOL(B, S) "s_mov_b64 exec, -1\n1:\n\t"
#define PW(R, O) WR1("%[pw]", #R, #O)
#define PW4(R, O) PW(R, O) PW(R+4, O+1024) PW(R+8, O+2048) PW(R+12, O+3072)
#define PX4(R, O) WR1("%[t]", #R, #O) WR1("%[t]", #R "+4", #O "+1024") WR1("%[t]", #R "+8", #O "+2048") WR1("%[t]", #R "+12", #O "+3072")
#define CSL(R) "buffer_load_dword " R ", %[ve], s[40:43], s49 offen sc1\n\ts_add_u32 s49, s49, 14336\n\t"
DI void phase_wo(rsrc_t rws, int w, unsigned lane, int mt, int nt, int ntg, u32x4& r0, u32x2& r1, Park& pk PROFARG) {
    const unsigned tg = w & 3, kh = w >> 2, t = lane & 15, g = lane >> 4, l16 = lane * 16, la = kh * 5120 + l16, e = w * 64 + lane, ro = nt & 3;
    const unsigned ab = WS_O + (mt * 4 + tg) * 20480 + kh * 10240, hb = WS_B + (mt >> 2) * QBS + nt * 256, tk = (mt & 3) * 4 + tg;
    u32x8 sa = {ab + ro * 2048, ab + (ro + 1) * 2048, ab + mod5(ro + 2) * 2048, ab + mod5(ro + 3) * 2048, ab + mod5(ro + 4) * 2048, WS_CS + ntg * 448, hb + tk * 20480, 0};
    const unsigned ex = tg * 5120 + kh * 2048 + l16;
    unsigned tmp;
    asm volatile(
        CSL("v92") CSL("v93") CSL("v94") CSL("v95") CSL("%[t]") SPIN0 VM(7) "s_barrier\n\t"
        RD5("96", "%[wa]", "0") RD5("116", "%[wa]", "10240")
        UNP(72, 248) UNP(74, 249) UNP(76, 250) UNP(78, 251) Z8("80") UNP(88, 252) UNP(90, 253)
        VM(5) TSA("20") TSS("45") WOL(32, s44) WOL(40, s45) WOL(48, s46) WOL(56, s47) WOL(64, s48) RD5("136", "%[wa]", "20480") RD5("156", "%[wa]", "30720")
        "s_waitcnt vmcnt(10) lgkmcnt(0)\n\tv_add_f32 v92, v92, v93\n\tv_add_f32 v94, v94, v95\n\tv_add_f32 v92, v92, %[t]\n\tv_add_f32 v92, v92, v94\n\tds_write_b32 %[vl], v92\n"
        WOC(0, 8, 32) TSA("8") WOK("96", "32", "%[wb]", "0") WOK("116", "36", "%[wb]", "10240")
        WOC(1, 6, 40) WOK("136", "40", "%[wb]", "20480") WOK("156", "44", "%[wb]", "30720")
        WOC(2, 4, 48) LGK(10) WOK("96", "48", "%[wc]", "0") WOK("116", "52", "%[wc]", "10240")
        WOC(3, 2, 56) LGK(10) WOE("136", "56") WOE("156", "60")
        WOC(4, 0, 64) LGK(0) WOE("96", "64") WOE("116", "68") TSA("9")
        "s_cmp_eq_u32 %[kh], 0\n\ts_nop 5\n\t" WR1("%[xw]", "80", "0") WR1("%[xw]", "84", "1024") "s_cbranch_scc1 1f\n\t" WR1("%[x4]", "88", "0")
        "1:\n\ts_waitcnt lgkmcnt(0)\n\ts_barrier\n\t" RD1("80", "%[xr]", "0") RD1("84", "%[xr]", "1024") RD1("92", "%[x4]", "0")
        LGK(2) PKADD("72", "80") PKADD("74", "82") LGK(1) PKADD("76", "84") PKADD("78", "86") CVT("248", "72") CVT("249", "74") CVT("250", "76") CVT("251", "78")
        "buffer_store_dwordx4 v[248:251], %[h0], s[40:43], s50 offen sc1\n\t" LGK(0) "s_cbranch_scc0 9f\n\t"
        PKADD("88", "92") PKADD("90", "94") CVT("252", "88") CVT("253", "90") "buffer_store_dwordx2 v[252:253], %[h1], s[40:43], s50 offen sc1\n\ts_branch 9f\n"
        WOT(0) WOT(1) WOT(2) WOT(3) WOT(4)
        "8:\n\t" VM(0) WOR(32, s44) WOR(40, s45) WOR(48, s46) WOR(56, s47) WOR(64, s48) VM(0) SPIN1 "s_setpc_b64 s[56:57]\n9:\n\t"
        PW4(176, 0) PW4(192, 4096) PW(208, 8192) PW(212, 9216)
        : "+{v[248:251]}"(r0), "+{v[252:253]}"(r1), "+{v[176:207]}"(pk.a), "+{v[208:239]}"(pk.b), "+{v[240:247]}"(pk.c), "+{s[44:51]}"(sa), [t] "=&v"(tmp) PFIO
        : "{s[40:43]}"(rws), [l16] "v"(l16), [wa] "v"(la + 122880), [wb] "v"(la + 20480), [wc] "v"(la + 81920), [kh] "s"(kh), [pw] "v"(20480 + park0(w) + l16),
          [ve] "v"(e < 112 ? (e ^ (((lane >> 1) ^ (lane >> 3)) & 1) * 10) * 4 : OOB), [vl] "v"(L_CS + (e < 112 ? e : 112) * 4),
          [xw] "v"(tg * 5120 + 2048 - kh * 2048 + l16), [xr] "v"(ex), [x4] "v"((tg == 2 ? 61440 : tg * 5120 + 4096) + l16),
          [h0] "v"((2 * g + kh) * 2048 + t * 16), [h1] "v"((8 + (g >> 1)) * 2048 + t * 16 + (g & 1) * 8)
        : "memory", "scc", "vcc", "s56", "s57", "s59", "s60", "s61", VCL32, VCL48, VCL64, VCL80, VCL96, VCL112, VCL128, VCL140, VCL160 PFCL);
    TS(10);
    float s = 0.f, q = 0.f;
#pragma unroll
    for (int k = 0; k < 6; ++k) {
        const bf2v v = __builtin_bit_cast(bf2v, k < 4 ? r0[k & 3] : r1[k & 1]);
        s = __builtin_amdgcn_fdot2_f32_bf16(v, __builtin_bit_cast(bf2v, 0x3f803f80u), s, false);
        q = __builtin_amdgcn_fdot2_f32_bf16(v, v, q, false);
    }
    const f32x2 y = swap32(s, q);
    const float z = sum16(y[0] + y[1]);
    const f32x2 v = swap32(z, z);
    st8(rws, g == 0 ? t * 16 + kh * 8 : OOB, hb + OFF_HS + tk * 2048, u32x2{__float_as_uint(v[0]), __float_as_uint(v[1])});
}

#define GW0 LGK(4)
#define GW1
#define GW2
#define GW3 LGK(5)
#define GW4
#define GW5 LGK(5)
#define GW6
#define GUN(MF, n, H, R, X) GW##n MF("140+8*" #n, "112+4*" #n, H) X MF("144+8*" #n, "112+4*" #n, H "+8") R
#define GUK(MF, H, V, O, D, X0, X1, X2, X3, X4, X5, X6) \
    GUN(MF, 0, H, RD1("112", V, O), X0) GUN(MF, 1, H, RD1("116", V, O "+" D), X1) GUN(MF, 2, H, RD1("120", V, O "+2*" D), X2) GUN(MF, 3, H, RD1("124", V, O "+3*" D), X3) \
    GUN(MF, 4, H, RD1("128", V, O "+4*" D), X4) GUN(MF, 5, H, RD1("132", V, O "+5*" D), X5) GUN(MF, 6, H, RD1("136", V, O "+6*" D), X6)
#define LH0(H, S) LD1(#H, "%[l16]", #S, "0")
#define LH1(H, S) LD1(#H "+4", "%[l16]", #S, "1024")
#define LH2(H, S) LD1(#H "+8", "%[lb]", #S, "0")
#define LH3(H, S) LD1(#H "+12", "%[lb]", #S, "1024")
#define LDH(H, S) LH0(H, S) LH1(H, S) LH2(H, S) LH3(H, S)
#define LH(H, S) LH0(H, S), LH1(H, S), LH2(H, S), LH3(H, S)
#define LS(R, O) LD1(#R, "%[l16]", "s54", #O)
#define LDST LS(32, 0) LS(36, 1024) LS(40, 2048) LS(44, 3072)
#define WL(R, O) LD1(#R, "%[l16]", "s52", #O)
#define WA "s_add_u32 s52, s52, 4096\n\t"
#define W15 WL(196, 0) WL(200, 1024) WL(204, 2048) WL(208, 3072) WA WL(212, 0) WL(216, 1024) WL(220, 2048) WL(224, 3072) WA WL(228, 0) WL(232, 1024) WL(236, 2048) WL(240, 3072) WA \
    WL(244, 0) WL(24, 1024) WL(28, 2048)
#define WEND "v_add_u32 %[lb], 4096, %[l16]\n\t" WL(32, 3072) LD1("36", "%[lb]", "s52", "0") LD1("40", "%[lb]", "s52", "1024") LD1("44", "%[z4]", "s52", "2048") LD1("96", "%[z4]", "s52", "3072") WA \
    LD1("100", "%[z4]", "s52", "0") LD1("104", "%[z4]", "s52", "1024") LD1("108", "%[z4]", "s52", "2048")
#define CKS(N, B, s) VM(N) MX3(B), MX2(B "+3"), MX2(B "+5"), MX2(B "+7"), MX2(B "+9"), MX2(B "+11"), MX2(B "+13") MXE(B "+15") "s_cbranch_scc1 4" #s "f\n3" #s ":\n\t"
#define GUK_(...) GUK(__VA_ARGS__)
#define GUS(MF, s, H, O1, PRE, O2, HN, N, ...) GUK_(MF, #H, "%[wa]", O1, "1024", __VA_ARGS__) PRE GUK_(MF16, #H "+4", "%[wa]", O2, "1024", CKS(N, #HN, s)) TSI
#define REDO(s, L, CK) "4" #s ":\n\ts_mov_b64 exec, vcc\n\t" L "s_mov_b64 exec, -1\n\t" VM(0) SPIN1 CK "s_cbranch_scc1 4" #s "b\n\ts_branch 3" #s "b\n"
#define WADV "v_add_u32 %[wa], 0xfc00, %[wa]\n\t"
#define HNX(A, B) "s_add_u32 s" #B ", s" #A ", 2048\n\ts_cmp_eq_u32 s" #B ", s55\n\ts_cselect_b32 s" #B ", %[hb], s" #B "\n\t"
#define SW32(A, B) "v_permlane32_swap_b32 v[" A "], v[" B "]\n\t"
#define SW16(A, B) "v_permlane16_swap_b32 v[" A "], v[" B "]\n\t"
#define EPF(A, C, T) "v_pk_fma_f32 v[" A ":" A "+1], v[" C ":" C "+1], v[16+4*" #T ":17+4*" #T "], v[" A ":" A "+1]\n\t"
#define EPT(A, C, T, D) EPF(A, C, T) EPF(A "+2", C "+2", T) "v_max_f32 v[" A "], 0, v[" A "]\n\tv_max_f32 v[" A "+1], 0, v[" A "+1]\n\t" \
    "v_pk_mul_f32 v[" A "+2:" A "+3], v[" A "+2:" A "+3], v[18+4*" #T ":19+4*" #T "]\n\t" \
    "v_pk_mul_f32 v[" A ":" A "+1], v[" A ":" A "+1], v[" A "+2:" A "+3]\n\t" CVT(D, A)
#define EPN(n, D) EPT("140+8*" #n, "112+4*" #n, 0, D) EPT("144+8*" #n, "112+4*" #n, 1, D "+1")
#define EP2(n, m, D, N) LGK(N) EPN(n, D) EPN(m, D "+2") SW32(D, D "+1") "s_nop 0\n\t" SW32(D "+2", D "+3") \
    "buffer_store_dwordx4 v[" D ":" D "+3], %[f0], s[40:43], s53 offen sc1\n\ts_add_u32 s53, s53, 15872\n\t"
#define LF0(F, S) LD1(F, "%[l16]", S, "0")
#define LF1(F, S) LD1(F "+4", "%[l16]", S, "1024")
#define LF2(F, S) LD1(F "+8", "%[l16]", S, "2048")
#define LF3(F, S) LD1(F "+12", "%[l16]", S, "3072")
#define LF4(F, S) LD1(F "+16", "%[lb]", S, "0")
#define LF5(F, S) LD1(F "+20", "%[lb]", S, "1024")
#define LF6(F, S) LD1(F "+24", "%[z4]", S, "2048")
#define LDF(F, S) LF0(F, S) LF1(F, S) LF2(F, S) LF3(F, S) LF4(F, S) LF5(F, S) LF6(F, S)
#define DK(MF, F, a, b, c, d, e) MF("168", #a, F) MF("172", #b, F) MF("176", #c, F) MF("180", #d, F) MF("184", #e, F)
#define DT(F, X0, X1, X2, X3, X4, X5, X6) DK(MF0, F, 196, 200, 204, 208, 212) X0 DK(MF16, F "+4", 216, 220, 224, 228, 232) X1 DK(MF16, F "+8", 236, 240, 244, 24, 28) X2 \
    DK(MF16, F "+12", 48, 52, 56, 60, 64) X3 DK(MF16, F "+16", 68, 72, 76, 80, 84) X4 DK(MF16, F "+20", 88, 92, 32, 36, 40) X5 DK(MF16, F "+24", 44, 96, 100, 104, 108) X6 TSI
#define DTL(F, S) DT(F, LF0(F, S), LF1(F, S), LF2(F, S), LF3(F, S), LF4(F, S), LF5(F, S), LF6(F, S))
#define DTE(F) DT(F, "", "", "", "", "", "", "")
#define DW(S) "v_add_u32 %[wa], " S ", %[l16]\n\t" WR1("%[wa]", "168", "0") WR1("%[wa]", "172", "1024") WR1("%[wa]", "176", "2048") WR1("%[wa]", "180", "3072") WR1("%[wa]", "184", "4096")
#define DC(N, F, s) VM(N) CK28(F) "s_cbranch_scc1 4" #s "f\n3" #s ":\n\t"
DI void phase_ffn(rsrc_t rws, int w, unsigned lane, int mt, int nt, int qg, int ntg, const Park& pk PROFARG) {
    const unsigned t = lane & 15, g = lane >> 4, l16 = lane * 16, qbb = WS_B + qg * QBS, wg = (w & 3) * 2 + (w >> 2), hb = qbb + wg * 40960, k0 = 7 * w - (w == 7);
    const unsigned fb = WS_B + (mt >> 2) * QBS + OFF_F + (mt & 3) * 4 * 55296 + k0 * 1024, fv = (g >> 1) * 55296 + t * 16;
    u32x8 sa; u32x4 sb = {WS_WD + nt * 276480 + k0 * 5120, qbb + OFF_F + wg * 110592 + ntg * 256, qbb + OFF_HS + wg * 4096, hb + 20480};
#pragma unroll
    for (int i = 0; i < 4; ++i) { sa[i] = fb + (i ^ (nt & 1)) * 55296; sa[4 + i] = (i ^ (nt & 1)) * 40960 + w * 5120; }
    unsigned wa = 20480 + l16, h0 = hb + (((ntg & 15) * 5) >> 3) * 2048, tmp = 20480 + park0(w) + l16, lb = l16 + 20480;
    u32x32 da = pk.a, db = pk.b; u32x8 dc = pk.c;
    asm volatile(
        PX4(216, 10240) PX4(232, 14336) SPIN0 HNX(64, 65) HNX(65, 66) HNX(66, 67) HNX(67, 68) HNX(68, 69) HNX(69, 70) HNX(70, 71) HNX(71, 72) HNX(72, 73)
        "s_cmp_eq_u32 %[ng], 31\n\ts_cbranch_scc1 7f\n\t" VM(1) TSA("21") LDH(32, s64) LDH(48, s65) LGK(0) "s_barrier\n\t"
        RD5("112", "%[wa]", "0") RD1("132", "%[wa]", "5120") RD1("136", "%[wa]", "6144")
        "1:\n\t" VM(4) CK16("32") "s_cbranch_scc1 2f\n\t" TSA("11") TSS("35") LDH(64, s66) LDH(80, s67) LDH(96, s68)
        GUS(MF0, 0, 32, "7168", "", "14336", 48, 12, "", "", "", "", "", "", "")
        GUS(MF16, 1, 48, "21504", "", "28672", 64, 12, LH(32, s69), "", "", "")
        GUS(MF16, 2, 64, "35840", "", "43008", 80, 13, LH(48, s70), WL(196, 0), "", "")
        GUS(MF16, 3, 80, "50176", "", "57344", 96, 15, LH(64, s71), WL(200, 1024), WL(204, 2048), "")
        WADV
        GUS(MF16, 4, 96, "0", "", "7168", 32, 18, LH(80, s72), WL(208, 3072) WA, WL(212, 0), WL(216, 1024))
        GUS(MF16, 5, 32, "14336", "", "21504", 48, 21, LH(96, s73), WL(220, 2048), WL(224, 3072) WA, WL(228, 0))
        GUS(MF16, 6, 48, "28672", "", "35840", 64, 23, LS(32, 0), LS(36, 1024), LS(40, 2048), LS(44, 3072), WL(232, 1024), WL(236, 2048), WL(240, 3072) WA)
        GUS(MF16, 7, 64, "43008", "", "50176", 80, 24, WL(244, 0), WL(24, 1024), WL(28, 2048), WL(48, 3072) WA, WL(52, 0), WL(56, 1024), WL(60, 2048))
        GUS(MF16, 8, 80, "57344", WADV, "0", 96, 21, WL(64, 3072) WA, WL(68, 0), WL(72, 1024), WL(76, 2048), "", "", "")
        GUK_(MF16, "96", "%[wa]", "7168", "1024", CKS(14, "32", 9))
        "v_lshrrev_b32 %[wa], 4, %[l16]\n\tv_and_b32 %[wa], 48, %[wa]\n\t"
        GUK(MF16, "100", "%[wa]", "14336", "64",
            WL(80, 3072) WA PKADD("32", "34") PKADD("36", "38") PKADD("40", "42") PKADD("44", "46"),
            WL(84, 0) PKADD("32", "36") PKADD("40", "44"),
            WL(88, 1024) SW32("32", "40") SW32("33", "41") PKADD("32", "40") "v_mov_b64 v[34:35], v[32:33]\n\t",
            WL(92, 2048) SW16("32", "34") SW16("33", "35") PKADD("32", "34") "v_mul_f32 v16, 0xbacccccd, v32\n\t",
            "v_mul_f32 v33, 0x3acccccd, v33\n\tv_fma_f32 v33, -v16, v16, v33\n\tv_max_f32 v33, 0, v33\n\tv_add_f32 v33, 0x3727c5ac, v33\n\t",
            "v_rcp_f32 v18, v33\n\tv_mov_b32 v20, v16\n\tv_mov_b32 v22, v18\n\t",
            SW32("16", "20") SW32("18", "22") "v_mov_b32 v17, v16\n\tv_mov_b32 v21, v20\n\tv_mov_b32 v19, v18\n\tv_mov_b32 v23, v22\n\t")
        TSA("12") WEND EP2(0, 1, "140", 5) EP2(2, 3, "156", 3) EP2(4, 5, "172", 1) LGK(0) EPN(6, "188") "s_nop 1\n\t" SW32("188", "189")
        "buffer_store_dwordx2 v[188:189], %[f1], s[40:43], s53 offen sc1\n"
        TSA("22") "6:\n\ts_barrier\n\t" TSA("23") LDF("112", "s44") LDF("140", "s45") DC(7, "112", 13) TSA("13") TSS("50")
        DTL("112", "s46") DC(7, "140", 10) DW("s48") DTL("140", "s47")
        "s_nop 5\n\t" DW("s49") DC(7, "112", 11) TSA("56") DTE("112") DC(0, "140", 12) DW("s50") DTE("140") "s_nop 6\n\t" DW("s51")
        "s_waitcnt lgkmcnt(0)\n\ts_barrier\n\ts_branch 9f\n"
        "2:\n\ts_mov_b64 exec, vcc\n\t" LDH(32, s64) LDH(48, s65) "s_mov_b64 exec, -1\n\t" SPIN1 "s_branch 1b\n"
        REDO(0, LDH(48, s65), CK16("48")) REDO(1, LDH(64, s66), CK16("64")) REDO(2, LDH(80, s67), CK16("80")) REDO(3, LDH(96, s68), CK16("96")) REDO(4, LDH(32, s69), CK16("32"))
        REDO(5, LDH(48, s70), CK16("48")) REDO(6, LDH(64, s71), CK16("64")) REDO(7, LDH(80, s72), CK16("80")) REDO(8, LDH(96, s73), CK16("96")) REDO(9, LDST, CK16("32"))
        REDO(10, LDF("140", "s45"), CK28("140")) REDO(11, LDF("112", "s46"), CK28("112")) REDO(12, LDF("140", "s47"), CK28("140"))
        REDO(13, LDF("112", "s44"), CK28("112"))
        "7:\n\t" LGK(0) W15 WL(48, 3072) WA WL(52, 0) WL(56, 1024) WL(60, 2048) WL(64, 3072) WA WL(68, 0) WL(72, 1024) WL(76, 2048) WL(80, 3072) WA WL(84, 0) WL(88, 1024) WL(92, 2048) WEND
        "1:\n\ts_sleep 4\n\t" LF0("112", "s44") SPIN1 VM(0) CK4("112") "s_cbranch_scc1 1b\n\ts_branch 6b\n9:"
        : "+{v[176:207]}"(da), "+{v[208:239]}"(db), "+{v[240:247]}"(dc), "+{s[52:55]}"(sb), "+{s64}"(h0), [wa] "+&v"(wa), [t] "+&v"(tmp), [lb] "+&v"(lb) PFIO
        : "{s[40:43]}"(rws), "{s[44:51]}"(sa), [l16] "v"(l16), [z4] "v"(w < 6 ? l16 + 4096 : OOB),
          [f0] "v"(fv + (g & 1) * 7936), [f1] "v"(ntg < 30 ? fv + (g & 1) * 8 : OOB), [ng] "s"(ntg), [hb] "s"(hb)
        : "memory", "scc", "vcc", "s59", "s60", "s61", "s65", "s66", "s67", "s68", "s69", "s70", "s71", "s72", "s73", "v16", "v17", "v18", "v19", "v20", "v21", "v22", "v23",
          "v24", "v25", "v26", "v27", "v28", "v29", "v30", "v31", VCL32, VCL48, VCL64, VCL80, VCL96, VCL112, VCL128, VCL140, VCL160 PFCL);
}
DI f32x4 sum8(unsigned a) {
    f32x4 v[8];
#pragma unroll
    for (int i = 0; i < 8; ++i) v[i] = ldsr<f32x4>(a + i * 5120);
    return ((v[0] + v[1]) + (v[2] + v[3])) + ((v[4] + v[5]) + (v[6] + v[7]));
}
DI u32x2 addres(f32x4 a, unsigned lo, unsigned hi) { return u32x2{pk2(a[0] + bflo(lo), a[1] + bfhi(lo)), pk2(a[2] + bflo(hi), a[3] + bfhi(hi))}; }

__global__ void __launch_bounds__(512, 2) block_kernel(Params) {
    u32x16 ka;
    asm volatile("s_load_dwordx16 %0, %1, 0" : "=s"(ka) : "s"(__builtin_amdgcn_kernarg_segment_ptr()) : "memory");
    __shared__ __attribute__((aligned(16))) char lds[163840];
    asm volatile("" :: "v"((LAS char*)lds) : "memory");
    const int lane = threadIdx.x & 63, w = __builtin_amdgcn_readfirstlane(threadIdx.x >> 6), b = blockIdx.x;
    unsigned pre;
    asm volatile("s_cmp_lg_u32 %1, %2\n\ts_cbranch_scc1 1f\n\ts_getpc_b64 s[4:5]\n.Lpc:\n\tv_lshlrev_b32 %0, 7, %3\n\tv_min_u32 %0, .Lend-.Lpc-4, %0\n\t"
                 "global_load_dword %0, %0, s[4:5]\n1:" : "=&{v200}"(pre) : "s"(w), "s"((b >> 3) & 3), "v"(threadIdx.x) : "memory", "scc", "s4", "s5");
    Params p;
    rsrc_t rws;
    const int xcd = b & 7, i = b >> 3;
    Retile rt;
    u32x2 tag;
    unsigned fq;
    {
        const int mt = (xcd >> 1) * 4 + (i & 3), nt = (xcd & 1) * 8 + (i >> 2), rot = (i >> 2) + (((i & 3) * 5 + 1) >> 1);
        QkvSrc c; QkvAdr e; u32x4 row; unsigned rowt, T;
        qkv_start(c, row, rowt, p, ka, mt, nt, rot < 10 ? rot : rot - 10, w, lane);
        rws = mkrs(p.ws, WS_END);
        asm volatile("s_load_dword %0, %2, %4\n\ts_load_dwordx16 %1, %3, %5" : "=&s"(T), "=&{s[60:75]}"(rt.t) : "s"(p.ws), "s"(&RT_TAB), "s"(WS_E), "s"((b * 8 + w) * 64) : "memory");
        TSW(60);
        retile_setup(rt, p, b, lane);
        qkv_setup(e, mt, nt, w, lane);
        fq = lane == 0 ? WS_FQ + (nt * 16 + mt) * 128 : OOB;
        TSW(61);
        asm volatile("s_waitcnt vmcnt(8) lgkmcnt(0)\n\ts_add_u32 %0, %0, 1" : "+s"(T), "+{s[60:75]}"(rt.t), "+v"(row), "+v"(rowt), "+v"(fq), "+{v200}"(pre) :: "memory", "scc");
        TSW(62);
        const u32x4 gr = row_stats(row, rowt, T);
        tag = u32x2{gr[2], gr[3]};
        TSW(63);
        if (nt == 15) {
            st16(rws, e.go, 0, gr);
            retile_issue(rt, w);
            asm volatile("s_waitcnt vmcnt(0)" ::: "memory");
        } else if (nt >= 10) qkv_body<true>(c, e, rt, rws, gr, tag, w PROFPASS);
        else qkv_body<false>(c, e, rt, rws, gr, tag, w PROFPASS);
    }
    const int mt = (xcd >> 1) * 8 + (i & 7), nt = (xcd & 1) * 4 + (i >> 3);
    const int qg = (xcd & 6) + ((i >> 2) & 1), ntg = (xcd & 1) * 16 + (i & 3) * 4 + (i >> 3);
    const int tg = w & 3, kh = w >> 2;
    auto rv0 = [&](unsigned l) { return (l & 15) * 1280 + (nt & 1) * 32 + kh * 64 + (l >> 4) * 16; };
    auto rv1 = [&](unsigned l) { return kh ? OOB : (l & 15) * 1280 + ((nt & 1) ? 0 : 128) + (l >> 4) * 8; };
    const unsigned rsb = (mt * 64 + tg * 16) * 1280 + nt * 160;
    u32x4 r0; u32x2 r1; Park pk;
    phase_attn(p, rt, rws, tag, fq, b, w, lane, nt, ntg, rv0(lane), rv1(lane), rsb, r0, r1, pk PROFPASS);
    unsigned ln;
    asm volatile("v_mbcnt_lo_u32_b32 %0, -1, 0\n\tv_mbcnt_hi_u32_b32 %0, -1, %0" : "=v"(ln) : "{v154}"(pk.d));
    phase_wo(rws, w, ln, mt, nt, ntg, r0, r1, pk PROFPASS);
    PIN(ln);
    unsigned o0 = rv0(ln), o1 = rv1(ln), a = tg * 40960 + ln * 16;
    PIN(o0); PIN(o1); PIN(a);
    phase_ffn(rws, w, ln, mt, nt, qg, ntg, pk PROFPASS);
    TS(14);
    {
        const rsrc_t ro = mkrs(p.out, 2048 * 1280);
        const u32x2 u = addres(sum8(a + kh * 2048), r0[0], r0[1]), v = addres(sum8(a + kh * 2048 + 1024), r0[2], r0[3]);
        st16(ro, o0, rsb, u32x4{u[0], u[1], v[0], v[1]});
        if (kh == 0) st8(ro, o1, rsb, addres(sum8(a + 4096), r1[0], r1[1]));
    }
    asm volatile(".Lend:" ::: "memory");
}

extern "C" void kernel_launch(void* const* d_in, const int* in_sizes, int n_in, void* d_out, int out_size, void* d_ws, size_t ws_size, hipStream_t stream) {
    Params p;
    p.x = (const char*)d_in[0]; p.wqkv = (const char*)d_in[1]; p.wo = (const char*)d_in[2];
    p.wg = (const char*)d_in[4]; p.wu = (const char*)d_in[5]; p.wd = (const char*)d_in[6];
    p.out = (char*)d_out; p.ws = (char*)d_ws;
    block_kernel<<<256, 512, 0, stream>>>(p);
}
```

```cpp
#include <hip/hip_runtime.h>
#include <hip/hip_bf16.h>
#include <stdint.h>

#define DI __device__ __forceinline__
typedef __attribute__((ext_vector_type(2))) __bf16 bf2v;
typedef __attribute__((ext_vector_type(8))) __bf16 bf8v;
typedef __attribute__((ext_vector_type(16))) __bf16 bf16v;
typedef __attribute__((ext_vector_type(4))) short s4v;
typedef __attribute__((ext_vector_type(2))) float f32x2;
typedef __attribute__((ext_vector_type(4))) float f32x4;
typedef __attribute__((ext_vector_type(8))) float f32x8;
typedef __attribute__((ext_vector_type(16))) float f32x16;
typedef __attribute__((ext_vector_type(32))) float f32x32;
typedef __attribute__((ext_vector_type(2))) unsigned u32x2;
typedef __attribute__((ext_vector_type(4))) unsigned u32x4;
typedef __attribute__((ext_vector_type(8))) unsigned u32x8;
typedef __attribute__((ext_vector_type(16))) unsigned u32x16;
typedef __attribute__((ext_vector_type(32))) unsigned u32x32;
typedef __amdgpu_buffer_rsrc_t rsrc_t;
#define LAS __attribute__((address_space(3)))

constexpr unsigned WS_Q = 0;
constexpr unsigned WS_KV = WS_Q + 32 * 81920;
constexpr unsigned WS_O = WS_KV + 32 * 163840;
constexpr unsigned WS_B = WS_O + 128 * 20480;
constexpr unsigned QBS = 32 * 40960, OFF_HS = 16 * 20480, OFF_F = OFF_HS + 16 * 2048;
constexpr unsigned WS_WO = WS_B + 8 * QBS;
constexpr unsigned WS_WD = WS_WO + 8 * 102400;
constexpr unsigned WS_WGU = WS_WD + 8 * 276480;
constexpr unsigned WS_CS = WS_WGU + 32 * 143360;
constexpr unsigned WS_FQ = WS_CS + 5 * 3584 * 4;
constexpr unsigned WS_FR = WS_FQ + 256 * 128;
constexpr unsigned WS_XS = WS_FR + 2048;
constexpr unsigned WS_E = WS_XS + 2048 * 16;
constexpr unsigned WS_END = WS_E + 128;
constexpr unsigned OOB = 0x40000000u;
constexpr int SPIN = 1 << 14;

struct Params {
    const char* x; const char* wqkv; const char* wo; const char* wg; const char* wu; const char* wd;
    char* out; char* ws;
};

#define SBAR() __builtin_amdgcn_sched_barrier(0)
#define PIN(x) asm volatile("" : "+v"(x))
#define WAITBAR(N) asm volatile("s_waitcnt vmcnt(" #N ") lgkmcnt(0)\n\ts_barrier" ::: "memory")
#define VBAR(N) asm volatile("s_waitcnt vmcnt(" #N ")\n\ts_barrier" ::: "memory")
#define LBAR() asm volatile("s_waitcnt lgkmcnt(0)\n\ts_barrier" ::: "memory")
#define BAR() asm volatile("s_barrier" ::: "memory")

DI rsrc_t mkrs(const char* p, unsigned bytes) { return __builtin_amdgcn_make_buffer_rsrc((void*)p, (short)0, (int)bytes, 0x00020000); }
template <class T> DI T ldsr(unsigned a) { return *(const LAS T*)(uintptr_t)a; }
template <class T> DI void ldsw(unsigned a, T v) { *(LAS T*)(uintptr_t)a = v; }
DI s4v ldstr(unsigned a) { return __builtin_amdgcn_ds_read_tr16_b64_v4i16((LAS s4v*)(uintptr_t)a); }
DI bf8v ldstr2(unsigned a, unsigned d) {
    s4v lo = ldstr(a), hi = ldstr(a + d);
    return __builtin_bit_cast(bf8v, __builtin_shufflevector(lo, hi, 0, 1, 2, 3, 4, 5, 6, 7));
}
DI f32x16 mfma32(bf8v a, bf8v b, f32x16 c) { return __builtin_amdgcn_mfma_f32_32x32x16_bf16(a, b, c, 0, 0, 0); }
DI f32x4 mfma16(bf8v a, bf8v b, f32x4 c) { return __builtin_amdgcn_mfma_f32_16x16x32_bf16(a, b, c, 0, 0, 0); }
DI bf8v asbf(u32x4 v) { return __builtin_bit_cast(bf8v, v); }
DI unsigned pk2(float lo, float hi) { f32x2 v = {lo, hi}; return __builtin_bit_cast(unsigned, __builtin_convertvector(v, bf2v)); }
DI float bflo(unsigned u) { return __uint_as_float(u << 16); }
DI float bfhi(unsigned u) { return __uint_as_float(u & 0xffff0000u); }
DI f32x2 swap32(float a, float b) { auto r = __builtin_amdgcn_permlane32_swap(__float_as_uint(a), __float_as_uint(b), false, false); return f32x2{__uint_as_float(r[0]), __uint_as_float(r[1])}; }
DI float sum16(float a) { auto r = __builtin_amdgcn_permlane16_swap(__float_as_uint(a), __float_as_uint(a), false, false); return __uint_as_float(r[0]) + __uint_as_float(r[1]); }
DI bool anyl(bool c) { return __builtin_amdgcn_ballot_w64(c) != 0; }
DI void st16(rsrc_t r, unsigned voff, unsigned soff, u32x4 v) { asm volatile("buffer_store_dwordx4 %0, %1, %2, %3 offen sc1\n\ts_nop 1" :: "v"(v), "v"(voff), "s"(r), "s"(soff) : "memory"); }
DI void st8(rsrc_t r, unsigned voff, unsigned soff, u32x2 v) { asm volatile("buffer_store_dwordx2 %0, %1, %2, %3 offen sc1" :: "v"(v), "v"(voff), "s"(r), "s"(soff) : "memory"); }
DI void st4(rsrc_t r, unsigned voff, unsigned soff, unsigned v) { asm volatile("buffer_store_dword %0, %1, %2, %3 offen sc1" :: "v"(v), "v"(voff), "s"(r), "s"(soff) : "memory"); }
DI u32x4 ld16c(rsrc_t r, unsigned voff, unsigned soff) { return __builtin_amdgcn_raw_buffer_load_b128(r, voff, soff, 16); }
DI u32x2 ld8c(rsrc_t r, unsigned voff, unsigned soff) { return __builtin_amdgcn_raw_buffer_load_b64(r, voff, soff, 16); }
DI void dma1(unsigned m0v, unsigned voff, rsrc_t r, unsigned soff) {
    asm volatile("s_mov_b32 m0, %0\n\ts_nop 0\n\tbuffer_load_dwordx4 %1, %2, %3 offen lds" :: "s"(m0v), "v"(voff), "s"(r), "s"(soff) : "memory"); }
DI void dma1c(unsigned m0v, unsigned voff, rsrc_t r, unsigned soff) {
    asm volatile("s_mov_b32 m0, %0\n\ts_nop 0\n\tbuffer_load_dwordx4 %1, %2, %3 offen sc1 lds" :: "s"(m0v), "v"(voff), "s"(r), "s"(soff) : "memory"); }
DI void dma3c(unsigned m0v, unsigned voff, rsrc_t r, unsigned soff) {
    asm volatile("s_mov_b32 m0, %0\n\ts_nop 0\n\tbuffer_load_dwordx4 %1, %2, %3 offen sc1 lds\n\tbuffer_load_dwordx4 %1, %2, %3 offen offset:1024 sc1 lds\n\t"
                 "buffer_load_dwordx4 %1, %2, %3 offen offset:2048 sc1 lds" :: "s"(m0v), "v"(voff), "s"(r), "s"(soff) : "memory"); }
DI void dma4c(unsigned m0v, unsigned voff, rsrc_t r, unsigned soff) {
    asm volatile("s_mov_b32 m0, %0\n\ts_nop 0\n\tbuffer_load_dwordx4 %1, %2, %3 offen sc1 lds\n\tbuffer_load_dwordx4 %1, %2, %3 offen offset:1024 sc1 lds\n\t"
                 "buffer_load_dwordx4 %1, %2, %3 offen offset:2048 sc1 lds\n\tbuffer_load_dwordx4 %1, %2, %3 offen offset:3072 sc1 lds" :: "s"(m0v), "v"(voff), "s"(r), "s"(soff) : "memory"); }
#define SPINS "0x4000"
#define PINS(x) asm volatile("" : "+s"(x))
#define CHK(B, TMP, DST) \
    "v_max3_u32 v" #TMP ", v[" #B "], v[" #B "+1], v[" #B "+2]\n\tv_max3_u32 v" #TMP ", v[" #B "+3], v[" #B "+4], v" #TMP "\n\t" \
    "v_max3_u32 v" #TMP ", v[" #B "+5], v[" #B "+6], v" #TMP "\n\tv_max3_u32 v" #TMP ", v[" #B "+7], v[" #B "+8], v" #TMP "\n\t" \
    "v_max3_u32 v" #TMP ", v[" #B "+9], v[" #B "+10], v" #TMP "\n\tv_max3_u32 v" #TMP ", v[" #B "+11], v[" #B "+12], v" #TMP "\n\t" \
    "v_max3_u32 v" #TMP ", v[" #B "+13], v[" #B "+14], v" #TMP "\n\tv_max_u32 v" #TMP ", v[" #B "+15], v" #TMP "\n\t" \
    "v_cmp_eq_u32 " DST ", -1, v" #TMP "\n\t"
#define LD1(R, V, S, O) "buffer_load_dwordx4 v[" R ":" R "+3], " V ", s[40:43], " S " offen offset:" O " sc1\n\t"
#define DM1(V, S, O) "buffer_load_dwordx4 " V ", s[40:43], " S " offen offset:" #O " sc1 lds\n\t"
#define DM3(V, S) DM1(V, S, 0) DM1(V, S, 1024) DM1(V, S, 2048)
#define DM4(V, S) DM3(V, S) DM1(V, S, 3072)
#define Z2(R) "v_mov_b64 v[" R ":" R "+1], 0\n\t"
#define Z4(R) Z2(R) Z2(R "+2")
#define Z8(R) Z4(R) Z4(R "+4")

#define VCL32 "v32", "v33", "v34", "v35", "v36", "v37", "v38", "v39", "v40", "v41", "v42", "v43", "v44", "v45", "v46", "v47"
#define VCL48 "v48", "v49", "v50", "v51", "v52", "v53", "v54", "v55", "v56", "v57", "v58", "v59", "v60", "v61", "v62", "v63"
#define VCL64 "v64", "v65", "v66", "v67", "v68", "v69", "v70", "v71", "v72", "v73", "v74", "v75", "v76", "v77", "v78", "v79"
#define VCL80 "v80", "v81", "v82", "v83", "v84", "v85", "v86", "v87", "v88", "v89", "v90", "v91", "v92", "v93", "v94", "v95"
#define VCL96 "v96", "v97", "v98", "v99", "v100", "v101", "v102", "v103", "v104", "v105", "v106", "v107", "v108", "v109", "v110", "v111"
#define VCL112 "v112", "v113", "v114", "v115", "v116", "v117", "v118", "v119", "v120", "v121", "v122", "v123", "v124", "v125", "v126", "v127"
#define VCL128 "v128", "v129", "v130", "v131", "v132", "v133", "v134", "v135", "v136", "v137", "v138", "v139"
#define VCL140 "v140", "v141", "v142", "v143", "v144", "v145", "v146", "v147", "v148", "v149", "v150", "v151", "v152", "v153", "v154", "v155", "v156", "v157", "v158", "v159"
#define VCL160 "v160", "v161", "v162", "v163", "v164", "v165", "v166", "v167", "v168", "v169", "v170", "v171", "v172", "v173", "v174", "v175"
#define VCL112L "v112", "v113", "v114", "v115", "v116", "v117", "v118", "v119"

#define PROFARG
#define PROFPASS
#define TSA(i)
#define PFIO
#define PFCL
#define TSM
#define PFM(i)
#define PFT
#define TS(i)

#define TSF(i)
#define TSI
#define TSS(i)
#define TSW(i)
#define TSP
#define TSQ(a, b)
constexpr unsigned L_RT = 0, L_SM = 32768, L_CP = 49152;
struct RtTab { unsigned v[2048][16]; };
constexpr RtTab make_rt() {
    RtTab t{};
    for (unsigned b = 0; b < 256; ++b) for (unsigned w = 0; w < 8; ++w) for (unsigned f = 0; f < 4; ++f) {
        unsigned A = 0, M = 0, SO = 0, R0 = 0, R1 = 0;
        if (b < 135) {
            const unsigned fg = b / 5, ks = (b % 5) * 4 + (w >> 1), j = ks >> 1, o = fg * 8 + (w & 1) * 4 + f;
            A = (320 * (ks & 1) + (j < 8 ? (j & 1) * 32 + (j >> 1) * 8 : 8 * j) + (f & 1) * 160) * 3456 + fg * 128 + (w & 1) * 64;
            M = (j < 8 ? 96 : 16) * 3456;
            SO = WS_WGU + (o / 7) * 143360 + ks * 7168 + (o % 7) * 1024;
            R0 = 16 * f;
        } else {
            const unsigned v = (b - 135) * 8 + w;
            const bool isWo = v < 200;
            const unsigned vv = v < 740 ? (isWo ? v : v - 200) : 0, ks = vv / 10, cu = vv % 10;
            const unsigned c = 4 * ks + f, j = c / 31, ng = c % 31;
            A = (isWo ? (ks >= 16 ? 40 * c - 2544 : 20 * (c >> 1) + 4 * (c & 1)) : 56 * ng + 16 * (j >> 1) + 4 * (j & 1)) * 1280 + cu * 128;
            M = (isWo ? (ks >= 16 ? 20 : 8) : (j < 6 ? 8 : 4)) * 1280;
            const unsigned seg = 2 * cu + (f >> 1), a = seg / 5, sm = seg % 5, fi = f & 1;
            const unsigned tile = 2 * a + (sm == 2 ? fi : sm > 2), blk = sm == 2 ? 4 : (sm == 1 || sm == 4) ? 2 + fi : fi;
            SO = isWo ? WS_WO + (tile * 100 + (ks >= 10 ? (ks - 10) * 10 + 5 + (blk < 4 ? blk ^ 2 : 4) : ks * 10 + blk)) * 1024 : WS_WD + (tile * 270 + ks * 5 + blk) * 1024;
            R0 = (f >> 1) * 64 + (sm == 2 ? 32 : 8) * fi; R1 = sm == 2 ? 8 : 16;
        }
        unsigned* r = t.v[b * 8 + w];
        r[f] = A + 3072 - 1024 * f; r[4 + f] = M; r[8 + f] = SO; r[12 + f] = (L_RT + w * 4096 + R0) | R1 << 16;
    }
    return t;
}
static __device__ const __attribute__((aligned(64))) RtTab RT_TAB = make_rt();
struct Retile { rsrc_t ra, rb; u32x16 t; unsigned vlo, hi; };
DI void retile_setup(Retile& r, const Params& p, unsigned b, unsigned lane) {
    const bool gu = b < 135;
    const char *pa, *pb; unsigned n;
    asm("s_cmpk_lt_u32 %3, 160\n\ts_cselect_b64 %0, %4, %5\n\ts_cselect_b32 %2, %9, %8\n\ts_cmpk_lt_u32 %3, 135\n\ts_cselect_b64 %1, %7, %0\n\ts_cselect_b64 %0, %6, %0\n\ts_cselect_b32 %2, %8, %2"
        : "=&s"(pa), "=&s"(pb), "=&s"(n) : "s"(b), "s"(p.wo), "s"(p.wd), "s"(p.wg), "s"(p.wu), "s"(640 * 3456 + 3072), "n"(640 * 1280 + 3072) : "scc");
    r.ra = mkrs(pa - 3072, n); r.rb = mkrs(pb - 3072, n);
    r.vlo = gu ? ((lane >> 2) & 7) * 3456 + (lane & 3) * 16 : ((lane >> 3) & 3) * 1280 + (lane & 7) * 16;
    r.hi = lane >> 5;
    PIN(r.vlo); PIN(r.hi); PINS(r.ra); PINS(r.rb);
}
DI void retile_issue(const Retile& r, int w) {
    asm volatile("s_mov_b32 m0, %0\n\ts_nop 0\n\tbuffer_load_dwordx4 %1, %5, 0 offen lds\n\tbuffer_load_dwordx4 %2, %5, 0 offen offset:1024 lds\n\t"
                 "buffer_load_dwordx4 %3, %6, 0 offen offset:2048 lds\n\tbuffer_load_dwordx4 %4, %6, 0 offen offset:3072 lds"
                 :: "s"(L_RT + w * 4096), "v"(r.vlo + r.t[0] + r.hi * r.t[4]), "v"(r.vlo + r.t[1] + r.hi * r.t[5]), "v"(r.vlo + r.t[2] + r.hi * r.t[6]),
                    "v"(r.vlo + r.t[3] + r.hi * r.t[7]), "s"(r.ra), "s"(r.rb) : "memory");
}
DI u32x32 retile_read(const Retile& r, unsigned b, int w, unsigned lane) {
    const u32x4 one = {0x3f803f80u, 0x3f803f80u, 0x3f803f80u, 0x3f803f80u};
    const unsigned f = lane & 3, row = 8 * (lane >> 4) + ((lane >> 2) & 3), dl = b < 135 ? 256 : 512;
    const unsigned rb = b < 135 ? (f >> 1) * 2048 + row * 64 + (f & 1) * 8 : row * 128;
    u32x32 o; f32x4 cp[4];
#pragma unroll
    for (int i = 0; i < 4; ++i) {
        const bf8v fr = ldstr2(rb + (r.t[12 + i] & 0xffff) + (r.t[12 + i] >> 16) * f, dl);
        cp[i] = mfma16(fr, asbf(one), f32x4{});
#pragma unroll
        for (int k = 0; k < 4; ++k) o[4 * i + k] = __builtin_bit_cast(u32x4, fr)[k];
    }
    if ((lane & 15) == 0) {
#pragma unroll
        for (int i = 0; i < 4; ++i) ldsw<f32x4>(L_CP + w * 256 + i * 64 + lane, cp[i]);
    }
    return o;
}

struct QkvSrc { rsrc_t rx, rw; unsigned xo[2], wo[2], sx, sw; };
#define QDX(V, O) "buffer_load_dwordx4 %[" #V "], %[rx], %[sx] offen offset:" #O " lds\n\t"
#define QDW(V, O) "buffer_load_dwordx4 %[" #V "], %[rw], %[sw] offen offset:" #O " lds\n\t"
#define QADV "s_add_u32 %[sx], %[sx], 128\n\ts_cmp_eq_u32 %[sx], 1280\n\ts_cselect_b32 %[sx], 0, %[sx]\n\ts_mul_i32 %[sw], %[sx], 1920\n\t"
#define QD4 QDX(x0, 0) QDX(x1, 1024) QDW(w0, 2048) QDW(w1, 3072) QADV "s_add_u32 m0, m0, 32768\n\t"
DI void qkv_start(QkvSrc& c, u32x4& row, unsigned& rowt, unsigned& T, u32x16& tab, Params& p, u32x16 ka, int b, int mt, int nt, int rot, int w, int lane) {
    const unsigned rr = lane >> 3, isv = nt >= 10;
    const unsigned tok = mt * 128 + (w >> 1) * 32 + ((w & 1) << (2 * isv)) + (rr & 4) * 2 + ((rr & 3) << (1 - isv));
    c.xo[0] = tok * 1280 + (((lane & 7) ^ (rr >> 1)) << 4);
    c.xo[1] = (c.xo[0] ^ 64) + 16 * 1280 - 1024;
    c.wo[0] = (8 * w + ((lane >> 2) & 3)) * 3840 + nt * 256 + (lane >> 4) * 64 + (((lane & 3) ^ ((w & 1) << 1)) << 4) + 1024;
    c.wo[1] = c.wo[0] + 4 * 3840 - 1024;
    c.sx = rot * 128; c.sw = rot * 245760;
    unsigned ro = (mt * 128 + nt * 8 + w) * 1280;
    asm volatile("s_waitcnt lgkmcnt(0)\n\ts_load_dwordx16 %5, %6, %7" : "+s"(ka), "+v"(c.xo[1]), "+v"(c.wo[1]), "+s"(c.sw), "+s"(ro), "=&{s[60:75]}"(tab) : "s"(&RT_TAB), "s"((b * 8 + w) * 64) : "memory");
    p = __builtin_bit_cast(Params, ka);
    asm volatile("s_load_dword %0, %1, %2" : "=&s"(T) : "s"(p.ws), "s"(WS_E) : "memory");
    c.rx = mkrs(p.x, 2048 * 1280); c.rw = mkrs(p.wqkv - 3072, 640 * 3840 + 3072);
    asm volatile("s_mov_b32 m0, %[dm]\n\tbuffer_load_dwordx4 %[r4], %[l16], %[rx], %[ro] offen\n\tbuffer_load_dword %[r1], %[l4], %[rx], %[ro] offen offset:1024\n\t"
                 "s_cmp_eq_u32 %[nt], 15\n\ts_cbranch_scc1 1f\n\t" QD4 "s_barrier\n\t" QD4 "s_branch 2f\n1:\n\ts_waitcnt vmcnt(0)\n2:"
                 : [r4] "=&v"(row), [r1] "=&v"(rowt), [sx] "+&s"(c.sx), [sw] "+&s"(c.sw)
                 : [x0] "v"(c.xo[0]), [x1] "v"(c.xo[1]), [w0] "v"(c.wo[0]), [w1] "v"(c.wo[1]), [rx] "s"(c.rx), [rw] "s"(c.rw), [dm] "s"(w * 4096),
                   [l16] "v"(lane * 16), [l4] "v"(lane * 4), [ro] "s"(ro), [nt] "s"(nt) : "memory", "scc");
}
template <int C> DI float rorsum(float v) { return v + __uint_as_float(__builtin_amdgcn_update_dpp(0u, __float_as_uint(v), 0x120 + C, 0xf, 0xf, true)); }
DI u32x4 row_stats(u32x4 row, unsigned rowt, unsigned T) {
    float s = 0.f, q = 0.f;
#pragma unroll
    for (int i = 0; i < 5; ++i) {
        const bf2v v = __builtin_bit_cast(bf2v, i < 4 ? row[i & 3] : rowt);
        s = __builtin_amdgcn_fdot2_f32_bf16(v, __builtin_bit_cast(bf2v, 0x3f803f80u), s, false);
        q = __builtin_amdgcn_fdot2_f32_bf16(v, v, q, false);
    }
    const f32x2 y = swap32(s, q);
    const float z = rorsum<1>(rorsum<2>(rorsum<4>(rorsum<8>(sum16(y[0] + y[1]))))) * (1.f / 640.f);
    const f32x2 v = swap32(z, z);
    const float ra = __builtin_amdgcn_rsqf(__builtin_fmaxf(v[1] - v[0] * v[0], 0.f) + 1e-5f);
    return u32x4{__float_as_uint(ra), __float_as_uint(-v[0] * ra), T, ~T};
}
#define MFQ(A, W, X) "v_mfma_f32_16x16x32_bf16 v[" A ":" A "+3], v[" W ":" W "+3], v[" X ":" X "+3], v[" A ":" A "+3]\n\t"
#define MFV(A, W, X) MFQ(A, X, W)
#define QTR(R, V, O) "ds_read_b64_tr_b16 v[" R ":" R "+1], %[" #V "] offset:" O "\n\t"
#define QRX(R, V, O) "ds_read_b128 v[" R ":" R "+3], %[" #V "] offset:" O "\n\t"
#define LGK(N) "s_waitcnt lgkmcnt(" #N ")\n\t"
#define QSEC(MF, P, Q, XA, WO, D1, D2, E1, E2) \
    "s_cmp_lt_u32 %[n], 8\n\t" LGK(4) MF("64", P, "68") QTR(Q, wa0, WO) MF("32", P, P "+8") QTR(Q "+2", wa0, WO "+1024") \
    "s_cbranch_scc0 1f\n\t" D1 "1:\n\t" MF("36", P, P "+12") QRX(Q "+8", XA, "0") LGK(5) MF("48", P "+4", P "+8") QRX(Q "+12", XA, "4096") \
    MF("52", P "+4", P "+12") QTR(Q "+4", wa1, WO) LGK(5) MF("40", P, P "+16") QTR(Q "+6", wa1, WO "+1024") \
    "s_cbranch_scc0 1f\n\t" D2 "1:\n\t" MF("56", P "+4", P "+16") QRX(Q "+16", XA, "8192") E1 MF("44", P, P "+20") QRX(Q "+20", XA, "12288") E2 MF("60", P "+4", P "+20")
#define QLDG "buffer_load_dwordx4 v[120:123], %[ga], %[rs], 0 offen sc1\n\t"
#define QADD(r) "v_add_u32 %[" #r "], %[d], %[" #r "]\n\t"
#define QRT(r, A, M) "v_mad_u32_u24 %[" #r "], %[hi], s" #M ", %[vlo]\n\tv_add_u32 %[" #r "], s" #A ", %[" #r "]\n\t"
#define QKV_ASM(MF, STRD) asm volatile( \
    "s_add_u32 m0, %[dm], 98304\n\ts_mov_b32 %[n], 0\n\tbuffer_store_dwordx4 %[gr], %[go], %[rs], 0 offen sc1\n\t" \
    Z8("32") Z8("40") Z8("48") Z8("56") Z4("64") "v_mov_b32 v68, 0x3f803f80\n\tv_mov_b32 v69, v68\n\tv_mov_b64 v[70:71], v[68:69]\n\t" \
    "s_waitcnt vmcnt(9)\n\ts_barrier\n\t" TSA("1") TSS("25") \
    QTR("72", wa0, "0") QTR("74", wa0, "1024") QRX("80", xa0, "0") QRX("84", xa0, "4096") QTR("76", wa1, "0") QTR("78", wa1, "1024") QRX("88", xa0, "8192") QRX("92", xa0, "12288") \
    QDX(x0, 0) QDX(x1, 1024) "s_branch 6f\n" \
    "3:\n\ts_cmp_gt_u32 %[n], 2\n\ts_cbranch_scc1 2f\n\ts_waitcnt vmcnt(9)\n\ts_branch 5f\n" \
    "2:\n\ts_cmp_eq_u32 %[n], 10\n\ts_cbranch_scc1 7f\n\ts_cmp_eq_u32 %[n], 7\n\ts_cbranch_scc1 2f\n\ts_waitcnt vmcnt(5)\n\ts_branch 5f\n" \
    "2:\n\t" QLDG QRT(x0, 60, 64) QRT(x1, 61, 65) QRT(w0, 62, 66) QRT(w1, 63, 67) \
    "s_mov_b64 s[44:45], s[52:53]\n\ts_mov_b32 s46, s54\n\ts_mov_b64 s[48:49], s[56:57]\n\ts_mov_b32 s50, s58\n\ts_mov_b32 %[sx], 0\n\ts_mov_b32 %[sw], 0\n" \
    "4:\n\ts_waitcnt vmcnt(8)\n5:\n\ts_barrier\n\t" \
    QSEC(MF, "96", "72", xa0, "0", QDX(x0, 0), QDX(x1, 1024), "", "") \
    "6:\n\t" \
    QSEC(MF, "72", "96", xa1, "16384", QDW(w0, 2048), QDW(w1, 3072), \
         QADV "s_bitcmp1_b32 0x42, %[n]\n\ts_cselect_b32 %[d], %[wr], %[up]\n\ts_add_u32 m0, m0, %[d]\n\ts_cmp_eq_u32 %[n], 4\n\ts_cselect_b32 %[d], %[wr], %[up]\n\ts_add_u32 %[n], %[n], 1\n\t", \
         QADD(xa0) QADD(xa1) QADD(wa0) QADD(wa1)) TSI \
    "s_bitcmp1_b32 0x686, %[n]\n\ts_cbranch_scc0 4b\n\ts_branch 3b\n" \
    "7:\n\ts_movk_i32 %[n], " SPINS "\n\t" LGK(4) MF("64", "96", "68") MF("32", "96", "104") "s_waitcnt vmcnt(4)\n" \
    "1:\n\tv_cmp_ne_u64 vcc, v[122:123], %[tag]\n\ts_cbranch_vccz 2f\n\t" QLDG "s_waitcnt vmcnt(0)\n\ts_sub_u32 %[n], %[n], 1\n\ts_cbranch_scc0 1b\n" \
    "2:\n\t" MF("36", "96", "108") "v_mul_f32 v120, %[sc], v120\n\tv_mul_f32 v121, %[sc], v121\n\t" LGK(2) MF("48", "100", "104") \
    "ds_write2_b32 %[pa], v120, v121 offset1:64\n\t" MF("52", "100", "108") "s_nop 1\n\tds_write_b128 %[ca], v[64:67]\n\t" LGK(2) \
    MF("40", "96", "112") STRD MF("56", "100", "112") MF("44", "96", "116") MF("60", "100", "116") \
    "s_waitcnt lgkmcnt(0)\n\ts_barrier\n\ts_nop 7" \
    : "=&{v[32:63]}"(A), "=&{v[64:67]}"(cs0), "=&{v[124:155]}"(S), [rx] "+{s[44:47]}"(c.rx), [rw] "+{s[48:51]}"(c.rw), [sx] "+&s"(c.sx), [sw] "+&s"(c.sw), \
      [x0] "+&v"(c.xo[0]), [x1] "+&v"(c.xo[1]), [w0] "+&v"(c.wo[0]), [w1] "+&v"(c.wo[1]), [xa0] "+&v"(e.xa0), [xa1] "+&v"(e.xa1), [wa0] "+&v"(e.wa0), [wa1] "+&v"(e.wa1), \
      [n] "=&s"(n), [d] "=&s"(d) PFIO \
    : "{s[52:55]}"(rt.ra), "{s[56:59]}"(rt.rb), [rs] "s"(rws), [dm] "s"(w * 4096), [wr] "s"(-131072), [up] "s"(32768), [sc] "s"(e.sc), [gr] "v"(gr), [go] "v"(e.go), [ga] "v"(e.ga), \
      "{s[60:75]}"(rt.t), [vlo] "v"(rt.vlo), [hi] "v"(rt.hi), [tag] "v"(tag), [pa] "v"(e.pa), [pr] "v"(e.pr), [ca] "v"(e.ca) \
    : "memory", "scc", "vcc", "v68", "v69", "v70", "v71", "v72", "v73", "v74", "v75", "v76", "v77", "v78", "v79", VCL80, VCL96, VCL112L, "v120", "v121", "v122", "v123" PFCL)
#define QS2(R, O) "ds_read2_b32 v[" #R ":" #R "+1], %[pr] offset0:" #O " offset1:" #O "\n\tds_read2_b32 v[" #R "+2:" #R "+3], %[pr] offset0:" #O "+64 offset1:" #O "+64\n\t"
struct QkvAdr { unsigned xa0, xa1, wa0, wa1, go, ga, pa, pr, ca, so[2], sb, sc; };
DI void qkv_setup(QkvAdr& e, int mt, int nt, int w, int lane) {
    const unsigned t = lane & 15, g = lane >> 4, T = w & 1, cb = w >> 1, g64 = 2 * mt + T;
    const bool isv = nt >= 10, isq = nt < 5;
    e.xa0 = 4 * T * 4096 + t * 128 + ((g ^ (t >> 1)) << 4); e.xa1 = e.xa0 ^ 64;
    e.wa0 = g * 4096 + 2048 + cb * 256 + ((lane >> 2) & 3) * 64 + ((g ^ T) & 1) * 32 + (lane & 3) * 8; e.wa1 = e.wa0 ^ 32;
    e.go = lane == 0 ? WS_XS + (mt * 128 + nt * 8 + w) * 16 : OOB;
    e.ga = WS_XS + (g64 * 64 + (g >> 1) * 32 + (isv ? 8 * (t >> 2) + 4 * (g & 1) + (t & 3) : 2 * t + (g & 1))) * 16;
    e.pa = L_SM + w * 2048 + lane * 4; e.pr = L_SM + w * 2048 + (isv ? g * 16 : t * 4); e.ca = L_SM + w * 2048 + 1024 + lane * 16;
    e.sc = isq ? __builtin_bit_cast(unsigned, 0.22360679774997896f * 1.4426950408889634f) : 0x3f800000u;
    e.sb = isq ? WS_Q + g64 * 2560 : WS_KV + g64 * 5120 + (isv ? 2560 : 0);
#pragma unroll
    for (int n = 0; n < 2; ++n) {
        if (isv) {
            const unsigned col = (nt - 10) * 128 + cb * 32 + 16 * (n ^ T) + t, head = (col * 3277) >> 16;
            e.so[n] = head * (163840 - 320) + g * 320 + col * 16;
        } else {
            const unsigned G = (isq ? nt : nt - 5) * 32 + cb * 8 + 4 * (n ^ T) + g, head = (G * 205) >> 10;
            e.so[n] = head * (isq ? 81920 - 2560 : 163840 - 2560) + G * 512 + t * 16;
        }
        PIN(e.so[n]);
    }
    PIN(e.xa0); PIN(e.xa1); PIN(e.wa0); PIN(e.wa1); PIN(e.go); PIN(e.ga); PIN(e.pa); PIN(e.pr); PIN(e.ca); PINS(e.sb); PINS(e.sc);
}
template <bool ISV>
DI void qkv_body(QkvSrc& c, QkvAdr& e, const Retile& rt, rsrc_t rws, u32x4 gr, u32x2 tag, int w PROFARG) {
    f32x32 A, S; f32x4 cs0; unsigned n, d;
    if (ISV) QKV_ASM(MFV, QRX("124", pr, "0") QRX("128", pr, "256") QRX("132", pr, "64") QRX("136", pr, "320") QRX("140", pr, "128") QRX("144", pr, "384") QRX("148", pr, "192") QRX("152", pr, "448"));
    else QKV_ASM(MFQ, QS2(124, 0) QS2(128, 16) QS2(132, 32) QS2(136, 48));
    TS(2);
    const f32x4 cs1 = ldsr<f32x4>(e.ca ^ 2048);
#pragma unroll
    for (int n = 0; n < 2; ++n) {
        const f32x4 cs = n ? cs1 : cs0;
#pragma unroll
        for (int q = 0; q < 2; ++q) {
            u32x4 o;
#pragma unroll
            for (int i = 0; i < 4; ++i) {
                const int k = 2 * q + (i >> 1), j = 2 * (i & 1), a = 16 * n + 4 * k + j;
                o[i] = ISV ? pk2(A[a] * S[8 * k + j] + S[8 * k + 4 + j] * cs[0], A[a + 1] * S[8 * k + j + 1] + S[8 * k + 5 + j] * cs[0])
                           : pk2(A[a] * S[4 * k] + S[4 * k + 2] * cs[j], A[a + 1] * S[4 * k + 1] + S[4 * k + 3] * cs[j + 1]);
            }
            asm volatile("buffer_store_dwordx4 %0, %1, %2, %3 offen offset:%4 sc1\n\ts_nop 1" :: "v"(o), "v"(e.so[n]), "s"(rws), "s"(e.sb), "n"(q * (ISV ? 1280 : 256)) : "memory");
        }
    }
    TS(58);
    asm volatile("s_waitcnt vmcnt(4)" ::: "memory");
    TS(59);
}

DI f32x16 qk_tile(u32x4 km, u32x2 kt, u32x4 qm, u32x2 qt, f32x16 c) {
    c = mfma32(asbf(km), asbf(qm), c);
    return __builtin_amdgcn_mfma_f32_32x32x8bf16_1k(__builtin_bit_cast(s4v, kt), __builtin_bit_cast(s4v, qt), c, 0, 0, 0);
}
DI float max16(const f32x16& s, float m) {
#pragma unroll
    for (int r = 0; r < 16; r += 2) m = __builtin_fmaxf(__builtin_fmaxf(s[r], s[r + 1]), m);
    return m;
}
DI f32x16 pv16(u32x4 v, f32x8 pr, f32x16 o) {
    return __builtin_amdgcn_smfmac_f32_32x32x32_bf16(asbf(v), __builtin_bit_cast(bf16v, pr), o, 0xDDDD, 0, 0);
}
#define EX4(P, S) "v_exp_f32 v[" #P "], v[" S "]\n\tv_exp_f32 v[" #P "+1], v[" S "+1]\n\tv_exp_f32 v[" #P "+2], v[" S "+2]\n\tv_exp_f32 v[" #P "+3], v[" S "+3]\n\t"
#define SMF(O, V, P) "v_smfmac_f32_32x32x32_bf16 v[" #O ":" #O "+15], v[" #V ":" #V "+3], v[" #P ":" #P "+7], %[idx]\n\t"
#define QKT(SN, AB) "v_mfma_f32_32x32x4_2b_bf16 v[" #SN ":" #SN "+31], v[156:157], v[152:153], 0 cbsz:1 abid:" #AB "\n\t"
#define QKA(SN) "v_mfma_f32_32x32x16_bf16 v[" #SN ":" #SN "+15], v[160:163], v[144:147], v[" #SN ":" #SN "+15]\n\t"
#define QKB(SN) "v_mfma_f32_32x32x16_bf16 v[" #SN "+16:" #SN "+31], v[160:163], v[148:151], v[" #SN "+16:" #SN "+31]\n\t"
#define STEP_(SC, VP, VC, L0, L1, L2, L3, L4) \
    L0 EX4(128, #SC) EX4(132, #SC "+4") SMF(112, VP, 136) EX4(136, #SC "+16") "s_waitcnt lgkmcnt(0)\n\t" SMF(96, 164, 128) EX4(140, #SC "+20") \
    L1 EX4(128, #SC "+8") SMF(112, 164, 136) L2 EX4(132, #SC "+12") L3 EX4(136, #SC "+24") SMF(96, VC, 128) L4 EX4(140, #SC "+28")
#define STEP(SC, SN, AB, VP, VC, KTRD, VRD, KRD, M1, M2) STEP_(SC, VP, VC, QKT(SN, AB) KTRD, QKA(SN) M1, VRD, QKB(SN) KRD, M2)
#define VR(VH, O) "s_mov_b64 exec, s[56:57]\n\tds_read_b128 v[164:167], %[av] offset:" O "\n\tds_read_b128 v[" #VH ":" #VH "+3], %[av] offset:" O "+640\n\ts_mov_b64 exec, -1\n\t"
#define KR(O) "ds_read_b64 v[160:161], %[a0] offset:" O "\n\tds_read_b64 v[162:163], %[a0] offset:" O "+512\n\t"
#define KT(O) "ds_read_b64 v[156:157], %[at] offset:" O "\n\t"
#define PRIO "s_cbranch_vccz 1f\n\ts_setprio 1\n1:\n\t"
#define PVC "s_mov_b32 vcc_lo, %[grp]\n\ts_mov_b32 vcc_hi, 0\n\t"
#define QUAD3(X, N, A0, B0, A1, B1, A2, B2) PRIO \
    STEP(32, 64, 1, 172, 168, KT(#X "+5120"), VR(172, #X "+1280"), KR(#X "+5120"), A0, B0) \
    STEP(64, 32, 0, 168, 172, "", VR(168, #X "+5120"), KR(#X "+5376"), A1, B1) \
    STEP(32, 64, 1, 172, 168, KT(#N), VR(172, #X "+6400"), KR(#N), A2, B2) "s_setprio 0\n\t"
#define QUAD(X, N, A0, B0, A1, B1, A2, B2, A3, B3) QUAD3(X, N, A0, B0, A1, B1, A2, B2) STEP(64, 32, 0, 168, 172, "", VR(168, #N), KR(#N "+256"), A3, B3) TSI
#define KVP_(D) "buffer_load_dwordx4 %[l16], s[40:43], s44 offen sc1 lds\n\ts_add_u32 m0, m0, " #D "\n\ts_add_u32 s44, s44, 4096\n\t"
#define KVP KVP_(4096)
#define LDQ(R, V, O) "buffer_load_dwordx2 v[" #R ":" #R "+1], %[" #V "], s[40:43], %[sq] offen offset:" #O " sc1\n\t"
#define STW(R, S) "buffer_store_dwordx4 v[" #R ":" #R "+3], %[vs], s[40:43], s" #S " offen sc1\n\t"
#define STM(V, S, O) "buffer_store_dwordx4 v[192:195], %[" #V "], s[40:43], " S " offen offset:" #O " sc1\n\t"
#define WOP(O) DM1("%[l16]", "s48", O)
#define WO5 "s_add_u32 m0, m0, 1024\n\ts_add_u32 s48, s48, 1024\n\t"
#define PK(R, O) LD1(#R, "%[l16]", "s52", #O)
#define PKA "s_add_u32 s52, s52, 2048\n\ts_cmp_eq_u32 s52, %[pe]\n\ts_cselect_b32 s52, %[pb], s52\n\t"
#define TCH(O) "buffer_load_dword v154, %[vt], s[40:43], 0 offen offset:" #O " sc1\n\t"
#define LDQ5 LDQ(144, va, 0) LDQ(146, va, 512) LDQ(148, vb, 0) LDQ(150, vb, 512) LDQ(152, vc, 2048)
#define RD1(W, V, O) "ds_read_b128 v[" W ":" W "+3], " V " offset:" O "\n\t"
#define VM_(N) "s_waitcnt vmcnt(" #N ")\n\t"
#define VM(N) VM_(N)
DI unsigned mod5(unsigned v) { return (0x21043210u >> (4 * v)) & 7; }
DI unsigned park0(int w) { return ((w * 35 + 2) >> 2) * 2048; }
struct Park { u32x32 a, b; u32x8 c; unsigned d; };

DI void phase_attn(const Params& p, const Retile& rt, rsrc_t rws, u32x2 tag, unsigned fq, int b, int w, int lane, int nt80, int ntg,
                   unsigned rv0, unsigned rv1, unsigned rsb, u32x4& r0, u32x2& r1, Park& pk PROFARG) {
    const int t = lane & 31, hh = lane >> 5, wl = w & 3, grp = w >> 2;
    const int head = (b & 7) * 4 + (b >> 6), qb = (b >> 3) & 7;
    const unsigned gb = grp * 61440, kvb = WS_KV + head * 163840;
    pk.a = retile_read(rt, b, w, lane);
    const unsigned l16 = lane * 16;
    unsigned va = hh * 1024 + grp * 256 + t * 8, dm = gb + wl * 1024, sq = WS_Q + head * 81920 + (qb * 4 + wl) * 2560;
    const int n0 = (head * 20 + ((lane & 16) ? 19 : 0)) >> 7;
    unsigned fo = WS_FQ + (w == 0 ? (5 + 5 * hh + n0) * 16 + (lane & 15) : n0 * 16 + 2 * qb + (lane & 1)) * 128;
    unsigned skv = kvb + grp * 81920 + wl * 1024;
    u32x8 qm; u32x2 qt;
    asm volatile("s_cmp_eq_u32 %[w], 2\n\ts_waitcnt vmcnt(0) lgkmcnt(0)\n\ts_barrier\n\ts_cbranch_scc0 1f\n\tbuffer_store_dwordx2 %[tag], %[fq], %[rs], 0 offen sc1\n1:"
                 : "+{v[176:207]}"(pk.a), "+v"(fo), "+v"(va), "+s"(skv), "+s"(sq), "+s"(dm) : [w] "s"(w), [tag] "v"(tag), [fq] "v"(fq), [rs] "s"(rws) : "memory", "scc");
    TS(3);
    SBAR();
    const unsigned a0 = gb + hh * 1024 + t * 8, at = gb + lane * 8 + 2048, av = gb + 2560 + hh * 320 + (t < 20 ? t : 0) * 16;
    const unsigned ones = t == 20 ? 0x3f803f80u : 0u;
    const unsigned wob = WS_WO + nt80 * 102400;
    u32x8 sa;
    sa[0] = skv;
    sa[2] = WS_O + b * 10240 + w * 1280;
    sa[1] = WS_B + qb * QBS + (head * 8 + w) * 1408;
    sa[3] = WS_B + qb * QBS + OFF_F + (head * 8 + w) * 3456;
    const unsigned mv = l16 + (lane < 16 ? sa[2] + 1024 : lane < 40 ? sa[1] + 768 : sa[3] + 2432);
    const unsigned ro = nt80 & 3, pb = WS_WGU + ntg * 143360, p0 = park0(w) + (((ntg & 15) * 5) >> 3) * 14336;
    sa[4] = wob + (grp + ro) * 20480 + wl * 5120; sa[5] = 122880 + w * 5120;
    sa[6] = wob + mod5(2 + 2 * grp + ro) * 20480 + wl * 5120; sa[7] = gb + 20480 + wl * 5120;
    u32x4 sb = {pb + p0 - ((int)p0 < 143360 ? 0 : 143360), rsb, wob + mod5(3 + ro) * 20480 + w * 2560, 40960u + w * 2560};
    const unsigned vt = lane < 17 ? WS_WD + nt80 * 276480 + qb * 34560 + (w * 17 + lane) * 256 : OOB;
    const unsigned vs = b < 135 ? l16 ^ (((lane >> 1) ^ (lane >> 3)) & 1) * 0xa0 : b * 8 + w < 1820 ? l16 : OOB;
    const unsigned vfs = (w == 2 && lane == 0) ? WS_FR + b * 8 : OOB, vfe = (b == 0 && w == 0 && lane == 0) ? WS_E : OOB, vfp = WS_FR + w * 256 + t * 8;
    const unsigned fg = (b * 205) >> 10;
    const unsigned vcs = (b < 135 && w < 2) ? WS_CS + ((b - fg * 5) * 3584 + fg * 128 + w * 64 + lane) * 4 : OOB;
    unsigned e0 = gb + wl * 3072 + l16, e1 = 61440 - gb + wl * 3072 + l16, ov = (t >> 4) * 20480 + (t & 15) * 16, ow = hh ? OOB : ov;
    unsigned ob = WS_O + (qb * 16 + wl * 4 + grp * 2) * 20480, oc = ob + (64 + (head >> 1)) * 256 + (head & 1) * 8;
    ov += hh * 256; ob += head * 512;
    f32x32 O;
    asm volatile(
        "ds_read2st64_b32 v[196:197], %[cp] offset1:2\n\tds_read2st64_b32 v[198:199], %[cp] offset0:4 offset1:6\n\t"
        Z8("96") Z8("104") Z8("112") Z8("120") Z8("136") "v_mov_b32 v164, %[one]\n\tv_mov_b32 v165, %[one]\n\tv_mov_b64 v[166:167], v[164:165]\n\tv_mov_b64 v[168:169], v[164:165]\n\t"
        "v_mov_b64 v[170:171], v[164:165]\n\tv_mov_b64 v[172:173], v[164:165]\n\tv_mov_b64 v[174:175], v[164:165]\n\tv_mov_b64 v[192:193], -1\n\tv_mov_b64 v[194:195], -1\n\t"
        "s_mov_b32 s56, 0xfffff\n\ts_mov_b32 s57, s56\n\ts_movk_i32 s58, " SPINS "\n\ts_mov_b32 m0, %[dm]\n\ts_cmp_gt_u32 %[w], 1\n\t" "s_cbranch_scc1 2f\n"
        "1:\n\tbuffer_load_dwordx2 v[32:33], %[fo], s[40:43], 0 offen sc1\n\ts_waitcnt vmcnt(0)\n\tv_cmp_ne_u64 vcc, v[32:33], %[tag]\n\t"
        "s_cbranch_vccz 2f\n\ts_sub_u32 s58, s58, 1\n\ts_cbranch_scc0 1b\n"
        "2:\n\ts_barrier\n\t" TSA("4") KVP LDQ5 KVP "s_barrier\n\t" KVP KVP
        LGK(0) "v_add_f32 v196, v196, v197\n\tv_add_f32 v198, v198, v199\n\tv_add_f32 v196, v196, v198\n\ts_movk_i32 s58, " SPINS "\n\t"
        "s_waitcnt vmcnt(2)\n\ts_barrier\n\t" TSA("5")
        KT("0") KR("0") VR(168, "0") "s_waitcnt lgkmcnt(4)\n\t" QKT(32, 0) "s_waitcnt lgkmcnt(2)\n\ts_nop 7\n\ts_nop 7\n\ts_nop 2\n\t" QKA(32) QKB(32) KR("256")
        PVC "s_waitcnt vmcnt(0)\n\ts_barrier\n\ts_nop 6\n\t" TSS("17") TSF("17")
        QUAD(0, 10240, KVP, KVP STW(176, 68), KVP, STW(180, 69) "buffer_store_dword v196, %[vcs], s[40:43], 0 offen sc1\n\t", KVP, STW(184, 70), KVP, STW(188, 71))
        "s_waitcnt vmcnt(8)\n\ts_barrier\n\t"
        QUAD(10240, 20480, KVP, STM(l16, "s46", 0), KVP, STM(mv, "0", 0), KVP, "", KVP, "")
        "s_waitcnt vmcnt(2)\n\ts_barrier\n\tbuffer_store_dwordx2 %[tag], %[vfs], s[40:43], 0 offen sc1\n\t"
        QUAD(20480, 30720, KVP, STM(l16, "s45", 0), KVP_(-57344), KVP, KVP, STM(l16, "s47", 0), KVP, KVP)
        QUAD(30720, 40960, KVP, "", "buffer_load_dwordx2 v[198:199], %[vfp], s[40:43], 0 offen sc1\n\t", STM(l16, "s47", 1024), "", STM(l16, "s47", 2048), "", "")
        "s_mov_b32 m0, s49\n\ts_waitcnt vmcnt(2)\n"
        "3:\n\tv_cmp_ne_u64 vcc, v[198:199], %[tag]\n\ts_cbranch_vccz 4f\n\t"
        "buffer_load_dwordx2 v[198:199], %[vfp], s[40:43], 0 offen sc1\n\ts_sub_u32 s58, s58, 1\n\ts_waitcnt vmcnt(0)\n\ts_cbranch_scc0 3b\n"
        "4:\n\t" PVC "s_barrier\n\t" TSA("6") "buffer_store_dwordx2 %[tag], %[vfe], s[40:43], 0 offen sc1\n\t"
        QUAD(40960, 51200, PK(176, 0), PK(180, 1024) PKA, PK(184, 0), PK(188, 1024) PKA, PK(192, 0), PK(196, 1024) PKA, PK(200, 0), PK(204, 1024) PKA)
        QUAD(51200, 0, PK(208, 0), PK(212, 1024) PKA, PK(216, 0), PK(220, 1024) PKA, PK(224, 0), PK(228, 1024) PKA, PK(232, 0), PK(236, 1024) PKA)
        QUAD(0, 10240, PK(240, 0), PK(244, 1024), WOP(0) WO5, WOP(0) TCH(0), WOP(1024), WOP(2048), WOP(3072) "s_mov_b32 m0, s51\n\ts_mov_b32 s48, s50\n\t", WOP(0) WO5)
        QUAD3(10240, 0, WOP(0) TCH(128), WOP(1024), WOP(2048), WOP(3072),
              "buffer_load_dwordx4 v[248:251], %[rv0], %[rx], s53 offen\n\t", "buffer_load_dwordx2 v[252:253], %[rv1], %[rx], s53 offen\n\t")
        STEP_(64, 168, 172, "", "", "", "", "") TSI
        "s_mov_b32 m0, s55\n\ts_waitcnt vmcnt(24)\n\ts_nop 0\n\t" SMF(112, 172, 136) "s_barrier\n\t"
        DM1("%[l16]", "s54", 0) DM1("%[l16]", "s54", 1024) "s_mov_b32 exec_hi, 0\n\t" DM1("%[l16]", "s54", 2048) "s_mov_b32 exec_hi, -1\n\ts_nop 7\n\ts_nop 7"
        : "=&{v[96:127]}"(O), "=&{v[144:151]}"(qm), "=&{v[152:153]}"(qt), "+{v[176:207]}"(pk.a), "=&{v[208:239]}"(pk.b), "=&{v[240:247]}"(pk.c), "=&{v[248:251]}"(r0), "=&{v[252:253]}"(r1), "=&{v154}"(pk.d),
          "+{s[44:51]}"(sa), "+{s[52:55]}"(sb), "+v"(e0), "+v"(e1), "+v"(ov), "+v"(ow), "+s"(ob), "+s"(oc) PFIO
        : "{s[40:43]}"(rws), "{s[60:75]}"(rt.t), [a0] "v"(a0), [at] "v"(at), [av] "v"(av), [l16] "v"(l16), [tag] "v"(tag), [vcs] "v"(vcs), [fo] "v"(fo), [sq] "s"(sq),
          [cp] "v"(L_CP + (w & 1) * 256 + lane * 4), [mv] "v"(mv), [vs] "v"(vs), [vt] "v"(vt), [pb] "s"(pb), [pe] "s"(pb + 143360), [one] "v"(ones), [idx] "v"(0xDDDD),
          [va] "v"(va), [vb] "v"(va ^ 256), [vc] "v"((lane * 8) ^ (grp * 256)), [vfs] "v"(vfs), [vfe] "v"(vfe), [vfp] "v"(vfp), [rv0] "v"(rv0), [rv1] "v"(rv1),
          [dm] "s"(dm), [w] "s"(w), [grp] "s"(grp), [rx] "s"(mkrs(p.x, 2048 * 1280))
        : "memory", "scc", "vcc", "s56", "s57", "s58", VCL32, VCL48, VCL64, VCL80, VCL128, "v140", "v141", "v142", "v143", "v156", "v157", VCL160 PFCL);
    TS(7);
    float o[12];
#pragma unroll
    for (int i = 0; i < 3; ++i) ldsw<f32x4>(e0 + i * 1024, f32x4{O[16 + 4 * i], O[17 + 4 * i], O[18 + 4 * i], O[19 + 4 * i]});
    LBAR();
#pragma unroll
    for (int i = 0; i < 3; ++i) {
        const f32x4 v = ldsr<f32x4>(e1 + i * 1024);
#pragma unroll
        for (int k = 0; k < 4; ++k) o[4 * i + k] = O[4 * i + k] + v[k];
    }
    {
        f32x2 z = {0.f, 0.f};
#pragma unroll
        for (int r = 0; r < 12; r += 2) z += f32x2{o[r], o[r + 1]} * 0.f;
        const float l = (hh ? o[8] : 1.f) + (z[0] + z[1]);
        const bool bad = !(l < 1.8446744e19f && l > 8.6736174e-19f);
        if (__builtin_expect(anyl(bad), 0)) {
            float m = -__builtin_inff();
            const unsigned ko = hh * 1024 + t * 8, vo = 2560 + hh * 320 + (t < 20 ? t : 0) * 16;
            const u32x4 qa = {qm[0], qm[1], qm[2], qm[3]}; const u32x2 qe = {hh ? 0u : qt[0], hh ? 0u : qt[1]};
            f32x16 c = {}, A = {};
#pragma unroll 1
            for (int it = 0; it < 128; ++it) {
                const unsigned sb = kvb + ((it & 63) >> 1) * 5120;
                const u32x2 k0 = ld8c(rws, ko, sb + (it & 1) * 256), k1 = ld8c(rws, ko + 512, sb + (it & 1) * 256), k2 = ld8c(rws, t * 8 + 2048, sb + (it & 1) * 256);
                f32x16 s = qk_tile(u32x4{k0[0], k0[1], k1[0], k1[1]}, k2, qa, qe, c);
                if (it < 64) {
                    m = max16(s, m);
                    if (it == 63) {
                        const f32x2 mm = swap32(m, m);
                        m = __builtin_fmaxf(mm[0], mm[1]);
#pragma unroll
                        for (int r = 0; r < 16; ++r) c[r] = -m;
                    }
                } else {
                    u32x4 v0 = ld16c(rws, vo, sb + (it & 1) * 1280), v1 = ld16c(rws, vo + 640, sb + (it & 1) * 1280);
#pragma unroll
                    for (int i = 0; i < 4; ++i) { v0[i] = t < 20 ? v0[i] : ones; v1[i] = t < 20 ? v1[i] : ones; }
#pragma unroll
                    for (int r = 0; r < 16; ++r) s[r] = __builtin_amdgcn_exp2f(s[r]);
                    A = pv16(v0, __builtin_shufflevector(s, s, 0, 1, 2, 3, 4, 5, 6, 7), A);
                    A = pv16(v1, __builtin_shufflevector(s, s, 8, 9, 10, 11, 12, 13, 14, 15), A);
                }
            }
#pragma unroll
            for (int r = 0; r < 12; ++r) o[r] = A[r];
        }
    }
    const float inv = __builtin_amdgcn_rcpf(swap32(o[8], o[8])[1]);
    st16(rws, ov, ob, u32x4{pk2(o[0] * inv, o[1] * inv), pk2(o[2] * inv, o[3] * inv), pk2(o[4] * inv, o[5] * inv), pk2(o[6] * inv, o[7] * inv)});
    st8(rws, ow, oc, u32x2{pk2(o[8] * inv, o[9] * inv), pk2(o[10] * inv, o[11] * inv)});
}

constexpr unsigned L_CS = 14336;
#define MF16(A, W, B) "v_mfma_f32_16x16x32_bf16 v[" A ":" A "+3], v[" W ":" W "+3], v[" B ":" B "+3], v[" A ":" A "+3]\n\t"
#define MF0(A, W, B) "v_mfma_f32_16x16x32_bf16 v[" A ":" A "+3], v[" W ":" W "+3], v[" B ":" B "+3], 0\n\t"
#define RD5(W, V, O) RD1(W, V, O) RD1(W "+4", V, O "+1024") RD1(W "+8", V, O "+2048") RD1(W "+12", V, O "+3072") RD1(W "+16", V, O "+4096")
#define WR1(V, R, O) "ds_write_b128 " V ", v[" R ":" R "+3] offset:" O "\n\t"
#define MX3(B) "v_max3_u32 %[t], v[" B "], v[" B "+1], v[" B "+2]\n\t"
#define MX2(B) "v_max3_u32 %[t], v[" B "], v[" B "+1], %[t]\n\t"
#define MXE(B) "v_max_u32 %[t], v[" B "], %[t]\n\tv_cmp_eq_u32 vcc, -1, %[t]\n\ts_and_b64 vcc, vcc, s[60:61]\n\t"
#define CK4(B) MX3(B) MXE(B "+3")
#define CK8(B) MX3(B) MX2(B "+3") MX2(B "+5") MXE(B "+7")
#define CK16(B) MX3(B) MX2(B "+3") MX2(B "+5") MX2(B "+7") MX2(B "+9") MX2(B "+11") MX2(B "+13") MXE(B "+15")
#define CK28(B) MX3(B) MX2(B "+3") MX2(B "+5") MX2(B "+7") MX2(B "+9") MX2(B "+11") MX2(B "+13") MX2(B "+15") MX2(B "+17") MX2(B "+19") MX2(B "+21") MX2(B "+23") MX2(B "+25") MXE(B "+27")
#define SPIN0 "s_movk_i32 s59, " SPINS "\n\ts_mov_b64 s[60:61], -1\n\t"
#define SPIN1 "s_sub_u32 s59, s59, 1\n\ts_cselect_b64 s[60:61], 0, s[60:61]\n\t"
#define UNP(D, S) "v_lshlrev_b32 v[" #D "], 16, v" #S "\n\tv_and_b32 v[" #D "+1], 0xffff0000, v" #S "\n\t"
#define PKADD(D, S) "v_pk_add_f32 v[" D ":" D "+1], v[" D ":" D "+1], v[" S ":" S "+1]\n\t"
#define CVT(D, S) "v_cvt_pk_bf16_f32 v[" D "], v[" S "], v[" S "+1]\n\t"
#define WON(n, W, B, R) MF16("72+4*" #n, W "+4*" #n, B) R
#define WOK(W, B, V, O) WON(0, W, B, RD1(W, V, O)) WON(1, W, B, RD1(W "+4", V, O "+1024")) WON(2, W, B, RD1(W "+8", V, O "+2048")) WON(3, W, B, RD1(W "+12", V, O "+3072")) WON(4, W, B, RD1(W "+16", V, O "+4096"))
#define WOE(W, B) WON(0, W, B, "") WON(1, W, B, "") WON(2, W, B, "") WON(3, W, B, "") WON(4, W, B, "")
#define WOL(B, S) LD1(#B, "%[l16]", #S, "0") LD1(#B "+4", "%[l16]", #S, "1024")
#define WOC(p, N, B) "1" #p ":\n\t" VM(N) CK8(#B) "s_cbranch_scc1 2" #p "f\n\t" TSI
#define WOT(p) "2" #p ":\n\ts_call_b64 s[56:57], 8f\n\ts_branch 1" #p "b\n"
#define WOR(B, S) CK8(#B) "s_cbranch_scc0 1f\n\ts_mov_b64 exec, vcc\n\t" WOL(B, S) "s_mov_b64 exec, -1\n1:\n\t"
#define PW(R, O) WR1("%[pw]", #R, #O)
#define PW4(R, O) PW(R, O) PW(R+4, O+1024) PW(R+8, O+2048) PW(R+12, O+3072)
#define PX4(R, O) WR1("%[t]", #R, #O) WR1("%[t]", #R "+4", #O "+1024") WR1("%[t]", #R "+8", #O "+2048") WR1("%[t]", #R "+12", #O "+3072")
#define CSL(R) "buffer_load_dword " R ", %[ve], s[40:43], s49 offen sc1\n\ts_add_u32 s49, s49, 14336\n\t"
DI void phase_wo(rsrc_t rws, int w, unsigned lane, int mt, int nt, int ntg, u32x4& r0, u32x2& r1, Park& pk PROFARG) {
    const unsigned tg = w & 3, kh = w >> 2, t = lane & 15, g = lane >> 4, l16 = lane * 16, la = kh * 5120 + l16, e = w * 64 + lane, ro = nt & 3;
    const unsigned ab = WS_O + (mt * 4 + tg) * 20480 + kh * 10240, hb = WS_B + (mt >> 2) * QBS + nt * 256, tk = (mt & 3) * 4 + tg;
    u32x8 sa = {ab + ro * 2048, ab + (ro + 1) * 2048, ab + mod5(ro + 2) * 2048, ab + mod5(ro + 3) * 2048, ab + mod5(ro + 4) * 2048, WS_CS + ntg * 448, hb + tk * 20480, 0};
    const unsigned ex = tg * 5120 + kh * 2048 + l16;
    unsigned tmp;
    asm volatile(
        CSL("v92") CSL("v93") CSL("v94") CSL("v95") CSL("%[t]") SPIN0 VM(7) "s_barrier\n\t"
        RD5("96", "%[wa]", "0") RD5("116", "%[wa]", "10240")
        UNP(72, 248) UNP(74, 249) UNP(76, 250) UNP(78, 251) Z8("80") UNP(88, 252) UNP(90, 253)
        VM(5) TSA("20") TSS("45") WOL(32, s44) WOL(40, s45) WOL(48, s46) WOL(56, s47) WOL(64, s48) RD5("136", "%[wa]", "20480") RD5("156", "%[wa]", "30720")
        "s_waitcnt vmcnt(10) lgkmcnt(0)\n\tv_add_f32 v92, v92, v93\n\tv_add_f32 v94, v94, v95\n\tv_add_f32 v92, v92, %[t]\n\tv_add_f32 v92, v92, v94\n\tds_write_b32 %[vl], v92\n"
        WOC(0, 8, 32) TSA("8") WOK("96", "32", "%[wb]", "0") WOK("116", "36", "%[wb]", "10240")
        WOC(1, 6, 40) WOK("136", "40", "%[wb]", "20480") WOK("156", "44", "%[wb]", "30720")
        WOC(2, 4, 48) LGK(10) WOK("96", "48", "%[wc]", "0") WOK("116", "52", "%[wc]", "10240")
        WOC(3, 2, 56) LGK(10) WOE("136", "56") WOE("156", "60")
        WOC(4, 0, 64) LGK(0) WOE("96", "64") WOE("116", "68") TSA("9")
        "s_cmp_eq_u32 %[kh], 0\n\ts_nop 5\n\t" WR1("%[xw]", "80", "0") WR1("%[xw]", "84", "1024") "s_cbranch_scc1 1f\n\t" WR1("%[x4]", "88", "0")
        "1:\n\ts_waitcnt lgkmcnt(0)\n\ts_barrier\n\t" RD1("80", "%[xr]", "0") RD1("84", "%[xr]", "1024") RD1("92", "%[x4]", "0")
        LGK(2) PKADD("72", "80") PKADD("74", "82") LGK(1) PKADD("76", "84") PKADD("78", "86") CVT("248", "72") CVT("249", "74") CVT("250", "76") CVT("251", "78")
        "buffer_store_dwordx4 v[248:251], %[h0], s[40:43], s50 offen sc1\n\t" LGK(0) "s_cbranch_scc0 9f\n\t"
        PKADD("88", "92") PKADD("90", "94") CVT("252", "88") CVT("253", "90") "buffer_store_dwordx2 v[252:253], %[h1], s[40:43], s50 offen sc1\n\ts_branch 9f\n"
        WOT(0) WOT(1) WOT(2) WOT(3) WOT(4)
        "8:\n\t" VM(0) WOR(32, s44) WOR(40, s45) WOR(48, s46) WOR(56, s47) WOR(64, s48) VM(0) SPIN1 "s_setpc_b64 s[56:57]\n9:\n\t"
        PW4(176, 0) PW4(192, 4096) PW(208, 8192) PW(212, 9216)
        : "+{v[248:251]}"(r0), "+{v[252:253]}"(r1), "+{v[176:207]}"(pk.a), "+{v[208:239]}"(pk.b), "+{v[240:247]}"(pk.c), "+{s[44:51]}"(sa), [t] "=&v"(tmp) PFIO
        : "{s[40:43]}"(rws), [l16] "v"(l16), [wa] "v"(la + 122880), [wb] "v"(la + 20480), [wc] "v"(la + 81920), [kh] "s"(kh), [pw] "v"(20480 + park0(w) + l16),
          [ve] "v"(e < 112 ? (e ^ (((lane >> 1) ^ (lane >> 3)) & 1) * 10) * 4 : OOB), [vl] "v"(L_CS + (e < 112 ? e : 112) * 4),
          [xw] "v"(tg * 5120 + 2048 - kh * 2048 + l16), [xr] "v"(ex), [x4] "v"((tg == 2 ? 61440 : tg * 5120 + 4096) + l16),
          [h0] "v"((2 * g + kh) * 2048 + t * 16), [h1] "v"((8 + (g >> 1)) * 2048 + t * 16 + (g & 1) * 8)
        : "memory", "scc", "vcc", "s56", "s57", "s59", "s60", "s61", VCL32, VCL48, VCL64, VCL80, VCL96, VCL112, VCL128, VCL140, VCL160 PFCL);
    TS(10);
    float s = 0.f, q = 0.f;
#pragma unroll
    for (int k = 0; k < 6; ++k) {
        const bf2v v = __builtin_bit_cast(bf2v, k < 4 ? r0[k & 3] : r1[k & 1]);
        s = __builtin_amdgcn_fdot2_f32_bf16(v, __builtin_bit_cast(bf2v, 0x3f803f80u), s, false);
        q = __builtin_amdgcn_fdot2_f32_bf16(v, v, q, false);
    }
    const f32x2 y = swap32(s, q);
    const float z = sum16(y[0] + y[1]);
    const f32x2 v = swap32(z, z);
    st8(rws, g == 0 ? t * 16 + kh * 8 : OOB, hb + OFF_HS + tk * 2048, u32x2{__float_as_uint(v[0]), __float_as_uint(v[1])});
}

#define GW0 LGK(4)
#define GW1
#define GW2
#define GW3 LGK(5)
#define GW4
#define GW5 LGK(5)
#define GW6
#define GUN(MF, n, H, R, X) GW##n MF("140+8*" #n, "112+4*" #n, H) X MF("144+8*" #n, "112+4*" #n, H "+8") R
#define GUK(MF, H, V, O, D, X0, X1, X2, X3, X4, X5, X6) \
    GUN(MF, 0, H, RD1("112", V, O), X0) GUN(MF, 1, H, RD1("116", V, O "+" D), X1) GUN(MF, 2, H, RD1("120", V, O "+2*" D), X2) GUN(MF, 3, H, RD1("124", V, O "+3*" D), X3) \
    GUN(MF, 4, H, RD1("128", V, O "+4*" D), X4) GUN(MF, 5, H, RD1("132", V, O "+5*" D), X5) GUN(MF, 6, H, RD1("136", V, O "+6*" D), X6)
#define LH0(H, S) LD1(#H, "%[l16]", #S, "0")
#define LH1(H, S) LD1(#H "+4", "%[l16]", #S, "1024")
#define LH2(H, S) LD1(#H "+8", "%[lb]", #S, "0")
#define LH3(H, S) LD1(#H "+12", "%[lb]", #S, "1024")
#define LDH(H, S) LH0(H, S) LH1(H, S) LH2(H, S) LH3(H, S)
#define LH(H, S) LH0(H, S), LH1(H, S), LH2(H, S), LH3(H, S)
#define LS(R, O) LD1(#R, "%[l16]", "s54", #O)
#define LDST LS(32, 0) LS(36, 1024) LS(40, 2048) LS(44, 3072)
#define WL(R, O) LD1(#R, "%[l16]", "s52", #O)
#define WA "s_add_u32 s52, s52, 4096\n\t"
#define W15 WL(196, 0) WL(200, 1024) WL(204, 2048) WL(208, 3072) WA WL(212, 0) WL(216, 1024) WL(220, 2048) WL(224, 3072) WA WL(228, 0) WL(232, 1024) WL(236, 2048) WL(240, 3072) WA \
    WL(244, 0) WL(24, 1024) WL(28, 2048)
#define WEND "v_add_u32 %[lb], 4096, %[l16]\n\t" WL(32, 3072) LD1("36", "%[lb]", "s52", "0") LD1("40", "%[lb]", "s52", "1024") LD1("44", "%[z4]", "s52", "2048") LD1("96", "%[z4]", "s52", "3072") WA \
    LD1("100", "%[z4]", "s52", "0") LD1("104", "%[z4]", "s52", "1024") LD1("108", "%[z4]", "s52", "2048")
#define CKS(N, B, s) VM(N) MX3(B), MX2(B "+3"), MX2(B "+5"), MX2(B "+7"), MX2(B "+9"), MX2(B "+11"), MX2(B "+13") MXE(B "+15") "s_cbranch_scc1 4" #s "f\n3" #s ":\n\t"
#define GUK_(...) GUK(__VA_ARGS__)
#define GUS(MF, s, H, O1, PRE, O2, HN, N, ...) GUK_(MF, #H, "%[wa]", O1, "1024", __VA_ARGS__) PRE GUK_(MF16, #H "+4", "%[wa]", O2, "1024", CKS(N, #HN, s)) TSI
#define REDO(s, L, CK) "4" #s ":\n\ts_mov_b64 exec, vcc\n\t" L "s_mov_b64 exec, -1\n\t" VM(0) SPIN1 CK "s_cbranch_scc1 4" #s "b\n\ts_branch 3" #s "b\n"
#define WADV "v_add_u32 %[wa], 0xfc00, %[wa]\n\t"
#define HNX(A, B) "s_add_u32 s" #B ", s" #A ", 2048\n\ts_cmp_eq_u32 s" #B ", s55\n\ts_cselect_b32 s" #B ", %[hb], s" #B "\n\t"
#define SW32(A, B) "v_permlane32_swap_b32 v[" A "], v[" B "]\n\t"
#define SW16(A, B) "v_permlane16_swap_b32 v[" A "], v[" B "]\n\t"
#define EPF(A, C, T) "v_pk_fma_f32 v[" A ":" A "+1], v[" C ":" C "+1], v[16+4*" #T ":17+4*" #T "], v[" A ":" A "+1]\n\t"
#define EPT(A, C, T, D) EPF(A, C, T) EPF(A "+2", C "+2", T) "v_max_f32 v[" A "], 0, v[" A "]\n\tv_max_f32 v[" A "+1], 0, v[" A "+1]\n\t" \
    "v_pk_mul_f32 v[" A "+2:" A "+3], v[" A "+2:" A "+3], v[18+4*" #T ":19+4*" #T "]\n\t" \
    "v_pk_mul_f32 v[" A ":" A "+1], v[" A ":" A "+1], v[" A "+2:" A "+3]\n\t" CVT(D, A)
#define EPN(n, D) EPT("140+8*" #n, "112+4*" #n, 0, D) EPT("144+8*" #n, "112+4*" #n, 1, D "+1")
#define EP2(n, m, D, N) LGK(N) EPN(n, D) EPN(m, D "+2") SW32(D, D "+1") "s_nop 0\n\t" SW32(D "+2", D "+3") \
    "buffer_store_dwordx4 v[" D ":" D "+3], %[f0], s[40:43], s53 offen sc1\n\ts_add_u32 s53, s53, 15872\n\t"
#define LF0(F, S) LD1(F, "%[l16]", S, "0")
#define LF1(F, S) LD1(F "+4", "%[l16]", S, "1024")
#define LF2(F, S) LD1(F "+8", "%[l16]", S, "2048")
#define LF3(F, S) LD1(F "+12", "%[l16]", S, "3072")
#define LF4(F, S) LD1(F "+16", "%[lb]", S, "0")
#define LF5(F, S) LD1(F "+20", "%[lb]", S, "1024")
#define LF6(F, S) LD1(F "+24", "%[z4]", S, "2048")
#define LDF(F, S) LF0(F, S) LF1(F, S) LF2(F, S) LF3(F, S) LF4(F, S) LF5(F, S) LF6(F, S)
#define DK(MF, F, a, b, c, d, e) MF("168", #a, F) MF("172", #b, F) MF("176", #c, F) MF("180", #d, F) MF("184", #e, F)
#define DT(F, X0, X1, X2, X3, X4, X5, X6) DK(MF0, F, 196, 200, 204, 208, 212) X0 DK(MF16, F "+4", 216, 220, 224, 228, 232) X1 DK(MF16, F "+8", 236, 240, 244, 24, 28) X2 \
    DK(MF16, F "+12", 48, 52, 56, 60, 64) X3 DK(MF16, F "+16", 68, 72, 76, 80, 84) X4 DK(MF16, F "+20", 88, 92, 32, 36, 40) X5 DK(MF16, F "+24", 44, 96, 100, 104, 108) X6 TSI
#define DTL(F, S) DT(F, LF0(F, S), LF1(F, S), LF2(F, S), LF3(F, S), LF4(F, S), LF5(F, S), LF6(F, S))
#define DTE(F) DT(F, "", "", "", "", "", "", "")
#define DW(S) "v_add_u32 %[wa], " S ", %[l16]\n\t" WR1("%[wa]", "168", "0") WR1("%[wa]", "172", "1024") WR1("%[wa]", "176", "2048") WR1("%[wa]", "180", "3072") WR1("%[wa]", "184", "4096")
#define DC(N, F, s) VM(N) CK28(F) "s_cbranch_scc1 4" #s "f\n3" #s ":\n\t"
#define KSW 0
#define TSWP 0
DI void phase_ffn(rsrc_t rws, int w, unsigned lane, int mt, int nt, int qg, int ntg, const Park& pk PROFARG) {
    const unsigned t = lane & 15, g = lane >> 4, l16 = lane * 16, qbb = WS_B + qg * QBS, wg = (w & 3) * 2 + (w >> 2), hb = qbb + wg * 40960, ks = w ^ (KSW), k0 = 7 * ks - (ks == 7);
    const unsigned fb = WS_B + (mt >> 2) * QBS + OFF_F + (mt & 3) * 4 * 55296 + k0 * 1024, fv = (g >> 1) * 55296 + t * 16;
    u32x8 sa; u32x4 sb = {WS_WD + nt * 276480 + k0 * 5120, qbb + OFF_F + wg * 110592 + ntg * 256, qbb + OFF_HS + wg * 4096, hb + 20480};
#pragma unroll
    for (int i = 0; i < 4; ++i) { sa[i] = fb + (i ^ (TSWP)) * 55296; sa[4 + i] = (i ^ (TSWP)) * 40960 + ks * 5120; }
    unsigned wa = 20480 + l16, h0 = hb + (((ntg & 15) * 5) >> 3) * 2048, tmp = 20480 + park0(w) + l16, lb = l16 + 20480;
    u32x32 da = pk.a, db = pk.b; u32x8 dc = pk.c;
    asm volatile(
        PX4(216, 10240) PX4(232, 14336) SPIN0 HNX(64, 65) HNX(65, 66) HNX(66, 67) HNX(67, 68) HNX(68, 69) HNX(69, 70) HNX(70, 71) HNX(71, 72) HNX(72, 73)
        "s_cmp_eq_u32 %[ng], 31\n\ts_cbranch_scc1 7f\n\t" VM(1) TSA("21") LDH(32, s64) LDH(48, s65) LGK(0) "s_barrier\n\t"
        RD5("112", "%[wa]", "0") RD1("132", "%[wa]", "5120") RD1("136", "%[wa]", "6144")
        "1:\n\t" VM(4) CK16("32") "s_cbranch_scc1 2f\n\t" TSA("11") TSS("35") LDH(64, s66) LDH(80, s67) LDH(96, s68)
        GUS(MF0, 0, 32, "7168", "", "14336", 48, 12, "", "", "", "", "", "", "")
        GUS(MF16, 1, 48, "21504", "", "28672", 64, 12, LH(32, s69), "", "", "")
        GUS(MF16, 2, 64, "35840", "", "43008", 80, 13, LH(48, s70), WL(196, 0), "", "")
        GUS(MF16, 3, 80, "50176", "", "57344", 96, 15, LH(64, s71), WL(200, 1024), WL(204, 2048), "")
        WADV
        GUS(MF16, 4, 96, "0", "", "7168", 32, 18, LH(80, s72), WL(208, 3072) WA, WL(212, 0), WL(216, 1024))
        GUS(MF16, 5, 32, "14336", "", "21504", 48, 21, LH(96, s73), WL(220, 2048), WL(224, 3072) WA, WL(228, 0))
        GUS(MF16, 6, 48, "28672", "", "35840", 64, 23, LS(32, 0), LS(36, 1024), LS(40, 2048), LS(44, 3072), WL(232, 1024), WL(236, 2048), WL(240, 3072) WA)
        GUS(MF16, 7, 64, "43008", "", "50176", 80, 24, WL(244, 0), WL(24, 1024), WL(28, 2048), WL(48, 3072) WA, WL(52, 0), WL(56, 1024), WL(60, 2048))
        GUS(MF16, 8, 80, "57344", WADV, "0", 96, 21, WL(64, 3072) WA, WL(68, 0), WL(72, 1024), WL(76, 2048), "", "", "")
        GUK_(MF16, "96", "%[wa]", "7168", "1024", CKS(14, "32", 9))
        "v_lshrrev_b32 %[wa], 4, %[l16]\n\tv_and_b32 %[wa], 48, %[wa]\n\t"
        GUK(MF16, "100", "%[wa]", "14336", "64",
            WL(80, 3072) WA PKADD("32", "34") PKADD("36", "38") PKADD("40", "42") PKADD("44", "46"),
            WL(84, 0) PKADD("32", "36") PKADD("40", "44"),
            WL(88, 1024) SW32("32", "40") SW32("33", "41") PKADD("32", "40") "v_mov_b64 v[34:35], v[32:33]\n\t",
            WL(92, 2048) SW16("32", "34") SW16("33", "35") PKADD("32", "34") "v_mul_f32 v16, 0xbacccccd, v32\n\t",
            "v_mul_f32 v33, 0x3acccccd, v33\n\tv_fma_f32 v33, -v16, v16, v33\n\tv_max_f32 v33, 0, v33\n\tv_add_f32 v33, 0x3727c5ac, v33\n\t",
            "v_rcp_f32 v18, v33\n\tv_mov_b32 v20, v16\n\tv_mov_b32 v22, v18\n\t",
            SW32("16", "20") SW32("18", "22") "v_mov_b32 v17, v16\n\tv_mov_b32 v21, v20\n\tv_mov_b32 v19, v18\n\tv_mov_b32 v23, v22\n\t")
        TSA("12") WEND EP2(0, 1, "140", 5) EP2(2, 3, "156", 3) EP2(4, 5, "172", 1) LGK(0) EPN(6, "188") "s_nop 1\n\t" SW32("188", "189")
        "buffer_store_dwordx2 v[188:189], %[f1], s[40:43], s53 offen sc1\n"
        TSA("22") "6:\n\ts_barrier\n\t" TSA("23") LDF("112", "s44") LDF("140", "s45")
        DC(7, "112", 13) TSA("13") TSS("50")
        DTL("112", "s46") DC(7, "140", 10) DW("s48") DTL("140", "s47")
        "s_nop 5\n\t" DW("s49") DC(7, "112", 11) TSA("56") DTE("112") DC(0, "140", 12) DW("s50") DTE("140") "s_nop 6\n\t" DW("s51")
        "s_waitcnt lgkmcnt(0)\n\ts_barrier\n\ts_branch 9f\n"
        "2:\n\ts_mov_b64 exec, vcc\n\t" LDH(32, s64) LDH(48, s65) "s_mov_b64 exec, -1\n\t" SPIN1 "s_branch 1b\n"
        REDO(0, LDH(48, s65), CK16("48")) REDO(1, LDH(64, s66), CK16("64")) REDO(2, LDH(80, s67), CK16("80")) REDO(3, LDH(96, s68), CK16("96")) REDO(4, LDH(32, s69), CK16("32"))
        REDO(5, LDH(48, s70), CK16("48")) REDO(6, LDH(64, s71), CK16("64")) REDO(7, LDH(80, s72), CK16("80")) REDO(8, LDH(96, s73), CK16("96")) REDO(9, LDST, CK16("32"))
        REDO(10, LDF("140", "s45"), CK28("140")) REDO(11, LDF("112", "s46"), CK28("112")) REDO(12, LDF("140", "s47"), CK28("140"))
        REDO(13, LDF("112", "s44"), CK28("112"))
        "7:\n\t" LGK(0) W15 WL(48, 3072) WA WL(52, 0) WL(56, 1024) WL(60, 2048) WL(64, 3072) WA WL(68, 0) WL(72, 1024) WL(76, 2048) WL(80, 3072) WA WL(84, 0) WL(88, 1024) WL(92, 2048) WEND
        "1:\n\ts_sleep 4\n\t" LF0("112", "s44") SPIN1 VM(0) CK4("112") "s_cbranch_scc1 1b\n\ts_branch 6b\n9:"
        : "+{v[176:207]}"(da), "+{v[208:239]}"(db), "+{v[240:247]}"(dc), "+{s[52:55]}"(sb), "+{s64}"(h0), [wa] "+&v"(wa), [t] "+&v"(tmp), [lb] "+&v"(lb) PFIO
        : "{s[40:43]}"(rws), "{s[44:51]}"(sa), [l16] "v"(l16), [z4] "v"(ks < 6 ? l16 + 4096 : OOB),
          [f0] "v"(fv + (g & 1) * 7936), [f1] "v"(ntg < 30 ? fv + (g & 1) * 8 : OOB), [ng] "s"(ntg), [hb] "s"(hb)
        : "memory", "scc", "vcc", "s59", "s60", "s61", "s65", "s66", "s67", "s68", "s69", "s70", "s71", "s72", "s73", "v16", "v17", "v18", "v19", "v20", "v21", "v22", "v23",
          "v24", "v25", "v26", "v27", "v28", "v29", "v30", "v31", VCL32, VCL48, VCL64, VCL80, VCL96, VCL112, VCL128, VCL140, VCL160 PFCL);
}
DI f32x4 sum8(unsigned a) {
    f32x4 v[8];
#pragma unroll
    for (int i = 0; i < 8; ++i) v[i] = ldsr<f32x4>(a + i * 5120);
    return ((v[0] + v[1]) + (v[2] + v[3])) + ((v[4] + v[5]) + (v[6] + v[7]));
}
DI u32x2 addres(f32x4 a, unsigned lo, unsigned hi) { return u32x2{pk2(a[0] + bflo(lo), a[1] + bfhi(lo)), pk2(a[2] + bflo(hi), a[3] + bfhi(hi))}; }

#define ROT 0
__global__ void __launch_bounds__(512, 2) block_kernel(Params) {
    u32x16 ka;
    asm volatile("s_load_dwordx16 %0, %1, 0" : "=s"(ka) : "s"(__builtin_amdgcn_kernarg_segment_ptr()) : "memory");
    __shared__ __attribute__((aligned(16))) char lds[163840];
    asm volatile("" :: "v"((LAS char*)lds) : "memory");
    const int lane = threadIdx.x & 63, w = __builtin_amdgcn_readfirstlane(threadIdx.x >> 6), b = blockIdx.x;
    unsigned pre;
    asm volatile("s_cmp_lg_u32 %1, %2\n\ts_cbranch_scc1 1f\n\ts_getpc_b64 s[4:5]\n.Lpc:\n\tv_lshlrev_b32 %0, 7, %3\n\tv_min_u32 %0, .Lend-.Lpc-4, %0\n\t"
                 "global_load_dword %0, %0, s[4:5]\n1:" : "=&{v200}"(pre) : "s"(w), "s"((b >> 3) & 3), "v"(threadIdx.x) : "memory", "scc", "s4", "s5");
    Params p;
    rsrc_t rws;
    const int xcd = b & 7, i = b >> 3;
    Retile rt;
    u32x2 tag;
    unsigned fq;
    {
        const int mt = (xcd >> 1) * 4 + (i & 3), nt = (xcd & 1) * 8 + (i >> 2), rot = ROT;
        QkvSrc c; QkvAdr e; u32x4 row; unsigned rowt, T;
        qkv_start(c, row, rowt, T, rt.t, p, ka, b, mt, nt, rot, w, lane);
        rws = mkrs(p.ws, WS_END);
        TSW(60);
        retile_setup(rt, p, b, lane);
        qkv_setup(e, mt, nt, w, lane);
        fq = lane == 0 ? WS_FQ + (nt * 16 + mt) * 128 : OOB;
        TSW(61);
        asm volatile("s_add_u32 m0, %[dm], 65536\n\ts_cmp_eq_u32 %[nt], 15\n\ts_cbranch_scc1 1f\n\t" QD4 "1:\n\ts_waitcnt vmcnt(12) lgkmcnt(0)\n\ts_add_u32 %0, %0, 1"
                     : "+s"(T), "+{s[60:75]}"(rt.t), "+v"(row), "+v"(rowt), "+v"(fq), "+{v200}"(pre), [sx] "+s"(c.sx), [sw] "+s"(c.sw)
                     : [x0] "v"(c.xo[0]), [x1] "v"(c.xo[1]), [w0] "v"(c.wo[0]), [w1] "v"(c.wo[1]), [rx] "s"(c.rx), [rw] "s"(c.rw), [dm] "s"(w * 4096), [nt] "s"(nt) : "memory", "scc");
        TSW(62);
        const u32x4 gr = row_stats(row, rowt, T);
        tag = u32x2{gr[2], gr[3]};
        TSW(63);
        if (nt == 15) {
            st16(rws, e.go, 0, gr);
            retile_issue(rt, w);
            asm volatile("s_waitcnt vmcnt(0)" ::: "memory");
        } else if (nt >= 10) qkv_body<true>(c, e, rt, rws, gr, tag, w PROFPASS);
        else qkv_body<false>(c, e, rt, rws, gr, tag, w PROFPASS);
    }
    const int mt = (xcd >> 1) * 8 + (i & 7), nt = (xcd & 1) * 4 + (i >> 3);
    const int qg = (xcd & 6) + ((i >> 2) & 1), ntg = (xcd & 1) * 16 + (i & 3) * 4 + (i >> 3);
    const int tg = w & 3, kh = w >> 2;
    auto rv0 = [&](unsigned l) { return (l & 15) * 1280 + (nt & 1) * 32 + kh * 64 + (l >> 4) * 16; };
    auto rv1 = [&](unsigned l) { return kh ? OOB : (l & 15) * 1280 + ((nt & 1) ? 0 : 128) + (l >> 4) * 8; };
    const unsigned rsb = (mt * 64 + tg * 16) * 1280 + nt * 160;
    u32x4 r0; u32x2 r1; Park pk;
    phase_attn(p, rt, rws, tag, fq, b, w, lane, nt, ntg, rv0(lane), rv1(lane), rsb, r0, r1, pk PROFPASS);
    unsigned ln;
    asm volatile("v_mbcnt_lo_u32_b32 %0, -1, 0\n\tv_mbcnt_hi_u32_b32 %0, -1, %0" : "=v"(ln) : "{v154}"(pk.d));
    phase_wo(rws, w, ln, mt, nt, ntg, r0, r1, pk PROFPASS);
    PIN(ln);
    unsigned o0 = rv0(ln), o1 = rv1(ln), a = tg * 40960 + ln * 16;
    PIN(o0); PIN(o1); PIN(a);
    phase_ffn(rws, w, ln, mt, nt, qg, ntg, pk PROFPASS);
    TS(14);
    {
        const rsrc_t ro = mkrs(p.out, 2048 * 1280);
        const u32x2 u = addres(sum8(a + kh * 2048), r0[0], r0[1]), v = addres(sum8(a + kh * 2048 + 1024), r0[2], r0[3]);
        st16(ro, o0, rsb, u32x4{u[0], u[1], v[0], v[1]});
        if (kh == 0) st8(ro, o1, rsb, addres(sum8(a + 4096), r1[0], r1[1]));
    }
    asm volatile(".Lend:" ::: "memory");
}

extern "C" void kernel_launch(void* const* d_in, const int* in_sizes, int n_in, void* d_out, int out_size, void* d_ws, size_t ws_size, hipStream_t stream) {
    Params p;
    p.x = (const char*)d_in[0]; p.wqkv = (const char*)d_in[1]; p.wo = (const char*)d_in[2];
    p.wg = (const char*)d_in[4]; p.wu = (const char*)d_in[5]; p.wd = (const char*)d_in[6];
    p.out = (char*)d_out; p.ws = (char*)d_ws;
    block_kernel<<<256, 512, 0, stream>>>(p);
}
```

```cpp
#include <hip/hip_runtime.h>
#include <hip/hip_bf16.h>
#include <stdint.h>

#define DI __device__ __forceinline__
typedef __attribute__((ext_vector_type(2))) __bf16 bf2v;
typedef __attribute__((ext_vector_type(8))) __bf16 bf8v;
typedef __attribute__((ext_vector_type(16))) __bf16 bf16v;
typedef __attribute__((ext_vector_type(4))) short s4v;
typedef __attribute__((ext_vector_type(2))) float f32x2;
typedef __attribute__((ext_vector_type(4))) float f32x4;
typedef __attribute__((ext_vector_type(8))) float f32x8;
typedef __attribute__((ext_vector_type(16))) float f32x16;
typedef __attribute__((ext_vector_type(32))) float f32x32;
typedef __attribute__((ext_vector_type(2))) unsigned u32x2;
typedef __attribute__((ext_vector_type(4))) unsigned u32x4;
typedef __attribute__((ext_vector_type(8))) unsigned u32x8;
typedef __attribute__((ext_vector_type(16))) unsigned u32x16;
typedef __attribute__((ext_vector_type(32))) unsigned u32x32;
typedef __amdgpu_buffer_rsrc_t rsrc_t;
#define LAS __attribute__((address_space(3)))

constexpr unsigned WS_Q = 0;
constexpr unsigned WS_KV = WS_Q + 32 * 81920;
constexpr unsigned WS_O = WS_KV + 32 * 163840;
constexpr unsigned WS_B = WS_O + 128 * 20480;
constexpr unsigned QBS = 32 * 40960, OFF_HS = 16 * 20480, OFF_F = OFF_HS + 16 * 2048;
constexpr unsigned WS_WO = WS_B + 8 * QBS;
constexpr unsigned WS_WD = WS_WO + 8 * 102400;
constexpr unsigned WS_WGU = WS_WD + 8 * 276480;
constexpr unsigned WS_CS = WS_WGU + 32 * 143360;
constexpr unsigned WS_FQ = WS_CS + 5 * 3584 * 4;
constexpr unsigned WS_FR = WS_FQ + 256 * 128;
constexpr unsigned WS_XS = WS_FR + 2048;
constexpr unsigned WS_E = WS_XS + 2048 * 16;
constexpr unsigned WS_END = WS_E + 128;
constexpr unsigned OOB = 0x40000000u;
constexpr int SPIN = 1 << 14;

struct Params {
    const char* x; const char* wqkv; const char* wo; const char* wg; const char* wu; const char* wd;
    char* out; char* ws;
};

#define SBAR() __builtin_amdgcn_sched_barrier(0)
#define PIN(x) asm volatile("" : "+v"(x))
#define WAITBAR(N) asm volatile("s_waitcnt vmcnt(" #N ") lgkmcnt(0)\n\ts_barrier" ::: "memory")
#define VBAR(N) asm volatile("s_waitcnt vmcnt(" #N ")\n\ts_barrier" ::: "memory")
#define LBAR() asm volatile("s_waitcnt lgkmcnt(0)\n\ts_barrier" ::: "memory")
#define BAR() asm volatile("s_barrier" ::: "memory")

DI rsrc_t mkrs(const char* p, unsigned bytes) { return __builtin_amdgcn_make_buffer_rsrc((void*)p, (short)0, (int)bytes, 0x00020000); }
template <class T> DI T ldsr(unsigned a) { return *(const LAS T*)(uintptr_t)a; }
template <class T> DI void ldsw(unsigned a, T v) { *(LAS T*)(uintptr_t)a = v; }
DI s4v ldstr(unsigned a) { return __builtin_amdgcn_ds_read_tr16_b64_v4i16((LAS s4v*)(uintptr_t)a); }
DI bf8v ldstr2(unsigned a, unsigned d) {
    s4v lo = ldstr(a), hi = ldstr(a + d);
    return __builtin_bit_cast(bf8v, __builtin_shufflevector(lo, hi, 0, 1, 2, 3, 4, 5, 6, 7));
}
DI f32x16 mfma32(bf8v a, bf8v b, f32x16 c) { return __builtin_amdgcn_mfma_f32_32x32x16_bf16(a, b, c, 0, 0, 0); }
DI f32x4 mfma16(bf8v a, bf8v b, f32x4 c) { return __builtin_amdgcn_mfma_f32_16x16x32_bf16(a, b, c, 0, 0, 0); }
DI bf8v asbf(u32x4 v) { return __builtin_bit_cast(bf8v, v); }
DI unsigned pk2(float lo, float hi) { f32x2 v = {lo, hi}; return __builtin_bit_cast(unsigned, __builtin_convertvector(v, bf2v)); }
DI float bflo(unsigned u) { return __uint_as_float(u << 16); }
DI float bfhi(unsigned u) { return __uint_as_float(u & 0xffff0000u); }
DI f32x2 swap32(float a, float b) { auto r = __builtin_amdgcn_permlane32_swap(__float_as_uint(a), __float_as_uint(b), false, false); return f32x2{__uint_as_float(r[0]), __uint_as_float(r[1])}; }
DI float sum16(float a) { auto r = __builtin_amdgcn_permlane16_swap(__float_as_uint(a), __float_as_uint(a), false, false); return __uint_as_float(r[0]) + __uint_as_float(r[1]); }
DI bool anyl(bool c) { return __builtin_amdgcn_ballot_w64(c) != 0; }
DI void st16(rsrc_t r, unsigned voff, unsigned soff, u32x4 v) { asm volatile("buffer_store_dwordx4 %0, %1, %2, %3 offen sc1\n\ts_nop 1" :: "v"(v), "v"(voff), "s"(r), "s"(soff) : "memory"); }
DI void st8(rsrc_t r, unsigned voff, unsigned soff, u32x2 v) { asm volatile("buffer_store_dwordx2 %0, %1, %2, %3 offen sc1" :: "v"(v), "v"(voff), "s"(r), "s"(soff) : "memory"); }
DI void st4(rsrc_t r, unsigned voff, unsigned soff, unsigned v) { asm volatile("buffer_store_dword %0, %1, %2, %3 offen sc1" :: "v"(v), "v"(voff), "s"(r), "s"(soff) : "memory"); }
DI u32x4 ld16c(rsrc_t r, unsigned voff, unsigned soff) { return __builtin_amdgcn_raw_buffer_load_b128(r, voff, soff, 16); }
DI u32x2 ld8c(rsrc_t r, unsigned voff, unsigned soff) { return __builtin_amdgcn_raw_buffer_load_b64(r, voff, soff, 16); }
DI void dma1(unsigned m0v, unsigned voff, rsrc_t r, unsigned soff) {
    asm volatile("s_mov_b32 m0, %0\n\ts_nop 0\n\tbuffer_load_dwordx4 %1, %2, %3 offen lds" :: "s"(m0v), "v"(voff), "s"(r), "s"(soff) : "memory"); }
DI void dma1c(unsigned m0v, unsigned voff, rsrc_t r, unsigned soff) {
    asm volatile("s_mov_b32 m0, %0\n\ts_nop 0\n\tbuffer_load_dwordx4 %1, %2, %3 offen sc1 lds" :: "s"(m0v), "v"(voff), "s"(r), "s"(soff) : "memory"); }
DI void dma3c(unsigned m0v, unsigned voff, rsrc_t r, unsigned soff) {
    asm volatile("s_mov_b32 m0, %0\n\ts_nop 0\n\tbuffer_load_dwordx4 %1, %2, %3 offen sc1 lds\n\tbuffer_load_dwordx4 %1, %2, %3 offen offset:1024 sc1 lds\n\t"
                 "buffer_load_dwordx4 %1, %2, %3 offen offset:2048 sc1 lds" :: "s"(m0v), "v"(voff), "s"(r), "s"(soff) : "memory"); }
DI void dma4c(unsigned m0v, unsigned voff, rsrc_t r, unsigned soff) {
    asm volatile("s_mov_b32 m0, %0\n\ts_nop 0\n\tbuffer_load_dwordx4 %1, %2, %3 offen sc1 lds\n\tbuffer_load_dwordx4 %1, %2, %3 offen offset:1024 sc1 lds\n\t"
                 "buffer_load_dwordx4 %1, %2, %3 offen offset:2048 sc1 lds\n\tbuffer_load_dwordx4 %1, %2, %3 offen offset:3072 sc1 lds" :: "s"(m0v), "v"(voff), "s"(r), "s"(soff) : "memory"); }
#define SPINS "0x4000"
#define PINS(x) asm volatile("" : "+s"(x))
#define CHK(B, TMP, DST) \
    "v_max3_u32 v" #TMP ", v[" #B "], v[" #B "+1], v[" #B "+2]\n\tv_max3_u32 v" #TMP ", v[" #B "+3], v[" #B "+4], v" #TMP "\n\t" \
    "v_max3_u32 v" #TMP ", v[" #B "+5], v[" #B "+6], v" #TMP "\n\tv_max3_u32 v" #TMP ", v[" #B "+7], v[" #B "+8], v" #TMP "\n\t" \
    "v_max3_u32 v" #TMP ", v[" #B "+9], v[" #B "+10], v" #TMP "\n\tv_max3_u32 v" #TMP ", v[" #B "+11], v[" #B "+12], v" #TMP "\n\t" \
    "v_max3_u32 v" #TMP ", v[" #B "+13], v[" #B "+14], v" #TMP "\n\tv_max_u32 v" #TMP ", v[" #B "+15], v" #TMP "\n\t" \
    "v_cmp_eq_u32 " DST ", -1, v" #TMP "\n\t"
#define LD1(R, V, S, O) "buffer_load_dwordx4 v[" R ":" R "+3], " V ", s[40:43], " S " offen offset:" O " sc1\n\t"
#define DM1(V, S, O) "buffer_load_dwordx4 " V ", s[40:43], " S " offen offset:" #O " sc1 lds\n\t"
#define DM3(V, S) DM1(V, S, 0) DM1(V, S, 1024) DM1(V, S, 2048)
#define DM4(V, S) DM3(V, S) DM1(V, S, 3072)
#define Z2(R) "v_mov_b64 v[" R ":" R "+1], 0\n\t"
#define Z4(R) Z2(R) Z2(R "+2")
#define Z8(R) Z4(R) Z4(R "+4")

#define VCL32 "v32", "v33", "v34", "v35", "v36", "v37", "v38", "v39", "v40", "v41", "v42", "v43", "v44", "v45", "v46", "v47"
#define VCL48 "v48", "v49", "v50", "v51", "v52", "v53", "v54", "v55", "v56", "v57", "v58", "v59", "v60", "v61", "v62", "v63"
#define VCL64 "v64", "v65", "v66", "v67", "v68", "v69", "v70", "v71", "v72", "v73", "v74", "v75", "v76", "v77", "v78", "v79"
#define VCL80 "v80", "v81", "v82", "v83", "v84", "v85", "v86", "v87", "v88", "v89", "v90", "v91", "v92", "v93", "v94", "v95"
#define VCL96 "v96", "v97", "v98", "v99", "v100", "v101", "v102", "v103", "v104", "v105", "v106", "v107", "v108", "v109", "v110", "v111"
#define VCL112 "v112", "v113", "v114", "v115", "v116", "v117", "v118", "v119", "v120", "v121", "v122", "v123", "v124", "v125", "v126", "v127"
#define VCL128 "v128", "v129", "v130", "v131", "v132", "v133", "v134", "v135", "v136", "v137", "v138", "v139"
#define VCL140 "v140", "v141", "v142", "v143", "v144", "v145", "v146", "v147", "v148", "v149", "v150", "v151", "v152", "v153", "v154", "v155", "v156", "v157", "v158", "v159"
#define VCL160 "v160", "v161", "v162", "v163", "v164", "v165", "v166", "v167", "v168", "v169", "v170", "v171", "v172", "v173", "v174", "v175"
#define VCL112L "v112", "v113", "v114", "v115", "v116", "v117", "v118", "v119"

#define PROFARG
#define PROFPASS
#define TSA(i)
#define PFIO
#define PFCL
#define TSM
#define PFM(i)
#define PFT
#define TS(i)

#define TSF(i)
#define TSI
#define TSS(i)
#define TSW(i)
#define TSP
#define TSQ(a, b)
constexpr unsigned L_RT = 0, L_SM = 32768, L_CP = 49152;
struct RtTab { unsigned v[2048][16]; };
constexpr RtTab make_rt() {
    RtTab t{};
    for (unsigned b = 0; b < 256; ++b) for (unsigned w = 0; w < 8; ++w) for (unsigned f = 0; f < 4; ++f) {
        unsigned A = 0, M = 0, SO = 0, R0 = 0, R1 = 0;
        if (b < 135) {
            const unsigned fg = b / 5, ks = (b % 5) * 4 + (w >> 1), j = ks >> 1, o = fg * 8 + (w & 1) * 4 + f;
            A = (320 * (ks & 1) + (j < 8 ? (j & 1) * 32 + (j >> 1) * 8 : 8 * j) + (f & 1) * 160) * 3456 + fg * 128 + (w & 1) * 64;
            M = (j < 8 ? 96 : 16) * 3456;
            SO = WS_WGU + (o / 7) * 143360 + ks * 7168 + (o % 7) * 1024;
            R0 = 16 * f;
        } else {
            const unsigned v = (b - 135) * 8 + w;
            const bool isWo = v < 200;
            const unsigned vv = v < 740 ? (isWo ? v : v - 200) : 0, ks = vv / 10, cu = vv % 10;
            const unsigned c = 4 * ks + f, j = c / 31, ng = c % 31;
            A = (isWo ? (ks >= 16 ? 40 * c - 2544 : 20 * (c >> 1) + 4 * (c & 1)) : 56 * ng + 16 * (j >> 1) + 4 * (j & 1)) * 1280 + cu * 128;
            M = (isWo ? (ks >= 16 ? 20 : 8) : (j < 6 ? 8 : 4)) * 1280;
            const unsigned seg = 2 * cu + (f >> 1), a = seg / 5, sm = seg % 5, fi = f & 1;
            const unsigned tile = 2 * a + (sm == 2 ? fi : sm > 2), blk = sm == 2 ? 4 : (sm == 1 || sm == 4) ? 2 + fi : fi;
            SO = isWo ? WS_WO + (tile * 100 + (ks >= 10 ? (ks - 10) * 10 + 5 + (blk < 4 ? blk ^ 2 : 4) : ks * 10 + blk)) * 1024 : WS_WD + (tile * 270 + ks * 5 + blk) * 1024;
            R0 = (f >> 1) * 64 + (sm == 2 ? 32 : 8) * fi; R1 = sm == 2 ? 8 : 16;
        }
        unsigned* r = t.v[b * 8 + w];
        r[f] = A + 3072 - 1024 * f; r[4 + f] = M; r[8 + f] = SO; r[12 + f] = (L_RT + w * 4096 + R0) | R1 << 16;
    }
    return t;
}
static __device__ const __attribute__((aligned(64))) RtTab RT_TAB = make_rt();
struct Retile { rsrc_t ra, rb; u32x16 t; unsigned vlo, hi; };
DI void retile_setup(Retile& r, const Params& p, unsigned b, unsigned lane) {
    const bool gu = b < 135;
    const char *pa, *pb; unsigned n;
    asm("s_cmpk_lt_u32 %3, 160\n\ts_cselect_b64 %0, %4, %5\n\ts_cselect_b32 %2, %9, %8\n\ts_cmpk_lt_u32 %3, 135\n\ts_cselect_b64 %1, %7, %0\n\ts_cselect_b64 %0, %6, %0\n\ts_cselect_b32 %2, %8, %2"
        : "=&s"(pa), "=&s"(pb), "=&s"(n) : "s"(b), "s"(p.wo), "s"(p.wd), "s"(p.wg), "s"(p.wu), "s"(640 * 3456 + 3072), "n"(640 * 1280 + 3072) : "scc");
    r.ra = mkrs(pa - 3072, n); r.rb = mkrs(pb - 3072, n);
    r.vlo = gu ? ((lane >> 2) & 7) * 3456 + (lane & 3) * 16 : ((lane >> 3) & 3) * 1280 + (lane & 7) * 16;
    r.hi = lane >> 5;
    PIN(r.vlo); PIN(r.hi); PINS(r.ra); PINS(r.rb);
}
DI void retile_issue(const Retile& r, int w) {
    asm volatile("s_mov_b32 m0, %0\n\ts_nop 0\n\tbuffer_load_dwordx4 %1, %5, 0 offen lds\n\tbuffer_load_dwordx4 %2, %5, 0 offen offset:1024 lds\n\t"
                 "buffer_load_dwordx4 %3, %6, 0 offen offset:2048 lds\n\tbuffer_load_dwordx4 %4, %6, 0 offen offset:3072 lds"
                 :: "s"(L_RT + w * 4096), "v"(r.vlo + r.t[0] + r.hi * r.t[4]), "v"(r.vlo + r.t[1] + r.hi * r.t[5]), "v"(r.vlo + r.t[2] + r.hi * r.t[6]),
                    "v"(r.vlo + r.t[3] + r.hi * r.t[7]), "s"(r.ra), "s"(r.rb) : "memory");
}
DI u32x32 retile_read(const Retile& r, unsigned b, int w, unsigned lane) {
    const u32x4 one = {0x3f803f80u, 0x3f803f80u, 0x3f803f80u, 0x3f803f80u};
    const unsigned f = lane & 3, row = 8 * (lane >> 4) + ((lane >> 2) & 3), dl = b < 135 ? 256 : 512;
    const unsigned rb = b < 135 ? (f >> 1) * 2048 + row * 64 + (f & 1) * 8 : row * 128;
    u32x32 o; f32x4 cp[4];
#pragma unroll
    for (int i = 0; i < 4; ++i) {
        const bf8v fr = ldstr2(rb + (r.t[12 + i] & 0xffff) + (r.t[12 + i] >> 16) * f, dl);
        cp[i] = mfma16(fr, asbf(one), f32x4{});
#pragma unroll
        for (int k = 0; k < 4; ++k) o[4 * i + k] = __builtin_bit_cast(u32x4, fr)[k];
    }
    if ((lane & 15) == 0) {
#pragma unroll
        for (int i = 0; i < 4; ++i) ldsw<f32x4>(L_CP + w * 256 + i * 64 + lane, cp[i]);
    }
    return o;
}

struct QkvSrc { rsrc_t rx, rw; unsigned xo[2], wo[2], sx, sw; };
#define QDX(V, O) "buffer_load_dwordx4 %[" #V "], %[rx], %[sx] offen offset:" #O " lds\n\t"
#define QDW(V, O) "buffer_load_dwordx4 %[" #V "], %[rw], %[sw] offen offset:" #O " lds\n\t"
#define QADV "s_add_u32 %[sx], %[sx], 128\n\ts_cmp_eq_u32 %[sx], 1280\n\ts_cselect_b32 %[sx], 0, %[sx]\n\ts_mul_i32 %[sw], %[sx], 1920\n\t"
#define QD4 QDX(x0, 0) QDX(x1, 1024) QDW(w0, 2048) QDW(w1, 3072) QADV "s_add_u32 m0, m0, 32768\n\t"
DI void qkv_start(QkvSrc& c, u32x4& row, unsigned& rowt, unsigned& T, u32x16& tab, Params& p, u32x16 ka, int b, int mt, int nt, int rot, int w, int lane) {
    const unsigned rr = lane >> 3, isv = nt >= 10;
    const unsigned tok = mt * 128 + (w >> 1) * 32 + ((w & 1) << (2 * isv)) + (rr & 4) * 2 + ((rr & 3) << (1 - isv));
    c.xo[0] = tok * 1280 + (((lane & 7) ^ (rr >> 1)) << 4);
    c.xo[1] = (c.xo[0] ^ 64) + 16 * 1280 - 1024;
    c.wo[0] = (8 * w + ((lane >> 2) & 3)) * 3840 + nt * 256 + (lane >> 4) * 64 + (((lane & 3) ^ ((w & 1) << 1)) << 4) + 1024;
    c.wo[1] = c.wo[0] + 4 * 3840 - 1024;
    c.sx = rot * 128; c.sw = rot * 245760;
    unsigned ro = (mt * 128 + nt * 8 + w) * 1280;
    asm volatile("s_waitcnt lgkmcnt(0)\n\ts_load_dwordx16 %5, %6, %7" : "+s"(ka), "+v"(c.xo[1]), "+v"(c.wo[1]), "+s"(c.sw), "+s"(ro), "=&{s[60:75]}"(tab) : "s"(&RT_TAB), "s"((b * 8 + w) * 64) : "memory");
    p = __builtin_bit_cast(Params, ka);
    asm volatile("s_load_dword %0, %1, %2" : "=&s"(T) : "s"(p.ws), "s"(WS_E) : "memory");
    c.rx = mkrs(p.x, 2048 * 1280); c.rw = mkrs(p.wqkv - 3072, 640 * 3840 + 3072);
    asm volatile("s_mov_b32 m0, %[dm]\n\tbuffer_load_dwordx4 %[r4], %[l16], %[rx], %[ro] offen\n\tbuffer_load_dword %[r1], %[l4], %[rx], %[ro] offen offset:1024\n\t"
                 "s_cmp_eq_u32 %[nt], 15\n\ts_cbranch_scc1 1f\n\t" QD4 "s_barrier\n\t" QD4 "s_branch 2f\n1:\n\ts_waitcnt vmcnt(0)\n2:"
                 : [r4] "=&v"(row), [r1] "=&v"(rowt), [sx] "+&s"(c.sx), [sw] "+&s"(c.sw)
                 : [x0] "v"(c.xo[0]), [x1] "v"(c.xo[1]), [w0] "v"(c.wo[0]), [w1] "v"(c.wo[1]), [rx] "s"(c.rx), [rw] "s"(c.rw), [dm] "s"(w * 4096),
                   [l16] "v"(lane * 16), [l4] "v"(lane * 4), [ro] "s"(ro), [nt] "s"(nt) : "memory", "scc");
}
template <int C> DI float rorsum(float v) { return v + __uint_as_float(__builtin_amdgcn_update_dpp(0u, __float_as_uint(v), 0x120 + C, 0xf, 0xf, true)); }
DI u32x4 row_stats(u32x4 row, unsigned rowt, unsigned T) {
    float s = 0.f, q = 0.f;
#pragma unroll
    for (int i = 0; i < 5; ++i) {
        const bf2v v = __builtin_bit_cast(bf2v, i < 4 ? row[i & 3] : rowt);
        s = __builtin_amdgcn_fdot2_f32_bf16(v, __builtin_bit_cast(bf2v, 0x3f803f80u), s, false);
        q = __builtin_amdgcn_fdot2_f32_bf16(v, v, q, false);
    }
    const f32x2 y = swap32(s, q);
    const float z = rorsum<1>(rorsum<2>(rorsum<4>(rorsum<8>(sum16(y[0] + y[1]))))) * (1.f / 640.f);
    const f32x2 v = swap32(z, z);
    const float ra = __builtin_amdgcn_rsqf(__builtin_fmaxf(v[1] - v[0] * v[0], 0.f) + 1e-5f);
    return u32x4{__float_as_uint(ra), __float_as_uint(-v[0] * ra), T, ~T};
}
#define MFQ(A, W, X) "v_mfma_f32_16x16x32_bf16 v[" A ":" A "+3], v[" W ":" W "+3], v[" X ":" X "+3], v[" A ":" A "+3]\n\t"
#define MFV(A, W, X) MFQ(A, X, W)
#define QTR(R, V, O) "ds_read_b64_tr_b16 v[" R ":" R "+1], %[" #V "] offset:" O "\n\t"
#define QRX(R, V, O) "ds_read_b128 v[" R ":" R "+3], %[" #V "] offset:" O "\n\t"
#define LGK(N) "s_waitcnt lgkmcnt(" #N ")\n\t"
#define QSEC(MF, P, Q, XA, WO, D1, D2, E1, E2) \
    "s_cmp_lt_u32 %[n], 8\n\t" LGK(4) MF("64", P, "68") QTR(Q, wa0, WO) MF("32", P, P "+8") QTR(Q "+2", wa0, WO "+1024") \
    "s_cbranch_scc0 1f\n\t" D1 "1:\n\t" MF("36", P, P "+12") QRX(Q "+8", XA, "0") LGK(5) MF("48", P "+4", P "+8") QRX(Q "+12", XA, "4096") \
    MF("52", P "+4", P "+12") QTR(Q "+4", wa1, WO) LGK(5) MF("40", P, P "+16") QTR(Q "+6", wa1, WO "+1024") \
    "s_cbranch_scc0 1f\n\t" D2 "1:\n\t" MF("56", P "+4", P "+16") QRX(Q "+16", XA, "8192") E1 MF("44", P, P "+20") QRX(Q "+20", XA, "12288") E2 MF("60", P "+4", P "+20")
#define QLDG "buffer_load_dwordx4 v[120:123], %[ga], %[rs], 0 offen sc1\n\t"
#define QADD(r) "v_add_u32 %[" #r "], %[d], %[" #r "]\n\t"
#define QRT(r, A, M) "v_mad_u32_u24 %[" #r "], %[hi], s" #M ", %[vlo]\n\tv_add_u32 %[" #r "], s" #A ", %[" #r "]\n\t"
#define QKV_ASM(MF, STRD) asm volatile( \
    "s_add_u32 m0, %[dm], 98304\n\ts_mov_b32 %[n], 0\n\tbuffer_store_dwordx4 %[gr], %[go], %[rs], 0 offen sc1\n\t" \
    Z8("32") Z8("40") Z8("48") Z8("56") Z4("64") "v_mov_b32 v68, 0x3f803f80\n\tv_mov_b32 v69, v68\n\tv_mov_b64 v[70:71], v[68:69]\n\t" \
    "s_waitcnt vmcnt(9)\n\ts_barrier\n\t" TSA("1") TSS("25") \
    QTR("72", wa0, "0") QTR("74", wa0, "1024") QRX("80", xa0, "0") QRX("84", xa0, "4096") QTR("76", wa1, "0") QTR("78", wa1, "1024") QRX("88", xa0, "8192") QRX("92", xa0, "12288") \
    QDX(x0, 0) QDX(x1, 1024) "s_branch 6f\n" \
    "3:\n\ts_cmp_gt_u32 %[n], 2\n\ts_cbranch_scc1 2f\n\ts_waitcnt vmcnt(9)\n\ts_branch 5f\n" \
    "2:\n\ts_cmp_eq_u32 %[n], 10\n\ts_cbranch_scc1 7f\n\ts_cmp_eq_u32 %[n], 7\n\ts_cbranch_scc1 2f\n\ts_waitcnt vmcnt(5)\n\ts_branch 5f\n" \
    "2:\n\t" QLDG QRT(x0, 60, 64) QRT(x1, 61, 65) QRT(w0, 62, 66) QRT(w1, 63, 67) \
    "s_mov_b64 s[44:45], s[52:53]\n\ts_mov_b32 s46, s54\n\ts_mov_b64 s[48:49], s[56:57]\n\ts_mov_b32 s50, s58\n\ts_mov_b32 %[sx], 0\n\ts_mov_b32 %[sw], 0\n" \
    "4:\n\ts_waitcnt vmcnt(8)\n5:\n\ts_barrier\n\t" \
    QSEC(MF, "96", "72", xa0, "0", QDX(x0, 0), QDX(x1, 1024), "", "") \
    "6:\n\t" \
    QSEC(MF, "72", "96", xa1, "16384", QDW(w0, 2048), QDW(w1, 3072), \
         QADV "s_bitcmp1_b32 0x42, %[n]\n\ts_cselect_b32 %[d], %[wr], %[up]\n\ts_add_u32 m0, m0, %[d]\n\ts_cmp_eq_u32 %[n], 4\n\ts_cselect_b32 %[d], %[wr], %[up]\n\ts_add_u32 %[n], %[n], 1\n\t", \
         QADD(xa0) QADD(xa1) QADD(wa0) QADD(wa1)) TSI \
    "s_bitcmp1_b32 0x686, %[n]\n\ts_cbranch_scc0 4b\n\ts_branch 3b\n" \
    "7:\n\ts_movk_i32 %[n], " SPINS "\n\t" LGK(4) MF("64", "96", "68") MF("32", "96", "104") "s_waitcnt vmcnt(4)\n" \
    "1:\n\tv_cmp_ne_u64 vcc, v[122:123], %[tag]\n\ts_cbranch_vccz 2f\n\t" QLDG "s_waitcnt vmcnt(0)\n\ts_sub_u32 %[n], %[n], 1\n\ts_cbranch_scc0 1b\n" \
    "2:\n\t" MF("36", "96", "108") "v_mul_f32 v120, %[sc], v120\n\tv_mul_f32 v121, %[sc], v121\n\t" LGK(2) MF("48", "100", "104") \
    "ds_write2_b32 %[pa], v120, v121 offset1:64\n\t" MF("52", "100", "108") "s_nop 1\n\tds_write_b128 %[ca], v[64:67]\n\t" LGK(2) \
    MF("40", "96", "112") STRD MF("56", "100", "112") MF("44", "96", "116") MF("60", "100", "116") \
    "s_waitcnt lgkmcnt(0)\n\ts_barrier\n\ts_nop 7" \
    : "=&{v[32:63]}"(A), "=&{v[64:67]}"(cs0), "=&{v[124:155]}"(S), [rx] "+{s[44:47]}"(c.rx), [rw] "+{s[48:51]}"(c.rw), [sx] "+&s"(c.sx), [sw] "+&s"(c.sw), \
      [x0] "+&v"(c.xo[0]), [x1] "+&v"(c.xo[1]), [w0] "+&v"(c.wo[0]), [w1] "+&v"(c.wo[1]), [xa0] "+&v"(e.xa0), [xa1] "+&v"(e.xa1), [wa0] "+&v"(e.wa0), [wa1] "+&v"(e.wa1), \
      [n] "=&s"(n), [d] "=&s"(d) PFIO \
    : "{s[52:55]}"(rt.ra), "{s[56:59]}"(rt.rb), [rs] "s"(rws), [dm] "s"(w * 4096), [wr] "s"(-131072), [up] "s"(32768), [sc] "s"(e.sc), [gr] "v"(gr), [go] "v"(e.go), [ga] "v"(e.ga), \
      "{s[60:75]}"(rt.t), [vlo] "v"(rt.vlo), [hi] "v"(rt.hi), [tag] "v"(tag), [pa] "v"(e.pa), [pr] "v"(e.pr), [ca] "v"(e.ca) \
    : "memory", "scc", "vcc", "v68", "v69", "v70", "v71", "v72", "v73", "v74", "v75", "v76", "v77", "v78", "v79", VCL80, VCL96, VCL112L, "v120", "v121", "v122", "v123" PFCL)
#define QS2(R, O) "ds_read2_b32 v[" #R ":" #R "+1], %[pr] offset0:" #O " offset1:" #O "\n\tds_read2_b32 v[" #R "+2:" #R "+3], %[pr] offset0:" #O "+64 offset1:" #O "+64\n\t"
struct QkvAdr { unsigned xa0, xa1, wa0, wa1, go, ga, pa, pr, ca, so[2], sb, sc; };
DI void qkv_setup(QkvAdr& e, int mt, int nt, int w, int lane) {
    const unsigned t = lane & 15, g = lane >> 4, T = w & 1, cb = w >> 1, g64 = 2 * mt + T;
    const bool isv = nt >= 10, isq = nt < 5;
    e.xa0 = 4 * T * 4096 + t * 128 + ((g ^ (t >> 1)) << 4); e.xa1 = e.xa0 ^ 64;
    e.wa0 = g * 4096 + 2048 + cb * 256 + ((lane >> 2) & 3) * 64 + ((g ^ T) & 1) * 32 + (lane & 3) * 8; e.wa1 = e.wa0 ^ 32;
    e.go = lane == 0 ? WS_XS + (mt * 128 + nt * 8 + w) * 16 : OOB;
    e.ga = WS_XS + (g64 * 64 + (g >> 1) * 32 + (isv ? 8 * (t >> 2) + 4 * (g & 1) + (t & 3) : 2 * t + (g & 1))) * 16;
    e.pa = L_SM + w * 2048 + lane * 4; e.pr = L_SM + w * 2048 + (isv ? g * 16 : t * 4); e.ca = L_SM + w * 2048 + 1024 + lane * 16;
    e.sc = isq ? __builtin_bit_cast(unsigned, 0.22360679774997896f * 1.4426950408889634f) : 0x3f800000u;
    e.sb = isq ? WS_Q + g64 * 2560 : WS_KV + g64 * 5120 + (isv ? 2560 : 0);
#pragma unroll
    for (int n = 0; n < 2; ++n) {
        if (isv) {
            const unsigned col = (nt - 10) * 128 + cb * 32 + 16 * (n ^ T) + t, head = (col * 3277) >> 16;
            e.so[n] = head * (163840 - 320) + g * 320 + col * 16;
        } else {
            const unsigned G = (isq ? nt : nt - 5) * 32 + cb * 8 + 4 * (n ^ T) + g, head = (G * 205) >> 10;
            e.so[n] = head * (isq ? 81920 - 2560 : 163840 - 2560) + G * 512 + t * 16;
        }
        PIN(e.so[n]);
    }
    PIN(e.xa0); PIN(e.xa1); PIN(e.wa0); PIN(e.wa1); PIN(e.go); PIN(e.ga); PIN(e.pa); PIN(e.pr); PIN(e.ca); PINS(e.sb); PINS(e.sc);
}
template <bool ISV>
DI void qkv_body(QkvSrc& c, QkvAdr& e, const Retile& rt, rsrc_t rws, u32x4 gr, u32x2 tag, int w PROFARG) {
    f32x32 A, S; f32x4 cs0; unsigned n, d;
    if (ISV) QKV_ASM(MFV, QRX("124", pr, "0") QRX("128", pr, "256") QRX("132", pr, "64") QRX("136", pr, "320") QRX("140", pr, "128") QRX("144", pr, "384") QRX("148", pr, "192") QRX("152", pr, "448"));
    else QKV_ASM(MFQ, QS2(124, 0) QS2(128, 16) QS2(132, 32) QS2(136, 48));
    TS(2);
    const f32x4 cs1 = ldsr<f32x4>(e.ca ^ 2048);
#pragma unroll
    for (int n = 0; n < 2; ++n) {
        const f32x4 cs = n ? cs1 : cs0;
#pragma unroll
        for (int q = 0; q < 2; ++q) {
            u32x4 o;
#pragma unroll
            for (int i = 0; i < 4; ++i) {
                const int k = 2 * q + (i >> 1), j = 2 * (i & 1), a = 16 * n + 4 * k + j;
                o[i] = ISV ? pk2(A[a] * S[8 * k + j] + S[8 * k + 4 + j] * cs[0], A[a + 1] * S[8 * k + j + 1] + S[8 * k + 5 + j] * cs[0])
                           : pk2(A[a] * S[4 * k] + S[4 * k + 2] * cs[j], A[a + 1] * S[4 * k + 1] + S[4 * k + 3] * cs[j + 1]);
            }
            asm volatile("buffer_store_dwordx4 %0, %1, %2, %3 offen offset:%4 sc1\n\ts_nop 1" :: "v"(o), "v"(e.so[n]), "s"(rws), "s"(e.sb), "n"(q * (ISV ? 1280 : 256)) : "memory");
        }
    }
    TS(58);
    asm volatile("s_waitcnt vmcnt(4)" ::: "memory");
    TS(59);
}

DI f32x16 qk_tile(u32x4 km, u32x2 kt, u32x4 qm, u32x2 qt, f32x16 c) {
    c = mfma32(asbf(km), asbf(qm), c);
    return __builtin_amdgcn_mfma_f32_32x32x8bf16_1k(__builtin_bit_cast(s4v, kt), __builtin_bit_cast(s4v, qt), c, 0, 0, 0);
}
DI float max16(const f32x16& s, float m) {
#pragma unroll
    for (int r = 0; r < 16; r += 2) m = __builtin_fmaxf(__builtin_fmaxf(s[r], s[r + 1]), m);
    return m;
}
DI f32x16 pv16(u32x4 v, f32x8 pr, f32x16 o) {
    return __builtin_amdgcn_smfmac_f32_32x32x32_bf16(asbf(v), __builtin_bit_cast(bf16v, pr), o, 0xDDDD, 0, 0);
}
#define EX4(P, S) "v_exp_f32 v[" #P "], v[" S "]\n\tv_exp_f32 v[" #P "+1], v[" S "+1]\n\tv_exp_f32 v[" #P "+2], v[" S "+2]\n\tv_exp_f32 v[" #P "+3], v[" S "+3]\n\t"
#define SMF(O, V, P) "v_smfmac_f32_32x32x32_bf16 v[" #O ":" #O "+15], v[" #V ":" #V "+3], v[" #P ":" #P "+7], %[idx]\n\t"
#define QKT(SN, AB) "v_mfma_f32_32x32x4_2b_bf16 v[" #SN ":" #SN "+31], v[156:157], v[152:153], 0 cbsz:1 abid:" #AB "\n\t"
#define QKA(SN) "v_mfma_f32_32x32x16_bf16 v[" #SN ":" #SN "+15], v[160:163], v[144:147], v[" #SN ":" #SN "+15]\n\t"
#define QKB(SN) "v_mfma_f32_32x32x16_bf16 v[" #SN "+16:" #SN "+31], v[160:163], v[148:151], v[" #SN "+16:" #SN "+31]\n\t"
#define STEP_(SC, VP, VC, L0, L1, L2, L3, L4) \
    L0 EX4(128, #SC) EX4(132, #SC "+4") SMF(112, VP, 136) EX4(136, #SC "+16") "s_waitcnt lgkmcnt(0)\n\t" SMF(96, 164, 128) EX4(140, #SC "+20") \
    L1 EX4(128, #SC "+8") SMF(112, 164, 136) L2 EX4(132, #SC "+12") L3 EX4(136, #SC "+24") SMF(96, VC, 128) L4 EX4(140, #SC "+28")
#define STEP(SC, SN, AB, VP, VC, KTRD, VRD, KRD, M1, M2) STEP_(SC, VP, VC, QKT(SN, AB) KTRD, QKA(SN) M1, VRD, QKB(SN) KRD, M2)
#define VR(VH, O) "s_mov_b64 exec, s[56:57]\n\tds_read_b128 v[164:167], %[av] offset:" O "\n\tds_read_b128 v[" #VH ":" #VH "+3], %[av] offset:" O "+640\n\ts_mov_b64 exec, -1\n\t"
#define KR(O) "ds_read_b64 v[160:161], %[a0] offset:" O "\n\tds_read_b64 v[162:163], %[a0] offset:" O "+512\n\t"
#define KT(O) "ds_read_b64 v[156:157], %[at] offset:" O "\n\t"
#define PRIO "s_cbranch_vccz 1f\n\ts_setprio 1\n1:\n\t"
#define PVC "s_mov_b32 vcc_lo, %[grp]\n\ts_mov_b32 vcc_hi, 0\n\t"
#define QUAD3(X, N, A0, B0, A1, B1, A2, B2) PRIO \
    STEP(32, 64, 1, 172, 168, KT(#X "+5120"), VR(172, #X "+1280"), KR(#X "+5120"), A0, B0) \
    STEP(64, 32, 0, 168, 172, "", VR(168, #X "+5120"), KR(#X "+5376"), A1, B1) \
    STEP(32, 64, 1, 172, 168, KT(#N), VR(172, #X "+6400"), KR(#N), A2, B2) "s_setprio 0\n\t"
#define QUAD(X, N, A0, B0, A1, B1, A2, B2, A3, B3) QUAD3(X, N, A0, B0, A1, B1, A2, B2) STEP(64, 32, 0, 168, 172, "", VR(168, #N), KR(#N "+256"), A3, B3) TSI
#define KVP_(D) "buffer_load_dwordx4 %[l16], s[40:43], s44 offen sc1 lds\n\ts_add_u32 m0, m0, " #D "\n\ts_add_u32 s44, s44, 4096\n\t"
#define KVP KVP_(4096)
#define LDQ(R, V, O) "buffer_load_dwordx2 v[" #R ":" #R "+1], %[" #V "], s[40:43], %[sq] offen offset:" #O " sc1\n\t"
#define STW(R, S) "buffer_store_dwordx4 v[" #R ":" #R "+3], %[vs], s[40:43], s" #S " offen sc1\n\t"
#define STM(V, S, O) "buffer_store_dwordx4 v[192:195], %[" #V "], s[40:43], " S " offen offset:" #O " sc1\n\t"
#define WOP(O) DM1("%[l16]", "s48", O)
#define WO5 "s_add_u32 m0, m0, 1024\n\ts_add_u32 s48, s48, 1024\n\t"
#define PK(R, O) LD1(#R, "%[l16]", "s52", #O)
#define PKA "s_add_u32 s52, s52, 2048\n\ts_cmp_eq_u32 s52, %[pe]\n\ts_cselect_b32 s52, %[pb], s52\n\t"
#define TCH(O) "buffer_load_dword v154, %[vt], s[40:43], 0 offen offset:" #O " sc1\n\t"
#define LDQ5 LDQ(144, va, 0) LDQ(146, va, 512) LDQ(148, vb, 0) LDQ(150, vb, 512) LDQ(152, vc, 2048)
#define RD1(W, V, O) "ds_read_b128 v[" W ":" W "+3], " V " offset:" O "\n\t"
#define VM_(N) "s_waitcnt vmcnt(" #N ")\n\t"
#define VM(N) VM_(N)
DI unsigned mod5(unsigned v) { return (0x21043210u >> (4 * v)) & 7; }
DI unsigned park0(int w) { return ((w * 35 + 2) >> 2) * 2048; }
struct Park { u32x32 a, b; u32x8 c; unsigned d; };

DI void phase_attn(const Params& p, const Retile& rt, rsrc_t rws, u32x2 tag, unsigned fq, int b, int w, int lane, int nt80, int ntg,
                   unsigned rv0, unsigned rv1, unsigned rsb, u32x4& r0, u32x2& r1, Park& pk PROFARG) {
    const int t = lane & 31, hh = lane >> 5, wl = w & 3, grp = w >> 2;
    const int head = (b & 7) * 4 + (b >> 6), qb = (b >> 3) & 7;
    const unsigned gb = grp * 61440, kvb = WS_KV + head * 163840;
    pk.a = retile_read(rt, b, w, lane);
    const unsigned l16 = lane * 16;
    unsigned va = hh * 1024 + grp * 256 + t * 8, dm = gb + wl * 1024, sq = WS_Q + head * 81920 + (qb * 4 + wl) * 2560;
    const int n0 = (head * 20 + ((lane & 16) ? 19 : 0)) >> 7;
    unsigned fo = WS_FQ + (w == 0 ? (5 + 5 * hh + n0) * 16 + (lane & 15) : n0 * 16 + 2 * qb + (lane & 1)) * 128;
    unsigned skv = kvb + grp * 81920 + wl * 1024;
    u32x8 qm; u32x2 qt;
    asm volatile("s_cmp_eq_u32 %[w], 2\n\ts_waitcnt vmcnt(0) lgkmcnt(0)\n\ts_barrier\n\ts_cbranch_scc0 1f\n\tbuffer_store_dwordx2 %[tag], %[fq], %[rs], 0 offen sc1\n1:"
                 : "+{v[176:207]}"(pk.a), "+v"(fo), "+v"(va), "+s"(skv), "+s"(sq), "+s"(dm) : [w] "s"(w), [tag] "v"(tag), [fq] "v"(fq), [rs] "s"(rws) : "memory", "scc");
    TS(3);
    SBAR();
    const unsigned a0 = gb + hh * 1024 + t * 8, at = gb + lane * 8 + 2048, av = gb + 2560 + hh * 320 + (t < 20 ? t : 0) * 16;
    const unsigned ones = t == 20 ? 0x3f803f80u : 0u;
    const unsigned wob = WS_WO + nt80 * 102400;
    u32x8 sa;
    sa[0] = skv;
    sa[2] = WS_O + b * 10240 + w * 1280;
    sa[1] = WS_B + qb * QBS + (head * 8 + w) * 1408;
    sa[3] = WS_B + qb * QBS + OFF_F + (head * 8 + w) * 3456;
    const unsigned mv = l16 + (lane < 16 ? sa[2] + 1024 : lane < 40 ? sa[1] + 768 : sa[3] + 2432);
    const unsigned ro = nt80 & 3, pb = WS_WGU + ntg * 143360, p0 = park0(w) + (((ntg & 15) * 5) >> 3) * 14336;
    sa[4] = wob + (grp + ro) * 20480 + wl * 5120; sa[5] = 122880 + w * 5120;
    sa[6] = wob + mod5(2 + 2 * grp + ro) * 20480 + wl * 5120; sa[7] = gb + 20480 + wl * 5120;
    u32x4 sb = {pb + p0 - ((int)p0 < 143360 ? 0 : 143360), rsb, wob + mod5(3 + ro) * 20480 + w * 2560, 40960u + w * 2560};
    const unsigned vt = lane < 17 ? WS_WD + nt80 * 276480 + qb * 34560 + (w * 17 + lane) * 256 : OOB;
    const unsigned vs = b < 135 ? l16 ^ (((lane >> 1) ^ (lane >> 3)) & 1) * 0xa0 : b * 8 + w < 1820 ? l16 : OOB;
    const unsigned vfs = (w == 2 && lane == 0) ? WS_FR + b * 8 : OOB, vfe = (b == 0 && w == 0 && lane == 0) ? WS_E : OOB, vfp = WS_FR + w * 256 + t * 8;
    const unsigned fg = (b * 205) >> 10;
    const unsigned vcs = (b < 135 && w < 2) ? WS_CS + ((b - fg * 5) * 3584 + fg * 128 + w * 64 + lane) * 4 : OOB;
    unsigned e0 = gb + wl * 3072 + l16, e1 = 61440 - gb + wl * 3072 + l16, ov = (t >> 4) * 20480 + (t & 15) * 16, ow = hh ? OOB : ov;
    unsigned ob = WS_O + (qb * 16 + wl * 4 + grp * 2) * 20480, oc = ob + (64 + (head >> 1)) * 256 + (head & 1) * 8;
    ov += hh * 256; ob += head * 512;
    f32x32 O;
    asm volatile(
        "ds_read2st64_b32 v[196:197], %[cp] offset1:2\n\tds_read2st64_b32 v[198:199], %[cp] offset0:4 offset1:6\n\t"
        Z8("96") Z8("104") Z8("112") Z8("120") Z8("136") "v_mov_b32 v164, %[one]\n\tv_mov_b32 v165, %[one]\n\tv_mov_b64 v[166:167], v[164:165]\n\tv_mov_b64 v[168:169], v[164:165]\n\t"
        "v_mov_b64 v[170:171], v[164:165]\n\tv_mov_b64 v[172:173], v[164:165]\n\tv_mov_b64 v[174:175], v[164:165]\n\tv_mov_b64 v[192:193], -1\n\tv_mov_b64 v[194:195], -1\n\t"
        "s_mov_b32 s56, 0xfffff\n\ts_mov_b32 s57, s56\n\ts_movk_i32 s58, " SPINS "\n\ts_mov_b32 m0, %[dm]\n\ts_cmp_gt_u32 %[w], 1\n\t" "s_cbranch_scc1 2f\n"
        "1:\n\tbuffer_load_dwordx2 v[32:33], %[fo], s[40:43], 0 offen sc1\n\ts_waitcnt vmcnt(0)\n\tv_cmp_ne_u64 vcc, v[32:33], %[tag]\n\t"
        "s_cbranch_vccz 2f\n\ts_sub_u32 s58, s58, 1\n\ts_cbranch_scc0 1b\n"
        "2:\n\ts_barrier\n\t" TSA("4") KVP LDQ5 KVP "s_barrier\n\t" KVP KVP
        LGK(0) "v_add_f32 v196, v196, v197\n\tv_add_f32 v198, v198, v199\n\tv_add_f32 v196, v196, v198\n\ts_movk_i32 s58, " SPINS "\n\t"
        "s_waitcnt vmcnt(2)\n\ts_barrier\n\t" TSA("5")
        KT("0") KR("0") VR(168, "0") "s_waitcnt lgkmcnt(4)\n\t" QKT(32, 0) "s_waitcnt lgkmcnt(2)\n\ts_nop 7\n\ts_nop 7\n\ts_nop 2\n\t" QKA(32) QKB(32) KR("256")
        PVC "s_waitcnt vmcnt(0)\n\ts_barrier\n\ts_nop 6\n\t" TSS("17") TSF("17")
        QUAD(0, 10240, KVP, KVP STW(176, 68), KVP, STW(180, 69) "buffer_store_dword v196, %[vcs], s[40:43], 0 offen sc1\n\t", KVP, STW(184, 70), KVP, STW(188, 71))
        "s_waitcnt vmcnt(8)\n\ts_barrier\n\t"
        QUAD(10240, 20480, KVP, STM(l16, "s46", 0), KVP, STM(mv, "0", 0), KVP, "", KVP, "")
        "s_waitcnt vmcnt(2)\n\ts_barrier\n\tbuffer_store_dwordx2 %[tag], %[vfs], s[40:43], 0 offen sc1\n\t"
        QUAD(20480, 30720, KVP, STM(l16, "s45", 0), KVP_(-57344), KVP, KVP, STM(l16, "s47", 0), KVP, KVP)
        QUAD(30720, 40960, KVP, "", "buffer_load_dwordx2 v[198:199], %[vfp], s[40:43], 0 offen sc1\n\t", STM(l16, "s47", 1024), "", STM(l16, "s47", 2048), "", "")
        "s_mov_b32 m0, s49\n\ts_waitcnt vmcnt(2)\n"
        "3:\n\tv_cmp_ne_u64 vcc, v[198:199], %[tag]\n\ts_cbranch_vccz 4f\n\t"
        "buffer_load_dwordx2 v[198:199], %[vfp], s[40:43], 0 offen sc1\n\ts_sub_u32 s58, s58, 1\n\ts_waitcnt vmcnt(0)\n\ts_cbranch_scc0 3b\n"
        "4:\n\t" PVC "s_barrier\n\t" TSA("6") "buffer_store_dwordx2 %[tag], %[vfe], s[40:43], 0 offen sc1\n\t"
        QUAD(40960, 51200, PK(176, 0), PK(180, 1024) PKA, PK(184, 0), PK(188, 1024) PKA, PK(192, 0), PK(196, 1024) PKA, PK(200, 0), PK(204, 1024) PKA)
        QUAD(51200, 0, PK(208, 0), PK(212, 1024) PKA, PK(216, 0), PK(220, 1024) PKA, PK(224, 0), PK(228, 1024) PKA, PK(232, 0), PK(236, 1024) PKA)
        QUAD(0, 10240, PK(240, 0), PK(244, 1024), WOP(0) WO5, WOP(0) TCH(0), WOP(1024), WOP(2048), WOP(3072) "s_mov_b32 m0, s51\n\ts_mov_b32 s48, s50\n\t", WOP(0) WO5)
        QUAD3(10240, 0, WOP(0) TCH(128), WOP(1024), WOP(2048), WOP(3072),
              "buffer_load_dwordx4 v[248:251], %[rv0], %[rx], s53 offen\n\t", "buffer_load_dwordx2 v[252:253], %[rv1], %[rx], s53 offen\n\t")
        STEP_(64, 168, 172, "", "", "", "", "") TSI
        "s_mov_b32 m0, s55\n\ts_waitcnt vmcnt(24)\n\ts_nop 0\n\t" SMF(112, 172, 136) "s_barrier\n\t"
        DM1("%[l16]", "s54", 0) DM1("%[l16]", "s54", 1024) "s_mov_b32 exec_hi, 0\n\t" DM1("%[l16]", "s54", 2048) "s_mov_b32 exec_hi, -1\n\ts_nop 7\n\ts_nop 7"
        : "=&{v[96:127]}"(O), "=&{v[144:151]}"(qm), "=&{v[152:153]}"(qt), "+{v[176:207]}"(pk.a), "=&{v[208:239]}"(pk.b), "=&{v[240:247]}"(pk.c), "=&{v[248:251]}"(r0), "=&{v[252:253]}"(r1), "=&{v154}"(pk.d),
          "+{s[44:51]}"(sa), "+{s[52:55]}"(sb), "+v"(e0), "+v"(e1), "+v"(ov), "+v"(ow), "+s"(ob), "+s"(oc) PFIO
        : "{s[40:43]}"(rws), "{s[60:75]}"(rt.t), [a0] "v"(a0), [at] "v"(at), [av] "v"(av), [l16] "v"(l16), [tag] "v"(tag), [vcs] "v"(vcs), [fo] "v"(fo), [sq] "s"(sq),
          [cp] "v"(L_CP + (w & 1) * 256 + lane * 4), [mv] "v"(mv), [vs] "v"(vs), [vt] "v"(vt), [pb] "s"(pb), [pe] "s"(pb + 143360), [one] "v"(ones), [idx] "v"(0xDDDD),
          [va] "v"(va), [vb] "v"(va ^ 256), [vc] "v"((lane * 8) ^ (grp * 256)), [vfs] "v"(vfs), [vfe] "v"(vfe), [vfp] "v"(vfp), [rv0] "v"(rv0), [rv1] "v"(rv1),
          [dm] "s"(dm), [w] "s"(w), [grp] "s"(grp), [rx] "s"(mkrs(p.x, 2048 * 1280))
        : "memory", "scc", "vcc", "s56", "s57", "s58", VCL32, VCL48, VCL64, VCL80, VCL128, "v140", "v141", "v142", "v143", "v156", "v157", VCL160 PFCL);
    TS(7);
    float o[12];
#pragma unroll
    for (int i = 0; i < 3; ++i) ldsw<f32x4>(e0 + i * 1024, f32x4{O[16 + 4 * i], O[17 + 4 * i], O[18 + 4 * i], O[19 + 4 * i]});
    LBAR();
#pragma unroll
    for (int i = 0; i < 3; ++i) {
        const f32x4 v = ldsr<f32x4>(e1 + i * 1024);
#pragma unroll
        for (int k = 0; k < 4; ++k) o[4 * i + k] = O[4 * i + k] + v[k];
    }
    {
        f32x2 z = {0.f, 0.f};
#pragma unroll
        for (int r = 0; r < 12; r += 2) z += f32x2{o[r], o[r + 1]} * 0.f;
        const float l = (hh ? o[8] : 1.f) + (z[0] + z[1]);
        const bool bad = !(l < 1.8446744e19f && l > 8.6736174e-19f);
        if (__builtin_expect(anyl(bad), 0)) {
            float m = -__builtin_inff();
            const unsigned ko = hh * 1024 + t * 8, vo = 2560 + hh * 320 + (t < 20 ? t : 0) * 16;
            const u32x4 qa = {qm[0], qm[1], qm[2], qm[3]}; const u32x2 qe = {hh ? 0u : qt[0], hh ? 0u : qt[1]};
            f32x16 c = {}, A = {};
#pragma unroll 1
            for (int it = 0; it < 128; ++it) {
                const unsigned sb = kvb + ((it & 63) >> 1) * 5120;
                const u32x2 k0 = ld8c(rws, ko, sb + (it & 1) * 256), k1 = ld8c(rws, ko + 512, sb + (it & 1) * 256), k2 = ld8c(rws, t * 8 + 2048, sb + (it & 1) * 256);
                f32x16 s = qk_tile(u32x4{k0[0], k0[1], k1[0], k1[1]}, k2, qa, qe, c);
                if (it < 64) {
                    m = max16(s, m);
                    if (it == 63) {
                        const f32x2 mm = swap32(m, m);
                        m = __builtin_fmaxf(mm[0], mm[1]);
#pragma unroll
                        for (int r = 0; r < 16; ++r) c[r] = -m;
                    }
                } else {
                    u32x4 v0 = ld16c(rws, vo, sb + (it & 1) * 1280), v1 = ld16c(rws, vo + 640, sb + (it & 1) * 1280);
#pragma unroll
                    for (int i = 0; i < 4; ++i) { v0[i] = t < 20 ? v0[i] : ones; v1[i] = t < 20 ? v1[i] : ones; }
#pragma unroll
                    for (int r = 0; r < 16; ++r) s[r] = __builtin_amdgcn_exp2f(s[r]);
                    A = pv16(v0, __builtin_shufflevector(s, s, 0, 1, 2, 3, 4, 5, 6, 7), A);
                    A = pv16(v1, __builtin_shufflevector(s, s, 8, 9, 10, 11, 12, 13, 14, 15), A);
                }
            }
#pragma unroll
            for (int r = 0; r < 12; ++r) o[r] = A[r];
        }
    }
    const float inv = __builtin_amdgcn_rcpf(swap32(o[8], o[8])[1]);
    st16(rws, ov, ob, u32x4{pk2(o[0] * inv, o[1] * inv), pk2(o[2] * inv, o[3] * inv), pk2(o[4] * inv, o[5] * inv), pk2(o[6] * inv, o[7] * inv)});
    st8(rws, ow, oc, u32x2{pk2(o[8] * inv, o[9] * inv), pk2(o[10] * inv, o[11] * inv)});
}

constexpr unsigned L_CS = 14336;
#define MF16(A, W, B) "v_mfma_f32_16x16x32_bf16 v[" A ":" A "+3], v[" W ":" W "+3], v[" B ":" B "+3], v[" A ":" A "+3]\n\t"
#define MF0(A, W, B) "v_mfma_f32_16x16x32_bf16 v[" A ":" A "+3], v[" W ":" W "+3], v[" B ":" B "+3], 0\n\t"
#define RD5(W, V, O) RD1(W, V, O) RD1(W "+4", V, O "+1024") RD1(W "+8", V, O "+2048") RD1(W "+12", V, O "+3072") RD1(W "+16", V, O "+4096")
#define WR1(V, R, O) "ds_write_b128 " V ", v[" R ":" R "+3] offset:" O "\n\t"
#define MX3(B) "v_max3_u32 %[t], v[" B "], v[" B "+1], v[" B "+2]\n\t"
#define MX2(B) "v_max3_u32 %[t], v[" B "], v[" B "+1], %[t]\n\t"
#define MXE(B) "v_max_u32 %[t], v[" B "], %[t]\n\tv_cmp_eq_u32 vcc, -1, %[t]\n\ts_and_b64 vcc, vcc, s[60:61]\n\t"
#define CK4(B) MX3(B) MXE(B "+3")
#define CK8(B) MX3(B) MX2(B "+3") MX2(B "+5") MXE(B "+7")
#define CK16(B) MX3(B) MX2(B "+3") MX2(B "+5") MX2(B "+7") MX2(B "+9") MX2(B "+11") MX2(B "+13") MXE(B "+15")
#define CK28(B) MX3(B) MX2(B "+3") MX2(B "+5") MX2(B "+7") MX2(B "+9") MX2(B "+11") MX2(B "+13") MX2(B "+15") MX2(B "+17") MX2(B "+19") MX2(B "+21") MX2(B "+23") MX2(B "+25") MXE(B "+27")
#define SPIN0 "s_movk_i32 s59, " SPINS "\n\ts_mov_b64 s[60:61], -1\n\t"
#define SPIN1 "s_sub_u32 s59, s59, 1\n\ts_cselect_b64 s[60:61], 0, s[60:61]\n\t"
#define UNP(D, S) "v_lshlrev_b32 v[" #D "], 16, v" #S "\n\tv_and_b32 v[" #D "+1], 0xffff0000, v" #S "\n\t"
#define PKADD(D, S) "v_pk_add_f32 v[" D ":" D "+1], v[" D ":" D "+1], v[" S ":" S "+1]\n\t"
#define CVT(D, S) "v_cvt_pk_bf16_f32 v[" D "], v[" S "], v[" S "+1]\n\t"
#define WON(n, W, B, R) MF16("72+4*" #n, W "+4*" #n, B) R
#define WOK(W, B, V, O) WON(0, W, B, RD1(W, V, O)) WON(1, W, B, RD1(W "+4", V, O "+1024")) WON(2, W, B, RD1(W "+8", V, O "+2048")) WON(3, W, B, RD1(W "+12", V, O "+3072")) WON(4, W, B, RD1(W "+16", V, O "+4096"))
#define WOE(W, B) WON(0, W, B, "") WON(1, W, B, "") WON(2, W, B, "") WON(3, W, B, "") WON(4, W, B, "")
#define WOL(B, S) LD1(#B, "%[l16]", #S, "0") LD1(#B "+4", "%[l16]", #S, "1024")
#define WOC(p, N, B) "1" #p ":\n\t" VM(N) CK8(#B) "s_cbranch_scc1 2" #p "f\n\t" TSI
#define WOT(p) "2" #p ":\n\ts_call_b64 s[56:57], 8f\n\ts_branch 1" #p "b\n"
#define WOR(B, S) CK8(#B) "s_cbranch_scc0 1f\n\ts_mov_b64 exec, vcc\n\t" WOL(B, S) "s_mov_b64 exec, -1\n1:\n\t"
#define PW(R, O) WR1("%[pw]", #R, #O)
#define PW4(R, O) PW(R, O) PW(R+4, O+1024) PW(R+8, O+2048) PW(R+12, O+3072)
#define PX4(R, O) WR1("%[t]", #R, #O) WR1("%[t]", #R "+4", #O "+1024") WR1("%[t]", #R "+8", #O "+2048") WR1("%[t]", #R "+12", #O "+3072")
#define CSL(R) "buffer_load_dword " R ", %[ve], s[40:43], s49 offen sc1\n\ts_add_u32 s49, s49, 14336\n\t"
DI void phase_wo(rsrc_t rws, int w, unsigned lane, int mt, int nt, int ntg, u32x4& r0, u32x2& r1, Park& pk PROFARG) {
    const unsigned tg = w & 3, kh = w >> 2, t = lane & 15, g = lane >> 4, l16 = lane * 16, la = kh * 5120 + l16, e = w * 64 + lane, ro = nt & 3;
    const unsigned ab = WS_O + (mt * 4 + tg) * 20480 + kh * 10240, hb = WS_B + (mt >> 2) * QBS + nt * 256, tk = (mt & 3) * 4 + tg;
    u32x8 sa = {ab + ro * 2048, ab + (ro + 1) * 2048, ab + mod5(ro + 2) * 2048, ab + mod5(ro + 3) * 2048, ab + mod5(ro + 4) * 2048, WS_CS + ntg * 448, hb + tk * 20480, 0};
    const unsigned ex = tg * 5120 + kh * 2048 + l16;
    unsigned tmp;
    asm volatile(
        CSL("v92") CSL("v93") CSL("v94") CSL("v95") CSL("%[t]") SPIN0 VM(7) "s_barrier\n\t"
        RD5("96", "%[wa]", "0") RD5("116", "%[wa]", "10240")
        UNP(72, 248) UNP(74, 249) UNP(76, 250) UNP(78, 251) Z8("80") UNP(88, 252) UNP(90, 253)
        VM(5) TSA("20") TSS("45") WOL(32, s44) WOL(40, s45) WOL(48, s46) WOL(56, s47) WOL(64, s48) RD5("136", "%[wa]", "20480") RD5("156", "%[wa]", "30720")
        "s_waitcnt vmcnt(10) lgkmcnt(0)\n\tv_add_f32 v92, v92, v93\n\tv_add_f32 v94, v94, v95\n\tv_add_f32 v92, v92, %[t]\n\tv_add_f32 v92, v92, v94\n\tds_write_b32 %[vl], v92\n"
        WOC(0, 8, 32) TSA("8") WOK("96", "32", "%[wb]", "0") WOK("116", "36", "%[wb]", "10240")
        WOC(1, 6, 40) WOK("136", "40", "%[wb]", "20480") WOK("156", "44", "%[wb]", "30720")
        WOC(2, 4, 48) LGK(10) WOK("96", "48", "%[wc]", "0") WOK("116", "52", "%[wc]", "10240")
        WOC(3, 2, 56) LGK(10) WOE("136", "56") WOE("156", "60")
        WOC(4, 0, 64) LGK(0) WOE("96", "64") WOE("116", "68") TSA("9")
        "s_cmp_eq_u32 %[kh], 0\n\ts_nop 5\n\t" WR1("%[xw]", "80", "0") WR1("%[xw]", "84", "1024") "s_cbranch_scc1 1f\n\t" WR1("%[x4]", "88", "0")
        "1:\n\ts_waitcnt lgkmcnt(0)\n\ts_barrier\n\t" RD1("80", "%[xr]", "0") RD1("84", "%[xr]", "1024") RD1("92", "%[x4]", "0")
        LGK(2) PKADD("72", "80") PKADD("74", "82") LGK(1) PKADD("76", "84") PKADD("78", "86") CVT("248", "72") CVT("249", "74") CVT("250", "76") CVT("251", "78")
        "buffer_store_dwordx4 v[248:251], %[h0], s[40:43], s50 offen sc1\n\t" LGK(0) "s_cbranch_scc0 9f\n\t"
        PKADD("88", "92") PKADD("90", "94") CVT("252", "88") CVT("253", "90") "buffer_store_dwordx2 v[252:253], %[h1], s[40:43], s50 offen sc1\n\ts_branch 9f\n"
        WOT(0) WOT(1) WOT(2) WOT(3) WOT(4)
        "8:\n\t" VM(0) WOR(32, s44) WOR(40, s45) WOR(48, s46) WOR(56, s47) WOR(64, s48) VM(0) SPIN1 "s_setpc_b64 s[56:57]\n9:\n\t"
        PW4(176, 0) PW4(192, 4096) PW(208, 8192) PW(212, 9216)
        : "+{v[248:251]}"(r0), "+{v[252:253]}"(r1), "+{v[176:207]}"(pk.a), "+{v[208:239]}"(pk.b), "+{v[240:247]}"(pk.c), "+{s[44:51]}"(sa), [t] "=&v"(tmp) PFIO
        : "{s[40:43]}"(rws), [l16] "v"(l16), [wa] "v"(la + 122880), [wb] "v"(la + 20480), [wc] "v"(la + 81920), [kh] "s"(kh), [pw] "v"(20480 + park0(w) + l16),
          [ve] "v"(e < 112 ? (e ^ (((lane >> 1) ^ (lane >> 3)) & 1) * 10) * 4 : OOB), [vl] "v"(L_CS + (e < 112 ? e : 112) * 4),
          [xw] "v"(tg * 5120 + 2048 - kh * 2048 + l16), [xr] "v"(ex), [x4] "v"((tg == 2 ? 61440 : tg * 5120 + 4096) + l16),
          [h0] "v"((2 * g + kh) * 2048 + t * 16), [h1] "v"((8 + (g >> 1)) * 2048 + t * 16 + (g & 1) * 8)
        : "memory", "scc", "vcc", "s56", "s57", "s59", "s60", "s61", VCL32, VCL48, VCL64, VCL80, VCL96, VCL112, VCL128, VCL140, VCL160 PFCL);
    TS(10);
    float s = 0.f, q = 0.f;
#pragma unroll
    for (int k = 0; k < 6; ++k) {
        const bf2v v = __builtin_bit_cast(bf2v, k < 4 ? r0[k & 3] : r1[k & 1]);
        s = __builtin_amdgcn_fdot2_f32_bf16(v, __builtin_bit_cast(bf2v, 0x3f803f80u), s, false);
        q = __builtin_amdgcn_fdot2_f32_bf16(v, v, q, false);
    }
    const f32x2 y = swap32(s, q);
    const float z = sum16(y[0] + y[1]);
    const f32x2 v = swap32(z, z);
    st8(rws, g == 0 ? t * 16 + kh * 8 : OOB, hb + OFF_HS + tk * 2048, u32x2{__float_as_uint(v[0]), __float_as_uint(v[1])});
}

#define GW0 LGK(4)
#define GW1
#define GW2
#define GW3 LGK(5)
#define GW4
#define GW5 LGK(5)
#define GW6
#define GUN(MF, n, H, R, X) GW##n MF("140+8*" #n, "112+4*" #n, H) X MF("144+8*" #n, "112+4*" #n, H "+8") R
#define GUK(MF, H, V, O, D, X0, X1, X2, X3, X4, X5, X6) \
    GUN(MF, 0, H, RD1("112", V, O), X0) GUN(MF, 1, H, RD1("116", V, O "+" D), X1) GUN(MF, 2, H, RD1("120", V, O "+2*" D), X2) GUN(MF, 3, H, RD1("124", V, O "+3*" D), X3) \
    GUN(MF, 4, H, RD1("128", V, O "+4*" D), X4) GUN(MF, 5, H, RD1("132", V, O "+5*" D), X5) GUN(MF, 6, H, RD1("136", V, O "+6*" D), X6)
#define LH0(H, S) LD1(#H, "%[l16]", #S, "0")
#define LH1(H, S) LD1(#H "+4", "%[l16]", #S, "1024")
#define LH2(H, S) LD1(#H "+8", "%[lb]", #S, "0")
#define LH3(H, S) LD1(#H "+12", "%[lb]", #S, "1024")
#define LDH(H, S) LH0(H, S) LH1(H, S) LH2(H, S) LH3(H, S)
#define LH(H, S) LH0(H, S), LH1(H, S), LH2(H, S), LH3(H, S)
#define LS(R, O) LD1(#R, "%[l16]", "s54", #O)
#define LDST LS(32, 0) LS(36, 1024) LS(40, 2048) LS(44, 3072)
#define WL(R, O) LD1(#R, "%[l16]", "s52", #O)
#define WA "s_add_u32 s52, s52, 4096\n\t"
#define W15 WL(196, 0) WL(200, 1024) WL(204, 2048) WL(208, 3072) WA WL(212, 0) WL(216, 1024) WL(220, 2048) WL(224, 3072) WA WL(228, 0) WL(232, 1024) WL(236, 2048) WL(240, 3072) WA \
    WL(244, 0) WL(24, 1024) WL(28, 2048)
#define WEND "v_add_u32 %[lb], 4096, %[l16]\n\t" WL(32, 3072) LD1("36", "%[lb]", "s52", "0") LD1("40", "%[lb]", "s52", "1024") LD1("44", "%[z4]", "s52", "2048") LD1("96", "%[z4]", "s52", "3072") WA \
    LD1("100", "%[z4]", "s52", "0") LD1("104", "%[z4]", "s52", "1024") LD1("108", "%[z4]", "s52", "2048")
#define CKS(N, B, s) VM(N) MX3(B), MX2(B "+3"), MX2(B "+5"), MX2(B "+7"), MX2(B "+9"), MX2(B "+11"), MX2(B "+13") MXE(B "+15") "s_cbranch_scc1 4" #s "f\n3" #s ":\n\t"
#define GUK_(...) GUK(__VA_ARGS__)
#define GUS(MF, s, H, O1, PRE, O2, HN, N, ...) GUK_(MF, #H, "%[wa]", O1, "1024", __VA_ARGS__) PRE GUK_(MF16, #H "+4", "%[wa]", O2, "1024", CKS(N, #HN, s)) TSI
#define REDO(s, L, CK) "4" #s ":\n\ts_mov_b64 exec, vcc\n\t" L "s_mov_b64 exec, -1\n\t" VM(0) SPIN1 CK "s_cbranch_scc1 4" #s "b\n\ts_branch 3" #s "b\n"
#define WADV "v_add_u32 %[wa], 0xfc00, %[wa]\n\t"
#define HNX(A, B) "s_add_u32 s" #B ", s" #A ", 2048\n\ts_cmp_eq_u32 s" #B ", s55\n\ts_cselect_b32 s" #B ", %[hb], s" #B "\n\t"
#define SW32(A, B) "v_permlane32_swap_b32 v[" A "], v[" B "]\n\t"
#define SW16(A, B) "v_permlane16_swap_b32 v[" A "], v[" B "]\n\t"
#define EPF(A, C, T) "v_pk_fma_f32 v[" A ":" A "+1], v[" C ":" C "+1], v[16+4*" #T ":17+4*" #T "], v[" A ":" A "+1]\n\t"
#define EPT(A, C, T, D) EPF(A, C, T) EPF(A "+2", C "+2", T) "v_max_f32 v[" A "], 0, v[" A "]\n\tv_max_f32 v[" A "+1], 0, v[" A "+1]\n\t" \
    "v_pk_mul_f32 v[" A "+2:" A "+3], v[" A "+2:" A "+3], v[18+4*" #T ":19+4*" #T "]\n\t" \
    "v_pk_mul_f32 v[" A ":" A "+1], v[" A ":" A "+1], v[" A "+2:" A "+3]\n\t" CVT(D, A)
#define EPN(n, D) EPT("140+8*" #n, "112+4*" #n, 0, D) EPT("144+8*" #n, "112+4*" #n, 1, D "+1")
#define EP2(n, m, D, N) LGK(N) EPN(n, D) EPN(m, D "+2") SW32(D, D "+1") "s_nop 0\n\t" SW32(D "+2", D "+3") \
    "buffer_store_dwordx4 v[" D ":" D "+3], %[f0], s[40:43], s53 offen sc1\n\ts_add_u32 s53, s53, 15872\n\t"
#define LF0(F, S) LD1(F, "%[l16]", S, "0")
#define LF1(F, S) LD1(F "+4", "%[l16]", S, "1024")
#define LF2(F, S) LD1(F "+8", "%[l16]", S, "2048")
#define LF3(F, S) LD1(F "+12", "%[l16]", S, "3072")
#define LF4(F, S) LD1(F "+16", "%[lb]", S, "0")
#define LF5(F, S) LD1(F "+20", "%[lb]", S, "1024")
#define LF6(F, S) LD1(F "+24", "%[z4]", S, "2048")
#define LDF(F, S) LF0(F, S) LF1(F, S) LF2(F, S) LF3(F, S) LF4(F, S) LF5(F, S) LF6(F, S)
#define DK(MF, F, a, b, c, d, e) MF("168", #a, F) MF("172", #b, F) MF("176", #c, F) MF("180", #d, F) MF("184", #e, F)
#define DT(F, X0, X1, X2, X3, X4, X5, X6) DK(MF0, F, 196, 200, 204, 208, 212) X0 DK(MF16, F "+4", 216, 220, 224, 228, 232) X1 DK(MF16, F "+8", 236, 240, 244, 24, 28) X2 \
    DK(MF16, F "+12", 48, 52, 56, 60, 64) X3 DK(MF16, F "+16", 68, 72, 76, 80, 84) X4 DK(MF16, F "+20", 88, 92, 32, 36, 40) X5 DK(MF16, F "+24", 44, 96, 100, 104, 108) X6 TSI
#define DTL(F, S) DT(F, LF0(F, S), LF1(F, S), LF2(F, S), LF3(F, S), LF4(F, S), LF5(F, S), LF6(F, S))
#define DTE(F) DT(F, "", "", "", "", "", "", "")
#define DW(S) "v_add_u32 %[wa], " S ", %[l16]\n\t" WR1("%[wa]", "168", "0") WR1("%[wa]", "172", "1024") WR1("%[wa]", "176", "2048") WR1("%[wa]", "180", "3072") WR1("%[wa]", "184", "4096")
#define DC(N, F, s) VM(N) CK28(F) "s_cbranch_scc1 4" #s "f\n3" #s ":\n\t"
#define KSW 0
#define TSWP 0
DI void phase_ffn(rsrc_t rws, int w, unsigned lane, int mt, int nt, int qg, int ntg, const Park& pk PROFARG) {
    const unsigned t = lane & 15, g = lane >> 4, l16 = lane * 16, qbb = WS_B + qg * QBS, wg = (w & 3) * 2 + (w >> 2), hb = qbb + wg * 40960, ks = w ^ (KSW), k0 = 7 * ks - (ks == 7);
    const unsigned fb = WS_B + (mt >> 2) * QBS + OFF_F + (mt & 3) * 4 * 55296 + k0 * 1024, fv = (g >> 1) * 55296 + t * 16;
    u32x8 sa; u32x4 sb = {WS_WD + nt * 276480 + k0 * 5120, qbb + OFF_F + wg * 110592 + ntg * 256, qbb + OFF_HS + wg * 4096, hb + 20480};
#pragma unroll
    for (int i = 0; i < 4; ++i) { sa[i] = fb + (i ^ (TSWP)) * 55296; sa[4 + i] = (i ^ (TSWP)) * 40960 + ks * 5120; }
    unsigned wa = 20480 + l16, h0 = hb + (((ntg & 15) * 5) >> 3) * 2048, tmp = 20480 + park0(w) + l16, lb = l16 + 20480;
    u32x32 da = pk.a, db = pk.b; u32x8 dc = pk.c;
    asm volatile(
        PX4(216, 10240) PX4(232, 14336) SPIN0 HNX(64, 65) HNX(65, 66) HNX(66, 67) HNX(67, 68) HNX(68, 69) HNX(69, 70) HNX(70, 71) HNX(71, 72) HNX(72, 73)
        "s_cmp_eq_u32 %[ng], 31\n\ts_cbranch_scc1 7f\n\t" VM(1) TSA("21") LDH(32, s64) LDH(48, s65) LGK(0) "s_barrier\n\t"
        RD5("112", "%[wa]", "0") RD1("132", "%[wa]", "5120") RD1("136", "%[wa]", "6144")
        "1:\n\t" VM(4) CK16("32") "s_cbranch_scc1 2f\n\t" TSA("11") TSS("35") LDH(64, s66) LDH(80, s67) LDH(96, s68)
        GUS(MF0, 0, 32, "7168", "", "14336", 48, 12, "", "", "", "", "", "", "")
        GUS(MF16, 1, 48, "21504", "", "28672", 64, 12, LH(32, s69), "", "", "")
        GUS(MF16, 2, 64, "35840", "", "43008", 80, 13, LH(48, s70), WL(196, 0), "", "")
        GUS(MF16, 3, 80, "50176", "", "57344", 96, 15, LH(64, s71), WL(200, 1024), WL(204, 2048), "")
        WADV
        GUS(MF16, 4, 96, "0", "", "7168", 32, 18, LH(80, s72), WL(208, 3072) WA, WL(212, 0), WL(216, 1024))
        GUS(MF16, 5, 32, "14336", "", "21504", 48, 21, LH(96, s73), WL(220, 2048), WL(224, 3072) WA, WL(228, 0))
        GUS(MF16, 6, 48, "28672", "", "35840", 64, 23, LS(32, 0), LS(36, 1024), LS(40, 2048), LS(44, 3072), WL(232, 1024), WL(236, 2048), WL(240, 3072) WA)
        GUS(MF16, 7, 64, "43008", "", "50176", 80, 24, WL(244, 0), WL(24, 1024), WL(28, 2048), WL(48, 3072) WA, WL(52, 0), WL(56, 1024), WL(60, 2048))
        GUS(MF16, 8, 80, "57344", WADV, "0", 96, 21, WL(64, 3072) WA, WL(68, 0), WL(72, 1024), WL(76, 2048), "", "", "")
        GUK_(MF16, "96", "%[wa]", "7168", "1024", CKS(14, "32", 9))
        "v_lshrrev_b32 %[wa], 4, %[l16]\n\tv_and_b32 %[wa], 48, %[wa]\n\t"
        GUK(MF16, "100", "%[wa]", "14336", "64",
            WL(80, 3072) WA PKADD("32", "34") PKADD("36", "38") PKADD("40", "42") PKADD("44", "46"),
            WL(84, 0) PKADD("32", "36") PKADD("40", "44"),
            WL(88, 1024) SW32("32", "40") SW32("33", "41") PKADD("32", "40") "v_mov_b64 v[34:35], v[32:33]\n\t",
            WL(92, 2048) SW16("32", "34") SW16("33", "35") PKADD("32", "34") "v_mul_f32 v16, 0xbacccccd, v32\n\t",
            "v_mul_f32 v33, 0x3acccccd, v33\n\tv_fma_f32 v33, -v16, v16, v33\n\tv_max_f32 v33, 0, v33\n\tv_add_f32 v33, 0x3727c5ac, v33\n\t",
            "v_rcp_f32 v18, v33\n\tv_mov_b32 v20, v16\n\tv_mov_b32 v22, v18\n\t",
            SW32("16", "20") SW32("18", "22") "v_mov_b32 v17, v16\n\tv_mov_b32 v21, v20\n\tv_mov_b32 v19, v18\n\tv_mov_b32 v23, v22\n\t")
        TSA("12") WEND EP2(0, 1, "140", 5) EP2(2, 3, "156", 3) EP2(4, 5, "172", 1) LGK(0) EPN(6, "188") "s_nop 1\n\t" SW32("188", "189")
        "buffer_store_dwordx2 v[188:189], %[f1], s[40:43], s53 offen sc1\n"
        TSA("22") "6:\n\ts_barrier\n\t" TSA("23") LDF("112", "s44") LDF("140", "s45")
        DC(7, "112", 13) TSA("13") TSS("50")
        DTL("112", "s46") DC(7, "140", 10) DW("s48") DTL("140", "s47")
        "s_nop 5\n\t" DW("s49") DC(7, "112", 11) TSA("56") DTE("112") DC(0, "140", 12) DW("s50") DTE("140") "s_nop 6\n\t" DW("s51")
        "s_waitcnt lgkmcnt(0)\n\ts_barrier\n\ts_branch 9f\n"
        "2:\n\ts_mov_b64 exec, vcc\n\t" LDH(32, s64) LDH(48, s65) "s_mov_b64 exec, -1\n\t" SPIN1 "s_branch 1b\n"
        REDO(0, LDH(48, s65), CK16("48")) REDO(1, LDH(64, s66), CK16("64")) REDO(2, LDH(80, s67), CK16("80")) REDO(3, LDH(96, s68), CK16("96")) REDO(4, LDH(32, s69), CK16("32"))
        REDO(5, LDH(48, s70), CK16("48")) REDO(6, LDH(64, s71), CK16("64")) REDO(7, LDH(80, s72), CK16("80")) REDO(8, LDH(96, s73), CK16("96")) REDO(9, LDST, CK16("32"))
        REDO(10, LDF("140", "s45"), CK28("140")) REDO(11, LDF("112", "s46"), CK28("112")) REDO(12, LDF("140", "s47"), CK28("140"))
        REDO(13, LDF("112", "s44"), CK28("112"))
        "7:\n\t" LGK(0) W15 WL(48, 3072) WA WL(52, 0) WL(56, 1024) WL(60, 2048) WL(64, 3072) WA WL(68, 0) WL(72, 1024) WL(76, 2048) WL(80, 3072) WA WL(84, 0) WL(88, 1024) WL(92, 2048) WEND
        "1:\n\ts_sleep 4\n\t" LF0("112", "s44") SPIN1 VM(0) CK4("112") "s_cbranch_scc1 1b\n\ts_branch 6b\n9:"
        : "+{v[176:207]}"(da), "+{v[208:239]}"(db), "+{v[240:247]}"(dc), "+{s[52:55]}"(sb), "+{s64}"(h0), [wa] "+&v"(wa), [t] "+&v"(tmp), [lb] "+&v"(lb) PFIO
        : "{s[40:43]}"(rws), "{s[44:51]}"(sa), [l16] "v"(l16), [z4] "v"(ks < 6 ? l16 + 4096 : OOB),
          [f0] "v"(fv + (g & 1) * 7936), [f1] "v"(ntg < 30 ? fv + (g & 1) * 8 : OOB), [ng] "s"(ntg), [hb] "s"(hb)
        : "memory", "scc", "vcc", "s59", "s60", "s61", "s65", "s66", "s67", "s68", "s69", "s70", "s71", "s72", "s73", "v16", "v17", "v18", "v19", "v20", "v21", "v22", "v23",
          "v24", "v25", "v26", "v27", "v28", "v29", "v30", "v31", VCL32, VCL48, VCL64, VCL80, VCL96, VCL112, VCL128, VCL140, VCL160 PFCL);
}
DI f32x4 sum8(unsigned a) {
    f32x4 v[8];
#pragma unroll
    for (int i = 0; i < 8; ++i) v[i] = ldsr<f32x4>(a + i * 5120);
    return ((v[0] + v[1]) + (v[2] + v[3])) + ((v[4] + v[5]) + (v[6] + v[7]));
}
DI u32x2 addres(f32x4 a, unsigned lo, unsigned hi) { return u32x2{pk2(a[0] + bflo(lo), a[1] + bfhi(lo)), pk2(a[2] + bflo(hi), a[3] + bfhi(hi))}; }

#define ROT 0
__global__ void __launch_bounds__(512, 2) block_kernel(Params) {
    u32x16 ka;
    asm volatile("s_load_dwordx16 %0, %1, 0" : "=s"(ka) : "s"(__builtin_amdgcn_kernarg_segment_ptr()) : "memory");
    __shared__ __attribute__((aligned(16))) char lds[163840];
    asm volatile("" :: "v"((LAS char*)lds) : "memory");
    const int lane = threadIdx.x & 63, w = __builtin_amdgcn_readfirstlane(threadIdx.x >> 6), b = blockIdx.x;
    unsigned pre;
    asm volatile("s_cmp_lg_u32 %1, %2\n\ts_cbranch_scc1 1f\n\ts_getpc_b64 s[4:5]\n.Lpc:\n\tv_lshlrev_b32 %0, 7, %3\n\tv_min_u32 %0, .Lend-.Lpc-4, %0\n\t"
                 "global_load_dword %0, %0, s[4:5]\n1:" : "=&{v200}"(pre) : "s"(w), "s"((b >> 3) & 3), "v"(threadIdx.x) : "memory", "scc", "s4", "s5");
    Params p;
    rsrc_t rws;
    const int xcd = b & 7, i = b >> 3;
    Retile rt;
    u32x2 tag;
    unsigned fq;
    {
        const int mt = (xcd >> 1) * 4 + (i & 3), nt = (xcd & 1) * 8 + (i >> 2), rot = ROT;
        QkvSrc c; QkvAdr e; u32x4 row; unsigned rowt, T;
        qkv_start(c, row, rowt, T, rt.t, p, ka, b, mt, nt, rot, w, lane);
        rws = mkrs(p.ws, WS_END);
        TSW(60);
        retile_setup(rt, p, b, lane);
        qkv_setup(e, mt, nt, w, lane);
        fq = lane == 0 ? WS_FQ + (nt * 16 + mt) * 128 : OOB;
        TSW(61);
        asm volatile("s_add_u32 m0, %[dm], 65536\n\ts_cmp_eq_u32 %[nt], 15\n\ts_cbranch_scc1 1f\n\t" QD4 "1:\n\ts_waitcnt vmcnt(12) lgkmcnt(0)\n\ts_add_u32 %0, %0, 1"
                     : "+s"(T), "+{s[60:75]}"(rt.t), "+v"(row), "+v"(rowt), "+v"(fq), "+{v200}"(pre), [sx] "+s"(c.sx), [sw] "+s"(c.sw)
                     : [x0] "v"(c.xo[0]), [x1] "v"(c.xo[1]), [w0] "v"(c.wo[0]), [w1] "v"(c.wo[1]), [rx] "s"(c.rx), [rw] "s"(c.rw), [dm] "s"(w * 4096), [nt] "s"(nt) : "memory", "scc");
        TSW(62);
        const u32x4 gr = row_stats(row, rowt, T);
        tag = u32x2{gr[2], gr[3]};
        TSW(63);
        if (nt == 15) {
            st16(rws, e.go, 0, gr);
            retile_issue(rt, w);
            asm volatile("s_waitcnt vmcnt(0)" ::: "memory");
        } else if (nt >= 10) qkv_body<true>(c, e, rt, rws, gr, tag, w PROFPASS);
        else qkv_body<false>(c, e, rt, rws, gr, tag, w PROFPASS);
    }
    const int mt = (xcd >> 1) * 8 + (i & 7), nt = (xcd & 1) * 4 + (i >> 3);
    const int qg = (xcd & 6) + ((i >> 2) & 1), ntg = (xcd & 1) * 16 + (i & 3) * 4 + (i >> 3);
    const int tg = w & 3, kh = w >> 2;
    auto rv0 = [&](unsigned l) { return (l & 15) * 1280 + (nt & 1) * 32 + kh * 64 + (l >> 4) * 16; };
    auto rv1 = [&](unsigned l) { return kh ? OOB : (l & 15) * 1280 + ((nt & 1) ? 0 : 128) + (l >> 4) * 8; };
    const unsigned rsb = (mt * 64 + tg * 16) * 1280 + nt * 160;
    u32x4 r0; u32x2 r1; Park pk;
    phase_attn(p, rt, rws, tag, fq, b, w, lane, nt, ntg, rv0(lane), rv1(lane), rsb, r0, r1, pk PROFPASS);
    unsigned ln;
    asm volatile("v_mbcnt_lo_u32_b32 %0, -1, 0\n\tv_mbcnt_hi_u32_b32 %0, -1, %0" : "=v"(ln) : "{v154}"(pk.d));
    phase_wo(rws, w, ln, mt, nt, ntg, r0, r1, pk PROFPASS);
    PIN(ln);
    unsigned o0 = rv0(ln), o1 = rv1(ln), a = tg * 40960 + ln * 16;
    PIN(o0); PIN(o1); PIN(a);
    phase_ffn(rws, w, ln, mt, nt, qg, ntg, pk PROFPASS);
    TS(14);
    {
        const rsrc_t ro = mkrs(p.out, 2048 * 1280);
        const u32x2 u = addres(sum8(a + kh * 2048), r0[0], r0[1]), v = addres(sum8(a + kh * 2048 + 1024), r0[2], r0[3]);
        st16(ro, o0, rsb, u32x4{u[0], u[1], v[0], v[1]});
        if (kh == 0) st8(ro, o1, rsb, addres(sum8(a + 4096), r1[0], r1[1]));
    }
    asm volatile(".Lend:" ::: "memory");
}

#define PRE 4
DI unsigned touch(const char* q, unsigned bytes, unsigned o) { return __builtin_amdgcn_raw_buffer_load_b32(mkrs(q, bytes), o, 0, 0); }
__global__ void __launch_bounds__(512) pre_kernel(Params p) {
    unsigned long long t0 = __builtin_amdgcn_s_memrealtime();
    const unsigned id = blockIdx.x * 512 + threadIdx.x, idx = (blockIdx.x >> 3) * 512 + threadIdx.x;
    unsigned s = 0;
    if (PRE & 1) {
        const unsigned o = idx * 4096;
        s += touch(p.x, 2048 * 1280, o) + touch(p.wqkv, 640 * 3840, o) + touch(p.wo, 640 * 1280, o) + touch(p.wg, 640 * 3456, o) + touch(p.wu, 640 * 3456, o) + touch(p.wd, 640 * 3456, o)
           + touch(p.out, 2048 * 1280, o) + touch(p.ws, WS_END, o);
    }
    if (PRE & 2) {
        const unsigned o = id * 64;
        s += touch(p.x, 2048 * 1280, o) + touch(p.wqkv, 640 * 3840, o) + touch(p.wo, 640 * 1280, o) + touch(p.wg, 640 * 3456, o) + touch(p.wu, 640 * 3456, o) + touch(p.wd, 640 * 3456, o);
    }
    if (PRE & 4) {
        const char* c;
        asm volatile("s_getpc_b64 s[60:61]\n\ts_add_u32 s60, s60, _Z12block_kernel6Params@rel32@lo+4\n\ts_addc_u32 s61, s61, _Z12block_kernel6Params@rel32@hi+12" : "={s[60:61]}"(c) :: "scc");
        s += touch(c, 31000, idx * 64) + touch((const char*)&RT_TAB, sizeof(RT_TAB), idx * 64);
    }
    if (PRE & 8) {
        const unsigned o = id * 64;
        s += touch(p.out, 2048 * 1280, o) + touch(p.ws, WS_END, o) + touch(p.ws, WS_END, o + (8 << 20)) + touch(p.ws, WS_END, o + (16 << 20)) + touch(p.ws, WS_END, o + (24 << 20));
    }
    asm volatile("" :: "v"(s));
    while (__builtin_amdgcn_s_memrealtime() - t0 < 1500) __builtin_amdgcn_s_sleep(1);
}
#define PRELAUNCH pre_kernel<<<256, 512, 0, stream>>>(p);

extern "C" void kernel_launch(void* const* d_in, const int* in_sizes, int n_in, void* d_out, int out_size, void* d_ws, size_t ws_size, hipStream_t stream) {
    Params p;
    p.x = (const char*)d_in[0]; p.wqkv = (const char*)d_in[1]; p.wo = (const char*)d_in[2];
    p.wg = (const char*)d_in[4]; p.wu = (const char*)d_in[5]; p.wd = (const char*)d_in[6];
    p.out = (char*)d_out; p.ws = (char*)d_ws + ((ws_size - WS_END) & ~(size_t)2097151);
    PRELAUNCH
    block_kernel<<<256, 512, 0, stream>>>(p);
}
```

```cpp
#define PROF
#define PADID "999"
#define PADT "0"
#include <hip/hip_runtime.h>
#include <hip/hip_bf16.h>
#include <stdint.h>

#define DI __device__ __forceinline__
typedef __attribute__((ext_vector_type(2))) __bf16 bf2v;
typedef __attribute__((ext_vector_type(8))) __bf16 bf8v;
typedef __attribute__((ext_vector_type(16))) __bf16 bf16v;
typedef __attribute__((ext_vector_type(4))) short s4v;
typedef __attribute__((ext_vector_type(2))) float f32x2;
typedef __attribute__((ext_vector_type(4))) float f32x4;
typedef __attribute__((ext_vector_type(8))) float f32x8;
typedef __attribute__((ext_vector_type(16))) float f32x16;
typedef __attribute__((ext_vector_type(32))) float f32x32;
typedef __attribute__((ext_vector_type(2))) unsigned u32x2;
typedef __attribute__((ext_vector_type(4))) unsigned u32x4;
typedef __attribute__((ext_vector_type(8))) unsigned u32x8;
typedef __attribute__((ext_vector_type(16))) unsigned u32x16;
typedef __attribute__((ext_vector_type(32))) unsigned u32x32;
typedef __amdgpu_buffer_rsrc_t rsrc_t;
#define LAS __attribute__((address_space(3)))

constexpr unsigned WS_Q = 0;
constexpr unsigned WS_KV = WS_Q + 32 * 81920;
constexpr unsigned WS_O = WS_KV + 32 * 163840;
constexpr unsigned WS_B = WS_O + 128 * 20480;
constexpr unsigned QBS = 32 * 40960, OFF_HS = 16 * 20480, OFF_F = OFF_HS + 16 * 2048;
constexpr unsigned WS_WO = WS_B + 8 * QBS;
constexpr unsigned WS_WD = WS_WO + 8 * 102400;
constexpr unsigned WS_WGU = WS_WD + 8 * 276480;
constexpr unsigned WS_CS = WS_WGU + 32 * 143360;
constexpr unsigned WS_FQ = WS_CS + 5 * 3584 * 4;
constexpr unsigned WS_FR = WS_FQ + 256 * 128;
constexpr unsigned WS_XS = WS_FR + 2048;
constexpr unsigned WS_E = WS_XS + 2048 * 16;
constexpr unsigned WS_END = WS_E + 128;
constexpr unsigned OOB = 0x40000000u;
constexpr int SPIN = 1 << 14;

struct Params {
    const char* x; const char* wqkv; const char* wo; const char* wg; const char* wu; const char* wd;
    char* out; char* ws;
};

#define SBAR() __builtin_amdgcn_sched_barrier(0)
#define PIN(x) asm volatile("" : "+v"(x))
#define WAITBAR(N) asm volatile("s_waitcnt vmcnt(" #N ") lgkmcnt(0)\n\ts_barrier" ::: "memory")
#define VBAR(N) asm volatile("s_waitcnt vmcnt(" #N ")\n\ts_barrier" ::: "memory")
#define LBAR() asm volatile("s_waitcnt lgkmcnt(0)\n\ts_barrier" ::: "memory")
#define BAR() asm volatile("s_barrier" ::: "memory")

DI rsrc_t mkrs(const char* p, unsigned bytes) { return __builtin_amdgcn_make_buffer_rsrc((void*)p, (short)0, (int)bytes, 0x00020000); }
template <class T> DI T ldsr(unsigned a) { return *(const LAS T*)(uintptr_t)a; }
template <class T> DI void ldsw(unsigned a, T v) { *(LAS T*)(uintptr_t)a = v; }
DI s4v ldstr(unsigned a) { return __builtin_amdgcn_ds_read_tr16_b64_v4i16((LAS s4v*)(uintptr_t)a); }
DI bf8v ldstr2(unsigned a, unsigned d) {
    s4v lo = ldstr(a), hi = ldstr(a + d);
    return __builtin_bit_cast(bf8v, __builtin_shufflevector(lo, hi, 0, 1, 2, 3, 4, 5, 6, 7));
}
DI f32x16 mfma32(bf8v a, bf8v b, f32x16 c) { return __builtin_amdgcn_mfma_f32_32x32x16_bf16(a, b, c, 0, 0, 0); }
DI f32x4 mfma16(bf8v a, bf8v b, f32x4 c) { return __builtin_amdgcn_mfma_f32_16x16x32_bf16(a, b, c, 0, 0, 0); }
DI bf8v asbf(u32x4 v) { return __builtin_bit_cast(bf8v, v); }
DI unsigned pk2(float lo, float hi) { f32x2 v = {lo, hi}; return __builtin_bit_cast(unsigned, __builtin_convertvector(v, bf2v)); }
DI float bflo(unsigned u) { return __uint_as_float(u << 16); }
DI float bfhi(unsigned u) { return __uint_as_float(u & 0xffff0000u); }
DI f32x2 swap32(float a, float b) { auto r = __builtin_amdgcn_permlane32_swap(__float_as_uint(a), __float_as_uint(b), false, false); return f32x2{__uint_as_float(r[0]), __uint_as_float(r[1])}; }
DI float sum16(float a) { auto r = __builtin_amdgcn_permlane16_swap(__float_as_uint(a), __float_as_uint(a), false, false); return __uint_as_float(r[0]) + __uint_as_float(r[1]); }
DI bool anyl(bool c) { return __builtin_amdgcn_ballot_w64(c) != 0; }
DI void st16(rsrc_t r, unsigned voff, unsigned soff, u32x4 v) { asm volatile("buffer_store_dwordx4 %0, %1, %2, %3 offen sc1\n\ts_nop 1" :: "v"(v), "v"(voff), "s"(r), "s"(soff) : "memory"); }
DI void st8(rsrc_t r, unsigned voff, unsigned soff, u32x2 v) { asm volatile("buffer_store_dwordx2 %0, %1, %2, %3 offen sc1" :: "v"(v), "v"(voff), "s"(r), "s"(soff) : "memory"); }
DI void st4(rsrc_t r, unsigned voff, unsigned soff, unsigned v) { asm volatile("buffer_store_dword %0, %1, %2, %3 offen sc1" :: "v"(v), "v"(voff), "s"(r), "s"(soff) : "memory"); }
DI u32x4 ld16c(rsrc_t r, unsigned voff, unsigned soff) { return __builtin_amdgcn_raw_buffer_load_b128(r, voff, soff, 16); }
DI u32x2 ld8c(rsrc_t r, unsigned voff, unsigned soff) { return __builtin_amdgcn_raw_buffer_load_b64(r, voff, soff, 16); }
DI void dma1(unsigned m0v, unsigned voff, rsrc_t r, unsigned soff) {
    asm volatile("s_mov_b32 m0, %0\n\ts_nop 0\n\tbuffer_load_dwordx4 %1, %2, %3 offen lds" :: "s"(m0v), "v"(voff), "s"(r), "s"(soff) : "memory"); }
DI void dma1c(unsigned m0v, unsigned voff, rsrc_t r, unsigned soff) {
    asm volatile("s_mov_b32 m0, %0\n\ts_nop 0\n\tbuffer_load_dwordx4 %1, %2, %3 offen sc1 lds" :: "s"(m0v), "v"(voff), "s"(r), "s"(soff) : "memory"); }
DI void dma3c(unsigned m0v, unsigned voff, rsrc_t r, unsigned soff) {
    asm volatile("s_mov_b32 m0, %0\n\ts_nop 0\n\tbuffer_load_dwordx4 %1, %2, %3 offen sc1 lds\n\tbuffer_load_dwordx4 %1, %2, %3 offen offset:1024 sc1 lds\n\t"
                 "buffer_load_dwordx4 %1, %2, %3 offen offset:2048 sc1 lds" :: "s"(m0v), "v"(voff), "s"(r), "s"(soff) : "memory"); }
DI void dma4c(unsigned m0v, unsigned voff, rsrc_t r, unsigned soff) {
    asm volatile("s_mov_b32 m0, %0\n\ts_nop 0\n\tbuffer_load_dwordx4 %1, %2, %3 offen sc1 lds\n\tbuffer_load_dwordx4 %1, %2, %3 offen offset:1024 sc1 lds\n\t"
                 "buffer_load_dwordx4 %1, %2, %3 offen offset:2048 sc1 lds\n\tbuffer_load_dwordx4 %1, %2, %3 offen offset:3072 sc1 lds" :: "s"(m0v), "v"(voff), "s"(r), "s"(soff) : "memory"); }
#define SPINS "0x4000"
#define PINS(x) asm volatile("" : "+s"(x))
#define CHK(B, TMP, DST) \
    "v_max3_u32 v" #TMP ", v[" #B "], v[" #B "+1], v[" #B "+2]\n\tv_max3_u32 v" #TMP ", v[" #B "+3], v[" #B "+4], v" #TMP "\n\t" \
    "v_max3_u32 v" #TMP ", v[" #B "+5], v[" #B "+6], v" #TMP "\n\tv_max3_u32 v" #TMP ", v[" #B "+7], v[" #B "+8], v" #TMP "\n\t" \
    "v_max3_u32 v" #TMP ", v[" #B "+9], v[" #B "+10], v" #TMP "\n\tv_max3_u32 v" #TMP ", v[" #B "+11], v[" #B "+12], v" #TMP "\n\t" \
    "v_max3_u32 v" #TMP ", v[" #B "+13], v[" #B "+14], v" #TMP "\n\tv_max_u32 v" #TMP ", v[" #B "+15], v" #TMP "\n\t" \
    "v_cmp_eq_u32 " DST ", -1, v" #TMP "\n\t"
#define LD1(R, V, S, O) "buffer_load_dwordx4 v[" R ":" R "+3], " V ", s[40:43], " S " offen offset:" O " sc1\n\t"
#define DM1(V, S, O) "buffer_load_dwordx4 " V ", s[40:43], " S " offen offset:" #O " sc1 lds\n\t"
#define DM3(V, S) DM1(V, S, 0) DM1(V, S, 1024) DM1(V, S, 2048)
#define DM4(V, S) DM3(V, S) DM1(V, S, 3072)
#define Z2(R) "v_mov_b64 v[" R ":" R "+1], 0\n\t"
#define Z4(R) Z2(R) Z2(R "+2")
#define Z8(R) Z4(R) Z4(R "+4")

#define VCL32 "v32", "v33", "v34", "v35", "v36", "v37", "v38", "v39", "v40", "v41", "v42", "v43", "v44", "v45", "v46", "v47"
#define VCL48 "v48", "v49", "v50", "v51", "v52", "v53", "v54", "v55", "v56", "v57", "v58", "v59", "v60", "v61", "v62", "v63"
#define VCL64 "v64", "v65", "v66", "v67", "v68", "v69", "v70", "v71", "v72", "v73", "v74", "v75", "v76", "v77", "v78", "v79"
#define VCL80 "v80", "v81", "v82", "v83", "v84", "v85", "v86", "v87", "v88", "v89", "v90", "v91", "v92", "v93", "v94", "v95"
#define VCL96 "v96", "v97", "v98", "v99", "v100", "v101", "v102", "v103", "v104", "v105", "v106", "v107", "v108", "v109", "v110", "v111"
#define VCL112 "v112", "v113", "v114", "v115", "v116", "v117", "v118", "v119", "v120", "v121", "v122", "v123", "v124", "v125", "v126", "v127"
#define VCL128 "v128", "v129", "v130", "v131", "v132", "v133", "v134", "v135", "v136", "v137", "v138", "v139"
#define VCL140 "v140", "v141", "v142", "v143", "v144", "v145", "v146", "v147", "v148", "v149", "v150", "v151", "v152", "v153", "v154", "v155", "v156", "v157", "v158", "v159"
#define VCL160 "v160", "v161", "v162", "v163", "v164", "v165", "v166", "v167", "v168", "v169", "v170", "v171", "v172", "v173", "v174", "v175"
#define VCL112L "v112", "v113", "v114", "v115", "v116", "v117", "v118", "v119"

constexpr unsigned WS_TS = 64u << 20;
#define PROFARG , u32x2& pf
#define PROFPASS , pf
#define TSA(i) "PADM " i "\n\t"
#define PADDEF ".macro PADM i\n.if \\i == 0\n\ts_memrealtime s[98:99]\n\ts_waitcnt lgkmcnt(0)\n\tv_writelane_b32 v254, s98, 0\n\t.endif\n\t" \
    ".if \\i == " PADID "\n\ts_cselect_b32 s95, 1, 0\n\tv_readlane_b32 s94, v254, 0\n9999:\n\ts_memrealtime s[98:99]\n\ts_waitcnt lgkmcnt(0)\n\ts_sub_u32 s98, s98, s94\n\ts_cmp_lt_u32 s98, " PADT "\n\ts_cbranch_scc1 9999b\n\ts_cmp_eq_u32 s95, 1\n\t.endif\n.endm\n\t"
#define PFIO , "+{v[254:255]}"(pf)
#define PFCL , "s92", "s93", "s94", "s95", "s96", "s97", "s98", "s99"
#define TSM "s_mov_b32 m0, s97\n\t" TSA("m0")
#define PFM(i) , [ti] "n"(i)
#define PFT "s_movk_i32 s97, %[ti]\n\t"
#define TSV(i) asm volatile("s_mov_b32 m0, %1\n\t" TSA("m0") : "+{v[254:255]}"(pf) : "s"(i) : "memory", "s94", "s95", "s98", "s99")
#define TS(i) asm volatile(TSA(#i) : "+{v[254:255]}"(pf) :: "memory", "s94", "s95", "s98", "s99")

#define TSF(i)
#define TSI
#define TSS(i)
#define TSW(i)
#define TSP
#define TSQ(a, b)
constexpr unsigned L_RT = 0, L_SM = 32768, L_CP = 49152;
struct RtTab { unsigned v[2048][16]; };
constexpr RtTab make_rt() {
    RtTab t{};
    for (unsigned b = 0; b < 256; ++b) for (unsigned w = 0; w < 8; ++w) for (unsigned f = 0; f < 4; ++f) {
        unsigned A = 0, M = 0, SO = 0, R0 = 0, R1 = 0;
        if (b < 135) {
            const unsigned fg = b / 5, ks = (b % 5) * 4 + (w >> 1), j = ks >> 1, o = fg * 8 + (w & 1) * 4 + f;
            A = (320 * (ks & 1) + (j < 8 ? (j & 1) * 32 + (j >> 1) * 8 : 8 * j) + (f & 1) * 160) * 3456 + fg * 128 + (w & 1) * 64;
            M = (j < 8 ? 96 : 16) * 3456;
            SO = WS_WGU + (o / 7) * 143360 + ks * 7168 + (o % 7) * 1024;
            R0 = 16 * f;
        } else {
            const unsigned v = (b - 135) * 8 + w;
            const bool isWo = v < 200;
            const unsigned vv = v < 740 ? (isWo ? v : v - 200) : 0, ks = vv / 10, cu = vv % 10;
            const unsigned c = 4 * ks + f, j = c / 31, ng = c % 31;
            A = (isWo ? (ks >= 16 ? 40 * c - 2544 : 20 * (c >> 1) + 4 * (c & 1)) : 56 * ng + 16 * (j >> 1) + 4 * (j & 1)) * 1280 + cu * 128;
            M = (isWo ? (ks >= 16 ? 20 : 8) : (j < 6 ? 8 : 4)) * 1280;
            const unsigned seg = 2 * cu + (f >> 1), a = seg / 5, sm = seg % 5, fi = f & 1;
            const unsigned tile = 2 * a + (sm == 2 ? fi : sm > 2), blk = sm == 2 ? 4 : (sm == 1 || sm == 4) ? 2 + fi : fi;
            SO = isWo ? WS_WO + (tile * 100 + (ks >= 10 ? (ks - 10) * 10 + 5 + (blk < 4 ? blk ^ 2 : 4) : ks * 10 + blk)) * 1024 : WS_WD + (tile * 270 + ks * 5 + blk) * 1024;
            R0 = (f >> 1) * 64 + (sm == 2 ? 32 : 8) * fi; R1 = sm == 2 ? 8 : 16;
        }
        unsigned* r = t.v[b * 8 + w];
        r[f] = A + 3072 - 1024 * f; r[4 + f] = M; r[8 + f] = SO; r[12 + f] = (L_RT + w * 4096 + R0) | R1 << 16;
    }
    return t;
}
static __device__ const __attribute__((aligned(64))) RtTab RT_TAB = make_rt();
struct Retile { rsrc_t ra, rb; u32x16 t; unsigned vlo, hi; };
DI void retile_setup(Retile& r, const Params& p, unsigned b, unsigned lane) {
    const bool gu = b < 135;
    const char *pa, *pb; unsigned n;
    asm("s_cmpk_lt_u32 %3, 160\n\ts_cselect_b64 %0, %4, %5\n\ts_cselect_b32 %2, %9, %8\n\ts_cmpk_lt_u32 %3, 135\n\ts_cselect_b64 %1, %7, %0\n\ts_cselect_b64 %0, %6, %0\n\ts_cselect_b32 %2, %8, %2"
        : "=&s"(pa), "=&s"(pb), "=&s"(n) : "s"(b), "s"(p.wo), "s"(p.wd), "s"(p.wg), "s"(p.wu), "s"(640 * 3456 + 3072), "n"(640 * 1280 + 3072) : "scc");
    r.ra = mkrs(pa - 3072, n); r.rb = mkrs(pb - 3072, n);
    r.vlo = gu ? ((lane >> 2) & 7) * 3456 + (lane & 3) * 16 : ((lane >> 3) & 3) * 1280 + (lane & 7) * 16;
    r.hi = lane >> 5;
    PIN(r.vlo); PIN(r.hi); PINS(r.ra); PINS(r.rb);
}
DI void retile_issue(const Retile& r, int w) {
    asm volatile("s_mov_b32 m0, %0\n\ts_nop 0\n\tbuffer_load_dwordx4 %1, %5, 0 offen lds\n\tbuffer_load_dwordx4 %2, %5, 0 offen offset:1024 lds\n\t"
                 "buffer_load_dwordx4 %3, %6, 0 offen offset:2048 lds\n\tbuffer_load_dwordx4 %4, %6, 0 offen offset:3072 lds"
                 :: "s"(L_RT + w * 4096), "v"(r.vlo + r.t[0] + r.hi * r.t[4]), "v"(r.vlo + r.t[1] + r.hi * r.t[5]), "v"(r.vlo + r.t[2] + r.hi * r.t[6]),
                    "v"(r.vlo + r.t[3] + r.hi * r.t[7]), "s"(r.ra), "s"(r.rb) : "memory");
}
DI u32x32 retile_read(const Retile& r, unsigned b, int w, unsigned lane) {
    const u32x4 one = {0x3f803f80u, 0x3f803f80u, 0x3f803f80u, 0x3f803f80u};
    const unsigned f = lane & 3, row = 8 * (lane >> 4) + ((lane >> 2) & 3), dl = b < 135 ? 256 : 512;
    const unsigned rb = b < 135 ? (f >> 1) * 2048 + row * 64 + (f & 1) * 8 : row * 128;
    u32x32 o; f32x4 cp[4];
#pragma unroll
    for (int i = 0; i < 4; ++i) {
        const bf8v fr = ldstr2(rb + (r.t[12 + i] & 0xffff) + (r.t[12 + i] >> 16) * f, dl);
        cp[i] = mfma16(fr, asbf(one), f32x4{});
#pragma unroll
        for (int k = 0; k < 4; ++k) o[4 * i + k] = __builtin_bit_cast(u32x4, fr)[k];
    }
    if ((lane & 15) == 0) {
#pragma unroll
        for (int i = 0; i < 4; ++i) ldsw<f32x4>(L_CP + w * 256 + i * 64 + lane, cp[i]);
    }
    return o;
}

struct QkvSrc { rsrc_t rx, rw; unsigned xo[2], wo[2], sx, sw; };
#define QDX(V, O) "buffer_load_dwordx4 %[" #V "], %[rx], %[sx] offen offset:" #O " lds\n\t"
#define QDW(V, O) "buffer_load_dwordx4 %[" #V "], %[rw], %[sw] offen offset:" #O " lds\n\t"
#define QADV "s_add_u32 %[sx], %[sx], 128\n\ts_cmp_eq_u32 %[sx], 1280\n\ts_cselect_b32 %[sx], 0, %[sx]\n\ts_mul_i32 %[sw], %[sx], 1920\n\t"
#define QD4 QDX(x0, 0) QDX(x1, 1024) QDW(w0, 2048) QDW(w1, 3072) QADV "s_add_u32 m0, m0, 32768\n\t"
DI void qkv_start(QkvSrc& c, u32x4& row, unsigned& rowt, unsigned& T, u32x16& tab, Params& p, u32x16 ka, int b, int mt, int nt, int rot, int w, int lane) {
    const unsigned rr = lane >> 3, isv = nt >= 10;
    const unsigned tok = mt * 128 + (w >> 1) * 32 + ((w & 1) << (2 * isv)) + (rr & 4) * 2 + ((rr & 3) << (1 - isv));
    c.xo[0] = tok * 1280 + (((lane & 7) ^ (rr >> 1)) << 4);
    c.xo[1] = (c.xo[0] ^ 64) + 16 * 1280 - 1024;
    c.wo[0] = (8 * w + ((lane >> 2) & 3)) * 3840 + nt * 256 + (lane >> 4) * 64 + (((lane & 3) ^ ((w & 1) << 1)) << 4) + 1024;
    c.wo[1] = c.wo[0] + 4 * 3840 - 1024;
    c.sx = rot * 128; c.sw = rot * 245760;
    unsigned ro = (mt * 128 + nt * 8 + w) * 1280;
    asm volatile("s_waitcnt lgkmcnt(0)\n\ts_load_dwordx16 %5, %6, %7" : "+s"(ka), "+v"(c.xo[1]), "+v"(c.wo[1]), "+s"(c.sw), "+s"(ro), "=&{s[60:75]}"(tab) : "s"(&RT_TAB), "s"((b * 8 + w) * 64) : "memory");
    p = __builtin_bit_cast(Params, ka);
    asm volatile("s_load_dword %0, %1, %2" : "=&s"(T) : "s"(p.ws), "s"(WS_E) : "memory");
    c.rx = mkrs(p.x, 2048 * 1280); c.rw = mkrs(p.wqkv - 3072, 640 * 3840 + 3072);
    asm volatile("s_mov_b32 m0, %[dm]\n\tbuffer_load_dwordx4 %[r4], %[l16], %[rx], %[ro] offen\n\tbuffer_load_dword %[r1], %[l4], %[rx], %[ro] offen offset:1024\n\t"
                 "s_cmp_eq_u32 %[nt], 15\n\ts_cbranch_scc1 1f\n\t" QD4 "s_barrier\n\t" QD4 "s_branch 2f\n1:\n\ts_waitcnt vmcnt(0)\n2:"
                 : [r4] "=&v"(row), [r1] "=&v"(rowt), [sx] "+&s"(c.sx), [sw] "+&s"(c.sw)
                 : [x0] "v"(c.xo[0]), [x1] "v"(c.xo[1]), [w0] "v"(c.wo[0]), [w1] "v"(c.wo[1]), [rx] "s"(c.rx), [rw] "s"(c.rw), [dm] "s"(w * 4096),
                   [l16] "v"(lane * 16), [l4] "v"(lane * 4), [ro] "s"(ro), [nt] "s"(nt) : "memory", "scc");
}
template <int C> DI float rorsum(float v) { return v + __uint_as_float(__builtin_amdgcn_update_dpp(0u, __float_as_uint(v), 0x120 + C, 0xf, 0xf, true)); }
DI u32x4 row_stats(u32x4 row, unsigned rowt, unsigned T) {
    float s = 0.f, q = 0.f;
#pragma unroll
    for (int i = 0; i < 5; ++i) {
        const bf2v v = __builtin_bit_cast(bf2v, i < 4 ? row[i & 3] : rowt);
        s = __builtin_amdgcn_fdot2_f32_bf16(v, __builtin_bit_cast(bf2v, 0x3f803f80u), s, false);
        q = __builtin_amdgcn_fdot2_f32_bf16(v, v, q, false);
    }
    const f32x2 y = swap32(s, q);
    const float z = rorsum<1>(rorsum<2>(rorsum<4>(rorsum<8>(sum16(y[0] + y[1]))))) * (1.f / 640.f);
    const f32x2 v = swap32(z, z);
    const float ra = __builtin_amdgcn_rsqf(__builtin_fmaxf(v[1] - v[0] * v[0], 0.f) + 1e-5f);
    return u32x4{__float_as_uint(ra), __float_as_uint(-v[0] * ra), T, ~T};
}
#define MFQ(A, W, X) "v_mfma_f32_16x16x32_bf16 v[" A ":" A "+3], v[" W ":" W "+3], v[" X ":" X "+3], v[" A ":" A "+3]\n\t"
#define MFV(A, W, X) MFQ(A, X, W)
#define QTR(R, V, O) "ds_read_b64_tr_b16 v[" R ":" R "+1], %[" #V "] offset:" O "\n\t"
#define QRX(R, V, O) "ds_read_b128 v[" R ":" R "+3], %[" #V "] offset:" O "\n\t"
#define LGK(N) "s_waitcnt lgkmcnt(" #N ")\n\t"
#define QSEC(MF, P, Q, XA, WO, D1, D2, E1, E2) \
    "s_cmp_lt_u32 %[n], 8\n\t" LGK(4) MF("64", P, "68") QTR(Q, wa0, WO) MF("32", P, P "+8") QTR(Q "+2", wa0, WO "+1024") \
    "s_cbranch_scc0 1f\n\t" D1 "1:\n\t" MF("36", P, P "+12") QRX(Q "+8", XA, "0") LGK(5) MF("48", P "+4", P "+8") QRX(Q "+12", XA, "4096") \
    MF("52", P "+4", P "+12") QTR(Q "+4", wa1, WO) LGK(5) MF("40", P, P "+16") QTR(Q "+6", wa1, WO "+1024") \
    "s_cbranch_scc0 1f\n\t" D2 "1:\n\t" MF("56", P "+4", P "+16") QRX(Q "+16", XA, "8192") E1 MF("44", P, P "+20") QRX(Q "+20", XA, "12288") E2 MF("60", P "+4", P "+20")
#define QLDG "buffer_load_dwordx4 v[120:123], %[ga], %[rs], 0 offen sc1\n\t"
#define QADD(r) "v_add_u32 %[" #r "], %[d], %[" #r "]\n\t"
#define QRT(r, A, M) "v_mad_u32_u24 %[" #r "], %[hi], s" #M ", %[vlo]\n\tv_add_u32 %[" #r "], s" #A ", %[" #r "]\n\t"
#define QKV_ASM(MF, STRD) asm volatile( \
    "s_add_u32 m0, %[dm], 98304\n\ts_mov_b32 %[n], 0\n\tbuffer_store_dwordx4 %[gr], %[go], %[rs], 0 offen sc1\n\t" \
    Z8("32") Z8("40") Z8("48") Z8("56") Z4("64") "v_mov_b32 v68, 0x3f803f80\n\tv_mov_b32 v69, v68\n\tv_mov_b64 v[70:71], v[68:69]\n\t" \
    "s_waitcnt vmcnt(9)\n\ts_barrier\n\t" TSA("1") TSS("25") \
    QTR("72", wa0, "0") QTR("74", wa0, "1024") QRX("80", xa0, "0") QRX("84", xa0, "4096") QTR("76", wa1, "0") QTR("78", wa1, "1024") QRX("88", xa0, "8192") QRX("92", xa0, "12288") \
    QDX(x0, 0) QDX(x1, 1024) "s_branch 6f\n" \
    "3:\n\ts_cmp_gt_u32 %[n], 2\n\ts_cbranch_scc1 2f\n\ts_waitcnt vmcnt(9)\n\ts_branch 5f\n" \
    "2:\n\ts_cmp_eq_u32 %[n], 10\n\ts_cbranch_scc1 7f\n\ts_cmp_eq_u32 %[n], 7\n\ts_cbranch_scc1 2f\n\ts_waitcnt vmcnt(5)\n\ts_branch 5f\n" \
    "2:\n\t" QLDG QRT(x0, 60, 64) QRT(x1, 61, 65) QRT(w0, 62, 66) QRT(w1, 63, 67) \
    "s_mov_b64 s[44:45], s[52:53]\n\ts_mov_b32 s46, s54\n\ts_mov_b64 s[48:49], s[56:57]\n\ts_mov_b32 s50, s58\n\ts_mov_b32 %[sx], 0\n\ts_mov_b32 %[sw], 0\n" \
    "4:\n\ts_waitcnt vmcnt(8)\n5:\n\ts_barrier\n\t" \
    QSEC(MF, "96", "72", xa0, "0", QDX(x0, 0), QDX(x1, 1024), "", "") \
    "6:\n\t" \
    QSEC(MF, "72", "96", xa1, "16384", QDW(w0, 2048), QDW(w1, 3072), \
         QADV "s_bitcmp1_b32 0x42, %[n]\n\ts_cselect_b32 %[d], %[wr], %[up]\n\ts_add_u32 m0, m0, %[d]\n\ts_cmp_eq_u32 %[n], 4\n\ts_cselect_b32 %[d], %[wr], %[up]\n\ts_add_u32 %[n], %[n], 1\n\t", \
         QADD(xa0) QADD(xa1) QADD(wa0) QADD(wa1)) TSI \
    "s_bitcmp1_b32 0x686, %[n]\n\ts_cbranch_scc0 4b\n\ts_branch 3b\n" \
    "7:\n\ts_movk_i32 %[n], " SPINS "\n\t" LGK(4) MF("64", "96", "68") MF("32", "96", "104") "s_waitcnt vmcnt(4)\n" \
    "1:\n\tv_cmp_ne_u64 vcc, v[122:123], %[tag]\n\ts_cbranch_vccz 2f\n\t" QLDG "s_waitcnt vmcnt(0)\n\ts_sub_u32 %[n], %[n], 1\n\ts_cbranch_scc0 1b\n" \
    "2:\n\t" MF("36", "96", "108") "v_mul_f32 v120, %[sc], v120\n\tv_mul_f32 v121, %[sc], v121\n\t" LGK(2) MF("48", "100", "104") \
    "ds_write2_b32 %[pa], v120, v121 offset1:64\n\t" MF("52", "100", "108") "s_nop 1\n\tds_write_b128 %[ca], v[64:67]\n\t" LGK(2) \
    MF("40", "96", "112") STRD MF("56", "100", "112") MF("44", "96", "116") MF("60", "100", "116") \
    "s_waitcnt lgkmcnt(0)\n\ts_barrier\n\ts_nop 7" \
    : "=&{v[32:63]}"(A), "=&{v[64:67]}"(cs0), "=&{v[124:155]}"(S), [rx] "+{s[44:47]}"(c.rx), [rw] "+{s[48:51]}"(c.rw), [sx] "+&s"(c.sx), [sw] "+&s"(c.sw), \
      [x0] "+&v"(c.xo[0]), [x1] "+&v"(c.xo[1]), [w0] "+&v"(c.wo[0]), [w1] "+&v"(c.wo[1]), [xa0] "+&v"(e.xa0), [xa1] "+&v"(e.xa1), [wa0] "+&v"(e.wa0), [wa1] "+&v"(e.wa1), \
      [n] "=&s"(n), [d] "=&s"(d) PFIO \
    : "{s[52:55]}"(rt.ra), "{s[56:59]}"(rt.rb), [rs] "s"(rws), [dm] "s"(w * 4096), [wr] "s"(-131072), [up] "s"(32768), [sc] "s"(e.sc), [gr] "v"(gr), [go] "v"(e.go), [ga] "v"(e.ga), \
      "{s[60:75]}"(rt.t), [vlo] "v"(rt.vlo), [hi] "v"(rt.hi), [tag] "v"(tag), [pa] "v"(e.pa), [pr] "v"(e.pr), [ca] "v"(e.ca) \
    : "memory", "scc", "vcc", "v68", "v69", "v70", "v71", "v72", "v73", "v74", "v75", "v76", "v77", "v78", "v79", VCL80, VCL96, VCL112L, "v120", "v121", "v122", "v123" PFCL)
#define QS2(R, O) "ds_read2_b32 v[" #R ":" #R "+1], %[pr] offset0:" #O " offset1:" #O "\n\tds_read2_b32 v[" #R "+2:" #R "+3], %[pr] offset0:" #O "+64 offset1:" #O "+64\n\t"
struct QkvAdr { unsigned xa0, xa1, wa0, wa1, go, ga, pa, pr, ca, so[2], sb, sc; };
DI void qkv_setup(QkvAdr& e, int mt, int nt, int w, int lane) {
    const unsigned t = lane & 15, g = lane >> 4, T = w & 1, cb = w >> 1, g64 = 2 * mt + T;
    const bool isv = nt >= 10, isq = nt < 5;
    e.xa0 = 4 * T * 4096 + t * 128 + ((g ^ (t >> 1)) << 4); e.xa1 = e.xa0 ^ 64;
    e.wa0 = g * 4096 + 2048 + cb * 256 + ((lane >> 2) & 3) * 64 + ((g ^ T) & 1) * 32 + (lane & 3) * 8; e.wa1 = e.wa0 ^ 32;
    e.go = lane == 0 ? WS_XS + (mt * 128 + nt * 8 + w) * 16 : OOB;
    e.ga = WS_XS + (g64 * 64 + (g >> 1) * 32 + (isv ? 8 * (t >> 2) + 4 * (g & 1) + (t & 3) : 2 * t + (g & 1))) * 16;
    e.pa = L_SM + w * 2048 + lane * 4; e.pr = L_SM + w * 2048 + (isv ? g * 16 : t * 4); e.ca = L_SM + w * 2048 + 1024 + lane * 16;
    e.sc = isq ? __builtin_bit_cast(unsigned, 0.22360679774997896f * 1.4426950408889634f) : 0x3f800000u;
    e.sb = isq ? WS_Q + g64 * 2560 : WS_KV + g64 * 5120 + (isv ? 2560 : 0);
#pragma unroll
    for (int n = 0; n < 2; ++n) {
        if (isv) {
            const unsigned col = (nt - 10) * 128 + cb * 32 + 16 * (n ^ T) + t, head = (col * 3277) >> 16;
            e.so[n] = head * (163840 - 320) + g * 320 + col * 16;
        } else {
            const unsigned G = (isq ? nt : nt - 5) * 32 + cb * 8 + 4 * (n ^ T) + g, head = (G * 205) >> 10;
            e.so[n] = head * (isq ? 81920 - 2560 : 163840 - 2560) + G * 512 + t * 16;
        }
        PIN(e.so[n]);
    }
    PIN(e.xa0); PIN(e.xa1); PIN(e.wa0); PIN(e.wa1); PIN(e.go); PIN(e.ga); PIN(e.pa); PIN(e.pr); PIN(e.ca); PINS(e.sb); PINS(e.sc);
}
template <bool ISV>
DI void qkv_body(QkvSrc& c, QkvAdr& e, const Retile& rt, rsrc_t rws, u32x4 gr, u32x2 tag, int w PROFARG) {
    f32x32 A, S; f32x4 cs0; unsigned n, d;
    if (ISV) QKV_ASM(MFV, QRX("124", pr, "0") QRX("128", pr, "256") QRX("132", pr, "64") QRX("136", pr, "320") QRX("140", pr, "128") QRX("144", pr, "384") QRX("148", pr, "192") QRX("152", pr, "448"));
    else QKV_ASM(MFQ, QS2(124, 0) QS2(128, 16) QS2(132, 32) QS2(136, 48));
    TS(2);
    const f32x4 cs1 = ldsr<f32x4>(e.ca ^ 2048);
#pragma unroll
    for (int n = 0; n < 2; ++n) {
        const f32x4 cs = n ? cs1 : cs0;
#pragma unroll
        for (int q = 0; q < 2; ++q) {
            u32x4 o;
#pragma unroll
            for (int i = 0; i < 4; ++i) {
                const int k = 2 * q + (i >> 1), j = 2 * (i & 1), a = 16 * n + 4 * k + j;
                o[i] = ISV ? pk2(A[a] * S[8 * k + j] + S[8 * k + 4 + j] * cs[0], A[a + 1] * S[8 * k + j + 1] + S[8 * k + 5 + j] * cs[0])
                           : pk2(A[a] * S[4 * k] + S[4 * k + 2] * cs[j], A[a + 1] * S[4 * k + 1] + S[4 * k + 3] * cs[j + 1]);
            }
            asm volatile("buffer_store_dwordx4 %0, %1, %2, %3 offen offset:%4 sc1\n\ts_nop 1" :: "v"(o), "v"(e.so[n]), "s"(rws), "s"(e.sb), "n"(q * (ISV ? 1280 : 256)) : "memory");
        }
    }
    TS(58);
    asm volatile("s_waitcnt vmcnt(4)" ::: "memory");
    TS(59);
}

DI f32x16 qk_tile(u32x4 km, u32x2 kt, u32x4 qm, u32x2 qt, f32x16 c) {
    c = mfma32(asbf(km), asbf(qm), c);
    return __builtin_amdgcn_mfma_f32_32x32x8bf16_1k(__builtin_bit_cast(s4v, kt), __builtin_bit_cast(s4v, qt), c, 0, 0, 0);
}
DI float max16(const f32x16& s, float m) {
#pragma unroll
    for (int r = 0; r < 16; r += 2) m = __builtin_fmaxf(__builtin_fmaxf(s[r], s[r + 1]), m);
    return m;
}
DI f32x16 pv16(u32x4 v, f32x8 pr, f32x16 o) {
    return __builtin_amdgcn_smfmac_f32_32x32x32_bf16(asbf(v), __builtin_bit_cast(bf16v, pr), o, 0xDDDD, 0, 0);
}
#define EX4(P, S) "v_exp_f32 v[" #P "], v[" S "]\n\tv_exp_f32 v[" #P "+1], v[" S "+1]\n\tv_exp_f32 v[" #P "+2], v[" S "+2]\n\tv_exp_f32 v[" #P "+3], v[" S "+3]\n\t"
#define SMF(O, V, P) "v_smfmac_f32_32x32x32_bf16 v[" #O ":" #O "+15], v[" #V ":" #V "+3], v[" #P ":" #P "+7], %[idx]\n\t"
#define QKT(SN, AB) "v_mfma_f32_32x32x4_2b_bf16 v[" #SN ":" #SN "+31], v[156:157], v[152:153], 0 cbsz:1 abid:" #AB "\n\t"
#define QKA(SN) "v_mfma_f32_32x32x16_bf16 v[" #SN ":" #SN "+15], v[160:163], v[144:147], v[" #SN ":" #SN "+15]\n\t"
#define QKB(SN) "v_mfma_f32_32x32x16_bf16 v[" #SN "+16:" #SN "+31], v[160:163], v[148:151], v[" #SN "+16:" #SN "+31]\n\t"
#define STEP_(SC, VP, VC, L0, L1, L2, L3, L4) \
    L0 EX4(128, #SC) EX4(132, #SC "+4") SMF(112, VP, 136) EX4(136, #SC "+16") "s_waitcnt lgkmcnt(0)\n\t" SMF(96, 164, 128) EX4(140, #SC "+20") \
    L1 EX4(128, #SC "+8") SMF(112, 164, 136) L2 EX4(132, #SC "+12") L3 EX4(136, #SC "+24") SMF(96, VC, 128) L4 EX4(140, #SC "+28")
#define STEP(SC, SN, AB, VP, VC, KTRD, VRD, KRD, M1, M2) STEP_(SC, VP, VC, QKT(SN, AB) KTRD, QKA(SN) M1, VRD, QKB(SN) KRD, M2)
#define VR(VH, O) "s_mov_b64 exec, s[56:57]\n\tds_read_b128 v[164:167], %[av] offset:" O "\n\tds_read_b128 v[" #VH ":" #VH "+3], %[av] offset:" O "+640\n\ts_mov_b64 exec, -1\n\t"
#define KR(O) "ds_read_b64 v[160:161], %[a0] offset:" O "\n\tds_read_b64 v[162:163], %[a0] offset:" O "+512\n\t"
#define KT(O) "ds_read_b64 v[156:157], %[at] offset:" O "\n\t"
#define PRIO "s_cbranch_vccz 1f\n\ts_setprio 1\n1:\n\t"
#define PVC "s_mov_b32 vcc_lo, %[grp]\n\ts_mov_b32 vcc_hi, 0\n\t"
#define QUAD3(X, N, A0, B0, A1, B1, A2, B2) PRIO \
    STEP(32, 64, 1, 172, 168, KT(#X "+5120"), VR(172, #X "+1280"), KR(#X "+5120"), A0, B0) \
    STEP(64, 32, 0, 168, 172, "", VR(168, #X "+5120"), KR(#X "+5376"), A1, B1) \
    STEP(32, 64, 1, 172, 168, KT(#N), VR(172, #X "+6400"), KR(#N), A2, B2) "s_setprio 0\n\t"
#define QUAD(X, N, A0, B0, A1, B1, A2, B2, A3, B3) QUAD3(X, N, A0, B0, A1, B1, A2, B2) STEP(64, 32, 0, 168, 172, "", VR(168, #N), KR(#N "+256"), A3, B3) TSI
#define KVP_(D) "buffer_load_dwordx4 %[l16], s[40:43], s44 offen sc1 lds\n\ts_add_u32 m0, m0, " #D "\n\ts_add_u32 s44, s44, 4096\n\t"
#define KVP KVP_(4096)
#define LDQ(R, V, O) "buffer_load_dwordx2 v[" #R ":" #R "+1], %[" #V "], s[40:43], %[sq] offen offset:" #O " sc1\n\t"
#define STW(R, S) "buffer_store_dwordx4 v[" #R ":" #R "+3], %[vs], s[40:43], s" #S " offen sc1\n\t"
#define STM(V, S, O) "buffer_store_dwordx4 v[192:195], %[" #V "], s[40:43], " S " offen offset:" #O " sc1\n\t"
#define WOP(O) DM1("%[l16]", "s48", O)
#define WO5 "s_add_u32 m0, m0, 1024\n\ts_add_u32 s48, s48, 1024\n\t"
#define PK(R, O) LD1(#R, "%[l16]", "s52", #O)
#define PKA "s_add_u32 s52, s52, 2048\n\ts_cmp_eq_u32 s52, %[pe]\n\ts_cselect_b32 s52, %[pb], s52\n\t"
#define TCH(O) "buffer_load_dword v154, %[vt], s[40:43], 0 offen offset:" #O " sc1\n\t"
#define LDQ5 LDQ(144, va, 0) LDQ(146, va, 512) LDQ(148, vb, 0) LDQ(150, vb, 512) LDQ(152, vc, 2048)
#define RD1(W, V, O) "ds_read_b128 v[" W ":" W "+3], " V " offset:" O "\n\t"
#define VM_(N) "s_waitcnt vmcnt(" #N ")\n\t"
#define VM(N) VM_(N)
DI unsigned mod5(unsigned v) { return (0x21043210u >> (4 * v)) & 7; }
DI unsigned park0(int w) { return ((w * 35 + 2) >> 2) * 2048; }
struct Park { u32x32 a, b; u32x8 c; unsigned d; };

DI void phase_attn(const Params& p, const Retile& rt, rsrc_t rws, u32x2 tag, unsigned fq, int b, int w, int lane, int nt80, int ntg,
                   unsigned rv0, unsigned rv1, unsigned rsb, u32x4& r0, u32x2& r1, Park& pk PROFARG) {
    const int t = lane & 31, hh = lane >> 5, wl = w & 3, grp = w >> 2;
    const int head = (b & 7) * 4 + (b >> 6), qb = (b >> 3) & 7;
    const unsigned gb = grp * 61440, kvb = WS_KV + head * 163840;
    pk.a = retile_read(rt, b, w, lane);
    const unsigned l16 = lane * 16;
    unsigned va = hh * 1024 + grp * 256 + t * 8, dm = gb + wl * 1024, sq = WS_Q + head * 81920 + (qb * 4 + wl) * 2560;
    const int n0 = (head * 20 + ((lane & 16) ? 19 : 0)) >> 7;
    unsigned fo = WS_FQ + (w == 0 ? (5 + 5 * hh + n0) * 16 + (lane & 15) : n0 * 16 + 2 * qb + (lane & 1)) * 128;
    unsigned skv = kvb + grp * 81920 + wl * 1024;
    u32x8 qm; u32x2 qt;
    asm volatile("s_cmp_eq_u32 %[w], 2\n\ts_waitcnt vmcnt(0) lgkmcnt(0)\n\ts_barrier\n\ts_cbranch_scc0 1f\n\tbuffer_store_dwordx2 %[tag], %[fq], %[rs], 0 offen sc1\n1:"
                 : "+{v[176:207]}"(pk.a), "+v"(fo), "+v"(va), "+s"(skv), "+s"(sq), "+s"(dm) : [w] "s"(w), [tag] "v"(tag), [fq] "v"(fq), [rs] "s"(rws) : "memory", "scc");
    TS(3);
    SBAR();
    const unsigned a0 = gb + hh * 1024 + t * 8, at = gb + lane * 8 + 2048, av = gb + 2560 + hh * 320 + (t < 20 ? t : 0) * 16;
    const unsigned ones = t == 20 ? 0x3f803f80u : 0u;
    const unsigned wob = WS_WO + nt80 * 102400;
    u32x8 sa;
    sa[0] = skv;
    sa[2] = WS_O + b * 10240 + w * 1280;
    sa[1] = WS_B + qb * QBS + (head * 8 + w) * 1408;
    sa[3] = WS_B + qb * QBS + OFF_F + (head * 8 + w) * 3456;
    const unsigned mv = l16 + (lane < 16 ? sa[2] + 1024 : lane < 40 ? sa[1] + 768 : sa[3] + 2432);
    const unsigned ro = nt80 & 3, pb = WS_WGU + ntg * 143360, p0 = park0(w) + (((ntg & 15) * 5) >> 3) * 14336;
    sa[4] = wob + (grp + ro) * 20480 + wl * 5120; sa[5] = 122880 + w * 5120;
    sa[6] = wob + mod5(2 + 2 * grp + ro) * 20480 + wl * 5120; sa[7] = gb + 20480 + wl * 5120;
    u32x4 sb = {pb + p0 - ((int)p0 < 143360 ? 0 : 143360), rsb, wob + mod5(3 + ro) * 20480 + w * 2560, 40960u + w * 2560};
    const unsigned vt = lane < 17 ? WS_WD + nt80 * 276480 + qb * 34560 + (w * 17 + lane) * 256 : OOB;
    const unsigned vs = b < 135 ? l16 ^ (((lane >> 1) ^ (lane >> 3)) & 1) * 0xa0 : b * 8 + w < 1820 ? l16 : OOB;
    const unsigned vfs = (w == 2 && lane == 0) ? WS_FR + b * 8 : OOB, vfe = (b == 0 && w == 0 && lane == 0) ? WS_E : OOB, vfp = WS_FR + w * 256 + t * 8;
    const unsigned fg = (b * 205) >> 10;
    const unsigned vcs = (b < 135 && w < 2) ? WS_CS + ((b - fg * 5) * 3584 + fg * 128 + w * 64 + lane) * 4 : OOB;
    unsigned e0 = gb + wl * 3072 + l16, e1 = 61440 - gb + wl * 3072 + l16, ov = (t >> 4) * 20480 + (t & 15) * 16, ow = hh ? OOB : ov;
    unsigned ob = WS_O + (qb * 16 + wl * 4 + grp * 2) * 20480, oc = ob + (64 + (head >> 1)) * 256 + (head & 1) * 8;
    ov += hh * 256; ob += head * 512;
    f32x32 O;
    asm volatile(
        "ds_read2st64_b32 v[196:197], %[cp] offset1:2\n\tds_read2st64_b32 v[198:199], %[cp] offset0:4 offset1:6\n\t"
        Z8("96") Z8("104") Z8("112") Z8("120") Z8("136") "v_mov_b32 v164, %[one]\n\tv_mov_b32 v165, %[one]\n\tv_mov_b64 v[166:167], v[164:165]\n\tv_mov_b64 v[168:169], v[164:165]\n\t"
        "v_mov_b64 v[170:171], v[164:165]\n\tv_mov_b64 v[172:173], v[164:165]\n\tv_mov_b64 v[174:175], v[164:165]\n\tv_mov_b64 v[192:193], -1\n\tv_mov_b64 v[194:195], -1\n\t"
        "s_mov_b32 s56, 0xfffff\n\ts_mov_b32 s57, s56\n\ts_movk_i32 s58, " SPINS "\n\ts_mov_b32 m0, %[dm]\n\ts_cmp_gt_u32 %[w], 1\n\t" "s_cbranch_scc1 2f\n"
        "1:\n\tbuffer_load_dwordx2 v[32:33], %[fo], s[40:43], 0 offen sc1\n\ts_waitcnt vmcnt(0)\n\tv_cmp_ne_u64 vcc, v[32:33], %[tag]\n\t"
        "s_cbranch_vccz 2f\n\ts_sub_u32 s58, s58, 1\n\ts_cbranch_scc0 1b\n"
        "2:\n\ts_barrier\n\t" TSA("4") KVP LDQ5 KVP "s_barrier\n\t" KVP KVP
        LGK(0) "v_add_f32 v196, v196, v197\n\tv_add_f32 v198, v198, v199\n\tv_add_f32 v196, v196, v198\n\ts_movk_i32 s58, " SPINS "\n\t"
        "s_waitcnt vmcnt(2)\n\ts_barrier\n\t" TSA("5")
        KT("0") KR("0") VR(168, "0") "s_waitcnt lgkmcnt(4)\n\t" QKT(32, 0) "s_waitcnt lgkmcnt(2)\n\ts_nop 7\n\ts_nop 7\n\ts_nop 2\n\t" QKA(32) QKB(32) KR("256")
        PVC "s_waitcnt vmcnt(0)\n\ts_barrier\n\ts_nop 6\n\t" TSS("17") TSF("17")
        QUAD(0, 10240, KVP, KVP STW(176, 68), KVP, STW(180, 69) "buffer_store_dword v196, %[vcs], s[40:43], 0 offen sc1\n\t", KVP, STW(184, 70), KVP, STW(188, 71))
        "s_waitcnt vmcnt(8)\n\ts_barrier\n\t"
        QUAD(10240, 20480, KVP, STM(l16, "s46", 0), KVP, STM(mv, "0", 0), KVP, "", KVP, "")
        "s_waitcnt vmcnt(2)\n\ts_barrier\n\tbuffer_store_dwordx2 %[tag], %[vfs], s[40:43], 0 offen sc1\n\t"
        QUAD(20480, 30720, KVP, STM(l16, "s45", 0), KVP_(-57344), KVP, KVP, STM(l16, "s47", 0), KVP, KVP)
        QUAD(30720, 40960, KVP, "", "buffer_load_dwordx2 v[198:199], %[vfp], s[40:43], 0 offen sc1\n\t", STM(l16, "s47", 1024), "", STM(l16, "s47", 2048), "", "")
        "s_mov_b32 m0, s49\n\ts_waitcnt vmcnt(2)\n"
        "3:\n\tv_cmp_ne_u64 vcc, v[198:199], %[tag]\n\ts_cbranch_vccz 4f\n\t"
        "buffer_load_dwordx2 v[198:199], %[vfp], s[40:43], 0 offen sc1\n\ts_sub_u32 s58, s58, 1\n\ts_waitcnt vmcnt(0)\n\ts_cbranch_scc0 3b\n"
        "4:\n\t" PVC "s_barrier\n\t" TSA("6") "buffer_store_dwordx2 %[tag], %[vfe], s[40:43], 0 offen sc1\n\t"
        QUAD(40960, 51200, PK(176, 0), PK(180, 1024) PKA, PK(184, 0), PK(188, 1024) PKA, PK(192, 0), PK(196, 1024) PKA, PK(200, 0), PK(204, 1024) PKA)
        QUAD(51200, 0, PK(208, 0), PK(212, 1024) PKA, PK(216, 0), PK(220, 1024) PKA, PK(224, 0), PK(228, 1024) PKA, PK(232, 0), PK(236, 1024) PKA)
        QUAD(0, 10240, PK(240, 0), PK(244, 1024), WOP(0) WO5, WOP(0) TCH(0), WOP(1024), WOP(2048), WOP(3072) "s_mov_b32 m0, s51\n\ts_mov_b32 s48, s50\n\t", WOP(0) WO5)
        QUAD3(10240, 0, WOP(0) TCH(128), WOP(1024), WOP(2048), WOP(3072),
              "buffer_load_dwordx4 v[248:251], %[rv0], %[rx], s53 offen\n\t", "buffer_load_dwordx2 v[252:253], %[rv1], %[rx], s53 offen\n\t")
        STEP_(64, 168, 172, "", "", "", "", "") TSI
        "s_mov_b32 m0, s55\n\ts_waitcnt vmcnt(24)\n\ts_nop 0\n\t" SMF(112, 172, 136) "s_barrier\n\t"
        DM1("%[l16]", "s54", 0) DM1("%[l16]", "s54", 1024) "s_mov_b32 exec_hi, 0\n\t" DM1("%[l16]", "s54", 2048) "s_mov_b32 exec_hi, -1\n\ts_nop 7\n\ts_nop 7"
        : "=&{v[96:127]}"(O), "=&{v[144:151]}"(qm), "=&{v[152:153]}"(qt), "+{v[176:207]}"(pk.a), "=&{v[208:239]}"(pk.b), "=&{v[240:247]}"(pk.c), "=&{v[248:251]}"(r0), "=&{v[252:253]}"(r1), "=&{v154}"(pk.d),
          "+{s[44:51]}"(sa), "+{s[52:55]}"(sb), "+v"(e0), "+v"(e1), "+v"(ov), "+v"(ow), "+s"(ob), "+s"(oc) PFIO
        : "{s[40:43]}"(rws), "{s[60:75]}"(rt.t), [a0] "v"(a0), [at] "v"(at), [av] "v"(av), [l16] "v"(l16), [tag] "v"(tag), [vcs] "v"(vcs), [fo] "v"(fo), [sq] "s"(sq),
          [cp] "v"(L_CP + (w & 1) * 256 + lane * 4), [mv] "v"(mv), [vs] "v"(vs), [vt] "v"(vt), [pb] "s"(pb), [pe] "s"(pb + 143360), [one] "v"(ones), [idx] "v"(0xDDDD),
          [va] "v"(va), [vb] "v"(va ^ 256), [vc] "v"((lane * 8) ^ (grp * 256)), [vfs] "v"(vfs), [vfe] "v"(vfe), [vfp] "v"(vfp), [rv0] "v"(rv0), [rv1] "v"(rv1),
          [dm] "s"(dm), [w] "s"(w), [grp] "s"(grp), [rx] "s"(mkrs(p.x, 2048 * 1280))
        : "memory", "scc", "vcc", "s56", "s57", "s58", VCL32, VCL48, VCL64, VCL80, VCL128, "v140", "v141", "v142", "v143", "v156", "v157", VCL160 PFCL);
    TS(7);
    float o[12];
#pragma unroll
    for (int i = 0; i < 3; ++i) ldsw<f32x4>(e0 + i * 1024, f32x4{O[16 + 4 * i], O[17 + 4 * i], O[18 + 4 * i], O[19 + 4 * i]});
    LBAR();
#pragma unroll
    for (int i = 0; i < 3; ++i) {
        const f32x4 v = ldsr<f32x4>(e1 + i * 1024);
#pragma unroll
        for (int k = 0; k < 4; ++k) o[4 * i + k] = O[4 * i + k] + v[k];
    }
    {
        f32x2 z = {0.f, 0.f};
#pragma unroll
        for (int r = 0; r < 12; r += 2) z += f32x2{o[r], o[r + 1]} * 0.f;
        const float l = (hh ? o[8] : 1.f) + (z[0] + z[1]);
        const bool bad = !(l < 1.8446744e19f && l > 8.6736174e-19f);
        if (__builtin_expect(anyl(bad), 0)) {
            float m = -__builtin_inff();
            const unsigned ko = hh * 1024 + t * 8, vo = 2560 + hh * 320 + (t < 20 ? t : 0) * 16;
            const u32x4 qa = {qm[0], qm[1], qm[2], qm[3]}; const u32x2 qe = {hh ? 0u : qt[0], hh ? 0u : qt[1]};
            f32x16 c = {}, A = {};
#pragma unroll 1
            for (int it = 0; it < 128; ++it) {
                const unsigned sb = kvb + ((it & 63) >> 1) * 5120;
                const u32x2 k0 = ld8c(rws, ko, sb + (it & 1) * 256), k1 = ld8c(rws, ko + 512, sb + (it & 1) * 256), k2 = ld8c(rws, t * 8 + 2048, sb + (it & 1) * 256);
                f32x16 s = qk_tile(u32x4{k0[0], k0[1], k1[0], k1[1]}, k2, qa, qe, c);
                if (it < 64) {
                    m = max16(s, m);
                    if (it == 63) {
                        const f32x2 mm = swap32(m, m);
                        m = __builtin_fmaxf(mm[0], mm[1]);
#pragma unroll
                        for (int r = 0; r < 16; ++r) c[r] = -m;
                    }
                } else {
                    u32x4 v0 = ld16c(rws, vo, sb + (it & 1) * 1280), v1 = ld16c(rws, vo + 640, sb + (it & 1) * 1280);
#pragma unroll
                    for (int i = 0; i < 4; ++i) { v0[i] = t < 20 ? v0[i] : ones; v1[i] = t < 20 ? v1[i] : ones; }
#pragma unroll
                    for (int r = 0; r < 16; ++r) s[r] = __builtin_amdgcn_exp2f(s[r]);
                    A = pv16(v0, __builtin_shufflevector(s, s, 0, 1, 2, 3, 4, 5, 6, 7), A);
                    A = pv16(v1, __builtin_shufflevector(s, s, 8, 9, 10, 11, 12, 13, 14, 15), A);
                }
            }
#pragma unroll
            for (int r = 0; r < 12; ++r) o[r] = A[r];
        }
    }
    const float inv = __builtin_amdgcn_rcpf(swap32(o[8], o[8])[1]);
    st16(rws, ov, ob, u32x4{pk2(o[0] * inv, o[1] * inv), pk2(o[2] * inv, o[3] * inv), pk2(o[4] * inv, o[5] * inv), pk2(o[6] * inv, o[7] * inv)});
    st8(rws, ow, oc, u32x2{pk2(o[8] * inv, o[9] * inv), pk2(o[10] * inv, o[11] * inv)});
}

constexpr unsigned L_CS = 14336;
#define MF16(A, W, B) "v_mfma_f32_16x16x32_bf16 v[" A ":" A "+3], v[" W ":" W "+3], v[" B ":" B "+3], v[" A ":" A "+3]\n\t"
#define MF0(A, W, B) "v_mfma_f32_16x16x32_bf16 v[" A ":" A "+3], v[" W ":" W "+3], v[" B ":" B "+3], 0\n\t"
#define RD5(W, V, O) RD1(W, V, O) RD1(W "+4", V, O "+1024") RD1(W "+8", V, O "+2048") RD1(W "+12", V, O "+3072") RD1(W "+16", V, O "+4096")
#define WR1(V, R, O) "ds_write_b128 " V ", v[" R ":" R "+3] offset:" O "\n\t"
#define MX3(B) "v_max3_u32 %[t], v[" B "], v[" B "+1], v[" B "+2]\n\t"
#define MX2(B) "v_max3_u32 %[t], v[" B "], v[" B "+1], %[t]\n\t"
#define MXE(B) "v_max_u32 %[t], v[" B "], %[t]\n\tv_cmp_eq_u32 vcc, -1, %[t]\n\ts_and_b64 vcc, vcc, s[60:61]\n\t"
#define CK4(B) MX3(B) MXE(B "+3")
#define CK8(B) MX3(B) MX2(B "+3") MX2(B "+5") MXE(B "+7")
#define CK16(B) MX3(B) MX2(B "+3") MX2(B "+5") MX2(B "+7") MX2(B "+9") MX2(B "+11") MX2(B "+13") MXE(B "+15")
#define CK28(B) MX3(B) MX2(B "+3") MX2(B "+5") MX2(B "+7") MX2(B "+9") MX2(B "+11") MX2(B "+13") MX2(B "+15") MX2(B "+17") MX2(B "+19") MX2(B "+21") MX2(B "+23") MX2(B "+25") MXE(B "+27")
#define SPIN0 "s_movk_i32 s59, " SPINS "\n\ts_mov_b64 s[60:61], -1\n\t"
#define SPIN1 "s_sub_u32 s59, s59, 1\n\ts_cselect_b64 s[60:61], 0, s[60:61]\n\t"
#define UNP(D, S) "v_lshlrev_b32 v[" #D "], 16, v" #S "\n\tv_and_b32 v[" #D "+1], 0xffff0000, v" #S "\n\t"
#define PKADD(D, S) "v_pk_add_f32 v[" D ":" D "+1], v[" D ":" D "+1], v[" S ":" S "+1]\n\t"
#define CVT(D, S) "v_cvt_pk_bf16_f32 v[" D "], v[" S "], v[" S "+1]\n\t"
#define WON(n, W, B, R) MF16("72+4*" #n, W "+4*" #n, B) R
#define WOK(W, B, V, O) WON(0, W, B, RD1(W, V, O)) WON(1, W, B, RD1(W "+4", V, O "+1024")) WON(2, W, B, RD1(W "+8", V, O "+2048")) WON(3, W, B, RD1(W "+12", V, O "+3072")) WON(4, W, B, RD1(W "+16", V, O "+4096"))
#define WOE(W, B) WON(0, W, B, "") WON(1, W, B, "") WON(2, W, B, "") WON(3, W, B, "") WON(4, W, B, "")
#define WOL(B, S) LD1(#B, "%[l16]", #S, "0") LD1(#B "+4", "%[l16]", #S, "1024")
#define WOC(p, N, B) "1" #p ":\n\t" VM(N) CK8(#B) "s_cbranch_scc1 2" #p "f\n\t" TSI
#define WOT(p) "2" #p ":\n\ts_call_b64 s[56:57], 8f\n\ts_branch 1" #p "b\n"
#define WOR(B, S) CK8(#B) "s_cbranch_scc0 1f\n\ts_mov_b64 exec, vcc\n\t" WOL(B, S) "s_mov_b64 exec, -1\n1:\n\t"
#define PW(R, O) WR1("%[pw]", #R, #O)
#define PW4(R, O) PW(R, O) PW(R+4, O+1024) PW(R+8, O+2048) PW(R+12, O+3072)
#define PX4(R, O) WR1("%[t]", #R, #O) WR1("%[t]", #R "+4", #O "+1024") WR1("%[t]", #R "+8", #O "+2048") WR1("%[t]", #R "+12", #O "+3072")
#define CSL(R) "buffer_load_dword " R ", %[ve], s[40:43], s49 offen sc1\n\ts_add_u32 s49, s49, 14336\n\t"
DI void phase_wo(rsrc_t rws, int w, unsigned lane, int mt, int nt, int ntg, u32x4& r0, u32x2& r1, Park& pk PROFARG) {
    const unsigned tg = w & 3, kh = w >> 2, t = lane & 15, g = lane >> 4, l16 = lane * 16, la = kh * 5120 + l16, e = w * 64 + lane, ro = nt & 3;
    const unsigned ab = WS_O + (mt * 4 + tg) * 20480 + kh * 10240, hb = WS_B + (mt >> 2) * QBS + nt * 256, tk = (mt & 3) * 4 + tg;
    u32x8 sa = {ab + ro * 2048, ab + (ro + 1) * 2048, ab + mod5(ro + 2) * 2048, ab + mod5(ro + 3) * 2048, ab + mod5(ro + 4) * 2048, WS_CS + ntg * 448, hb + tk * 20480, 0};
    const unsigned ex = tg * 5120 + kh * 2048 + l16;
    unsigned tmp;
    asm volatile(
        CSL("v92") CSL("v93") CSL("v94") CSL("v95") CSL("%[t]") SPIN0 VM(7) "s_barrier\n\t"
        RD5("96", "%[wa]", "0") RD5("116", "%[wa]", "10240")
        UNP(72, 248) UNP(74, 249) UNP(76, 250) UNP(78, 251) Z8("80") UNP(88, 252) UNP(90, 253)
        VM(5) TSA("20") TSS("45") WOL(32, s44) WOL(40, s45) WOL(48, s46) WOL(56, s47) WOL(64, s48) RD5("136", "%[wa]", "20480") RD5("156", "%[wa]", "30720")
        "s_waitcnt vmcnt(10) lgkmcnt(0)\n\tv_add_f32 v92, v92, v93\n\tv_add_f32 v94, v94, v95\n\tv_add_f32 v92, v92, %[t]\n\tv_add_f32 v92, v92, v94\n\tds_write_b32 %[vl], v92\n"
        WOC(0, 8, 32) TSA("8") WOK("96", "32", "%[wb]", "0") WOK("116", "36", "%[wb]", "10240")
        WOC(1, 6, 40) WOK("136", "40", "%[wb]", "20480") WOK("156", "44", "%[wb]", "30720")
        WOC(2, 4, 48) LGK(10) WOK("96", "48", "%[wc]", "0") WOK("116", "52", "%[wc]", "10240")
        WOC(3, 2, 56) LGK(10) WOE("136", "56") WOE("156", "60")
        WOC(4, 0, 64) LGK(0) WOE("96", "64") WOE("116", "68") TSA("9")
        "s_cmp_eq_u32 %[kh], 0\n\ts_nop 5\n\t" WR1("%[xw]", "80", "0") WR1("%[xw]", "84", "1024") "s_cbranch_scc1 1f\n\t" WR1("%[x4]", "88", "0")
        "1:\n\ts_waitcnt lgkmcnt(0)\n\ts_barrier\n\t" RD1("80", "%[xr]", "0") RD1("84", "%[xr]", "1024") RD1("92", "%[x4]", "0")
        LGK(2) PKADD("72", "80") PKADD("74", "82") LGK(1) PKADD("76", "84") PKADD("78", "86") CVT("248", "72") CVT("249", "74") CVT("250", "76") CVT("251", "78")
        "buffer_store_dwordx4 v[248:251], %[h0], s[40:43], s50 offen sc1\n\t" LGK(0) "s_cbranch_scc0 9f\n\t"
        PKADD("88", "92") PKADD("90", "94") CVT("252", "88") CVT("253", "90") "buffer_store_dwordx2 v[252:253], %[h1], s[40:43], s50 offen sc1\n\ts_branch 9f\n"
        WOT(0) WOT(1) WOT(2) WOT(3) WOT(4)
        "8:\n\t" VM(0) WOR(32, s44) WOR(40, s45) WOR(48, s46) WOR(56, s47) WOR(64, s48) VM(0) SPIN1 "s_setpc_b64 s[56:57]\n9:\n\t"
        PW4(176, 0) PW4(192, 4096) PW(208, 8192) PW(212, 9216)
        : "+{v[248:251]}"(r0), "+{v[252:253]}"(r1), "+{v[176:207]}"(pk.a), "+{v[208:239]}"(pk.b), "+{v[240:247]}"(pk.c), "+{s[44:51]}"(sa), [t] "=&v"(tmp) PFIO
        : "{s[40:43]}"(rws), [l16] "v"(l16), [wa] "v"(la + 122880), [wb] "v"(la + 20480), [wc] "v"(la + 81920), [kh] "s"(kh), [pw] "v"(20480 + park0(w) + l16),
          [ve] "v"(e < 112 ? (e ^ (((lane >> 1) ^ (lane >> 3)) & 1) * 10) * 4 : OOB), [vl] "v"(L_CS + (e < 112 ? e : 112) * 4),
          [xw] "v"(tg * 5120 + 2048 - kh * 2048 + l16), [xr] "v"(ex), [x4] "v"((tg == 2 ? 61440 : tg * 5120 + 4096) + l16),
          [h0] "v"((2 * g + kh) * 2048 + t * 16), [h1] "v"((8 + (g >> 1)) * 2048 + t * 16 + (g & 1) * 8)
        : "memory", "scc", "vcc", "s56", "s57", "s59", "s60", "s61", VCL32, VCL48, VCL64, VCL80, VCL96, VCL112, VCL128, VCL140, VCL160 PFCL);
    TS(10);
    float s = 0.f, q = 0.f;
#pragma unroll
    for (int k = 0; k < 6; ++k) {
        const bf2v v = __builtin_bit_cast(bf2v, k < 4 ? r0[k & 3] : r1[k & 1]);
        s = __builtin_amdgcn_fdot2_f32_bf16(v, __builtin_bit_cast(bf2v, 0x3f803f80u), s, false);
        q = __builtin_amdgcn_fdot2_f32_bf16(v, v, q, false);
    }
    const f32x2 y = swap32(s, q);
    const float z = sum16(y[0] + y[1]);
    const f32x2 v = swap32(z, z);
    st8(rws, g == 0 ? t * 16 + kh * 8 : OOB, hb + OFF_HS + tk * 2048, u32x2{__float_as_uint(v[0]), __float_as_uint(v[1])});
}

#define GW0 LGK(4)
#define GW1
#define GW2
#define GW3 LGK(5)
#define GW4
#define GW5 LGK(5)
#define GW6
#define GUN(MF, n, H, R, X) GW##n MF("140+8*" #n, "112+4*" #n, H) X MF("144+8*" #n, "112+4*" #n, H "+8") R
#define GUK(MF, H, V, O, D, X0, X1, X2, X3, X4, X5, X6) \
    GUN(MF, 0, H, RD1("112", V, O), X0) GUN(MF, 1, H, RD1("116", V, O "+" D), X1) GUN(MF, 2, H, RD1("120", V, O "+2*" D), X2) GUN(MF, 3, H, RD1("124", V, O "+3*" D), X3) \
    GUN(MF, 4, H, RD1("128", V, O "+4*" D), X4) GUN(MF, 5, H, RD1("132", V, O "+5*" D), X5) GUN(MF, 6, H, RD1("136", V, O "+6*" D), X6)
#define LH0(H, S) LD1(#H, "%[l16]", #S, "0")
#define LH1(H, S) LD1(#H "+4", "%[l16]", #S, "1024")
#define LH2(H, S) LD1(#H "+8", "%[lb]", #S, "0")
#define LH3(H, S) LD1(#H "+12", "%[lb]", #S, "1024")
#define LDH(H, S) LH0(H, S) LH1(H, S) LH2(H, S) LH3(H, S)
#define LH(H, S) LH0(H, S), LH1(H, S), LH2(H, S), LH3(H, S)
#define LS(R, O) LD1(#R, "%[l16]", "s54", #O)
#define LDST LS(32, 0) LS(36, 1024) LS(40, 2048) LS(44, 3072)
#define WL(R, O) LD1(#R, "%[l16]", "s52", #O)
#define WA "s_add_u32 s52, s52, 4096\n\t"
#define W15 WL(196, 0) WL(200, 1024) WL(204, 2048) WL(208, 3072) WA WL(212, 0) WL(216, 1024) WL(220, 2048) WL(224, 3072) WA WL(228, 0) WL(232, 1024) WL(236, 2048) WL(240, 3072) WA \
    WL(244, 0) WL(24, 1024) WL(28, 2048)
#define WEND "v_add_u32 %[lb], 4096, %[l16]\n\t" WL(32, 3072) LD1("36", "%[lb]", "s52", "0") LD1("40", "%[lb]", "s52", "1024") LD1("44", "%[z4]", "s52", "2048") LD1("96", "%[z4]", "s52", "3072") WA \
    LD1("100", "%[z4]", "s52", "0") LD1("104", "%[z4]", "s52", "1024") LD1("108", "%[z4]", "s52", "2048")
#define CKS(N, B, s) VM(N) MX3(B), MX2(B "+3"), MX2(B "+5"), MX2(B "+7"), MX2(B "+9"), MX2(B "+11"), MX2(B "+13") MXE(B "+15") "s_cbranch_scc1 4" #s "f\n3" #s ":\n\t"
#define GUK_(...) GUK(__VA_ARGS__)
#define GUS(MF, s, H, O1, PRE, O2, HN, N, ...) GUK_(MF, #H, "%[wa]", O1, "1024", __VA_ARGS__) PRE GUK_(MF16, #H "+4", "%[wa]", O2, "1024", CKS(N, #HN, s)) TSI
#define REDO(s, L, CK) "4" #s ":\n\ts_mov_b64 exec, vcc\n\t" L "s_mov_b64 exec, -1\n\t" VM(0) SPIN1 CK "s_cbranch_scc1 4" #s "b\n\ts_branch 3" #s "b\n"
#define WADV "v_add_u32 %[wa], 0xfc00, %[wa]\n\t"
#define HNX(A, B) "s_add_u32 s" #B ", s" #A ", 2048\n\ts_cmp_eq_u32 s" #B ", s55\n\ts_cselect_b32 s" #B ", %[hb], s" #B "\n\t"
#define SW32(A, B) "v_permlane32_swap_b32 v[" A "], v[" B "]\n\t"
#define SW16(A, B) "v_permlane16_swap_b32 v[" A "], v[" B "]\n\t"
#define EPF(A, C, T) "v_pk_fma_f32 v[" A ":" A "+1], v[" C ":" C "+1], v[16+4*" #T ":17+4*" #T "], v[" A ":" A "+1]\n\t"
#define EPT(A, C, T, D) EPF(A, C, T) EPF(A "+2", C "+2", T) "v_max_f32 v[" A "], 0, v[" A "]\n\tv_max_f32 v[" A "+1], 0, v[" A "+1]\n\t" \
    "v_pk_mul_f32 v[" A "+2:" A "+3], v[" A "+2:" A "+3], v[18+4*" #T ":19+4*" #T "]\n\t" \
    "v_pk_mul_f32 v[" A ":" A "+1], v[" A ":" A "+1], v[" A "+2:" A "+3]\n\t" CVT(D, A)
#define EPN(n, D) EPT("140+8*" #n, "112+4*" #n, 0, D) EPT("144+8*" #n, "112+4*" #n, 1, D "+1")
#define EP2(n, m, D, N) LGK(N) EPN(n, D) EPN(m, D "+2") SW32(D, D "+1") "s_nop 0\n\t" SW32(D "+2", D "+3") \
    "buffer_store_dwordx4 v[" D ":" D "+3], %[f0], s[40:43], s53 offen sc1\n\ts_add_u32 s53, s53, 15872\n\t"
#define LF0(F, S) LD1(F, "%[l16]", S, "0")
#define LF1(F, S) LD1(F "+4", "%[l16]", S, "1024")
#define LF2(F, S) LD1(F "+8", "%[l16]", S, "2048")
#define LF3(F, S) LD1(F "+12", "%[l16]", S, "3072")
#define LF4(F, S) LD1(F "+16", "%[lb]", S, "0")
#define LF5(F, S) LD1(F "+20", "%[lb]", S, "1024")
#define LF6(F, S) LD1(F "+24", "%[z4]", S, "2048")
#define LDF(F, S) LF0(F, S) LF1(F, S) LF2(F, S) LF3(F, S) LF4(F, S) LF5(F, S) LF6(F, S)
#define DK(MF, F, a, b, c, d, e) MF("168", #a, F) MF("172", #b, F) MF("176", #c, F) MF("180", #d, F) MF("184", #e, F)
#define DT(F, X0, X1, X2, X3, X4, X5, X6) DK(MF0, F, 196, 200, 204, 208, 212) X0 DK(MF16, F "+4", 216, 220, 224, 228, 232) X1 DK(MF16, F "+8", 236, 240, 244, 24, 28) X2 \
    DK(MF16, F "+12", 48, 52, 56, 60, 64) X3 DK(MF16, F "+16", 68, 72, 76, 80, 84) X4 DK(MF16, F "+20", 88, 92, 32, 36, 40) X5 DK(MF16, F "+24", 44, 96, 100, 104, 108) X6 TSI
#define DTL(F, S) DT(F, LF0(F, S), LF1(F, S), LF2(F, S), LF3(F, S), LF4(F, S), LF5(F, S), LF6(F, S))
#define DTE(F) DT(F, "", "", "", "", "", "", "")
#define DW(S) "v_add_u32 %[wa], " S ", %[l16]\n\t" WR1("%[wa]", "168", "0") WR1("%[wa]", "172", "1024") WR1("%[wa]", "176", "2048") WR1("%[wa]", "180", "3072") WR1("%[wa]", "184", "4096")
#define DC(N, F, s) VM(N) CK28(F) "s_cbranch_scc1 4" #s "f\n3" #s ":\n\t"
#define KSW 0
#define TSWP 0
DI void phase_ffn(rsrc_t rws, int w, unsigned lane, int mt, int nt, int qg, int ntg, const Park& pk PROFARG) {
    const unsigned t = lane & 15, g = lane >> 4, l16 = lane * 16, qbb = WS_B + qg * QBS, wg = (w & 3) * 2 + (w >> 2), hb = qbb + wg * 40960, ks = w ^ (KSW), k0 = 7 * ks - (ks == 7);
    const unsigned fb = WS_B + (mt >> 2) * QBS + OFF_F + (mt & 3) * 4 * 55296 + k0 * 1024, fv = (g >> 1) * 55296 + t * 16;
    u32x8 sa; u32x4 sb = {WS_WD + nt * 276480 + k0 * 5120, qbb + OFF_F + wg * 110592 + ntg * 256, qbb + OFF_HS + wg * 4096, hb + 20480};
#pragma unroll
    for (int i = 0; i < 4; ++i) { sa[i] = fb + (i ^ (TSWP)) * 55296; sa[4 + i] = (i ^ (TSWP)) * 40960 + ks * 5120; }
    unsigned wa = 20480 + l16, h0 = hb + (((ntg & 15) * 5) >> 3) * 2048, tmp = 20480 + park0(w) + l16, lb = l16 + 20480;
    u32x32 da = pk.a, db = pk.b; u32x8 dc = pk.c;
    asm volatile(
        PX4(216, 10240) PX4(232, 14336) SPIN0 HNX(64, 65) HNX(65, 66) HNX(66, 67) HNX(67, 68) HNX(68, 69) HNX(69, 70) HNX(70, 71) HNX(71, 72) HNX(72, 73)
        "s_cmp_eq_u32 %[ng], 31\n\ts_cbranch_scc1 7f\n\t" VM(1) TSA("21") LDH(32, s64) LDH(48, s65) LGK(0) "s_barrier\n\t"
        RD5("112", "%[wa]", "0") RD1("132", "%[wa]", "5120") RD1("136", "%[wa]", "6144")
        "1:\n\t" VM(4) CK16("32") "s_cbranch_scc1 2f\n\t" TSA("11") TSS("35") LDH(64, s66) LDH(80, s67) LDH(96, s68)
        GUS(MF0, 0, 32, "7168", "", "14336", 48, 12, "", "", "", "", "", "", "")
        GUS(MF16, 1, 48, "21504", "", "28672", 64, 12, LH(32, s69), "", "", "")
        GUS(MF16, 2, 64, "35840", "", "43008", 80, 13, LH(48, s70), WL(196, 0), "", "")
        GUS(MF16, 3, 80, "50176", "", "57344", 96, 15, LH(64, s71), WL(200, 1024), WL(204, 2048), "")
        WADV
        GUS(MF16, 4, 96, "0", "", "7168", 32, 18, LH(80, s72), WL(208, 3072) WA, WL(212, 0), WL(216, 1024))
        GUS(MF16, 5, 32, "14336", "", "21504", 48, 21, LH(96, s73), WL(220, 2048), WL(224, 3072) WA, WL(228, 0))
        GUS(MF16, 6, 48, "28672", "", "35840", 64, 23, LS(32, 0), LS(36, 1024), LS(40, 2048), LS(44, 3072), WL(232, 1024), WL(236, 2048), WL(240, 3072) WA)
        GUS(MF16, 7, 64, "43008", "", "50176", 80, 24, WL(244, 0), WL(24, 1024), WL(28, 2048), WL(48, 3072) WA, WL(52, 0), WL(56, 1024), WL(60, 2048))
        GUS(MF16, 8, 80, "57344", WADV, "0", 96, 21, WL(64, 3072) WA, WL(68, 0), WL(72, 1024), WL(76, 2048), "", "", "")
        GUK_(MF16, "96", "%[wa]", "7168", "1024", CKS(14, "32", 9))
        "v_lshrrev_b32 %[wa], 4, %[l16]\n\tv_and_b32 %[wa], 48, %[wa]\n\t"
        GUK(MF16, "100", "%[wa]", "14336", "64",
            WL(80, 3072) WA PKADD("32", "34") PKADD("36", "38") PKADD("40", "42") PKADD("44", "46"),
            WL(84, 0) PKADD("32", "36") PKADD("40", "44"),
            WL(88, 1024) SW32("32", "40") SW32("33", "41") PKADD("32", "40") "v_mov_b64 v[34:35], v[32:33]\n\t",
            WL(92, 2048) SW16("32", "34") SW16("33", "35") PKADD("32", "34") "v_mul_f32 v16, 0xbacccccd, v32\n\t",
            "v_mul_f32 v33, 0x3acccccd, v33\n\tv_fma_f32 v33, -v16, v16, v33\n\tv_max_f32 v33, 0, v33\n\tv_add_f32 v33, 0x3727c5ac, v33\n\t",
            "v_rcp_f32 v18, v33\n\tv_mov_b32 v20, v16\n\tv_mov_b32 v22, v18\n\t",
            SW32("16", "20") SW32("18", "22") "v_mov_b32 v17, v16\n\tv_mov_b32 v21, v20\n\tv_mov_b32 v19, v18\n\tv_mov_b32 v23, v22\n\t")
        TSA("12") WEND EP2(0, 1, "140", 5) EP2(2, 3, "156", 3) EP2(4, 5, "172", 1) LGK(0) EPN(6, "188") "s_nop 1\n\t" SW32("188", "189")
        "buffer_store_dwordx2 v[188:189], %[f1], s[40:43], s53 offen sc1\n"
        TSA("22") "6:\n\ts_barrier\n\t" TSA("23") LDF("112", "s44") LDF("140", "s45")
        DC(7, "112", 13) TSA("13") TSS("50")
        DTL("112", "s46") DC(7, "140", 10) DW("s48") DTL("140", "s47")
        "s_nop 5\n\t" DW("s49") DC(7, "112", 11) TSA("56") DTE("112") DC(0, "140", 12) DW("s50") DTE("140") "s_nop 6\n\t" DW("s51")
        "s_waitcnt lgkmcnt(0)\n\ts_barrier\n\ts_branch 9f\n"
        "2:\n\ts_mov_b64 exec, vcc\n\t" LDH(32, s64) LDH(48, s65) "s_mov_b64 exec, -1\n\t" SPIN1 "s_branch 1b\n"
        REDO(0, LDH(48, s65), CK16("48")) REDO(1, LDH(64, s66), CK16("64")) REDO(2, LDH(80, s67), CK16("80")) REDO(3, LDH(96, s68), CK16("96")) REDO(4, LDH(32, s69), CK16("32"))
        REDO(5, LDH(48, s70), CK16("48")) REDO(6, LDH(64, s71), CK16("64")) REDO(7, LDH(80, s72), CK16("80")) REDO(8, LDH(96, s73), CK16("96")) REDO(9, LDST, CK16("32"))
        REDO(10, LDF("140", "s45"), CK28("140")) REDO(11, LDF("112", "s46"), CK28("112")) REDO(12, LDF("140", "s47"), CK28("140"))
        REDO(13, LDF("112", "s44"), CK28("112"))
        "7:\n\t" LGK(0) W15 WL(48, 3072) WA WL(52, 0) WL(56, 1024) WL(60, 2048) WL(64, 3072) WA WL(68, 0) WL(72, 1024) WL(76, 2048) WL(80, 3072) WA WL(84, 0) WL(88, 1024) WL(92, 2048) WEND
        "1:\n\ts_sleep 4\n\t" LF0("112", "s44") SPIN1 VM(0) CK4("112") "s_cbranch_scc1 1b\n\ts_branch 6b\n9:"
        : "+{v[176:207]}"(da), "+{v[208:239]}"(db), "+{v[240:247]}"(dc), "+{s[52:55]}"(sb), "+{s64}"(h0), [wa] "+&v"(wa), [t] "+&v"(tmp), [lb] "+&v"(lb) PFIO
        : "{s[40:43]}"(rws), "{s[44:51]}"(sa), [l16] "v"(l16), [z4] "v"(ks < 6 ? l16 + 4096 : OOB),
          [f0] "v"(fv + (g & 1) * 7936), [f1] "v"(ntg < 30 ? fv + (g & 1) * 8 : OOB), [ng] "s"(ntg), [hb] "s"(hb)
        : "memory", "scc", "vcc", "s59", "s60", "s61", "s65", "s66", "s67", "s68", "s69", "s70", "s71", "s72", "s73", "v16", "v17", "v18", "v19", "v20", "v21", "v22", "v23",
          "v24", "v25", "v26", "v27", "v28", "v29", "v30", "v31", VCL32, VCL48, VCL64, VCL80, VCL96, VCL112, VCL128, VCL140, VCL160 PFCL);
}
DI f32x4 sum8(unsigned a) {
    f32x4 v[8];
#pragma unroll
    for (int i = 0; i < 8; ++i) v[i] = ldsr<f32x4>(a + i * 5120);
    return ((v[0] + v[1]) + (v[2] + v[3])) + ((v[4] + v[5]) + (v[6] + v[7]));
}
DI u32x2 addres(f32x4 a, unsigned lo, unsigned hi) { return u32x2{pk2(a[0] + bflo(lo), a[1] + bfhi(lo)), pk2(a[2] + bflo(hi), a[3] + bfhi(hi))}; }

#define ROT 0
__global__ void __launch_bounds__(512, 2) block_kernel(Params) {
    u32x16 ka;
    asm volatile(PADDEF "s_load_dwordx16 %0, %1, 0" : "=s"(ka) : "s"(__builtin_amdgcn_kernarg_segment_ptr()) : "memory");
    __shared__ __attribute__((aligned(16))) char lds[163840];
    asm volatile("" :: "v"((LAS char*)lds) : "memory");
    const int lane = threadIdx.x & 63, w = __builtin_amdgcn_readfirstlane(threadIdx.x >> 6), b = blockIdx.x;
    unsigned pre;
    asm volatile("s_cmp_lg_u32 %1, %2\n\ts_cbranch_scc1 1f\n\ts_getpc_b64 s[4:5]\n.Lpc:\n\tv_lshlrev_b32 %0, 7, %3\n\tv_min_u32 %0, .Lend-.Lpc-4, %0\n\t"
                 "global_load_dword %0, %0, s[4:5]\n1:" : "=&{v200}"(pre) : "s"(w), "s"((b >> 3) & 3), "v"(threadIdx.x) : "memory", "scc", "s4", "s5");
    u32x2 pf = {0, 0};
    TS(0);
    Params p;
    rsrc_t rws;
    const int xcd = b & 7, i = b >> 3;
    Retile rt;
    u32x2 tag;
    unsigned fq;
    {
        const int mt = (xcd >> 1) * 4 + (i & 3), nt = (xcd & 1) * 8 + (i >> 2), rot = ROT;
        QkvSrc c; QkvAdr e; u32x4 row; unsigned rowt, T;
        qkv_start(c, row, rowt, T, rt.t, p, ka, b, mt, nt, rot, w, lane);
        rws = mkrs(p.ws, WS_END);
        TSW(60);
        retile_setup(rt, p, b, lane);
        qkv_setup(e, mt, nt, w, lane);
        fq = lane == 0 ? WS_FQ + (nt * 16 + mt) * 128 : OOB;
        TSW(61);
        asm volatile("s_add_u32 m0, %[dm], 65536\n\ts_cmp_eq_u32 %[nt], 15\n\ts_cbranch_scc1 1f\n\t" QD4 "1:\n\ts_waitcnt vmcnt(12) lgkmcnt(0)\n\ts_add_u32 %0, %0, 1"
                     : "+s"(T), "+{s[60:75]}"(rt.t), "+v"(row), "+v"(rowt), "+v"(fq), "+{v200}"(pre), [sx] "+s"(c.sx), [sw] "+s"(c.sw)
                     : [x0] "v"(c.xo[0]), [x1] "v"(c.xo[1]), [w0] "v"(c.wo[0]), [w1] "v"(c.wo[1]), [rx] "s"(c.rx), [rw] "s"(c.rw), [dm] "s"(w * 4096), [nt] "s"(nt) : "memory", "scc");
        TSW(62);
        const u32x4 gr = row_stats(row, rowt, T);
        tag = u32x2{gr[2], gr[3]};
        TSW(63);
        if (nt == 15) {
            st16(rws, e.go, 0, gr);
            retile_issue(rt, w);
            asm volatile("s_waitcnt vmcnt(0)" ::: "memory");
        } else if (nt >= 10) qkv_body<true>(c, e, rt, rws, gr, tag, w PROFPASS);
        else qkv_body<false>(c, e, rt, rws, gr, tag, w PROFPASS);
    }
    const int mt = (xcd >> 1) * 8 + (i & 7), nt = (xcd & 1) * 4 + (i >> 3);
    const int qg = (xcd & 6) + ((i >> 2) & 1), ntg = (xcd & 1) * 16 + (i & 3) * 4 + (i >> 3);
    const int tg = w & 3, kh = w >> 2;
    auto rv0 = [&](unsigned l) { return (l & 15) * 1280 + (nt & 1) * 32 + kh * 64 + (l >> 4) * 16; };
    auto rv1 = [&](unsigned l) { return kh ? OOB : (l & 15) * 1280 + ((nt & 1) ? 0 : 128) + (l >> 4) * 8; };
    const unsigned rsb = (mt * 64 + tg * 16) * 1280 + nt * 160;
    u32x4 r0; u32x2 r1; Park pk;
    phase_attn(p, rt, rws, tag, fq, b, w, lane, nt, ntg, rv0(lane), rv1(lane), rsb, r0, r1, pk PROFPASS);
    unsigned ln;
    asm volatile("v_mbcnt_lo_u32_b32 %0, -1, 0\n\tv_mbcnt_hi_u32_b32 %0, -1, %0" : "=v"(ln) : "{v154}"(pk.d));
    phase_wo(rws, w, ln, mt, nt, ntg, r0, r1, pk PROFPASS);
    PIN(ln);
    unsigned o0 = rv0(ln), o1 = rv1(ln), a = tg * 40960 + ln * 16;
    PIN(o0); PIN(o1); PIN(a);
    phase_ffn(rws, w, ln, mt, nt, qg, ntg, pk PROFPASS);
    TS(14);
    {
        const rsrc_t ro = mkrs(p.out, 2048 * 1280);
        const u32x2 u = addres(sum8(a + kh * 2048), r0[0], r0[1]), v = addres(sum8(a + kh * 2048 + 1024), r0[2], r0[3]);
        st16(ro, o0, rsb, u32x4{u[0], u[1], v[0], v[1]});
        if (kh == 0) st8(ro, o1, rsb, addres(sum8(a + 4096), r1[0], r1[1]));
    }
    TS(15);
    asm volatile("s_waitcnt vmcnt(0)" ::: "memory");
    TS(16);
    unsigned l2; asm volatile("v_mbcnt_lo_u32_b32 %0, -1, 0\n\tv_mbcnt_hi_u32_b32 %0, -1, %0" : "=v"(l2));
    ((u32x2*)(p.ws + WS_TS))[(b * 8 + w) * 64 + l2] = pf;
    asm volatile(".Lend:" ::: "memory");
}

extern "C" void kernel_launch(void* const* d_in, const int* in_sizes, int n_in, void* d_out, int out_size, void* d_ws, size_t ws_size, hipStream_t stream) {
    Params p;
    p.x = (const char*)d_in[0]; p.wqkv = (const char*)d_in[1]; p.wo = (const char*)d_in[2];
    p.wg = (const char*)d_in[4]; p.wu = (const char*)d_in[5]; p.wd = (const char*)d_in[6];
    p.out = (char*)d_out; p.ws = (char*)d_ws;
    block_kernel<<<256, 512, 0, stream>>>(p);
}
```

```cpp
#include <hip/hip_runtime.h>
#include <hip/hip_bf16.h>
#include <stdint.h>

#define DI __device__ __forceinline__
typedef __attribute__((ext_vector_type(2))) __bf16 bf2v;
typedef __attribute__((ext_vector_type(8))) __bf16 bf8v;
typedef __attribute__((ext_vector_type(16))) __bf16 bf16v;
typedef __attribute__((ext_vector_type(4))) short s4v;
typedef __attribute__((ext_vector_type(2))) float f32x2;
typedef __attribute__((ext_vector_type(4))) float f32x4;
typedef __attribute__((ext_vector_type(8))) float f32x8;
typedef __attribute__((ext_vector_type(16))) float f32x16;
typedef __attribute__((ext_vector_type(32))) float f32x32;
typedef __attribute__((ext_vector_type(2))) unsigned u32x2;
typedef __attribute__((ext_vector_type(4))) unsigned u32x4;
typedef __attribute__((ext_vector_type(8))) unsigned u32x8;
typedef __attribute__((ext_vector_type(16))) unsigned u32x16;
typedef __attribute__((ext_vector_type(32))) unsigned u32x32;
typedef __amdgpu_buffer_rsrc_t rsrc_t;
#define LAS __attribute__((address_space(3)))

constexpr unsigned WS_Q = 0;
constexpr unsigned WS_KV = WS_Q + 32 * 81920;
constexpr unsigned WS_O = WS_KV + 32 * 163840;
constexpr unsigned WS_B = WS_O + 128 * 20480;
constexpr unsigned QBS = 32 * 40960, OFF_HS = 16 * 20480, OFF_F = OFF_HS + 16 * 2048;
constexpr unsigned WS_WO = WS_B + 8 * QBS;
constexpr unsigned WS_WD = WS_WO + 8 * 102400;
constexpr unsigned WS_WGU = WS_WD + 8 * 276480;
constexpr unsigned WS_CS = WS_WGU + 32 * 143360;
constexpr unsigned WS_FQ = WS_CS + 5 * 3584 * 4;
constexpr unsigned WS_FR = WS_FQ + 256 * 128;
constexpr unsigned WS_XS = WS_FR + 2048;
constexpr unsigned WS_E = WS_XS + 2048 * 16;
constexpr unsigned WS_END = WS_E + 128;
constexpr unsigned OOB = 0x40000000u;
constexpr int SPIN = 1 << 14;

struct Params {
    const char* x; const char* wqkv; const char* wo; const char* wg; const char* wu; const char* wd;
    char* out; char* ws;
};

#define SBAR() __builtin_amdgcn_sched_barrier(0)
#define PIN(x) asm volatile("" : "+v"(x))
#define WAITBAR(N) asm volatile("s_waitcnt vmcnt(" #N ") lgkmcnt(0)\n\ts_barrier" ::: "memory")
#define VBAR(N) asm volatile("s_waitcnt vmcnt(" #N ")\n\ts_barrier" ::: "memory")
#define LBAR() asm volatile("s_waitcnt lgkmcnt(0)\n\ts_barrier" ::: "memory")
#define BAR() asm volatile("s_barrier" ::: "memory")

DI rsrc_t mkrs(const char* p, unsigned bytes) { return __builtin_amdgcn_make_buffer_rsrc((void*)p, (short)0, (int)bytes, 0x00020000); }
template <class T> DI T ldsr(unsigned a) { return *(const LAS T*)(uintptr_t)a; }
template <class T> DI void ldsw(unsigned a, T v) { *(LAS T*)(uintptr_t)a = v; }
DI s4v ldstr(unsigned a) { return __builtin_amdgcn_ds_read_tr16_b64_v4i16((LAS s4v*)(uintptr_t)a); }
DI bf8v ldstr2(unsigned a, unsigned d) {
    s4v lo = ldstr(a), hi = ldstr(a + d);
    return __builtin_bit_cast(bf8v, __builtin_shufflevector(lo, hi, 0, 1, 2, 3, 4, 5, 6, 7));
}
DI f32x16 mfma32(bf8v a, bf8v b, f32x16 c) { return __builtin_amdgcn_mfma_f32_32x32x16_bf16(a, b, c, 0, 0, 0); }
DI f32x4 mfma16(bf8v a, bf8v b, f32x4 c) { return __builtin_amdgcn_mfma_f32_16x16x32_bf16(a, b, c, 0, 0, 0); }
DI bf8v asbf(u32x4 v) { return __builtin_bit_cast(bf8v, v); }
DI unsigned pk2(float lo, float hi) { f32x2 v = {lo, hi}; return __builtin_bit_cast(unsigned, __builtin_convertvector(v, bf2v)); }
DI float bflo(unsigned u) { return __uint_as_float(u << 16); }
DI float bfhi(unsigned u) { return __uint_as_float(u & 0xffff0000u); }
DI f32x2 swap32(float a, float b) { auto r = __builtin_amdgcn_permlane32_swap(__float_as_uint(a), __float_as_uint(b), false, false); return f32x2{__uint_as_float(r[0]), __uint_as_float(r[1])}; }
DI float sum16(float a) { auto r = __builtin_amdgcn_permlane16_swap(__float_as_uint(a), __float_as_uint(a), false, false); return __uint_as_float(r[0]) + __uint_as_float(r[1]); }
DI bool anyl(bool c) { return __builtin_amdgcn_ballot_w64(c) != 0; }
DI void st16(rsrc_t r, unsigned voff, unsigned soff, u32x4 v) { asm volatile("buffer_store_dwordx4 %0, %1, %2, %3 offen sc1\n\ts_nop 1" :: "v"(v), "v"(voff), "s"(r), "s"(soff) : "memory"); }
DI void st8(rsrc_t r, unsigned voff, unsigned soff, u32x2 v) { asm volatile("buffer_store_dwordx2 %0, %1, %2, %3 offen sc1" :: "v"(v), "v"(voff), "s"(r), "s"(soff) : "memory"); }
DI void st4(rsrc_t r, unsigned voff, unsigned soff, unsigned v) { asm volatile("buffer_store_dword %0, %1, %2, %3 offen sc1" :: "v"(v), "v"(voff), "s"(r), "s"(soff) : "memory"); }
DI u32x4 ld16c(rsrc_t r, unsigned voff, unsigned soff) { return __builtin_amdgcn_raw_buffer_load_b128(r, voff, soff, 16); }
DI u32x2 ld8c(rsrc_t r, unsigned voff, unsigned soff) { return __builtin_amdgcn_raw_buffer_load_b64(r, voff, soff, 16); }
DI void dma1(unsigned m0v, unsigned voff, rsrc_t r, unsigned soff) {
    asm volatile("s_mov_b32 m0, %0\n\ts_nop 0\n\tbuffer_load_dwordx4 %1, %2, %3 offen lds" :: "s"(m0v), "v"(voff), "s"(r), "s"(soff) : "memory"); }
DI void dma1c(unsigned m0v, unsigned voff, rsrc_t r, unsigned soff) {
    asm volatile("s_mov_b32 m0, %0\n\ts_nop 0\n\tbuffer_load_dwordx4 %1, %2, %3 offen sc1 lds" :: "s"(m0v), "v"(voff), "s"(r), "s"(soff) : "memory"); }
DI void dma3c(unsigned m0v, unsigned voff, rsrc_t r, unsigned soff) {
    asm volatile("s_mov_b32 m0, %0\n\ts_nop 0\n\tbuffer_load_dwordx4 %1, %2, %3 offen sc1 lds\n\tbuffer_load_dwordx4 %1, %2, %3 offen offset:1024 sc1 lds\n\t"
                 "buffer_load_dwordx4 %1, %2, %3 offen offset:2048 sc1 lds" :: "s"(m0v), "v"(voff), "s"(r), "s"(soff) : "memory"); }
DI void dma4c(unsigned m0v, unsigned voff, rsrc_t r, unsigned soff) {
    asm volatile("s_mov_b32 m0, %0\n\ts_nop 0\n\tbuffer_load_dwordx4 %1, %2, %3 offen sc1 lds\n\tbuffer_load_dwordx4 %1, %2, %3 offen offset:1024 sc1 lds\n\t"
                 "buffer_load_dwordx4 %1, %2, %3 offen offset:2048 sc1 lds\n\tbuffer_load_dwordx4 %1, %2, %3 offen offset:3072 sc1 lds" :: "s"(m0v), "v"(voff), "s"(r), "s"(soff) : "memory"); }
#define SPINS "0x4000"
#define PINS(x) asm volatile("" : "+s"(x))
#define CHK(B, TMP, DST) \
    "v_max3_u32 v" #TMP ", v[" #B "], v[" #B "+1], v[" #B "+2]\n\tv_max3_u32 v" #TMP ", v[" #B "+3], v[" #B "+4], v" #TMP "\n\t" \
    "v_max3_u32 v" #TMP ", v[" #B "+5], v[" #B "+6], v" #TMP "\n\tv_max3_u32 v" #TMP ", v[" #B "+7], v[" #B "+8], v" #TMP "\n\t" \
    "v_max3_u32 v" #TMP ", v[" #B "+9], v[" #B "+10], v" #TMP "\n\tv_max3_u32 v" #TMP ", v[" #B "+11], v[" #B "+12], v" #TMP "\n\t" \
    "v_max3_u32 v" #TMP ", v[" #B "+13], v[" #B "+14], v" #TMP "\n\tv_max_u32 v" #TMP ", v[" #B "+15], v" #TMP "\n\t" \
    "v_cmp_eq_u32 " DST ", -1, v" #TMP "\n\t"
#define LD1(R, V, S, O) "buffer_load_dwordx4 v[" R ":" R "+3], " V ", s[40:43], " S " offen offset:" O " sc1\n\t"
#define DM1(V, S, O) "buffer_load_dwordx4 " V ", s[40:43], " S " offen offset:" #O " sc1 lds\n\t"
#define DM3(V, S) DM1(V, S, 0) DM1(V, S, 1024) DM1(V, S, 2048)
#define DM4(V, S) DM3(V, S) DM1(V, S, 3072)
#define Z2(R) "v_mov_b64 v[" R ":" R "+1], 0\n\t"
#define Z4(R) Z2(R) Z2(R "+2")
#define Z8(R) Z4(R) Z4(R "+4")

#define VCL32 "v32", "v33", "v34", "v35", "v36", "v37", "v38", "v39", "v40", "v41", "v42", "v43", "v44", "v45", "v46", "v47"
#define VCL48 "v48", "v49", "v50", "v51", "v52", "v53", "v54", "v55", "v56", "v57", "v58", "v59", "v60", "v61", "v62", "v63"
#define VCL64 "v64", "v65", "v66", "v67", "v68", "v69", "v70", "v71", "v72", "v73", "v74", "v75", "v76", "v77", "v78", "v79"
#define VCL80 "v80", "v81", "v82", "v83", "v84", "v85", "v86", "v87", "v88", "v89", "v90", "v91", "v92", "v93", "v94", "v95"
#define VCL96 "v96", "v97", "v98", "v99", "v100", "v101", "v102", "v103", "v104", "v105", "v106", "v107", "v108", "v109", "v110", "v111"
#define VCL112 "v112", "v113", "v114", "v115", "v116", "v117", "v118", "v119", "v120", "v121", "v122", "v123", "v124", "v125", "v126", "v127"
#define VCL128 "v128", "v129", "v130", "v131", "v132", "v133", "v134", "v135", "v136", "v137", "v138", "v139"
#define VCL140 "v140", "v141", "v142", "v143", "v144", "v145", "v146", "v147", "v148", "v149", "v150", "v151", "v152", "v153", "v154", "v155", "v156", "v157", "v158", "v159"
#define VCL160 "v160", "v161", "v162", "v163", "v164", "v165", "v166", "v167", "v168", "v169", "v170", "v171", "v172", "v173", "v174", "v175"
#define VCL112L "v112", "v113", "v114", "v115", "v116", "v117", "v118", "v119"

#define PROFARG
#define PROFPASS
#define TSA(i)
#define PFIO
#define PFCL
#define TSM
#define PFM(i)
#define PFT
#define TS(i)

#define TSF(i)
#define TSI
#define TSS(i)
#define TSW(i)
#define TSP
#define TSQ(a, b)
constexpr unsigned L_RT = 0, L_SM = 32768, L_CP = 49152;
struct RtTab { unsigned v[2048][16]; };
constexpr RtTab make_rt() {
    RtTab t{};
    for (unsigned b = 0; b < 256; ++b) for (unsigned w = 0; w < 8; ++w) for (unsigned f = 0; f < 4; ++f) {
        unsigned A = 0, M = 0, SO = 0, R0 = 0, R1 = 0;
        if (b < 135) {
            const unsigned fg = b / 5, ks = (b % 5) * 4 + (w >> 1), j = ks >> 1, o = fg * 8 + (w & 1) * 4 + f;
            A = (320 * (ks & 1) + (j < 8 ? (j & 1) * 32 + (j >> 1) * 8 : 8 * j) + (f & 1) * 160) * 3456 + fg * 128 + (w & 1) * 64;
            M = (j < 8 ? 96 : 16) * 3456;
            SO = WS_WGU + (o / 7) * 143360 + ks * 7168 + (o % 7) * 1024;
            R0 = 16 * f;
        } else {
            const unsigned v = (b - 135) * 8 + w;
            const bool isWo = v < 200;
            const unsigned vv = v < 740 ? (isWo ? v : v - 200) : 0, ks = vv / 10, cu = vv % 10;
            const unsigned c = 4 * ks + f, j = c / 31, ng = c % 31;
            A = (isWo ? (ks >= 16 ? 40 * c - 2544 : 20 * (c >> 1) + 4 * (c & 1)) : 56 * ng + 16 * (j >> 1) + 4 * (j & 1)) * 1280 + cu * 128;
            M = (isWo ? (ks >= 16 ? 20 : 8) : (j < 6 ? 8 : 4)) * 1280;
            const unsigned seg = 2 * cu + (f >> 1), a = seg / 5, sm = seg % 5, fi = f & 1;
            const unsigned tile = 2 * a + (sm == 2 ? fi : sm > 2), blk = sm == 2 ? 4 : (sm == 1 || sm == 4) ? 2 + fi : fi;
            SO = isWo ? WS_WO + (tile * 100 + (ks >= 10 ? (ks - 10) * 10 + 5 + (blk < 4 ? blk ^ 2 : 4) : ks * 10 + blk)) * 1024 : WS_WD + (tile * 270 + ks * 5 + blk) * 1024;
            R0 = (f >> 1) * 64 + (sm == 2 ? 32 : 8) * fi; R1 = sm == 2 ? 8 : 16;
        }
        unsigned* r = t.v[b * 8 + w];
        r[f] = A + 3072 - 1024 * f; r[4 + f] = M; r[8 + f] = SO; r[12 + f] = (L_RT + w * 4096 + R0) | R1 << 16;
    }
    return t;
}
static __device__ const __attribute__((aligned(64))) RtTab RT_TAB = make_rt();
struct Retile { rsrc_t ra, rb; u32x16 t; unsigned vlo, hi; };
DI void retile_setup(Retile& r, const Params& p, unsigned b, unsigned lane) {
    const bool gu = b < 135;
    const char *pa, *pb; unsigned n;
    asm("s_cmpk_lt_u32 %3, 160\n\ts_cselect_b64 %0, %4, %5\n\ts_cselect_b32 %2, %9, %8\n\ts_cmpk_lt_u32 %3, 135\n\ts_cselect_b64 %1, %7, %0\n\ts_cselect_b64 %0, %6, %0\n\ts_cselect_b32 %2, %8, %2"
        : "=&s"(pa), "=&s"(pb), "=&s"(n) : "s"(b), "s"(p.wo), "s"(p.wd), "s"(p.wg), "s"(p.wu), "s"(640 * 3456 + 3072), "n"(640 * 1280 + 3072) : "scc");
    r.ra = mkrs(pa - 3072, n); r.rb = mkrs(pb - 3072, n);
    r.vlo = gu ? ((lane >> 2) & 7) * 3456 + (lane & 3) * 16 : ((lane >> 3) & 3) * 1280 + (lane & 7) * 16;
    r.hi = lane >> 5;
    PIN(r.vlo); PIN(r.hi); PINS(r.ra); PINS(r.rb);
}
DI void retile_issue(const Retile& r, int w) {
    asm volatile("s_mov_b32 m0, %0\n\ts_nop 0\n\tbuffer_load_dwordx4 %1, %5, 0 offen lds\n\tbuffer_load_dwordx4 %2, %5, 0 offen offset:1024 lds\n\t"
                 "buffer_load_dwordx4 %3, %6, 0 offen offset:2048 lds\n\tbuffer_load_dwordx4 %4, %6, 0 offen offset:3072 lds"
                 :: "s"(L_RT + w * 4096), "v"(r.vlo + r.t[0] + r.hi * r.t[4]), "v"(r.vlo + r.t[1] + r.hi * r.t[5]), "v"(r.vlo + r.t[2] + r.hi * r.t[6]),
                    "v"(r.vlo + r.t[3] + r.hi * r.t[7]), "s"(r.ra), "s"(r.rb) : "memory");
}
DI u32x32 retile_read(const Retile& r, unsigned b, int w, unsigned lane) {
    const u32x4 one = {0x3f803f80u, 0x3f803f80u, 0x3f803f80u, 0x3f803f80u};
    const unsigned f = lane & 3, row = 8 * (lane >> 4) + ((lane >> 2) & 3), dl = b < 135 ? 256 : 512;
    const unsigned rb = b < 135 ? (f >> 1) * 2048 + row * 64 + (f & 1) * 8 : row * 128;
    u32x32 o; f32x4 cp[4];
#pragma unroll
    for (int i = 0; i < 4; ++i) {
        const bf8v fr = ldstr2(rb + (r.t[12 + i] & 0xffff) + (r.t[12 + i] >> 16) * f, dl);
        cp[i] = mfma16(fr, asbf(one), f32x4{});
#pragma unroll
        for (int k = 0; k < 4; ++k) o[4 * i + k] = __builtin_bit_cast(u32x4, fr)[k];
    }
    if ((lane & 15) == 0) {
#pragma unroll
        for (int i = 0; i < 4; ++i) ldsw<f32x4>(L_CP + w * 256 + i * 64 + lane, cp[i]);
    }
    return o;
}

struct QkvSrc { rsrc_t rx, rw; unsigned xo[2], wo[2], sx, sw; };
#define QDX(V, O) "buffer_load_dwordx4 %[" #V "], %[rx], %[sx] offen offset:" #O " lds\n\t"
#define QDW(V, O) "buffer_load_dwordx4 %[" #V "], %[rw], %[sw] offen offset:" #O " lds\n\t"
#define QADV "s_add_u32 %[sx], %[sx], 128\n\ts_cmp_eq_u32 %[sx], 1280\n\ts_cselect_b32 %[sx], 0, %[sx]\n\ts_mul_i32 %[sw], %[sx], 1920\n\t"
#define QD4 QDX(x0, 0) QDX(x1, 1024) QDW(w0, 2048) QDW(w1, 3072) QADV "s_add_u32 m0, m0, 32768\n\t"
DI void qkv_start(QkvSrc& c, u32x4& row, unsigned& rowt, unsigned& T, u32x16& tab, Params& p, u32x16 ka, int b, int mt, int nt, int rot, int w, int lane) {
    const unsigned rr = lane >> 3, isv = nt >= 10;
    const unsigned tok = mt * 128 + (w >> 1) * 32 + ((w & 1) << (2 * isv)) + (rr & 4) * 2 + ((rr & 3) << (1 - isv));
    c.xo[0] = tok * 1280 + (((lane & 7) ^ (rr >> 1)) << 4);
    c.xo[1] = (c.xo[0] ^ 64) + 16 * 1280 - 1024;
    c.wo[0] = (8 * w + ((lane >> 2) & 3)) * 3840 + nt * 256 + (lane >> 4) * 64 + (((lane & 3) ^ ((w & 1) << 1)) << 4) + 1024;
    c.wo[1] = c.wo[0] + 4 * 3840 - 1024;
    c.sx = rot * 128; c.sw = rot * 245760;
    unsigned ro = (mt * 128 + nt * 8 + w) * 1280;
    asm volatile("s_waitcnt lgkmcnt(0)\n\ts_load_dwordx16 %5, %6, %7" : "+s"(ka), "+v"(c.xo[1]), "+v"(c.wo[1]), "+s"(c.sw), "+s"(ro), "=&{s[60:75]}"(tab) : "s"(&RT_TAB), "s"((b * 8 + w) * 64) : "memory");
    p = __builtin_bit_cast(Params, ka);
    asm volatile("s_load_dword %0, %1, %2" : "=&s"(T) : "s"(p.ws), "s"(WS_E) : "memory");
    c.rx = mkrs(p.x, 2048 * 1280); c.rw = mkrs(p.wqkv - 3072, 640 * 3840 + 3072);
    asm volatile("s_mov_b32 m0, %[dm]\n\tbuffer_load_dwordx4 %[r4], %[l16], %[rx], %[ro] offen\n\tbuffer_load_dword %[r1], %[l4], %[rx], %[ro] offen offset:1024\n\t"
                 "s_cmp_eq_u32 %[nt], 15\n\ts_cbranch_scc1 1f\n\t" QD4 "s_barrier\n\t" QD4 "s_branch 2f\n1:\n\ts_waitcnt vmcnt(0)\n2:"
                 : [r4] "=&v"(row), [r1] "=&v"(rowt), [sx] "+&s"(c.sx), [sw] "+&s"(c.sw)
                 : [x0] "v"(c.xo[0]), [x1] "v"(c.xo[1]), [w0] "v"(c.wo[0]), [w1] "v"(c.wo[1]), [rx] "s"(c.rx), [rw] "s"(c.rw), [dm] "s"(w * 4096),
                   [l16] "v"(lane * 16), [l4] "v"(lane * 4), [ro] "s"(ro), [nt] "s"(nt) : "memory", "scc");
}
template <int C> DI float rorsum(float v) { return v + __uint_as_float(__builtin_amdgcn_update_dpp(0u, __float_as_uint(v), 0x120 + C, 0xf, 0xf, true)); }
DI u32x4 row_stats(u32x4 row, unsigned rowt, unsigned T) {
    float s = 0.f, q = 0.f;
#pragma unroll
    for (int i = 0; i < 5; ++i) {
        const bf2v v = __builtin_bit_cast(bf2v, i < 4 ? row[i & 3] : rowt);
        s = __builtin_amdgcn_fdot2_f32_bf16(v, __builtin_bit_cast(bf2v, 0x3f803f80u), s, false);
        q = __builtin_amdgcn_fdot2_f32_bf16(v, v, q, false);
    }
    const f32x2 y = swap32(s, q);
    const float z = rorsum<1>(rorsum<2>(rorsum<4>(rorsum<8>(sum16(y[0] + y[1]))))) * (1.f / 640.f);
    const f32x2 v = swap32(z, z);
    const float ra = __builtin_amdgcn_rsqf(__builtin_fmaxf(v[1] - v[0] * v[0], 0.f) + 1e-5f);
    return u32x4{__float_as_uint(ra), __float_as_uint(-v[0] * ra), T, ~T};
}
#define MFQ(A, W, X) "v_mfma_f32_16x16x32_bf16 v[" A ":" A "+3], v[" W ":" W "+3], v[" X ":" X "+3], v[" A ":" A "+3]\n\t"
#define MFV(A, W, X) MFQ(A, X, W)
#define QTR(R, V, O) "ds_read_b64_tr_b16 v[" R ":" R "+1], %[" #V "] offset:" O "\n\t"
#define QRX(R, V, O) "ds_read_b128 v[" R ":" R "+3], %[" #V "] offset:" O "\n\t"
#define LGK(N) "s_waitcnt lgkmcnt(" #N ")\n\t"
#define QSEC(MF, P, Q, XA, WO, D1, D2, E1, E2) \
    "s_cmp_lt_u32 %[n], 8\n\t" LGK(4) MF("64", P, "68") QTR(Q, wa0, WO) MF("32", P, P "+8") QTR(Q "+2", wa0, WO "+1024") \
    "s_cbranch_scc0 1f\n\t" D1 "1:\n\t" MF("36", P, P "+12") QRX(Q "+8", XA, "0") LGK(5) MF("48", P "+4", P "+8") QRX(Q "+12", XA, "4096") \
    MF("52", P "+4", P "+12") QTR(Q "+4", wa1, WO) LGK(5) MF("40", P, P "+16") QTR(Q "+6", wa1, WO "+1024") \
    "s_cbranch_scc0 1f\n\t" D2 "1:\n\t" MF("56", P "+4", P "+16") QRX(Q "+16", XA, "8192") E1 MF("44", P, P "+20") QRX(Q "+20", XA, "12288") E2 MF("60", P "+4", P "+20")
#define QLDG "buffer_load_dwordx4 v[120:123], %[ga], %[rs], 0 offen sc1\n\t"
#define QADD(r) "v_add_u32 %[" #r "], %[d], %[" #r "]\n\t"
#define QRT(r, A, M) "v_mad_u32_u24 %[" #r "], %[hi], s" #M ", %[vlo]\n\tv_add_u32 %[" #r "], s" #A ", %[" #r "]\n\t"
#define QKV_ASM(MF, STRD) asm volatile( \
    "s_add_u32 m0, %[dm], 98304\n\ts_mov_b32 %[n], 0\n\tbuffer_store_dwordx4 %[gr], %[go], %[rs], 0 offen sc1\n\t" \
    Z8("32") Z8("40") Z8("48") Z8("56") Z4("64") "v_mov_b32 v68, 0x3f803f80\n\tv_mov_b32 v69, v68\n\tv_mov_b64 v[70:71], v[68:69]\n\t" \
    "s_waitcnt vmcnt(9)\n\ts_barrier\n\t" TSA("1") TSS("25") \
    QTR("72", wa0, "0") QTR("74", wa0, "1024") QRX("80", xa0, "0") QRX("84", xa0, "4096") QTR("76", wa1, "0") QTR("78", wa1, "1024") QRX("88", xa0, "8192") QRX("92", xa0, "12288") \
    QDX(x0, 0) QDX(x1, 1024) "s_branch 6f\n" \
    "3:\n\ts_cmp_gt_u32 %[n], 2\n\ts_cbranch_scc1 2f\n\ts_waitcnt vmcnt(9)\n\ts_branch 5f\n" \
    "2:\n\ts_cmp_eq_u32 %[n], 10\n\ts_cbranch_scc1 7f\n\ts_cmp_eq_u32 %[n], 7\n\ts_cbranch_scc1 2f\n\ts_waitcnt vmcnt(5)\n\ts_branch 5f\n" \
    "2:\n\t" QLDG QRT(x0, 60, 64) QRT(x1, 61, 65) QRT(w0, 62, 66) QRT(w1, 63, 67) \
    "s_mov_b64 s[44:45], s[52:53]\n\ts_mov_b32 s46, s54\n\ts_mov_b64 s[48:49], s[56:57]\n\ts_mov_b32 s50, s58\n\ts_mov_b32 %[sx], 0\n\ts_mov_b32 %[sw], 0\n" \
    "4:\n\ts_waitcnt vmcnt(8)\n5:\n\ts_barrier\n\t" \
    QSEC(MF, "96", "72", xa0, "0", QDX(x0, 0), QDX(x1, 1024), "", "") \
    "6:\n\t" \
    QSEC(MF, "72", "96", xa1, "16384", QDW(w0, 2048), QDW(w1, 3072), \
         QADV "s_bitcmp1_b32 0x42, %[n]\n\ts_cselect_b32 %[d], %[wr], %[up]\n\ts_add_u32 m0, m0, %[d]\n\ts_cmp_eq_u32 %[n], 4\n\ts_cselect_b32 %[d], %[wr], %[up]\n\ts_add_u32 %[n], %[n], 1\n\t", \
         QADD(xa0) QADD(xa1) QADD(wa0) QADD(wa1)) TSI \
    "s_bitcmp1_b32 0x686, %[n]\n\ts_cbranch_scc0 4b\n\ts_branch 3b\n" \
    "7:\n\ts_movk_i32 %[n], " SPINS "\n\t" LGK(4) MF("64", "96", "68") MF("32", "96", "104") "s_waitcnt vmcnt(4)\n" \
    "1:\n\tv_cmp_ne_u64 vcc, v[122:123], %[tag]\n\ts_cbranch_vccz 2f\n\t" QLDG "s_waitcnt vmcnt(0)\n\ts_sub_u32 %[n], %[n], 1\n\ts_cbranch_scc0 1b\n" \
    "2:\n\t" MF("36", "96", "108") "v_mul_f32 v120, %[sc], v120\n\tv_mul_f32 v121, %[sc], v121\n\t" LGK(2) MF("48", "100", "104") \
    "ds_write2_b32 %[pa], v120, v121 offset1:64\n\t" MF("52", "100", "108") "s_nop 1\n\tds_write_b128 %[ca], v[64:67]\n\t" LGK(2) \
    MF("40", "96", "112") STRD MF("56", "100", "112") MF("44", "96", "116") MF("60", "100", "116") \
    "s_waitcnt lgkmcnt(0)\n\ts_barrier\n\ts_nop 7" \
    : "=&{v[32:63]}"(A), "=&{v[64:67]}"(cs0), "=&{v[124:155]}"(S), [rx] "+{s[44:47]}"(c.rx), [rw] "+{s[48:51]}"(c.rw), [sx] "+&s"(c.sx), [sw] "+&s"(c.sw), \
      [x0] "+&v"(c.xo[0]), [x1] "+&v"(c.xo[1]), [w0] "+&v"(c.wo[0]), [w1] "+&v"(c.wo[1]), [xa0] "+&v"(e.xa0), [xa1] "+&v"(e.xa1), [wa0] "+&v"(e.wa0), [wa1] "+&v"(e.wa1), \
      [n] "=&s"(n), [d] "=&s"(d) PFIO \
    : "{s[52:55]}"(rt.ra), "{s[56:59]}"(rt.rb), [rs] "s"(rws), [dm] "s"(w * 4096), [wr] "s"(-131072), [up] "s"(32768), [sc] "s"(e.sc), [gr] "v"(gr), [go] "v"(e.go), [ga] "v"(e.ga), \
      "{s[60:75]}"(rt.t), [vlo] "v"(rt.vlo), [hi] "v"(rt.hi), [tag] "v"(tag), [pa] "v"(e.pa), [pr] "v"(e.pr), [ca] "v"(e.ca) \
    : "memory", "scc", "vcc", "v68", "v69", "v70", "v71", "v72", "v73", "v74", "v75", "v76", "v77", "v78", "v79", VCL80, VCL96, VCL112L, "v120", "v121", "v122", "v123" PFCL)
#define QS2(R, O) "ds_read2_b32 v[" #R ":" #R "+1], %[pr] offset0:" #O " offset1:" #O "\n\tds_read2_b32 v[" #R "+2:" #R "+3], %[pr] offset0:" #O "+64 offset1:" #O "+64\n\t"
struct QkvAdr { unsigned xa0, xa1, wa0, wa1, go, ga, pa, pr, ca, so[2], sb, sc; };
DI void qkv_setup(QkvAdr& e, int mt, int nt, int w, int lane) {
    const unsigned t = lane & 15, g = lane >> 4, T = w & 1, cb = w >> 1, g64 = 2 * mt + T;
    const bool isv = nt >= 10, isq = nt < 5;
    e.xa0 = 4 * T * 4096 + t * 128 + ((g ^ (t >> 1)) << 4); e.xa1 = e.xa0 ^ 64;
    e.wa0 = g * 4096 + 2048 + cb * 256 + ((lane >> 2) & 3) * 64 + ((g ^ T) & 1) * 32 + (lane & 3) * 8; e.wa1 = e.wa0 ^ 32;
    e.go = lane == 0 ? WS_XS + (mt * 128 + nt * 8 + w) * 16 : OOB;
    e.ga = WS_XS + (g64 * 64 + (g >> 1) * 32 + (isv ? 8 * (t >> 2) + 4 * (g & 1) + (t & 3) : 2 * t + (g & 1))) * 16;
    e.pa = L_SM + w * 2048 + lane * 4; e.pr = L_SM + w * 2048 + (isv ? g * 16 : t * 4); e.ca = L_SM + w * 2048 + 1024 + lane * 16;
    e.sc = isq ? __builtin_bit_cast(unsigned, 0.22360679774997896f * 1.4426950408889634f) : 0x3f800000u;
    e.sb = isq ? WS_Q + g64 * 2560 : WS_KV + g64 * 5120 + (isv ? 2560 : 0);
#pragma unroll
    for (int n = 0; n < 2; ++n) {
        if (isv) {
            const unsigned col = (nt - 10) * 128 + cb * 32 + 16 * (n ^ T) + t, head = (col * 3277) >> 16;
            e.so[n] = head * (163840 - 320) + g * 320 + col * 16;
        } else {
            const unsigned G = (isq ? nt : nt - 5) * 32 + cb * 8 + 4 * (n ^ T) + g, head = (G * 205) >> 10;
            e.so[n] = head * (isq ? 81920 - 2560 : 163840 - 2560) + G * 512 + t * 16;
        }
        PIN(e.so[n]);
    }
    PIN(e.xa0); PIN(e.xa1); PIN(e.wa0); PIN(e.wa1); PIN(e.go); PIN(e.ga); PIN(e.pa); PIN(e.pr); PIN(e.ca); PINS(e.sb); PINS(e.sc);
}
template <bool ISV>
DI void qkv_body(QkvSrc& c, QkvAdr& e, const Retile& rt, rsrc_t rws, u32x4 gr, u32x2 tag, int w PROFARG) {
    f32x32 A, S; f32x4 cs0; unsigned n, d;
    if (ISV) QKV_ASM(MFV, QRX("124", pr, "0") QRX("128", pr, "256") QRX("132", pr, "64") QRX("136", pr, "320") QRX("140", pr, "128") QRX("144", pr, "384") QRX("148", pr, "192") QRX("152", pr, "448"));
    else QKV_ASM(MFQ, QS2(124, 0) QS2(128, 16) QS2(132, 32) QS2(136, 48));
    TS(2);
    const f32x4 cs1 = ldsr<f32x4>(e.ca ^ 2048);
#pragma unroll
    for (int n = 0; n < 2; ++n) {
        const f32x4 cs = n ? cs1 : cs0;
#pragma unroll
        for (int q = 0; q < 2; ++q) {
            u32x4 o;
#pragma unroll
            for (int i = 0; i < 4; ++i) {
                const int k = 2 * q + (i >> 1), j = 2 * (i & 1), a = 16 * n + 4 * k + j;
                o[i] = ISV ? pk2(A[a] * S[8 * k + j] + S[8 * k + 4 + j] * cs[0], A[a + 1] * S[8 * k + j + 1] + S[8 * k + 5 + j] * cs[0])
                           : pk2(A[a] * S[4 * k] + S[4 * k + 2] * cs[j], A[a + 1] * S[4 * k + 1] + S[4 * k + 3] * cs[j + 1]);
            }
            asm volatile("buffer_store_dwordx4 %0, %1, %2, %3 offen offset:%4 sc1\n\ts_nop 1" :: "v"(o), "v"(e.so[n]), "s"(rws), "s"(e.sb), "n"(q * (ISV ? 1280 : 256)) : "memory");
        }
    }
    TS(58);
    asm volatile("s_waitcnt vmcnt(4)" ::: "memory");
    TS(59);
}

DI f32x16 qk_tile(u32x4 km, u32x2 kt, u32x4 qm, u32x2 qt, f32x16 c) {
    c = mfma32(asbf(km), asbf(qm), c);
    return __builtin_amdgcn_mfma_f32_32x32x8bf16_1k(__builtin_bit_cast(s4v, kt), __builtin_bit_cast(s4v, qt), c, 0, 0, 0);
}
DI float max16(const f32x16& s, float m) {
#pragma unroll
    for (int r = 0; r < 16; r += 2) m = __builtin_fmaxf(__builtin_fmaxf(s[r], s[r + 1]), m);
    return m;
}
DI f32x16 pv16(u32x4 v, f32x8 pr, f32x16 o) {
    return __builtin_amdgcn_smfmac_f32_32x32x32_bf16(asbf(v), __builtin_bit_cast(bf16v, pr), o, 0xDDDD, 0, 0);
}
#define EX4(P, S) "v_exp_f32 v[" #P "], v[" S "]\n\tv_exp_f32 v[" #P "+1], v[" S "+1]\n\tv_exp_f32 v[" #P "+2], v[" S "+2]\n\tv_exp_f32 v[" #P "+3], v[" S "+3]\n\t"
#define SMF(O, V, P) "v_smfmac_f32_32x32x32_bf16 v[" #O ":" #O "+15], v[" #V ":" #V "+3], v[" #P ":" #P "+7], %[idx]\n\t"
#define QKT(SN, AB) "v_mfma_f32_32x32x4_2b_bf16 v[" #SN ":" #SN "+31], v[156:157], v[152:153], 0 cbsz:1 abid:" #AB "\n\t"
#define QKA(SN) "v_mfma_f32_32x32x16_bf16 v[" #SN ":" #SN "+15], v[160:163], v[144:147], v[" #SN ":" #SN "+15]\n\t"
#define QKB(SN) "v_mfma_f32_32x32x16_bf16 v[" #SN "+16:" #SN "+31], v[160:163], v[148:151], v[" #SN "+16:" #SN "+31]\n\t"
#define STEP_(SC, VP, VC, L0, L1, L2, L3, L4) \
    L0 EX4(128, #SC) EX4(132, #SC "+4") SMF(112, VP, 136) EX4(136, #SC "+16") "s_waitcnt lgkmcnt(0)\n\t" SMF(96, 164, 128) EX4(140, #SC "+20") \
    L1 EX4(128, #SC "+8") SMF(112, 164, 136) L2 EX4(132, #SC "+12") L3 EX4(136, #SC "+24") SMF(96, VC, 128) L4 EX4(140, #SC "+28")
#define STEP(SC, SN, AB, VP, VC, KTRD, VRD, KRD, M1, M2) STEP_(SC, VP, VC, QKT(SN, AB) KTRD, QKA(SN) M1, VRD, QKB(SN) KRD, M2)
#define VR(VH, O) "s_mov_b64 exec, s[56:57]\n\tds_read_b128 v[164:167], %[av] offset:" O "\n\tds_read_b128 v[" #VH ":" #VH "+3], %[av] offset:" O "+640\n\ts_mov_b64 exec, -1\n\t"
#define KR(O) "ds_read_b64 v[160:161], %[a0] offset:" O "\n\tds_read_b64 v[162:163], %[a0] offset:" O "+512\n\t"
#define KT(O) "ds_read_b64 v[156:157], %[at] offset:" O "\n\t"
#define PRIO "s_cbranch_vccz 1f\n\ts_setprio 1\n1:\n\t"
#define PVC "s_mov_b32 vcc_lo, %[grp]\n\ts_mov_b32 vcc_hi, 0\n\t"
#define QUAD3(X, N, A0, B0, A1, B1, A2, B2) \
    STEP(32, 64, 1, 172, 168, KT(#X "+5120"), VR(172, #X "+1280"), KR(#X "+5120"), A0, B0) \
    STEP(64, 32, 0, 168, 172, "", VR(168, #X "+5120"), KR(#X "+5376"), A1, B1) \
    STEP(32, 64, 1, 172, 168, KT(#N), VR(172, #X "+6400"), KR(#N), A2, B2)
#define QUAD(X, N, A0, B0, A1, B1, A2, B2, A3, B3) QUAD3(X, N, A0, B0, A1, B1, A2, B2) STEP(64, 32, 0, 168, 172, "", VR(168, #N), KR(#N "+256"), A3, B3) TSI
#define QUADP(X, N, A0, B0, A1, B1, A2, B2, A3, B3) PRIO QUAD(X, N, A0, B0, A1, B1, A2, B2 "s_setprio 0\n\t", A3, B3)
#define KVP_(D) "buffer_load_dwordx4 %[l16], s[40:43], s44 offen sc1 lds\n\ts_add_u32 m0, m0, " #D "\n\ts_add_u32 s44, s44, 4096\n\t"
#define KVP KVP_(4096)
#define LDQ(R, V, O) "buffer_load_dwordx2 v[" #R ":" #R "+1], %[" #V "], s[40:43], %[sq] offen offset:" #O " sc1\n\t"
#define STW(R, S) "buffer_store_dwordx4 v[" #R ":" #R "+3], %[vs], s[40:43], s" #S " offen sc1\n\t"
#define STM(V, S, O) "buffer_store_dwordx4 v[192:195], %[" #V "], s[40:43], " S " offen offset:" #O " sc1\n\t"
#define WO4 "s_cbranch_vccnz 5f\n\t" DM4("%[l16]", "s48") "s_add_u32 m0, m0, 4096\n\ts_add_u32 s48, s48, 4096\n5:\n\t"
#define PK(R, O) LD1(#R, "%[l16]", "s52", #O)
#define PKA "s_add_u32 s52, s52, 2048\n\ts_cmp_eq_u32 s52, %[pe]\n\ts_cselect_b32 s52, %[pb], s52\n\t"
#define TCH(O) "buffer_load_dword v154, %[vt], s[40:43], 0 offen offset:" #O " sc1\n\t"
#define LDQ5 LDQ(144, va, 0) LDQ(146, va, 512) LDQ(148, vb, 0) LDQ(150, vb, 512) LDQ(152, vc, 2048)
#define RD1(W, V, O) "ds_read_b128 v[" W ":" W "+3], " V " offset:" O "\n\t"
#define VM_(N) "s_waitcnt vmcnt(" #N ")\n\t"
#define VM(N) VM_(N)
DI unsigned mod5(unsigned v) { return (0x21043210u >> (4 * v)) & 7; }
DI unsigned wrapw(unsigned v) { return v - (v < 143360 ? 0 : 143360); }
constexpr unsigned L_CS = 52224, L_FL = L_CS + 512;
#define FSET(V, A, O) "ds_write_b32 " A ", " V " offset:" #O "\n\t"
#define FGET(V, A, O) "ds_read_b32 " V ", " A " offset:" #O "\n\t"
#define FTST(V, C, L) "v_cmp_ne_u32 vcc, -1, " V "\n\ts_sub_u32 " C ", " C ", 1\n\ts_cselect_b64 vcc, 0, vcc\n\ts_cbranch_vccnz " L "\n\t"
struct Park { u32x32 a, b; u32x8 c; unsigned d; };

DI void phase_attn(const Params& p, const Retile& rt, rsrc_t rws, u32x2 tag, unsigned fq, int b, int w, int lane, int nt80, int ntg,
                   unsigned rv0, unsigned rv1, unsigned rsb, u32x4& r0, u32x2& r1, Park& pk PROFARG) {
    const int t = lane & 31, hh = lane >> 5, wl = w & 3, grp = w >> 2, kh = w & 1;
    const int head = (b & 7) * 4 + (b >> 6), qb = (b >> 3) & 7;
    const unsigned gb = kh * 61440, kvb = WS_KV + head * 163840;
    pk.a = retile_read(rt, b, w, lane);
    const unsigned l16 = lane * 16;
    unsigned va = hh * 1024 + grp * 256 + t * 8 + kh * 2560, dm = gb + (w >> 1) * 1024, sq = WS_Q + head * 81920 + (qb * 4 + (w & 2)) * 2560;
    const int n0 = (head * 20 + ((lane & 16) ? 19 : 0)) >> 7;
    unsigned fo = WS_FQ + (w == 0 ? (5 + 5 * hh + n0) * 16 + (lane & 15) : n0 * 16 + 2 * qb + (lane & 1)) * 128;
    unsigned skv = kvb + kh * 81920 + (w >> 1) * 1024;
    u32x8 qm; u32x2 qt;
    asm volatile("s_cmp_eq_u32 %[w], 2\n\ts_waitcnt vmcnt(0) lgkmcnt(0)\n\ts_barrier\n\ts_cbranch_scc0 1f\n\tbuffer_store_dwordx2 %[tag], %[fq], %[rs], 0 offen sc1\n1:"
                 : "+{v[176:207]}"(pk.a), "+v"(fo), "+v"(va), "+s"(skv), "+s"(sq), "+s"(dm) : [w] "s"(w), [tag] "v"(tag), [fq] "v"(fq), [rs] "s"(rws) : "memory", "scc");
    TS(3);
    SBAR();
    const unsigned a0 = gb + hh * 1024 + t * 8, at = gb + lane * 8 + 2048, av = gb + 2560 + hh * 320 + (t < 20 ? t : 0) * 16;
    const unsigned ones = t == 20 ? 0x3f803f80u : 0u;
    const unsigned wob = WS_WO + nt80 * 102400;
    u32x8 sa;
    sa[0] = skv;
    sa[2] = WS_O + b * 10240 + w * 1280;
    sa[1] = WS_B + qb * QBS + (head * 8 + w) * 1408;
    sa[3] = WS_B + qb * QBS + OFF_F + (head * 8 + w) * 3456;
    const unsigned mv = l16 + (lane < 16 ? sa[2] + 1024 : lane < 40 ? sa[1] + 768 : sa[3] + 2432);
    const unsigned ro = nt80 & 3, pb = WS_WGU + ntg * 143360, p0 = (((ntg & 15) * 5) >> 3) * 14336, f0 = grp ? 68 + 18 * wl : 14 * wl;
    sa[4] = wob + mod5(ro + wl + (wl != 0)) * 20480; sa[5] = ((0x4176u >> (4 * wl)) & 7) * 20480;
    sa[6] = pb + wrapw(p0 + (grp ? f0 + 14 : 56 + 3 * wl) * 1024); sa[7] = L_FL + w * 16;
    u32x2 sb = {pb + wrapw(p0 + f0 * 1024), rsb};
    const unsigned vt = lane < 17 ? WS_WD + nt80 * 276480 + qb * 34560 + (w * 17 + lane) * 256 : OOB;
    const unsigned vs = b < 135 ? l16 ^ (((lane >> 1) ^ (lane >> 3)) & 1) * 0xa0 : b * 8 + w < 1820 ? l16 : OOB;
    const unsigned vfs = (w == 2 && lane == 0) ? WS_FR + b * 8 : OOB, vfe = (b == 0 && w == 0 && lane == 0) ? WS_E : OOB, vfp = WS_FR + w * 256 + t * 8;
    const unsigned fg = (b * 205) >> 10;
    const unsigned vcs = (b < 135 && w < 2) ? WS_CS + ((b - fg * 5) * 3584 + fg * 128 + w * 64 + lane) * 4 : OOB;
    const unsigned ma = 40960 + (w & 2) * 30720 + grp * 6144 + l16;
    unsigned ov = (t >> 4) * 20480 + (t & 15) * 16, ow = hh ? OOB : ov;
    unsigned ob = WS_O + (qb * 16 + wl * 4 + grp * 2) * 20480, oc = ob + (64 + (head >> 1)) * 256 + (head & 1) * 8;
    ov += hh * 256; ob += head * 512;
    f32x16 O, P;
    asm volatile(
        "ds_read2st64_b32 v[196:197], %[cp] offset1:2\n\tds_read2st64_b32 v[198:199], %[cp] offset0:4 offset1:6\n\t"
        Z8("96") Z8("104") Z8("112") Z8("120") Z8("136") "v_mov_b32 v164, %[one]\n\tv_mov_b32 v165, %[one]\n\tv_mov_b64 v[166:167], v[164:165]\n\tv_mov_b64 v[168:169], v[164:165]\n\t"
        "v_mov_b64 v[170:171], v[164:165]\n\tv_mov_b64 v[172:173], v[164:165]\n\tv_mov_b64 v[174:175], v[164:165]\n\tv_mov_b64 v[192:193], -1\n\tv_mov_b64 v[194:195], -1\n\t"
        "s_mov_b32 s56, 0xfffff\n\ts_mov_b32 s57, s56\n\ts_movk_i32 s58, " SPINS "\n\ts_mov_b32 m0, %[dm]\n\ts_cmp_gt_u32 %[w], 1\n\t" "s_cbranch_scc1 2f\n"
        "1:\n\tbuffer_load_dwordx2 v[32:33], %[fo], s[40:43], 0 offen sc1\n\ts_waitcnt vmcnt(0)\n\tv_cmp_ne_u64 vcc, v[32:33], %[tag]\n\t"
        "s_cbranch_vccz 2f\n\ts_sub_u32 s58, s58, 1\n\ts_cbranch_scc0 1b\n"
        "2:\n\ts_barrier\n\t" TSA("4") KVP LDQ5 KVP "s_barrier\n\t" KVP KVP
        LGK(0) "v_add_f32 v196, v196, v197\n\tv_add_f32 v198, v198, v199\n\tv_add_f32 v196, v196, v198\n\ts_movk_i32 s58, " SPINS "\n\t"
        "s_waitcnt vmcnt(2)\n\ts_barrier\n\t" TSA("5")
        KT("0") KR("0") VR(168, "0") "s_waitcnt lgkmcnt(4)\n\t" QKT(32, 0) "s_waitcnt lgkmcnt(2)\n\ts_nop 7\n\ts_nop 7\n\ts_nop 2\n\t" QKA(32) QKB(32) KR("256")
        PVC "s_waitcnt vmcnt(0)\n\ts_barrier\n\ts_nop 6\n\t" TSS("17") TSF("17")
        QUADP(0, 10240, KVP, KVP STW(176, 68), KVP, STW(180, 69) "buffer_store_dword v196, %[vcs], s[40:43], 0 offen sc1\n\t", KVP, STW(184, 70), KVP, STW(188, 71))
        "s_waitcnt vmcnt(8)\n\ts_barrier\n\t"
        QUADP(10240, 20480, KVP, STM(l16, "s46", 0), KVP, STM(mv, "0", 0), KVP, "", KVP, "")
        "s_waitcnt vmcnt(2)\n\ts_barrier\n\tbuffer_store_dwordx2 %[tag], %[vfs], s[40:43], 0 offen sc1\n\t"
        QUADP(20480, 30720, KVP, STM(l16, "s45", 0), KVP_(-57344), KVP, KVP, STM(l16, "s47", 0), KVP, KVP)
        QUADP(30720, 40960, KVP, "buffer_load_dwordx4 v[248:251], %[rv0], %[rx], s53 offen\n\tbuffer_load_dwordx2 v[252:253], %[rv1], %[rx], s53 offen\n\t", "buffer_load_dwordx2 v[198:199], %[vfp], s[40:43], 0 offen sc1\n\t", STM(l16, "s47", 1024), "", STM(l16, "s47", 2048), "", "")
        "s_mov_b32 m0, s49\n\ts_waitcnt vmcnt(2)\n"
        "3:\n\tv_cmp_ne_u64 vcc, v[198:199], %[tag]\n\ts_cbranch_vccz 4f\n\t"
        "buffer_load_dwordx2 v[198:199], %[vfp], s[40:43], 0 offen sc1\n\ts_sub_u32 s58, s58, 1\n\ts_waitcnt vmcnt(0)\n\ts_cbranch_scc0 3b\n"
        "4:\n\t" PVC "s_barrier\n\t" TSA("6") "buffer_store_dwordx2 %[tag], %[vfe], s[40:43], 0 offen sc1\n\t"
        TSS("25")
        QUADP(40960, 51200, PK(176, 0), PK(180, 1024) PKA, PK(184, 0) TCH(0), PK(188, 1024) PKA, PK(192, 0) PK(196, 1024) PKA, PK(200, 0), PK(204, 1024) PKA, PK(208, 0))
        QUAD(51200, 0, PK(212, 1024) PKA, PK(216, 0), PK(220, 1024) PKA TCH(128), PK(224, 0), PK(228, 1024) "s_mov_b32 s52, s50\n\t" PK(232, 0), PK(236, 1024) PKA, PK(240, 0), PK(244, 1024))
        "s_cbranch_vccz 5f\n\ts_waitcnt vmcnt(20)\n\ts_barrier\n5:\n\t" TSF("30")
        QUAD(0, 10240, WO4, "", WO4, "", WO4, "", WO4, "")
        QUAD3(10240, 0, WO4, "", "", "", "", "")
        STEP_(64, 168, 172, "", "v_mov_b32 v45, s51\n\t", "v_mov_b32 v41, 0\n\t", "v_mov_b64 v[42:43], 0\n\t", "v_mov_b32 v44, -1\n\t") TSI
        "v_xor_b32 v46, 16, v45\n\ts_waitcnt vmcnt(0)\n\ts_setprio 1\n\t" SMF(112, 172, 136) "s_cbranch_vccnz 5f\n\ts_barrier\n"
        "5:\n\ts_movk_i32 s58, " SPINS "\n\ts_nop 7\n\ts_nop 4\n\t" TSA("7")
        "ds_write_b128 %[e0], v[112:115]\n\tds_write_b128 %[e0], v[116:119] offset:1024\n\tds_write_b32 %[e0], v120 offset:2048\n\t"
        "s_mov_b32 exec_hi, 0\n\tds_write_b128 %[e0], v[120:123] offset:2048\n\t" FSET("v44", "v45", 4)
        "5:\n\t" FGET("v47", "v46", 4) RD1("40", "%[e1]", "2048") "s_mov_b32 exec_hi, -1\n\t" RD1("32", "%[e1]", "0") RD1("36", "%[e1]", "1024") "ds_read_b32 v40, %[e1] offset:2048\n\t"
        "s_mov_b32 exec_hi, 0\n\t" LGK(4) FTST("v47", "s58", "5b") "s_mov_b32 exec_hi, -1\n\t" LGK(0)
        : "=&{v[96:111]}"(O), "=&{v[32:47]}"(P), "=&{v[144:151]}"(qm), "=&{v[152:153]}"(qt), "+{v[176:207]}"(pk.a), "=&{v[208:239]}"(pk.b), "=&{v[240:247]}"(pk.c), "=&{v[248:251]}"(r0), "=&{v[252:253]}"(r1), "=&{v154}"(pk.d),
          "+{s[44:51]}"(sa), "+{s[52:53]}"(sb), "+v"(ov), "+v"(ow), "+s"(ob), "+s"(oc) PFIO
        : "{s[40:43]}"(rws), "{s[60:75]}"(rt.t), [a0] "v"(a0), [at] "v"(at), [av] "v"(av), [l16] "v"(l16), [tag] "v"(tag), [vcs] "v"(vcs), [fo] "v"(fo), [sq] "s"(sq),
          [cp] "v"(L_CP + (w & 1) * 256 + lane * 4), [mv] "v"(mv), [vs] "v"(vs), [vt] "v"(vt), [pb] "s"(pb), [pe] "s"(pb + 143360), [one] "v"(ones), [idx] "v"(0xDDDD),
          [va] "v"(va), [vb] "v"(va + 2560 - kh * 5120), [vc] "v"(grp * 256 + t * 8 + (hh ^ kh) * 2560), [e0] "v"(ma + kh * 3072), [e1] "v"(ma + 3072 - kh * 3072), [vfs] "v"(vfs), [vfe] "v"(vfe), [vfp] "v"(vfp), [rv0] "v"(rv0), [rv1] "v"(rv1),
          [dm] "s"(dm), [w] "s"(w), [grp] "s"(grp), [rx] "s"(mkrs(p.x, 2048 * 1280))
        : "memory", "scc", "vcc", "s56", "s57", "s58", VCL48, VCL64, VCL80, VCL112, VCL128, "v140", "v141", "v142", "v143", "v156", "v157", VCL160 PFCL);
    float o[12];
#pragma unroll
    for (int i = 0; i < 12; ++i) o[i] = O[i] + P[i];
    {
        f32x2 z = {0.f, 0.f};
#pragma unroll
        for (int r = 0; r < 12; r += 2) z += f32x2{o[r], o[r + 1]} * 0.f;
        const float l = (hh ? o[8] : 1.f) + (z[0] + z[1]);
        const bool bad = !(l < 1.8446744e19f && l > 8.6736174e-19f);
        if (__builtin_expect(anyl(bad), 0)) {
            float m = -__builtin_inff();
            const unsigned ko = hh * 1024 + t * 8, vo = 2560 + hh * 320 + (t < 20 ? t : 0) * 16;
            const u32x4 qa = {qm[0], qm[1], qm[2], qm[3]}; const u32x2 qe = {hh ? 0u : qt[0], hh ? 0u : qt[1]};
            f32x16 c = {}, A = {};
#pragma unroll 1
            for (int it = 0; it < 128; ++it) {
                const unsigned sb = kvb + ((it & 63) >> 1) * 5120;
                const u32x2 k0 = ld8c(rws, ko, sb + (it & 1) * 256), k1 = ld8c(rws, ko + 512, sb + (it & 1) * 256), k2 = ld8c(rws, t * 8 + 2048, sb + (it & 1) * 256);
                f32x16 s = qk_tile(u32x4{k0[0], k0[1], k1[0], k1[1]}, k2, qa, qe, c);
                if (it < 64) {
                    m = max16(s, m);
                    if (it == 63) {
                        const f32x2 mm = swap32(m, m);
                        m = __builtin_fmaxf(mm[0], mm[1]);
#pragma unroll
                        for (int r = 0; r < 16; ++r) c[r] = -m;
                    }
                } else {
                    u32x4 v0 = ld16c(rws, vo, sb + (it & 1) * 1280), v1 = ld16c(rws, vo + 640, sb + (it & 1) * 1280);
#pragma unroll
                    for (int i = 0; i < 4; ++i) { v0[i] = t < 20 ? v0[i] : ones; v1[i] = t < 20 ? v1[i] : ones; }
#pragma unroll
                    for (int r = 0; r < 16; ++r) s[r] = __builtin_amdgcn_exp2f(s[r]);
                    A = pv16(v0, __builtin_shufflevector(s, s, 0, 1, 2, 3, 4, 5, 6, 7), A);
                    A = pv16(v1, __builtin_shufflevector(s, s, 8, 9, 10, 11, 12, 13, 14, 15), A);
                }
            }
#pragma unroll
            for (int r = 0; r < 12; ++r) o[r] = A[r];
        }
    }
    const float inv = __builtin_amdgcn_rcpf(swap32(o[8], o[8])[1]);
    st16(rws, ov, ob, u32x4{pk2(o[0] * inv, o[1] * inv), pk2(o[2] * inv, o[3] * inv), pk2(o[4] * inv, o[5] * inv), pk2(o[6] * inv, o[7] * inv)});
    st8(rws, ow, oc, u32x2{pk2(o[8] * inv, o[9] * inv), pk2(o[10] * inv, o[11] * inv)});
}

#define MF16(A, W, B) "v_mfma_f32_16x16x32_bf16 v[" A ":" A "+3], v[" W ":" W "+3], v[" B ":" B "+3], v[" A ":" A "+3]\n\t"
#define MF0(A, W, B) "v_mfma_f32_16x16x32_bf16 v[" A ":" A "+3], v[" W ":" W "+3], v[" B ":" B "+3], 0\n\t"
#define RD5(W, V, O) RD1(W, V, O) RD1(W "+4", V, O "+1024") RD1(W "+8", V, O "+2048") RD1(W "+12", V, O "+3072") RD1(W "+16", V, O "+4096")
#define WR1(V, R, O) "ds_write_b128 " V ", v[" R ":" R "+3] offset:" O "\n\t"
#define MX3(B) "v_max3_u32 %[t], v[" B "], v[" B "+1], v[" B "+2]\n\t"
#define MX2(B) "v_max3_u32 %[t], v[" B "], v[" B "+1], %[t]\n\t"
#define MXE(B) "v_max_u32 %[t], v[" B "], %[t]\n\tv_cmp_eq_u32 vcc, -1, %[t]\n\ts_and_b64 vcc, vcc, s[60:61]\n\t"
#define CK4(B) MX3(B) MXE(B "+3")
#define CK8(B) MX3(B) MX2(B "+3") MX2(B "+5") MXE(B "+7")
#define CK16(B) MX3(B) MX2(B "+3") MX2(B "+5") MX2(B "+7") MX2(B "+9") MX2(B "+11") MX2(B "+13") MXE(B "+15")
#define CK28(B) MX3(B) MX2(B "+3") MX2(B "+5") MX2(B "+7") MX2(B "+9") MX2(B "+11") MX2(B "+13") MX2(B "+15") MX2(B "+17") MX2(B "+19") MX2(B "+21") MX2(B "+23") MX2(B "+25") MXE(B "+27")
#define SPIN0 "s_movk_i32 s59, " SPINS "\n\ts_mov_b64 s[60:61], -1\n\t"
#define SPIN1 "s_sub_u32 s59, s59, 1\n\ts_cselect_b64 s[60:61], 0, s[60:61]\n\t"
#define UNP(D, S) "v_lshlrev_b32 v[" #D "], 16, v" #S "\n\tv_and_b32 v[" #D "+1], 0xffff0000, v" #S "\n\t"
#define PKADD(D, S) "v_pk_add_f32 v[" D ":" D "+1], v[" D ":" D "+1], v[" S ":" S "+1]\n\t"
#define CVT(D, S) "v_cvt_pk_bf16_f32 v[" D "], v[" S "], v[" S "+1]\n\t"
#define WON(n, W, B, R) MF16("72+4*" #n, W "+4*" #n, B) R
#define WOK(W, B, V, O) WON(0, W, B, RD1(W, V, O)) WON(1, W, B, RD1(W "+4", V, O "+1024")) WON(2, W, B, RD1(W "+8", V, O "+2048")) WON(3, W, B, RD1(W "+12", V, O "+3072")) WON(4, W, B, RD1(W "+16", V, O "+4096"))
#define WOE(W, B) WON(0, W, B, "") WON(1, W, B, "") WON(2, W, B, "") WON(3, W, B, "") WON(4, W, B, "")
#define WOL(B, S) LD1(#B, "%[l16]", #S, "0") LD1(#B "+4", "%[l16]", #S, "1024")
#define WOC(p, N, B) "1" #p ":\n\t" VM(N) CK8(#B) "s_cbranch_scc1 2" #p "f\n\t" TSI
#define WOT(p) "2" #p ":\n\ts_call_b64 s[56:57], 8f\n\ts_branch 1" #p "b\n"
#define WOR(B, S) CK8(#B) "s_cbranch_scc0 1f\n\ts_mov_b64 exec, vcc\n\t" WOL(B, S) "s_mov_b64 exec, -1\n1:\n\t"
#define PW2(V, R, O) WR1(V, #R, #O) WR1(V, #R "+4", #O "+1024")
#define LD5(R) LD1(#R, "%[l16]", "s51", "0") LD1(#R "+4", "%[l16]", "s51", "1024") LD1(#R "+8", "%[l16]", "s51", "2048") LD1(#R "+12", "%[l16]", "s51", "3072") \
    "s_add_u32 s51, s51, 4096\n\t" LD1(#R "+16", "%[l16]", "s51", "0") "s_add_u32 s51, s51, 6144\n\t"
#define CSL(R) "buffer_load_dword " R ", %[ve], s[40:43], s49 offen sc1\n\ts_add_u32 s49, s49, 14336\n\t"
DI void phase_wo(rsrc_t rws, int w, unsigned lane, int mt, int nt, int ntg, u32x4& r0, u32x2& r1, Park& pk PROFARG) {
    const unsigned tg = w >> 1, kh = w & 1, wl = w & 3, grp = w >> 2, t = lane & 15, g = lane >> 4, l16 = lane * 16, la = kh * 5120 + l16, e = (w ^ 4) * 64 + lane, ro = nt & 3;
    const unsigned ab = WS_O + (mt * 4 + tg) * 20480 + kh * 10240, hb = WS_B + (mt >> 2) * QBS + nt * 256, tk = (mt & 3) * 4 + tg;
    u32x8 sa = {ab + ro * 2048, ab + (ro + 1) * 2048, ab + mod5(ro + 2) * 2048, ab + mod5(ro + 3) * 2048, ab + mod5(ro + 4) * 2048, WS_CS + ntg * 448, hb + tk * 20480,
                WS_WO + nt * 102400 + (ro + 1) * 20480 + kh * 5120};
    const unsigned ma = 40960 + (w & 2) * 30720 + grp * 6144 + l16;
    const unsigned q0 = ((grp ? 0x8a785014u : 0x70423400u) >> (8 * wl)) & 255, q1 = w == 3 ? 14 : q0 + 8, q2 = grp ? q0 + 14 : 40 + (w & 2) * 30 + kh * 3;
    unsigned tmp, cs;
    asm volatile(
        CSL("v92") CSL("v93") CSL("v94") CSL("v95") CSL("%[cs]") LD5(136) LD5(156) SPIN0
        RD5("96", "%[wa]", "0") RD5("116", "%[wa]", "10240")
        UNP(72, 248) UNP(74, 249) UNP(76, 250) UNP(78, 251) Z8("80") UNP(88, 252) UNP(90, 253)
        VM(15) TSA("20") TSS("45") WOL(32, s44) WOL(40, s45) WOL(48, s46) WOL(56, s47) WOL(64, s48)
        "s_waitcnt vmcnt(10) lgkmcnt(0)\n\tv_add_f32 v92, v92, v93\n\tv_add_f32 v94, v94, v95\n\tv_add_f32 v92, v92, %[cs]\n\tv_add_f32 %[cs], v92, v94\n"
        WOC(0, 8, 32) TSA("8") WOK("96", "32", "%[wa]", "20480") WOK("116", "36", "%[wa]", "30720")
        WOC(1, 6, 40) WOK("136", "40", "%[wb]", "0") WOK("156", "44", "%[wb]", "10240")
        WOC(2, 4, 48) LGK(10) WOK("96", "48", "%[wc]", "0") WOK("116", "52", "%[wc]", "10240")
        WOC(3, 2, 56) LGK(10) WOE("136", "56") WOE("156", "60")
        WOC(4, 0, 64) LGK(0) WOE("96", "64") WOE("116", "68") TSA("9")
        "s_cmp_eq_u32 %[kh], 0\n\ts_movk_i32 s59, " SPINS "\n\tv_mov_b32 v32, -1\n\ts_nop 3\n\t" WR1("%[xw]", "80", "0") WR1("%[xw]", "84", "1024") "s_cbranch_scc1 1f\n\t" WR1("%[xw]", "88", "2048")
        "1:\n\t" FSET("v32", "%[fw]", 8)
        "1:\n\t" FGET("%[t]", "%[fp]", 8) RD1("80", "%[xr]", "0") RD1("84", "%[xr]", "1024") RD1("92", "%[xr]", "2048") LGK(3) FTST("%[t]", "s59", "1b") "s_cmp_eq_u32 %[kh], 0\n\t"
        LGK(2) PKADD("72", "80") PKADD("74", "82") LGK(1) PKADD("76", "84") PKADD("78", "86") CVT("248", "72") CVT("249", "74") CVT("250", "76") CVT("251", "78")
        "buffer_store_dwordx4 v[248:251], %[h0], s[40:43], s50 offen sc1\n\t" LGK(0) "s_cbranch_scc0 9f\n\t"
        PKADD("88", "92") PKADD("90", "94") CVT("252", "88") CVT("253", "90") "buffer_store_dwordx2 v[252:253], %[h1], s[40:43], s50 offen sc1\n\ts_branch 9f\n"
        WOT(0) WOT(1) WOT(2) WOT(3) WOT(4)
        "8:\n\t" VM(0) WOR(32, s44) WOR(40, s45) WOR(48, s46) WOR(56, s47) WOR(64, s48) VM(0) SPIN1 "s_setpc_b64 s[56:57]\n9:\n\t"
        TSA("31") FSET("v32", "%[fw]", 12) "ds_write_b32 %[vl], %[cs]\n\ts_cmp_eq_u32 %[grp], 0\n\ts_cbranch_scc1 1f\n\t" WR1("%[p2]", "244", "3072") "s_branch 2f\n"
        "1:\n\ts_sleep 1\n\t" FGET("%[t]", "%[fs]", 0) LGK(0) FTST("%[t]", "s59", "1b")
        "2:\n\t" PW2("%[p0]", 176, 0) PW2("%[p0]", 184, 2048) PW2("%[p0]", 192, 4096) PW2("%[p0]", 200, 6144) PW2("%[p1]", 208, 0) PW2("%[p1]", 216, 2048) PW2("%[p1]", 224, 4096)
        PW2("%[p2]", 232, 0) WR1("%[p2]", "240", "2048") FSET("v32", "%[fw]", 132) "s_setprio 0"
        : "+{v[248:251]}"(r0), "+{v[252:253]}"(r1), "+{v[176:207]}"(pk.a), "+{v[208:239]}"(pk.b), "+{v[240:247]}"(pk.c), "+{s[44:51]}"(sa), [t] "=&v"(tmp), [cs] "=&v"(cs) PFIO
        : "{s[40:43]}"(rws), [l16] "v"(l16), [wa] "v"(la + 122880), [wb] "v"(la + 20480), [wc] "v"(la + 81920), [kh] "s"(kh), [grp] "s"(grp),
          [p0] "v"(q0 * 1024 + l16), [p1] "v"(q1 * 1024 + l16), [p2] "v"(q2 * 1024 + l16),
          [fw] "v"(L_FL + w * 16), [fp] "v"(L_FL + (l16 & (grp * 64 + 48))), [fs] "v"(L_FL + (l16 & 112) + ((lane & 4) ? 4 : 12)),
          [ve] "v"(e < 112 ? (e ^ (((lane >> 1) ^ (lane >> 3)) & 1) * 10) * 4 : OOB), [vl] "v"(e < 112 ? L_CS + e * 4 : L_FL + 260),
          [xw] "v"(ma + 3072 - kh * 3072), [xr] "v"(ma + kh * 3072),
          [h0] "v"((2 * g + kh) * 2048 + t * 16), [h1] "v"((8 + (g >> 1)) * 2048 + t * 16 + (g & 1) * 8)
        : "memory", "scc", "vcc", "s56", "s57", "s59", "s60", "s61", VCL32, VCL48, VCL64, VCL80, VCL96, VCL112, VCL128, VCL140, VCL160 PFCL);
    TS(10);
    float s = 0.f, q = 0.f;
#pragma unroll
    for (int k = 0; k < 6; ++k) {
        const bf2v v = __builtin_bit_cast(bf2v, k < 4 ? r0[k & 3] : r1[k & 1]);
        s = __builtin_amdgcn_fdot2_f32_bf16(v, __builtin_bit_cast(bf2v, 0x3f803f80u), s, false);
        q = __builtin_amdgcn_fdot2_f32_bf16(v, v, q, false);
    }
    const f32x2 y = swap32(s, q);
    const float z = sum16(y[0] + y[1]);
    const f32x2 v = swap32(z, z);
    st8(rws, g == 0 ? t * 16 + kh * 8 : OOB, hb + OFF_HS + tk * 2048, u32x2{__float_as_uint(v[0]), __float_as_uint(v[1])});
}

#define GW0 LGK(4)
#define GW1
#define GW2
#define GW3 LGK(5)
#define GW4
#define GW5 LGK(5)
#define GW6
#define GUN(MF, n, H, R, X) GW##n MF("140+8*" #n, "112+4*" #n, H) X MF("144+8*" #n, "112+4*" #n, H "+8") R
#define RDW_(LO, HI, KB) ".if (7*(\\k)+\\n >= " #LO ") && (7*(\\k)+\\n < " #HI ")\n.set .Lwa, (7*(\\k)+\\n-" #LO "+" #KB ")*1024\n.endif\n"
#define RDWDEF ".macro RDW k, n, a0, a1\n" RDW_(0, 14, 0) RDW_(14, 42, 52) RDW_(42, 50, 112) RDW_(50, 56, 14) RDW_(56, 62, 40) RDW_(62, 68, 100) RDW_(68, 86, 20) RDW_(86, 104, 80) RDW_(104, 140, 88) \
    ".if 7*(\\k)+\\n == 104\nv_add_u32 \\a1, 0x8000, \\a1\n.endif\n.if (\\k) == 20\n.set .Lwa, 65536+52224+64*\\n\n.endif\n" \
    ".if .Lwa < 65536\nds_read_b128 v[112+4*\\n:115+4*\\n], \\a0 offset:.Lwa\n.else\nds_read_b128 v[112+4*\\n:115+4*\\n], \\a1 offset:.Lwa-65536\n.endif\n.endm\n\t"
#define RDW(K, n) "RDW " K ", " #n ", %[l16], %[wa]\n\t"
#define GUK(MF, H, K, X0, X1, X2, X3, X4, X5, X6) \
    GUN(MF, 0, H, RDW(K, 0), X0) GUN(MF, 1, H, RDW(K, 1), X1) GUN(MF, 2, H, RDW(K, 2), X2) GUN(MF, 3, H, RDW(K, 3), X3) \
    GUN(MF, 4, H, RDW(K, 4), X4) GUN(MF, 5, H, RDW(K, 5), X5) GUN(MF, 6, H, RDW(K, 6), X6)
#define LH0(H, S) LD1(#H, "%[l16]", #S, "0")
#define LH1(H, S) LD1(#H "+4", "%[l16]", #S, "1024")
#define LH2(H, S) LD1(#H "+8", "%[lb]", #S, "0")
#define LH3(H, S) LD1(#H "+12", "%[lb]", #S, "1024")
#define LDH(H, S) LH0(H, S) LH1(H, S) LH2(H, S) LH3(H, S)
#define LH(H, S) LH0(H, S), LH1(H, S), LH2(H, S), LH3(H, S)
#define LS(R, O) LD1(#R, "%[l16]", "s54", #O)
#define LDST LS(32, 0) LS(36, 1024) LS(40, 2048) LS(44, 3072)
#define WL(R, O) LD1(#R, "%[l16]", "s52", #O)
#define WA "s_add_u32 s52, s52, 4096\n\t"
#define W15 WL(196, 0) WL(200, 1024) WL(204, 2048) WL(208, 3072) WA WL(212, 0) WL(216, 1024) WL(220, 2048) WL(224, 3072) WA WL(228, 0) WL(232, 1024) WL(236, 2048) WL(240, 3072) WA \
    WL(244, 0) WL(24, 1024) WL(28, 2048)
#define WEND "v_add_u32 %[lb], 4096, %[l16]\n\t" WL(32, 3072) LD1("36", "%[lb]", "s52", "0") LD1("40", "%[lb]", "s52", "1024") LD1("44", "%[z4]", "s52", "2048") LD1("96", "%[z4]", "s52", "3072") WA \
    LD1("100", "%[z4]", "s52", "0") LD1("104", "%[z4]", "s52", "1024") LD1("108", "%[z4]", "s52", "2048")
#define CKS(N, B, s) VM(N) MX3(B), MX2(B "+3"), MX2(B "+5"), MX2(B "+7"), MX2(B "+9"), MX2(B "+11"), MX2(B "+13") MXE(B "+15") "s_cbranch_scc1 4" #s "f\n3" #s ":\n\t"
#define GUK_(...) GUK(__VA_ARGS__)
#define GUS(MF, s, H, PRE, HN, N, ...) GUK_(MF, #H, "2*" #s "+1", __VA_ARGS__) PRE GUK_(MF16, #H "+4", "2*" #s "+2", CKS(N, #HN, s)) TSI
#define REDO(s, L, CK) "4" #s ":\n\ts_mov_b64 exec, vcc\n\t" L "s_mov_b64 exec, -1\n\t" VM(0) SPIN1 CK "s_cbranch_scc1 4" #s "b\n\ts_branch 3" #s "b\n"
#define HNX(A, B) "s_add_u32 s" #B ", s" #A ", 2048\n\ts_cmp_eq_u32 s" #B ", s55\n\ts_cselect_b32 s" #B ", %[hb], s" #B "\n\t"
#define SW32(A, B) "v_permlane32_swap_b32 v[" A "], v[" B "]\n\t"
#define SW16(A, B) "v_permlane16_swap_b32 v[" A "], v[" B "]\n\t"
#define EPF(A, C, T) "v_pk_fma_f32 v[" A ":" A "+1], v[" C ":" C "+1], v[16+4*" #T ":17+4*" #T "], v[" A ":" A "+1]\n\t"
#define EPT(A, C, T, D) EPF(A, C, T) EPF(A "+2", C "+2", T) "v_max_f32 v[" A "], 0, v[" A "]\n\tv_max_f32 v[" A "+1], 0, v[" A "+1]\n\t" \
    "v_pk_mul_f32 v[" A "+2:" A "+3], v[" A "+2:" A "+3], v[18+4*" #T ":19+4*" #T "]\n\t" \
    "v_pk_mul_f32 v[" A ":" A "+1], v[" A ":" A "+1], v[" A "+2:" A "+3]\n\t" CVT(D, A)
#define EPN(n, D) EPT("140+8*" #n, "112+4*" #n, 0, D) EPT("144+8*" #n, "112+4*" #n, 1, D "+1")
#define EP2(n, m, D, N) LGK(N) EPN(n, D) EPN(m, D "+2") SW32(D, D "+1") "s_nop 0\n\t" SW32(D "+2", D "+3") \
    "buffer_store_dwordx4 v[" D ":" D "+3], %[f0], s[40:43], s53 offen sc1\n\ts_add_u32 s53, s53, 15872\n\t"
#define LF0(F, S) LD1(F, "%[l16]", S, "0")
#define LF1(F, S) LD1(F "+4", "%[l16]", S, "1024")
#define LF2(F, S) LD1(F "+8", "%[l16]", S, "2048")
#define LF3(F, S) LD1(F "+12", "%[l16]", S, "3072")
#define LF4(F, S) LD1(F "+16", "%[lb]", S, "0")
#define LF5(F, S) LD1(F "+20", "%[lb]", S, "1024")
#define LF6(F, S) LD1(F "+24", "%[z4]", S, "2048")
#define LDF(F, S) LF0(F, S) LF1(F, S) LF2(F, S) LF3(F, S) LF4(F, S) LF5(F, S) LF6(F, S)
#define DK(MF, F, a, b, c, d, e) MF("168", #a, F) MF("172", #b, F) MF("176", #c, F) MF("180", #d, F) MF("184", #e, F)
#define DT(F, X0, X1, X2, X3, X4, X5, X6) DK(MF0, F, 196, 200, 204, 208, 212) X0 DK(MF16, F "+4", 216, 220, 224, 228, 232) X1 DK(MF16, F "+8", 236, 240, 244, 24, 28) X2 \
    DK(MF16, F "+12", 48, 52, 56, 60, 64) X3 DK(MF16, F "+16", 68, 72, 76, 80, 84) X4 DK(MF16, F "+20", 88, 92, 32, 36, 40) X5 DK(MF16, F "+24", 44, 96, 100, 104, 108) X6 TSI
#define DTL(F, S) DT(F, LF0(F, S), LF1(F, S), LF2(F, S), LF3(F, S), LF4(F, S), LF5(F, S), LF6(F, S))
#define DTE(F) DT(F, "", "", "", "", "", "", "")
#define DW(S) "v_add_u32 %[wa], " S ", %[l16]\n\t" WR1("%[wa]", "168", "0") WR1("%[wa]", "172", "1024") WR1("%[wa]", "176", "2048") WR1("%[wa]", "180", "3072") WR1("%[wa]", "184", "4096")
#define DC(N, F, s) VM(N) CK28(F) "s_cbranch_scc1 4" #s "f\n3" #s ":\n\t"
DI void phase_ffn(rsrc_t rws, int w, unsigned lane, int mt, int nt, int qg, int ntg, const Park& pk PROFARG) {
    const unsigned t = lane & 15, g = lane >> 4, l16 = lane * 16, qbb = WS_B + qg * QBS, wg = (w & 3) * 2 + (w >> 2), hb = qbb + wg * 40960, k0 = 7 * w - (w == 7);
    const unsigned fb = WS_B + (mt >> 2) * QBS + OFF_F + (mt & 3) * 4 * 55296 + k0 * 1024, fv = (g >> 1) * 55296 + t * 16;
    u32x8 sa; u32x4 sb = {WS_WD + nt * 276480 + k0 * 5120, qbb + OFF_F + wg * 110592 + ntg * 256, qbb + OFF_HS + wg * 4096, hb + 20480};
#pragma unroll
    for (int i = 0; i < 4; ++i) { sa[i] = fb + i * 55296; sa[4 + i] = i * 40960 + w * 5120; }
    unsigned wa = 65536 + l16, h0 = hb + (((ntg & 15) * 5) >> 3) * 2048, tmp = L_FL + (l16 & ((w & 4) * 16 + 48)), lb = l16 + 20480;
    u32x32 da = pk.a, db = pk.b; u32x8 dc = pk.c;
    asm volatile(
        RDWDEF SPIN0 HNX(64, 65) HNX(65, 66) HNX(66, 67) HNX(67, 68) HNX(68, 69) HNX(69, 70) HNX(70, 71) HNX(71, 72) HNX(72, 73)
        "v_and_b32 v17, 0x70, %[l16]\n\ts_mov_b32 s58, s59\n\ts_cmp_eq_u32 %[ng], 31\n\ts_cbranch_scc1 7f\n\t" VM(1) TSA("21") LDH(32, s64) LDH(48, s65)
        "1:\n\t" FGET("v16", "%[t]", 132) LGK(0) FTST("v16", "s58", "1b")
        RDW("0", 0) RDW("0", 1) RDW("0", 2) RDW("0", 3) RDW("0", 4) RDW("0", 5) RDW("0", 6)
        "1:\n\t" VM(4) CK16("32") "s_cbranch_scc1 2f\n\t" TSA("11") TSS("35") LDH(64, s66) LDH(80, s67) LDH(96, s68)
        GUS(MF0, 0, 32, "", 48, 12, "", "", "", "", "", "", "")
        GUS(MF16, 1, 48, "", 64, 12, LH(32, s69), "", "", "")
        GUS(MF16, 2, 64, "", 80, 13, LH(48, s70), WL(196, 0), "", "")
        GUS(MF16, 3, 80, FGET("v16", "v17", 52868), 96, 15, LH(64, s71), WL(200, 1024), WL(204, 2048), "")
        GUS(MF16, 4, 96, "", 32, 18, LH(80, s72), WL(208, 3072) WA, WL(212, 0) "v_cmp_ne_u32 vcc, -1, v16\n\ts_cbranch_vccnz 51f\n50:\n\t" TSA("57"), WL(216, 1024))
        GUS(MF16, 5, 32, "", 48, 21, LH(96, s73), WL(220, 2048), WL(224, 3072) WA, WL(228, 0))
        GUS(MF16, 6, 48, "", 64, 23, LS(32, 0), LS(36, 1024), LS(40, 2048), LS(44, 3072), WL(232, 1024), WL(236, 2048), WL(240, 3072) WA)
        GUS(MF16, 7, 64, "", 80, 24, WL(244, 0), WL(24, 1024), WL(28, 2048), WL(48, 3072) WA, WL(52, 0), WL(56, 1024), WL(60, 2048))
        GUS(MF16, 8, 80, "", 96, 21, WL(64, 3072) WA, WL(68, 0), WL(72, 1024), WL(76, 2048), "", "", "")
        GUK_(MF16, "96", "19", CKS(14, "32", 9))
        "v_lshrrev_b32 %[wa], 4, %[l16]\n\tv_and_b32 %[wa], 48, %[wa]\n\t"
        GUK(MF16, "100", "20",
            WL(80, 3072) WA PKADD("32", "34") PKADD("36", "38") PKADD("40", "42") PKADD("44", "46"),
            WL(84, 0) PKADD("32", "36") PKADD("40", "44"),
            WL(88, 1024) SW32("32", "40") SW32("33", "41") PKADD("32", "40") "v_mov_b64 v[34:35], v[32:33]\n\t",
            WL(92, 2048) SW16("32", "34") SW16("33", "35") PKADD("32", "34") "v_mul_f32 v16, 0xbacccccd, v32\n\t",
            "v_mul_f32 v33, 0x3acccccd, v33\n\tv_fma_f32 v33, -v16, v16, v33\n\tv_max_f32 v33, 0, v33\n\tv_add_f32 v33, 0x3727c5ac, v33\n\t",
            "v_rcp_f32 v18, v33\n\tv_mov_b32 v20, v16\n\tv_mov_b32 v22, v18\n\t",
            SW32("16", "20") SW32("18", "22") "v_mov_b32 v17, v16\n\tv_mov_b32 v21, v20\n\tv_mov_b32 v19, v18\n\tv_mov_b32 v23, v22\n\t")
        TSA("12") WEND EP2(0, 1, "140", 5) EP2(2, 3, "156", 3) EP2(4, 5, "172", 1) LGK(0) EPN(6, "188") "s_nop 1\n\t" SW32("188", "189")
        "buffer_store_dwordx2 v[188:189], %[f1], s[40:43], s53 offen sc1\n"
        TSA("22") "6:\n\ts_barrier\n\t" TSA("23") LDF("112", "s44") LDF("140", "s45")
        DC(7, "112", 13) TSA("13") TSS("50")
        DTL("112", "s46") DC(7, "140", 10) DW("s48") DTL("140", "s47")
        "s_nop 5\n\t" DW("s49") DC(7, "112", 11) TSA("56") DTE("112") DC(0, "140", 12) DW("s50") DTE("140") "s_nop 6\n\t" DW("s51")
        "s_waitcnt lgkmcnt(0)\n\ts_barrier\n\ts_branch 9f\n"
        "2:\n\ts_mov_b64 exec, vcc\n\t" LDH(32, s64) LDH(48, s65) "s_mov_b64 exec, -1\n\t" SPIN1 "s_branch 1b\n"
        "51:\n\t" FGET("v16", "v17", 52868) LGK(0) FTST("v16", "s58", "51b") "s_branch 50b\n"
        REDO(0, LDH(48, s65), CK16("48")) REDO(1, LDH(64, s66), CK16("64")) REDO(2, LDH(80, s67), CK16("80")) REDO(3, LDH(96, s68), CK16("96")) REDO(4, LDH(32, s69), CK16("32"))
        REDO(5, LDH(48, s70), CK16("48")) REDO(6, LDH(64, s71), CK16("64")) REDO(7, LDH(80, s72), CK16("80")) REDO(8, LDH(96, s73), CK16("96")) REDO(9, LDST, CK16("32"))
        REDO(10, LDF("140", "s45"), CK28("140")) REDO(11, LDF("112", "s46"), CK28("112")) REDO(12, LDF("140", "s47"), CK28("140"))
        REDO(13, LDF("112", "s44"), CK28("112"))
        "7:\n\t" LGK(0) W15 WL(48, 3072) WA WL(52, 0) WL(56, 1024) WL(60, 2048) WL(64, 3072) WA WL(68, 0) WL(72, 1024) WL(76, 2048) WL(80, 3072) WA WL(84, 0) WL(88, 1024) WL(92, 2048) WEND
        "1:\n\ts_sleep 4\n\t" LF0("112", "s44") SPIN1 VM(0) CK4("112") "s_cbranch_scc1 1b\n\ts_branch 6b\n9:"
        : "+{v[176:207]}"(da), "+{v[208:239]}"(db), "+{v[240:247]}"(dc), "+{s[52:55]}"(sb), "+{s64}"(h0), [wa] "+&v"(wa), [t] "+&v"(tmp), [lb] "+&v"(lb) PFIO
        : "{s[40:43]}"(rws), "{s[44:51]}"(sa), [l16] "v"(l16), [z4] "v"(w < 6 ? l16 + 4096 : OOB),
          [f0] "v"(fv + (g & 1) * 7936), [f1] "v"(ntg < 30 ? fv + (g & 1) * 8 : OOB), [ng] "s"(ntg), [hb] "s"(hb)
        : "memory", "scc", "vcc", "s58", "s59", "s60", "s61", "s65", "s66", "s67", "s68", "s69", "s70", "s71", "s72", "s73", "v16", "v17", "v18", "v19", "v20", "v21", "v22", "v23",
          "v24", "v25", "v26", "v27", "v28", "v29", "v30", "v31", VCL32, VCL48, VCL64, VCL80, VCL96, VCL112, VCL128, VCL140, VCL160 PFCL);
}
DI f32x4 sum8(unsigned a) {
    f32x4 v[8];
#pragma unroll
    for (int i = 0; i < 8; ++i) v[i] = ldsr<f32x4>(a + i * 5120);
    return ((v[0] + v[1]) + (v[2] + v[3])) + ((v[4] + v[5]) + (v[6] + v[7]));
}
DI u32x2 addres(f32x4 a, unsigned lo, unsigned hi) { return u32x2{pk2(a[0] + bflo(lo), a[1] + bfhi(lo)), pk2(a[2] + bflo(hi), a[3] + bfhi(hi))}; }

#define ROT 0
__global__ void __launch_bounds__(512, 2) block_kernel(Params) {
    u32x16 ka;
    asm volatile("s_load_dwordx16 %0, %1, 0" : "=s"(ka) : "s"(__builtin_amdgcn_kernarg_segment_ptr()) : "memory");
    __shared__ __attribute__((aligned(16))) char lds[163840];
    asm volatile("" :: "v"((LAS char*)lds) : "memory");
    const int lane = threadIdx.x & 63, w = __builtin_amdgcn_readfirstlane(threadIdx.x >> 6), b = blockIdx.x;
    unsigned pre;
    asm volatile("s_cmp_lg_u32 %1, %2\n\ts_cbranch_scc1 1f\n\ts_getpc_b64 s[4:5]\n.Lpc:\n\tv_lshlrev_b32 %0, 7, %3\n\tv_min_u32 %0, .Lend-.Lpc-4, %0\n\t"
                 "global_load_dword %0, %0, s[4:5]\n1:" : "=&{v200}"(pre) : "s"(w), "s"((b >> 3) & 3), "v"(threadIdx.x) : "memory", "scc", "s4", "s5");
    Params p;
    rsrc_t rws;
    const int xcd = b & 7, i = b >> 3;
    Retile rt;
    u32x2 tag;
    unsigned fq;
    {
        const int mt = (xcd >> 1) * 4 + (i & 3), nt = (xcd & 1) * 8 + (i >> 2), rot = ROT;
        QkvSrc c; QkvAdr e; u32x4 row; unsigned rowt, T;
        qkv_start(c, row, rowt, T, rt.t, p, ka, b, mt, nt, rot, w, lane);
        rws = mkrs(p.ws, WS_END);
        TSW(60);
        retile_setup(rt, p, b, lane);
        qkv_setup(e, mt, nt, w, lane);
        fq = lane == 0 ? WS_FQ + (nt * 16 + mt) * 128 : OOB;
        TSW(61);
        asm volatile("s_add_u32 m0, %[dm], 65536\n\ts_cmp_eq_u32 %[nt], 15\n\ts_cbranch_scc1 1f\n\t" QD4 "1:\n\ts_waitcnt vmcnt(12) lgkmcnt(0)\n\ts_add_u32 %0, %0, 1"
                     : "+s"(T), "+{s[60:75]}"(rt.t), "+v"(row), "+v"(rowt), "+v"(fq), "+{v200}"(pre), [sx] "+s"(c.sx), [sw] "+s"(c.sw)
                     : [x0] "v"(c.xo[0]), [x1] "v"(c.xo[1]), [w0] "v"(c.wo[0]), [w1] "v"(c.wo[1]), [rx] "s"(c.rx), [rw] "s"(c.rw), [dm] "s"(w * 4096), [nt] "s"(nt) : "memory", "scc");
        TSW(62);
        const u32x4 gr = row_stats(row, rowt, T);
        tag = u32x2{gr[2], gr[3]};
        TSW(63);
        if (nt == 15) {
            st16(rws, e.go, 0, gr);
            retile_issue(rt, w);
            asm volatile("s_waitcnt vmcnt(0)" ::: "memory");
        } else if (nt >= 10) qkv_body<true>(c, e, rt, rws, gr, tag, w PROFPASS);
        else qkv_body<false>(c, e, rt, rws, gr, tag, w PROFPASS);
    }
    const int mt = (xcd >> 1) * 8 + (i & 7), nt = (xcd & 1) * 4 + (i >> 3);
    const int qg = (xcd & 6) + ((i >> 2) & 1), ntg = (xcd & 1) * 16 + (i & 3) * 4 + (i >> 3);
    const int tg = w >> 1, kh = w & 1;
    auto rv0 = [&](unsigned l) { return (l & 15) * 1280 + (nt & 1) * 32 + kh * 64 + (l >> 4) * 16; };
    auto rv1 = [&](unsigned l) { return kh ? OOB : (l & 15) * 1280 + ((nt & 1) ? 0 : 128) + (l >> 4) * 8; };
    const unsigned rsb = (mt * 64 + tg * 16) * 1280 + nt * 160;
    u32x4 r0; u32x2 r1; Park pk;
    phase_attn(p, rt, rws, tag, fq, b, w, lane, nt, ntg, rv0(lane), rv1(lane), rsb, r0, r1, pk PROFPASS);
    unsigned ln;
    asm volatile("v_mbcnt_lo_u32_b32 %0, -1, 0\n\tv_mbcnt_hi_u32_b32 %0, -1, %0" : "=v"(ln) : "{v154}"(pk.d));
    phase_wo(rws, w, ln, mt, nt, ntg, r0, r1, pk PROFPASS);
    PIN(ln);
    unsigned o0 = rv0(ln), o1 = rv1(ln), a = tg * 40960 + ln * 16;
    PIN(o0); PIN(o1); PIN(a);
    phase_ffn(rws, w, ln, mt, nt, qg, ntg, pk PROFPASS);
    TS(14);
    {
        const rsrc_t ro = mkrs(p.out, 2048 * 1280);
        const u32x2 u = addres(sum8(a + kh * 2048), r0[0], r0[1]), v = addres(sum8(a + kh * 2048 + 1024), r0[2], r0[3]);
        st16(ro, o0, rsb, u32x4{u[0], u[1], v[0], v[1]});
        if (kh == 0) st8(ro, o1, rsb, addres(sum8(a + 4096), r1[0], r1[1]));
    }
    asm volatile(".Lend:" ::: "memory");
}

extern "C" void kernel_launch(void* const* d_in, const int* in_sizes, int n_in, void* d_out, int out_size, void* d_ws, size_t ws_size, hipStream_t stream) {
    Params p;
    p.x = (const char*)d_in[0]; p.wqkv = (const char*)d_in[1]; p.wo = (const char*)d_in[2];
    p.wg = (const char*)d_in[4]; p.wu = (const char*)d_in[5]; p.wd = (const char*)d_in[6];
    p.out = (char*)d_out; p.ws = (char*)d_ws + ((ws_size - WS_END) & ~(size_t)2097151);
    block_kernel<<<256, 512, 0, stream>>>(p);
}
```

```cpp
#include <hip/hip_runtime.h>
#include <hip/hip_bf16.h>
#include <stdint.h>

#define DI __device__ __forceinline__
typedef __attribute__((ext_vector_type(2))) __bf16 bf2v;
typedef __attribute__((ext_vector_type(8))) __bf16 bf8v;
typedef __attribute__((ext_vector_type(16))) __bf16 bf16v;
typedef __attribute__((ext_vector_type(4))) short s4v;
typedef __attribute__((ext_vector_type(2))) float f32x2;
typedef __attribute__((ext_vector_type(4))) float f32x4;
typedef __attribute__((ext_vector_type(8))) float f32x8;
typedef __attribute__((ext_vector_type(16))) float f32x16;
typedef __attribute__((ext_vector_type(32))) float f32x32;
typedef __attribute__((ext_vector_type(2))) unsigned u32x2;
typedef __attribute__((ext_vector_type(4))) unsigned u32x4;
typedef __attribute__((ext_vector_type(8))) unsigned u32x8;
typedef __attribute__((ext_vector_type(16))) unsigned u32x16;
typedef __attribute__((ext_vector_type(32))) unsigned u32x32;
typedef __amdgpu_buffer_rsrc_t rsrc_t;
#define LAS __attribute__((address_space(3)))

constexpr unsigned WS_Q = 0;
constexpr unsigned WS_KV = WS_Q + 32 * 81920;
constexpr unsigned WS_O = WS_KV + 32 * 163840;
constexpr unsigned WS_B = WS_O + 128 * 20480;
constexpr unsigned QBS = 32 * 40960, OFF_HS = 16 * 20480, OFF_F = OFF_HS + 16 * 2048;
constexpr unsigned WS_WO = WS_B + 8 * QBS;
constexpr unsigned WS_WD = WS_WO + 8 * 102400;
constexpr unsigned WS_WGU = WS_WD + 8 * 276480;
constexpr unsigned WS_CS = WS_WGU + 32 * 143360;
constexpr unsigned WS_FQ = WS_CS + 5 * 3584 * 4;
constexpr unsigned WS_FR = WS_FQ + 256 * 128;
constexpr unsigned WS_XS = WS_FR + 2048;
constexpr unsigned WS_E = WS_XS + 2048 * 16;
constexpr unsigned WS_END = WS_E + 128;
constexpr unsigned OOB = 0x40000000u;
constexpr int SPIN = 1 << 14;

struct Params {
    const char* x; const char* wqkv; const char* wo; const char* wg; const char* wu; const char* wd;
    char* out; char* ws;
};

#define SBAR() __builtin_amdgcn_sched_barrier(0)
#define PIN(x) asm volatile("" : "+v"(x))
#define WAITBAR(N) asm volatile("s_waitcnt vmcnt(" #N ") lgkmcnt(0)\n\ts_barrier" ::: "memory")
#define VBAR(N) asm volatile("s_waitcnt vmcnt(" #N ")\n\ts_barrier" ::: "memory")
#define LBAR() asm volatile("s_waitcnt lgkmcnt(0)\n\ts_barrier" ::: "memory")
#define BAR() asm volatile("s_barrier" ::: "memory")

DI rsrc_t mkrs(const char* p, unsigned bytes) { return __builtin_amdgcn_make_buffer_rsrc((void*)p, (short)0, (int)bytes, 0x00020000); }
template <class T> DI T ldsr(unsigned a) { return *(const LAS T*)(uintptr_t)a; }
template <class T> DI void ldsw(unsigned a, T v) { *(LAS T*)(uintptr_t)a = v; }
DI s4v ldstr(unsigned a) { return __builtin_amdgcn_ds_read_tr16_b64_v4i16((LAS s4v*)(uintptr_t)a); }
DI bf8v ldstr2(unsigned a, unsigned d) {
    s4v lo = ldstr(a), hi = ldstr(a + d);
    return __builtin_bit_cast(bf8v, __builtin_shufflevector(lo, hi, 0, 1, 2, 3, 4, 5, 6, 7));
}
DI f32x16 mfma32(bf8v a, bf8v b, f32x16 c) { return __builtin_amdgcn_mfma_f32_32x32x16_bf16(a, b, c, 0, 0, 0); }
DI f32x4 mfma16(bf8v a, bf8v b, f32x4 c) { return __builtin_amdgcn_mfma_f32_16x16x32_bf16(a, b, c, 0, 0, 0); }
DI bf8v asbf(u32x4 v) { return __builtin_bit_cast(bf8v, v); }
DI unsigned pk2(float lo, float hi) { f32x2 v = {lo, hi}; return __builtin_bit_cast(unsigned, __builtin_convertvector(v, bf2v)); }
DI float bflo(unsigned u) { return __uint_as_float(u << 16); }
DI float bfhi(unsigned u) { return __uint_as_float(u & 0xffff0000u); }
DI f32x2 swap32(float a, float b) { auto r = __builtin_amdgcn_permlane32_swap(__float_as_uint(a), __float_as_uint(b), false, false); return f32x2{__uint_as_float(r[0]), __uint_as_float(r[1])}; }
DI float sum16(float a) { auto r = __builtin_amdgcn_permlane16_swap(__float_as_uint(a), __float_as_uint(a), false, false); return __uint_as_float(r[0]) + __uint_as_float(r[1]); }
DI bool anyl(bool c) { return __builtin_amdgcn_ballot_w64(c) != 0; }
DI void st16(rsrc_t r, unsigned voff, unsigned soff, u32x4 v) { asm volatile("buffer_store_dwordx4 %0, %1, %2, %3 offen sc1\n\ts_nop 1" :: "v"(v), "v"(voff), "s"(r), "s"(soff) : "memory"); }
DI void st8(rsrc_t r, unsigned voff, unsigned soff, u32x2 v) { asm volatile("buffer_store_dwordx2 %0, %1, %2, %3 offen sc1" :: "v"(v), "v"(voff), "s"(r), "s"(soff) : "memory"); }
DI void st4(rsrc_t r, unsigned voff, unsigned soff, unsigned v) { asm volatile("buffer_store_dword %0, %1, %2, %3 offen sc1" :: "v"(v), "v"(voff), "s"(r), "s"(soff) : "memory"); }
DI u32x4 ld16c(rsrc_t r, unsigned voff, unsigned soff) { return __builtin_amdgcn_raw_buffer_load_b128(r, voff, soff, 16); }
DI u32x2 ld8c(rsrc_t r, unsigned voff, unsigned soff) { return __builtin_amdgcn_raw_buffer_load_b64(r, voff, soff, 16); }
DI void dma1(unsigned m0v, unsigned voff, rsrc_t r, unsigned soff) {
    asm volatile("s_mov_b32 m0, %0\n\ts_nop 0\n\tbuffer_load_dwordx4 %1, %2, %3 offen lds" :: "s"(m0v), "v"(voff), "s"(r), "s"(soff) : "memory"); }
DI void dma1c(unsigned m0v, unsigned voff, rsrc_t r, unsigned soff) {
    asm volatile("s_mov_b32 m0, %0\n\ts_nop 0\n\tbuffer_load_dwordx4 %1, %2, %3 offen sc1 lds" :: "s"(m0v), "v"(voff), "s"(r), "s"(soff) : "memory"); }
DI void dma3c(unsigned m0v, unsigned voff, rsrc_t r, unsigned soff) {
    asm volatile("s_mov_b32 m0, %0\n\ts_nop 0\n\tbuffer_load_dwordx4 %1, %2, %3 offen sc1 lds\n\tbuffer_load_dwordx4 %1, %2, %3 offen offset:1024 sc1 lds\n\t"
                 "buffer_load_dwordx4 %1, %2, %3 offen offset:2048 sc1 lds" :: "s"(m0v), "v"(voff), "s"(r), "s"(soff) : "memory"); }
DI void dma4c(unsigned m0v, unsigned voff, rsrc_t r, unsigned soff) {
    asm volatile("s_mov_b32 m0, %0\n\ts_nop 0\n\tbuffer_load_dwordx4 %1, %2, %3 offen sc1 lds\n\tbuffer_load_dwordx4 %1, %2, %3 offen offset:1024 sc1 lds\n\t"
                 "buffer_load_dwordx4 %1, %2, %3 offen offset:2048 sc1 lds\n\tbuffer_load_dwordx4 %1, %2, %3 offen offset:3072 sc1 lds" :: "s"(m0v), "v"(voff), "s"(r), "s"(soff) : "memory"); }
#define SH 0
#define SLO 0
#define SF 0
#define RF 0
#define RH 0
#define RO 0
#define R7 4
#define P7 "s44"
#define SLP_(n) ".if " #n "\ns_sleep " #n "\n.endif\n\t"
#define SLP(n) SLP_(n)
#define SPINS "0xfffff"
#define PINS(x) asm volatile("" : "+s"(x))
#define CHK(B, TMP, DST) \
    "v_max3_u32 v" #TMP ", v[" #B "], v[" #B "+1], v[" #B "+2]\n\tv_max3_u32 v" #TMP ", v[" #B "+3], v[" #B "+4], v" #TMP "\n\t" \
    "v_max3_u32 v" #TMP ", v[" #B "+5], v[" #B "+6], v" #TMP "\n\tv_max3_u32 v" #TMP ", v[" #B "+7], v[" #B "+8], v" #TMP "\n\t" \
    "v_max3_u32 v" #TMP ", v[" #B "+9], v[" #B "+10], v" #TMP "\n\tv_max3_u32 v" #TMP ", v[" #B "+11], v[" #B "+12], v" #TMP "\n\t" \
    "v_max3_u32 v" #TMP ", v[" #B "+13], v[" #B "+14], v" #TMP "\n\tv_max_u32 v" #TMP ", v[" #B "+15], v" #TMP "\n\t" \
    "v_cmp_eq_u32 " DST ", -1, v" #TMP "\n\t"
#define LD1(R, V, S, O) "buffer_load_dwordx4 v[" R ":" R "+3], " V ", s[40:43], " S " offen offset:" O " sc1\n\t"
#define DM1(V, S, O) "buffer_load_dwordx4 " V ", s[40:43], " S " offen offset:" #O " sc1 lds\n\t"
#define DM3(V, S) DM1(V, S, 0) DM1(V, S, 1024) DM1(V, S, 2048)
#define DM4(V, S) DM3(V, S) DM1(V, S, 3072)
#define Z2(R) "v_mov_b64 v[" R ":" R "+1], 0\n\t"
#define Z4(R) Z2(R) Z2(R "+2")
#define Z8(R) Z4(R) Z4(R "+4")

#define VCL32 "v32", "v33", "v34", "v35", "v36", "v37", "v38", "v39", "v40", "v41", "v42", "v43", "v44", "v45", "v46", "v47"
#define VCL48 "v48", "v49", "v50", "v51", "v52", "v53", "v54", "v55", "v56", "v57", "v58", "v59", "v60", "v61", "v62", "v63"
#define VCL64 "v64", "v65", "v66", "v67", "v68", "v69", "v70", "v71", "v72", "v73", "v74", "v75", "v76", "v77", "v78", "v79"
#define VCL80 "v80", "v81", "v82", "v83", "v84", "v85", "v86", "v87", "v88", "v89", "v90", "v91", "v92", "v93", "v94", "v95"
#define VCL96 "v96", "v97", "v98", "v99", "v100", "v101", "v102", "v103", "v104", "v105", "v106", "v107", "v108", "v109", "v110", "v111"
#define VCL112 "v112", "v113", "v114", "v115", "v116", "v117", "v118", "v119", "v120", "v121", "v122", "v123", "v124", "v125", "v126", "v127"
#define VCL128 "v128", "v129", "v130", "v131", "v132", "v133", "v134", "v135", "v136", "v137", "v138", "v139"
#define VCL140 "v140", "v141", "v142", "v143", "v144", "v145", "v146", "v147", "v148", "v149", "v150", "v151", "v152", "v153", "v154", "v155", "v156", "v157", "v158", "v159"
#define VCL160 "v160", "v161", "v162", "v163", "v164", "v165", "v166", "v167", "v168", "v169", "v170", "v171", "v172", "v173", "v174", "v175"
#define VCL112L "v112", "v113", "v114", "v115", "v116", "v117", "v118", "v119"

#define PROFARG
#define PROFPASS
#define TSA(i)
#define PFIO
#define PFCL
#define TSM
#define PFM(i)
#define PFT
#define TS(i)

#define TSF(i)
#define TSI
#define TSS(i)
#define TSW(i)
#define TSP
#define TSQ(a, b)
constexpr unsigned L_RT = 0, L_SM = 32768, L_CP = 49152;
struct RtTab { unsigned v[2048][16]; };
constexpr RtTab make_rt() {
    RtTab t{};
    for (unsigned b = 0; b < 256; ++b) for (unsigned w = 0; w < 8; ++w) for (unsigned f = 0; f < 4; ++f) {
        unsigned A = 0, M = 0, SO = 0, R0 = 0, R1 = 0;
        if (b < 135) {
            const unsigned fg = b / 5, ks = (b % 5) * 4 + (w >> 1), j = ks >> 1, o = fg * 8 + (w & 1) * 4 + f;
            A = (320 * (ks & 1) + (j < 8 ? (j & 1) * 32 + (j >> 1) * 8 : 8 * j) + (f & 1) * 160) * 3456 + fg * 128 + (w & 1) * 64;
            M = (j < 8 ? 96 : 16) * 3456;
            SO = WS_WGU + (o / 7) * 143360 + (ks + 20 - (o / 7 % 16 * 5 >> 3) * 2) % 20 * 7168 + (o % 7) * 1024;
            R0 = 16 * f;
        } else {
            const unsigned v = (b - 135) * 8 + w;
            const bool isWo = v < 200;
            const unsigned vv = v < 740 ? (isWo ? v : v - 200) : 0, ks = vv / 10, cu = vv % 10;
            const unsigned c = 4 * ks + f, j = c / 31, ng = c % 31;
            A = (isWo ? (ks >= 16 ? 40 * c - 2544 : 20 * (c >> 1) + 4 * (c & 1)) : 56 * ng + 16 * (j >> 1) + 4 * (j & 1)) * 1280 + cu * 128;
            M = (isWo ? (ks >= 16 ? 20 : 8) : (j < 6 ? 8 : 4)) * 1280;
            const unsigned seg = 2 * cu + (f >> 1), a = seg / 5, sm = seg % 5, fi = f & 1;
            const unsigned tile = 2 * a + (sm == 2 ? fi : sm > 2), blk = sm == 2 ? 4 : (sm == 1 || sm == 4) ? 2 + fi : fi;
            SO = isWo ? WS_WO + (tile * 100 + (ks >= 10 ? (ks - 10) * 10 + 5 + (blk < 4 ? blk ^ 2 : 4) : ks * 10 + blk)) * 1024 : WS_WD + (tile * 270 + ks * 5 + blk) * 1024;
            R0 = (f >> 1) * 64 + (sm == 2 ? 32 : 8) * fi; R1 = sm == 2 ? 8 : 16;
        }
        unsigned* r = t.v[b * 8 + w];
        r[f] = A + 3072 - 1024 * f; r[4 + f] = M; r[8 + f] = SO; r[12 + f] = (L_RT + w * 4096 + R0) | R1 << 16;
    }
    return t;
}
static __device__ const __attribute__((aligned(64))) RtTab RT_TAB = make_rt();
struct Retile { rsrc_t ra, rb; u32x16 t; unsigned vlo, hi; };
DI void retile_setup(Retile& r, const Params& p, unsigned b, unsigned lane) {
    const bool gu = b < 135;
    const char *pa, *pb; unsigned n;
    asm("s_cmpk_lt_u32 %3, 160\n\ts_cselect_b64 %0, %4, %5\n\ts_cselect_b32 %2, %9, %8\n\ts_cmpk_lt_u32 %3, 135\n\ts_cselect_b64 %1, %7, %0\n\ts_cselect_b64 %0, %6, %0\n\ts_cselect_b32 %2, %8, %2"
        : "=&s"(pa), "=&s"(pb), "=&s"(n) : "s"(b), "s"(p.wo), "s"(p.wd), "s"(p.wg), "s"(p.wu), "s"(640 * 3456 + 3072), "n"(640 * 1280 + 3072) : "scc");
    r.ra = mkrs(pa - 3072, n); r.rb = mkrs(pb - 3072, n);
    r.vlo = gu ? ((lane >> 2) & 7) * 3456 + (lane & 3) * 16 : ((lane >> 3) & 3) * 1280 + (lane & 7) * 16;
    r.hi = lane >> 5;
    PIN(r.vlo); PIN(r.hi); PINS(r.ra); PINS(r.rb);
}
DI void retile_issue(const Retile& r, int w) {
    asm volatile("s_mov_b32 m0, %0\n\ts_nop 0\n\tbuffer_load_dwordx4 %1, %5, 0 offen lds\n\tbuffer_load_dwordx4 %2, %5, 0 offen offset:1024 lds\n\t"
                 "buffer_load_dwordx4 %3, %6, 0 offen offset:2048 lds\n\tbuffer_load_dwordx4 %4, %6, 0 offen offset:3072 lds"
                 :: "s"(L_RT + w * 4096), "v"(r.vlo + r.t[0] + r.hi * r.t[4]), "v"(r.vlo + r.t[1] + r.hi * r.t[5]), "v"(r.vlo + r.t[2] + r.hi * r.t[6]),
                    "v"(r.vlo + r.t[3] + r.hi * r.t[7]), "s"(r.ra), "s"(r.rb) : "memory");
}
DI u32x32 retile_read(const Retile& r, unsigned b, int w, unsigned lane) {
    const u32x4 one = {0x3f803f80u, 0x3f803f80u, 0x3f803f80u, 0x3f803f80u};
    const unsigned f = lane & 3, row = 8 * (lane >> 4) + ((lane >> 2) & 3), dl = b < 135 ? 256 : 512;
    const unsigned rb = b < 135 ? (f >> 1) * 2048 + row * 64 + (f & 1) * 8 : row * 128;
    u32x32 o; f32x4 cp[4];
#pragma unroll
    for (int i = 0; i < 4; ++i) {
        const bf8v fr = ldstr2(rb + (r.t[12 + i] & 0xffff) + (r.t[12 + i] >> 16) * f, dl);
        cp[i] = mfma16(fr, asbf(one), f32x4{});
#pragma unroll
        for (int k = 0; k < 4; ++k) o[4 * i + k] = __builtin_bit_cast(u32x4, fr)[k];
    }
    if ((lane & 15) == 0) {
#pragma unroll
        for (int i = 0; i < 4; ++i) ldsw<f32x4>(L_CP + w * 256 + i * 64 + lane, cp[i]);
    }
    return o;
}

struct QkvSrc { rsrc_t rx, rw; unsigned xo[2], wo[2], sx, sw; };
#define QDX(V, O) "buffer_load_dwordx4 %[" #V "], %[rx], %[sx] offen offset:" #O " lds\n\t"
#define QDW(V, O) "buffer_load_dwordx4 %[" #V "], %[rw], %[sw] offen offset:" #O " lds\n\t"
#define QADV "s_add_u32 %[sx], %[sx], 128\n\ts_cmp_eq_u32 %[sx], 1280\n\ts_cselect_b32 %[sx], 0, %[sx]\n\ts_mul_i32 %[sw], %[sx], 1920\n\t"
#define QD4 QDX(x0, 0) QDX(x1, 1024) QDW(w0, 2048) QDW(w1, 3072) QADV "s_add_u32 m0, m0, 32768\n\t"
DI void qkv_start(QkvSrc& c, u32x4& row, unsigned& rowt, unsigned& T, u32x16& tab, Params& p, u32x16 ka, int b, int mt, int nt, int rot, int w, int lane) {
    const unsigned rr = lane >> 3, isv = nt >= 10;
    const unsigned tok = mt * 128 + (w >> 1) * 32 + ((w & 1) << (2 * isv)) + (rr & 4) * 2 + ((rr & 3) << (1 - isv));
    c.xo[0] = tok * 1280 + (((lane & 7) ^ (rr >> 1)) << 4);
    c.xo[1] = (c.xo[0] ^ 64) + 16 * 1280 - 1024;
    c.wo[0] = (8 * w + ((lane >> 2) & 3)) * 3840 + nt * 256 + (lane >> 4) * 64 + (((lane & 3) ^ ((w & 1) << 1)) << 4) + 1024;
    c.wo[1] = c.wo[0] + 4 * 3840 - 1024;
    c.sx = rot * 128; c.sw = rot * 245760;
    unsigned ro = (mt * 128 + nt * 8 + w) * 1280;
    asm volatile("s_waitcnt lgkmcnt(0)\n\ts_load_dwordx16 %5, %6, %7" : "+s"(ka), "+v"(c.xo[1]), "+v"(c.wo[1]), "+s"(c.sw), "+s"(ro), "=&{s[60:75]}"(tab) : "s"(&RT_TAB), "s"((b * 8 + w) * 64) : "memory");
    p = __builtin_bit_cast(Params, ka);
    asm volatile("s_load_dword %0, %1, %2" : "=&s"(T) : "s"(p.ws), "s"(WS_E) : "memory");
    c.rx = mkrs(p.x, 2048 * 1280); c.rw = mkrs(p.wqkv - 3072, 640 * 3840 + 3072);
    asm volatile("s_mov_b32 m0, %[dm]\n\tbuffer_load_dwordx4 %[r4], %[l16], %[rx], %[ro] offen\n\tbuffer_load_dword %[r1], %[l4], %[rx], %[ro] offen offset:1024\n\t"
                 "s_cmp_eq_u32 %[nt], 15\n\ts_cbranch_scc1 1f\n\t" QD4 "s_barrier\n\t" QD4 "s_branch 2f\n1:\n\ts_waitcnt vmcnt(0)\n2:"
                 : [r4] "=&v"(row), [r1] "=&v"(rowt), [sx] "+&s"(c.sx), [sw] "+&s"(c.sw)
                 : [x0] "v"(c.xo[0]), [x1] "v"(c.xo[1]), [w0] "v"(c.wo[0]), [w1] "v"(c.wo[1]), [rx] "s"(c.rx), [rw] "s"(c.rw), [dm] "s"(w * 4096),
                   [l16] "v"(lane * 16), [l4] "v"(lane * 4), [ro] "s"(ro), [nt] "s"(nt) : "memory", "scc");
}
template <int C> DI float rorsum(float v) { return v + __uint_as_float(__builtin_amdgcn_update_dpp(0u, __float_as_uint(v), 0x120 + C, 0xf, 0xf, true)); }
DI u32x4 row_stats(u32x4 row, unsigned rowt, unsigned T) {
    float s = 0.f, q = 0.f;
#pragma unroll
    for (int i = 0; i < 5; ++i) {
        const bf2v v = __builtin_bit_cast(bf2v, i < 4 ? row[i & 3] : rowt);
        s = __builtin_amdgcn_fdot2_f32_bf16(v, __builtin_bit_cast(bf2v, 0x3f803f80u), s, false);
        q = __builtin_amdgcn_fdot2_f32_bf16(v, v, q, false);
    }
    const f32x2 y = swap32(s, q);
    const float z = rorsum<1>(rorsum<2>(rorsum<4>(rorsum<8>(sum16(y[0] + y[1]))))) * (1.f / 640.f);
    const f32x2 v = swap32(z, z);
    const float ra = __builtin_amdgcn_rsqf(__builtin_fmaxf(v[1] - v[0] * v[0], 0.f) + 1e-5f);
    return u32x4{__float_as_uint(ra), __float_as_uint(-v[0] * ra), T, ~T};
}
#define MFQ(A, W, X) "v_mfma_f32_16x16x32_bf16 v[" A ":" A "+3], v[" W ":" W "+3], v[" X ":" X "+3], v[" A ":" A "+3]\n\t"
#define MFV(A, W, X) MFQ(A, X, W)
#define QTR(R, V, O) "ds_read_b64_tr_b16 v[" R ":" R "+1], %[" #V "] offset:" O "\n\t"
#define QRX(R, V, O) "ds_read_b128 v[" R ":" R "+3], %[" #V "] offset:" O "\n\t"
#define LGK(N) "s_waitcnt lgkmcnt(" #N ")\n\t"
#define QSEC(MF, P, Q, XA, WO, D1, D2, E1, E2) \
    "s_cmp_lt_u32 %[n], 8\n\t" LGK(4) MF("64", P, "68") QTR(Q, wa0, WO) MF("32", P, P "+8") QTR(Q "+2", wa0, WO "+1024") \
    "s_cbranch_scc0 1f\n\t" D1 "1:\n\t" MF("36", P, P "+12") QRX(Q "+8", XA, "0") LGK(5) MF("48", P "+4", P "+8") QRX(Q "+12", XA, "4096") \
    MF("52", P "+4", P "+12") QTR(Q "+4", wa1, WO) LGK(5) MF("40", P, P "+16") QTR(Q "+6", wa1, WO "+1024") \
    "s_cbranch_scc0 1f\n\t" D2 "1:\n\t" MF("56", P "+4", P "+16") QRX(Q "+16", XA, "8192") E1 MF("44", P, P "+20") QRX(Q "+20", XA, "12288") E2 MF("60", P "+4", P "+20")
#define QLDG "buffer_load_dwordx4 v[120:123], %[ga], %[rs], 0 offen sc1\n\t"
#define QADD(r) "v_add_u32 %[" #r "], %[d], %[" #r "]\n\t"
#define QRT(r, A, M) "v_mad_u32_u24 %[" #r "], %[hi], s" #M ", %[vlo]\n\tv_add_u32 %[" #r "], s" #A ", %[" #r "]\n\t"
#define QKV_ASM(MF, STRD) asm volatile( \
    "s_add_u32 m0, %[dm], 98304\n\ts_mov_b32 %[n], 0\n\tbuffer_store_dwordx4 %[gr], %[go], %[rs], 0 offen sc1\n\t" \
    Z8("32") Z8("40") Z8("48") Z8("56") Z4("64") "v_mov_b32 v68, 0x3f803f80\n\tv_mov_b32 v69, v68\n\tv_mov_b64 v[70:71], v[68:69]\n\t" \
    "s_waitcnt vmcnt(9)\n\ts_barrier\n\t" TSA("1") TSS("25") \
    QTR("72", wa0, "0") QTR("74", wa0, "1024") QRX("80", xa0, "0") QRX("84", xa0, "4096") QTR("76", wa1, "0") QTR("78", wa1, "1024") QRX("88", xa0, "8192") QRX("92", xa0, "12288") \
    QDX(x0, 0) QDX(x1, 1024) "s_branch 6f\n" \
    "3:\n\ts_cmp_gt_u32 %[n], 2\n\ts_cbranch_scc1 2f\n\ts_waitcnt vmcnt(9)\n\ts_branch 5f\n" \
    "2:\n\ts_cmp_eq_u32 %[n], 10\n\ts_cbranch_scc1 7f\n\ts_cmp_eq_u32 %[n], 7\n\ts_cbranch_scc1 2f\n\ts_waitcnt vmcnt(5)\n\ts_branch 5f\n" \
    "2:\n\t" QLDG QRT(x0, 60, 64) QRT(x1, 61, 65) QRT(w0, 62, 66) QRT(w1, 63, 67) \
    "s_mov_b64 s[44:45], s[52:53]\n\ts_mov_b32 s46, s54\n\ts_mov_b64 s[48:49], s[56:57]\n\ts_mov_b32 s50, s58\n\ts_mov_b32 %[sx], 0\n\ts_mov_b32 %[sw], 0\n" \
    "4:\n\ts_waitcnt vmcnt(8)\n5:\n\ts_barrier\n\t" \
    QSEC(MF, "96", "72", xa0, "0", QDX(x0, 0), QDX(x1, 1024), "", "") \
    "6:\n\t" \
    QSEC(MF, "72", "96", xa1, "16384", QDW(w0, 2048), QDW(w1, 3072), \
         QADV "s_bitcmp1_b32 0x42, %[n]\n\ts_cselect_b32 %[d], %[wr], %[up]\n\ts_add_u32 m0, m0, %[d]\n\ts_cmp_eq_u32 %[n], 4\n\ts_cselect_b32 %[d], %[wr], %[up]\n\ts_add_u32 %[n], %[n], 1\n\t", \
         QADD(xa0) QADD(xa1) QADD(wa0) QADD(wa1)) TSI \
    "s_bitcmp1_b32 0x686, %[n]\n\ts_cbranch_scc0 4b\n\ts_branch 3b\n" \
    "7:\n\ts_mov_b32 %[n], " SPINS "\n\t" LGK(4) MF("64", "96", "68") MF("32", "96", "104") "s_waitcnt vmcnt(4)\n" \
    "1:\n\tv_cmp_ne_u64 vcc, v[122:123], %[tag]\n\ts_cbranch_vccz 2f\n\t" QLDG "s_waitcnt vmcnt(0)\n\ts_sub_u32 %[n], %[n], 1\n\ts_cbranch_scc0 1b\n" \
    "2:\n\t" MF("36", "96", "108") "v_mul_f32 v120, %[sc], v120\n\tv_mul_f32 v121, %[sc], v121\n\t" LGK(2) MF("48", "100", "104") \
    "ds_write2_b32 %[pa], v120, v121 offset1:64\n\t" MF("52", "100", "108") "s_nop 1\n\tds_write_b128 %[ca], v[64:67]\n\t" LGK(2) \
    MF("40", "96", "112") STRD MF("56", "100", "112") MF("44", "96", "116") MF("60", "100", "116") \
    "s_waitcnt lgkmcnt(0)\n\ts_barrier\n\ts_nop 7" \
    : "=&{v[32:63]}"(A), "=&{v[64:67]}"(cs0), "=&{v[124:155]}"(S), [rx] "+{s[44:47]}"(c.rx), [rw] "+{s[48:51]}"(c.rw), [sx] "+&s"(c.sx), [sw] "+&s"(c.sw), \
      [x0] "+&v"(c.xo[0]), [x1] "+&v"(c.xo[1]), [w0] "+&v"(c.wo[0]), [w1] "+&v"(c.wo[1]), [xa0] "+&v"(e.xa0), [xa1] "+&v"(e.xa1), [wa0] "+&v"(e.wa0), [wa1] "+&v"(e.wa1), \
      [n] "=&s"(n), [d] "=&s"(d) PFIO \
    : "{s[52:55]}"(rt.ra), "{s[56:59]}"(rt.rb), [rs] "s"(rws), [dm] "s"(w * 4096), [wr] "s"(-131072), [up] "s"(32768), [sc] "s"(e.sc), [gr] "v"(gr), [go] "v"(e.go), [ga] "v"(e.ga), \
      "{s[60:75]}"(rt.t), [vlo] "v"(rt.vlo), [hi] "v"(rt.hi), [tag] "v"(tag), [pa] "v"(e.pa), [pr] "v"(e.pr), [ca] "v"(e.ca) \
    : "memory", "scc", "vcc", "v68", "v69", "v70", "v71", "v72", "v73", "v74", "v75", "v76", "v77", "v78", "v79", VCL80, VCL96, VCL112L, "v120", "v121", "v122", "v123" PFCL)
#define QS2(R, O) "ds_read2_b32 v[" #R ":" #R "+1], %[pr] offset0:" #O " offset1:" #O "\n\tds_read2_b32 v[" #R "+2:" #R "+3], %[pr] offset0:" #O "+64 offset1:" #O "+64\n\t"
struct QkvAdr { unsigned xa0, xa1, wa0, wa1, go, ga, pa, pr, ca, so[2], sb, sc; };
DI void qkv_setup(QkvAdr& e, int mt, int nt, int w, int lane) {
    const unsigned t = lane & 15, g = lane >> 4, T = w & 1, cb = w >> 1, g64 = 2 * mt + T;
    const bool isv = nt >= 10, isq = nt < 5;
    e.xa0 = 4 * T * 4096 + t * 128 + ((g ^ (t >> 1)) << 4); e.xa1 = e.xa0 ^ 64;
    e.wa0 = g * 4096 + 2048 + cb * 256 + ((lane >> 2) & 3) * 64 + ((g ^ T) & 1) * 32 + (lane & 3) * 8; e.wa1 = e.wa0 ^ 32;
    e.go = lane == 0 ? WS_XS + (mt * 128 + nt * 8 + w) * 16 : OOB;
    e.ga = WS_XS + (g64 * 64 + (g >> 1) * 32 + (isv ? 8 * (t >> 2) + 4 * (g & 1) + (t & 3) : 2 * t + (g & 1))) * 16;
    e.pa = L_SM + w * 2048 + lane * 4; e.pr = L_SM + w * 2048 + (isv ? g * 16 : t * 4); e.ca = L_SM + w * 2048 + 1024 + lane * 16;
    e.sc = isq ? __builtin_bit_cast(unsigned, 0.22360679774997896f * 1.4426950408889634f) : 0x3f800000u;
    e.sb = isq ? WS_Q + g64 * 2560 : WS_KV + g64 * 5120 + (isv ? 2560 : 0);
#pragma unroll
    for (int n = 0; n < 2; ++n) {
        if (isv) {
            const unsigned col = (nt - 10) * 128 + cb * 32 + 16 * (n ^ T) + t, head = (col * 3277) >> 16;
            e.so[n] = head * (163840 - 320) + g * 320 + col * 16;
        } else {
            const unsigned G = (isq ? nt : nt - 5) * 32 + cb * 8 + 4 * (n ^ T) + g, head = (G * 205) >> 10;
            e.so[n] = head * (isq ? 81920 - 2560 : 163840 - 2560) + G * 512 + t * 16;
        }
        PIN(e.so[n]);
    }
    PIN(e.xa0); PIN(e.xa1); PIN(e.wa0); PIN(e.wa1); PIN(e.go); PIN(e.ga); PIN(e.pa); PIN(e.pr); PIN(e.ca); PINS(e.sb); PINS(e.sc);
}
template <bool ISV>
DI void qkv_body(QkvSrc& c, QkvAdr& e, const Retile& rt, rsrc_t rws, u32x4 gr, u32x2 tag, int w PROFARG) {
    f32x32 A, S; f32x4 cs0; unsigned n, d;
    if (ISV) QKV_ASM(MFV, QRX("124", pr, "0") QRX("128", pr, "256") QRX("132", pr, "64") QRX("136", pr, "320") QRX("140", pr, "128") QRX("144", pr, "384") QRX("148", pr, "192") QRX("152", pr, "448"));
    else QKV_ASM(MFQ, QS2(124, 0) QS2(128, 16) QS2(132, 32) QS2(136, 48));
    TS(2);
    const f32x4 cs1 = ldsr<f32x4>(e.ca ^ 2048);
#pragma unroll
    for (int n = 0; n < 2; ++n) {
        const f32x4 cs = n ? cs1 : cs0;
#pragma unroll
        for (int q = 0; q < 2; ++q) {
            u32x4 o;
#pragma unroll
            for (int i = 0; i < 4; ++i) {
                const int k = 2 * q + (i >> 1), j = 2 * (i & 1), a = 16 * n + 4 * k + j;
                o[i] = ISV ? pk2(A[a] * S[8 * k + j] + S[8 * k + 4 + j] * cs[0], A[a + 1] * S[8 * k + j + 1] + S[8 * k + 5 + j] * cs[0])
                           : pk2(A[a] * S[4 * k] + S[4 * k + 2] * cs[j], A[a + 1] * S[4 * k + 1] + S[4 * k + 3] * cs[j + 1]);
            }
            asm volatile("buffer_store_dwordx4 %0, %1, %2, %3 offen offset:%4 sc1\n\ts_nop 1" :: "v"(o), "v"(e.so[n]), "s"(rws), "s"(e.sb), "n"(q * (ISV ? 1280 : 256)) : "memory");
        }
    }
    TS(58);
    asm volatile("s_waitcnt vmcnt(4)" ::: "memory");
    TS(59);
}

DI f32x16 qk_tile(u32x4 km, u32x2 kt, u32x4 qm, u32x2 qt, f32x16 c) {
    c = mfma32(asbf(km), asbf(qm), c);
    return __builtin_amdgcn_mfma_f32_32x32x8bf16_1k(__builtin_bit_cast(s4v, kt), __builtin_bit_cast(s4v, qt), c, 0, 0, 0);
}
DI float max16(const f32x16& s, float m) {
#pragma unroll
    for (int r = 0; r < 16; r += 2) m = __builtin_fmaxf(__builtin_fmaxf(s[r], s[r + 1]), m);
    return m;
}
DI f32x16 pv16(u32x4 v, f32x8 pr, f32x16 o) {
    return __builtin_amdgcn_smfmac_f32_32x32x32_bf16(asbf(v), __builtin_bit_cast(bf16v, pr), o, 0xDDDD, 0, 0);
}
#define EX4(P, S) "v_exp_f32 v[" #P "], v[" S "]\n\tv_exp_f32 v[" #P "+1], v[" S "+1]\n\tv_exp_f32 v[" #P "+2], v[" S "+2]\n\tv_exp_f32 v[" #P "+3], v[" S "+3]\n\t"
#define SMF(O, V, P) "v_smfmac_f32_32x32x32_bf16 v[" #O ":" #O "+15], v[" #V ":" #V "+3], v[" #P ":" #P "+7], %[idx]\n\t"
#define QKT(SN, AB) "v_mfma_f32_32x32x4_2b_bf16 v[" #SN ":" #SN "+31], v[156:157], v[152:153], 0 cbsz:1 abid:" #AB "\n\t"
#define QKA(SN) "v_mfma_f32_32x32x16_bf16 v[" #SN ":" #SN "+15], v[160:163], v[144:147], v[" #SN ":" #SN "+15]\n\t"
#define QKB(SN) "v_mfma_f32_32x32x16_bf16 v[" #SN "+16:" #SN "+31], v[160:163], v[148:151], v[" #SN "+16:" #SN "+31]\n\t"
#define STEP_(SC, VP, VC, L0, L1, L2, L3, L4) \
    L0 EX4(128, #SC) EX4(132, #SC "+4") SMF(112, VP, 136) EX4(136, #SC "+16") "s_waitcnt lgkmcnt(0)\n\t" SMF(96, 164, 128) EX4(140, #SC "+20") \
    L1 EX4(128, #SC "+8") SMF(112, 164, 136) L2 EX4(132, #SC "+12") L3 EX4(136, #SC "+24") SMF(96, VC, 128) L4 EX4(140, #SC "+28")
#define STEP(SC, SN, AB, VP, VC, KTRD, VRD, KRD, M1, M2) STEP_(SC, VP, VC, QKT(SN, AB) KTRD, QKA(SN) M1, VRD, QKB(SN) KRD, M2)
#define VR(VH, O) "s_mov_b64 exec, s[56:57]\n\tds_read_b128 v[164:167], %[av] offset:" O "\n\tds_read_b128 v[" #VH ":" #VH "+3], %[av] offset:" O "+640\n\ts_mov_b64 exec, -1\n\t"
#define KR(O) "ds_read_b64 v[160:161], %[a0] offset:" O "\n\tds_read_b64 v[162:163], %[a0] offset:" O "+512\n\t"
#define KT(O) "ds_read_b64 v[156:157], %[at] offset:" O "\n\t"
#define PRIO "s_cbranch_vccz 1f\n\ts_setprio 1\n1:\n\t"
#define PVC "s_mov_b32 vcc_lo, %[grp]\n\ts_mov_b32 vcc_hi, 0\n\t"
#define QUAD3(X, N, A0, B0, A1, B1, A2, B2) \
    STEP(32, 64, 1, 172, 168, KT(#X "+5120"), VR(172, #X "+1280"), KR(#X "+5120"), A0, B0) \
    STEP(64, 32, 0, 168, 172, "", VR(168, #X "+5120"), KR(#X "+5376"), A1, B1) \
    STEP(32, 64, 1, 172, 168, KT(#N), VR(172, #X "+6400"), KR(#N), A2, B2)
#define QUAD(X, N, A0, B0, A1, B1, A2, B2, A3, B3) QUAD3(X, N, A0, B0, A1, B1, A2, B2) STEP(64, 32, 0, 168, 172, "", VR(168, #N), KR(#N "+256"), A3, B3) TSI
#define QUADP(X, N, A0, B0, A1, B1, A2, B2, A3, B3) PRIO QUAD(X, N, A0, B0, A1, B1, A2, B2 "s_setprio 0\n\t", A3, B3)
#define KVP_(D) "buffer_load_dwordx4 %[l16], s[40:43], s44 offen sc1 lds\n\ts_add_u32 m0, m0, " #D "\n\ts_add_u32 s44, s44, 4096\n\t"
#define KVP KVP_(4096)
#define LDQ(R, V, O) "buffer_load_dwordx2 v[" #R ":" #R "+1], %[" #V "], s[40:43], %[sq] offen offset:" #O " sc1\n\t"
#define STW(R, S) "buffer_store_dwordx4 v[" #R ":" #R "+3], %[vs], s[40:43], s" #S " offen sc1\n\t"
#define STM(V, S, O) "buffer_store_dwordx4 v[192:195], %[" #V "], s[40:43], " S " offen offset:" #O " sc1\n\t"
#define WO4 "s_cbranch_vccnz 5f\n\t" DM4("%[l16]", "s48") "s_add_u32 m0, m0, 4096\n\ts_add_u32 s48, s48, 4096\n5:\n\t"
#define PK(R, O) LD1(#R, "%[l16]", "s52", #O)
#define PKA "s_add_u32 s52, s52, 4096\n\t"
#define TCH(O) "buffer_load_dword v154, %[vt], s[40:43], 0 offen offset:" #O " sc1\n\t"
#define LDQ5 LDQ(144, va, 0) LDQ(146, va, 512) LDQ(148, vb, 0) LDQ(150, vb, 512) LDQ(152, vc, 2048)
#define RD1(W, V, O) "ds_read_b128 v[" W ":" W "+3], " V " offset:" O "\n\t"
#define VM_(N) "s_waitcnt vmcnt(" #N ")\n\t"
#define VM(N) VM_(N)
DI unsigned mod5(unsigned v) { return (0x21043210u >> (4 * v)) & 7; }
constexpr unsigned L_CS = 52224, L_FL = L_CS + 512;
#define FSET(V, A, O) "ds_write_b32 " A ", " V " offset:" #O "\n\t"
#define FGET(V, A, O) "ds_read_b32 " V ", " A " offset:" #O "\n\t"
#define FTST(V, C, L) "v_cmp_ne_u32 vcc, -1, " V "\n\ts_sub_u32 " C ", " C ", 1\n\ts_cselect_b64 vcc, 0, vcc\n\ts_cbranch_vccnz " L "\n\t"
#define XP1
#define XP2
struct Park { u32x32 a, b; u32x8 c; unsigned d; };

DI void phase_attn(const Params& p, const Retile& rt, rsrc_t rws, u32x2 tag, unsigned fq, int b, int w, int lane, int nt80, int ntg,
                   unsigned rv0, unsigned rv1, unsigned rsb, u32x4& r0, u32x2& r1, Park& pk PROFARG) {
    const int t = lane & 31, hh = lane >> 5, wl = w & 3, grp = w >> 2, kh = w & 1;
    const int head = (b & 7) * 4 + (b >> 6), qb = (b >> 3) & 7;
    const unsigned gb = kh * 61440, kvb = WS_KV + head * 163840;
    pk.a = retile_read(rt, b, w, lane);
    const unsigned l16 = lane * 16;
    unsigned va = hh * 1024 + grp * 256 + t * 8 + kh * 2560, dm = gb + (w >> 1) * 1024, sq = WS_Q + head * 81920 + (qb * 4 + (w & 2)) * 2560;
    const int n0 = (head * 20 + ((lane & 16) ? 19 : 0)) >> 7;
    unsigned fo = WS_FQ + (w == 0 ? (5 + 5 * hh + n0) * 16 + (lane & 15) : n0 * 16 + 2 * qb + (lane & 1)) * 128;
    unsigned skv = kvb + kh * 81920 + (w >> 1) * 1024;
    u32x8 qm; u32x2 qt;
    asm volatile("s_cmp_eq_u32 %[w], 2\n\ts_waitcnt vmcnt(0) lgkmcnt(0)\n\ts_barrier\n\ts_cbranch_scc0 1f\n\tbuffer_store_dwordx2 %[tag], %[fq], %[rs], 0 offen sc1\n1:"
                 : "+{v[176:207]}"(pk.a), "+v"(fo), "+v"(va), "+s"(skv), "+s"(sq), "+s"(dm) : [w] "s"(w), [tag] "v"(tag), [fq] "v"(fq), [rs] "s"(rws) : "memory", "scc");
    TS(3);
    SBAR();
    const unsigned a0 = gb + hh * 1024 + t * 8, at = gb + lane * 8 + 2048, av = gb + 2560 + hh * 320 + (t < 20 ? t : 0) * 16;
    const unsigned ones = t == 20 ? 0x3f803f80u : 0u;
    const unsigned wob = WS_WO + nt80 * 102400;
    u32x8 sa;
    sa[0] = skv;
    sa[2] = WS_O + b * 10240 + w * 1280;
    sa[1] = WS_B + qb * QBS + (head * 8 + w) * 1408;
    sa[3] = WS_B + qb * QBS + OFF_F + (head * 8 + w) * 3456;
    const unsigned mv = l16 + (lane < 16 ? sa[2] + 1024 : lane < 40 ? sa[1] + 768 : sa[3] + 2432);
    const unsigned ro = nt80 & 3, pb = WS_WGU + ntg * 143360 + (grp ? 68 + 5 * wl : 17 * wl) * 1024;
    sa[4] = wob + mod5(ro + wl + (wl != 0)) * 20480; sa[5] = ((0x4176u >> (4 * wl)) & 7) * 20480;
    sa[6] = pb + (grp ? 20 + 8 * wl : 5) * 1024; sa[7] = L_FL + w * 16;
    u32x2 sb = {pb, rsb};
    const unsigned vt = lane < 17 ? WS_WD + nt80 * 276480 + qb * 34560 + (w * 17 + lane) * 256 : OOB;
    const unsigned vs = b < 135 ? l16 ^ (((lane >> 1) ^ (lane >> 3)) & 1) * 0xa0 : b * 8 + w < 1820 ? l16 : OOB;
    const unsigned vfs = (w == 2 && lane == 0) ? WS_FR + b * 8 : OOB, vfe = (b == 0 && w == 0 && lane == 0) ? WS_E : OOB, vfp = WS_FR + w * 256 + t * 8;
    const unsigned fg = (b * 205) >> 10;
    const unsigned vcs = (b < 135 && w < 2) ? WS_CS + ((b - fg * 5) * 3584 + fg * 128 + w * 64 + lane) * 4 : OOB;
    const unsigned ma = 40960 + (w & 2) * 30720 + grp * 6144 + l16;
    unsigned ov = (t >> 4) * 20480 + (t & 15) * 16, ow = hh ? OOB : ov;
    unsigned ob = WS_O + (qb * 16 + wl * 4 + grp * 2) * 20480, oc = ob + (64 + (head >> 1)) * 256 + (head & 1) * 8;
    ov += hh * 256; ob += head * 512;
    f32x16 O, P;
    asm volatile(
        "ds_read2st64_b32 v[196:197], %[cp] offset1:2\n\tds_read2st64_b32 v[198:199], %[cp] offset0:4 offset1:6\n\t"
        Z8("96") Z8("104") Z8("112") Z8("120") Z8("136") "v_mov_b32 v164, %[one]\n\tv_mov_b32 v165, %[one]\n\tv_mov_b64 v[166:167], v[164:165]\n\tv_mov_b64 v[168:169], v[164:165]\n\t"
        "v_mov_b64 v[170:171], v[164:165]\n\tv_mov_b64 v[172:173], v[164:165]\n\tv_mov_b64 v[174:175], v[164:165]\n\tv_mov_b64 v[192:193], -1\n\tv_mov_b64 v[194:195], -1\n\t"
        "s_mov_b32 s56, 0xfffff\n\ts_mov_b32 s57, s56\n\ts_mov_b32 s58, " SPINS "\n\ts_mov_b32 m0, %[dm]\n\ts_cmp_gt_u32 %[w], 1\n\t" "s_cbranch_scc1 2f\n"
        "1:\n\tbuffer_load_dwordx2 v[32:33], %[fo], s[40:43], 0 offen sc1\n\ts_waitcnt vmcnt(0)\n\tv_cmp_ne_u64 vcc, v[32:33], %[tag]\n\t"
        "s_cbranch_vccz 2f\n\ts_sub_u32 s58, s58, 1\n\ts_cbranch_scc0 1b\n"
        "2:\n\ts_barrier\n\t" TSA("4") KVP LDQ5 KVP "s_barrier\n\t" KVP KVP
        LGK(0) "v_add_f32 v196, v196, v197\n\tv_add_f32 v198, v198, v199\n\tv_add_f32 v196, v196, v198\n\ts_mov_b32 s58, " SPINS "\n\t"
        "s_waitcnt vmcnt(2)\n\ts_barrier\n\t" TSA("5")
        KT("0") KR("0") VR(168, "0") "s_waitcnt lgkmcnt(4)\n\t" QKT(32, 0) "s_waitcnt lgkmcnt(2)\n\ts_nop 7\n\ts_nop 7\n\ts_nop 2\n\t" QKA(32) QKB(32) KR("256")
        PVC "s_waitcnt vmcnt(0)\n\ts_barrier\n\ts_nop 6\n\t" TSS("17") TSF("17")
        QUADP(0, 10240, KVP, KVP STW(176, 68), KVP, STW(180, 69) "buffer_store_dword v196, %[vcs], s[40:43], 0 offen sc1\n\t", KVP, STW(184, 70), KVP, STW(188, 71))
        "s_waitcnt vmcnt(8)\n\ts_barrier\n\t"
        QUADP(10240, 20480, KVP, STM(l16, "s46", 0), KVP, STM(mv, "0", 0), KVP, "", KVP, "")
        "s_waitcnt vmcnt(2)\n\ts_barrier\n\tbuffer_store_dwordx2 %[tag], %[vfs], s[40:43], 0 offen sc1\n\t"
        QUADP(20480, 30720, KVP, STM(l16, "s45", 0), KVP_(-57344), KVP, KVP, STM(l16, "s47", 0), KVP, KVP)
        QUADP(30720, 40960, KVP, "buffer_load_dwordx4 v[248:251], %[rv0], %[rx], s53 offen\n\tbuffer_load_dwordx2 v[252:253], %[rv1], %[rx], s53 offen\n\t", "buffer_load_dwordx2 v[198:199], %[vfp], s[40:43], 0 offen sc1\n\t", STM(l16, "s47", 1024), "", STM(l16, "s47", 2048), "", "")
        "s_mov_b32 m0, s49\n\ts_waitcnt vmcnt(2)\n"
        "3:\n\tv_cmp_ne_u64 vcc, v[198:199], %[tag]\n\ts_cbranch_vccz 4f\n\t"
        "buffer_load_dwordx2 v[198:199], %[vfp], s[40:43], 0 offen sc1\n\ts_sub_u32 s58, s58, 1\n\ts_waitcnt vmcnt(0)\n\ts_cbranch_scc0 3b\n"
        "4:\n\t" PVC "s_barrier\n\t" TSA("6") "buffer_store_dwordx2 %[tag], %[vfe], s[40:43], 0 offen sc1\n\t"
        TSS("25")
        QUADP(40960, 51200, PK(176, 0), PK(180, 1024), PK(184, 2048) TCH(0), PK(188, 3072) PKA, PK(192, 0) "s_mov_b32 s52, s50\n\t" PK(196, 0), PK(200, 1024), PK(204, 2048), PK(208, 3072) PKA)
        QUAD(51200, 0, PK(212, 0), PK(216, 1024), PK(220, 2048) TCH(128), PK(224, 3072) PKA, PK(228, 0) PK(232, 1024), PK(236, 2048), PK(240, 3072) PKA, PK(244, 0))
        "s_cbranch_vccz 5f\n\ts_waitcnt vmcnt(20)\n\ts_barrier\n\t" XP1 "5:\n\t" TSF("30")
        QUAD(0, 10240, WO4, "", WO4, "", WO4, "", WO4, "")
        QUAD3(10240, 0, WO4 XP2, "", "", "", "", "")
        STEP_(64, 168, 172, "", "v_mov_b32 v45, s51\n\t", "v_mov_b32 v41, 0\n\t", "v_mov_b64 v[42:43], 0\n\t", "v_mov_b32 v44, -1\n\t") TSI
        "v_xor_b32 v46, 16, v45\n\ts_waitcnt vmcnt(0)\n\ts_setprio 1\n\t" SMF(112, 172, 136) "s_cbranch_vccnz 5f\n\ts_barrier\n"
        "5:\n\ts_mov_b32 s58, " SPINS "\n\ts_nop 7\n\ts_nop 4\n\t" TSA("7")
        "ds_write_b128 %[e0], v[112:115]\n\tds_write_b128 %[e0], v[116:119] offset:1024\n\tds_write_b32 %[e0], v120 offset:2048\n\t"
        "s_mov_b32 exec_hi, 0\n\tds_write_b128 %[e0], v[120:123] offset:2048\n\t" FSET("v44", "v45", 4)
        "5:\n\t" FGET("v47", "v46", 4) RD1("40", "%[e1]", "2048") "s_mov_b32 exec_hi, -1\n\t" RD1("32", "%[e1]", "0") RD1("36", "%[e1]", "1024") "ds_read_b32 v40, %[e1] offset:2048\n\t"
        "s_mov_b32 exec_hi, 0\n\t" LGK(4) FTST("v47", "s58", "5b") "s_mov_b32 exec_hi, -1\n\t" LGK(0)
        : "=&{v[96:111]}"(O), "=&{v[32:47]}"(P), "=&{v[144:151]}"(qm), "=&{v[152:153]}"(qt), "+{v[176:207]}"(pk.a), "=&{v[208:239]}"(pk.b), "=&{v[240:247]}"(pk.c), "=&{v[248:251]}"(r0), "=&{v[252:253]}"(r1), "=&{v154}"(pk.d),
          "+{s[44:51]}"(sa), "+{s[52:53]}"(sb), "+v"(ov), "+v"(ow), "+s"(ob), "+s"(oc) PFIO
        : "{s[40:43]}"(rws), "{s[60:75]}"(rt.t), [a0] "v"(a0), [at] "v"(at), [av] "v"(av), [l16] "v"(l16), [tag] "v"(tag), [vcs] "v"(vcs), [fo] "v"(fo), [sq] "s"(sq),
          [cp] "v"(L_CP + (w & 1) * 256 + lane * 4), [mv] "v"(mv), [vs] "v"(vs), [vt] "v"(vt), [one] "v"(ones), [idx] "v"(0xDDDD),
          [va] "v"(va), [vb] "v"(va + 2560 - kh * 5120), [vc] "v"(grp * 256 + t * 8 + (hh ^ kh) * 2560), [e0] "v"(ma + kh * 3072), [e1] "v"(ma + 3072 - kh * 3072), [vfs] "v"(vfs), [vfe] "v"(vfe), [vfp] "v"(vfp), [rv0] "v"(rv0), [rv1] "v"(rv1),
          [dm] "s"(dm), [w] "s"(w), [grp] "s"(grp), [rx] "s"(mkrs(p.x, 2048 * 1280))
        : "memory", "scc", "vcc", "s56", "s57", "s58", VCL48, VCL64, VCL80, VCL112, VCL128, "v140", "v141", "v142", "v143", "v156", "v157", VCL160 PFCL);
    float o[12];
#pragma unroll
    for (int i = 0; i < 12; ++i) o[i] = O[i] + P[i];
    {
        f32x2 z = {0.f, 0.f};
#pragma unroll
        for (int r = 0; r < 12; r += 2) z += f32x2{o[r], o[r + 1]} * 0.f;
        const float l = (hh ? o[8] : 1.f) + (z[0] + z[1]);
        const bool bad = !(l < 1.8446744e19f && l > 8.6736174e-19f);
        if (__builtin_expect(anyl(bad), 0)) {
            float m = -__builtin_inff();
            const unsigned ko = hh * 1024 + t * 8, vo = 2560 + hh * 320 + (t < 20 ? t : 0) * 16;
            const u32x4 qa = {qm[0], qm[1], qm[2], qm[3]}; const u32x2 qe = {hh ? 0u : qt[0], hh ? 0u : qt[1]};
            f32x16 c = {}, A = {};
#pragma unroll 1
            for (int it = 0; it < 128; ++it) {
                const unsigned sb = kvb + ((it & 63) >> 1) * 5120;
                const u32x2 k0 = ld8c(rws, ko, sb + (it & 1) * 256), k1 = ld8c(rws, ko + 512, sb + (it & 1) * 256), k2 = ld8c(rws, t * 8 + 2048, sb + (it & 1) * 256);
                f32x16 s = qk_tile(u32x4{k0[0], k0[1], k1[0], k1[1]}, k2, qa, qe, c);
                if (it < 64) {
                    m = max16(s, m);
                    if (it == 63) {
                        const f32x2 mm = swap32(m, m);
                        m = __builtin_fmaxf(mm[0], mm[1]);
#pragma unroll
                        for (int r = 0; r < 16; ++r) c[r] = -m;
                    }
                } else {
                    u32x4 v0 = ld16c(rws, vo, sb + (it & 1) * 1280), v1 = ld16c(rws, vo + 640, sb + (it & 1) * 1280);
#pragma unroll
                    for (int i = 0; i < 4; ++i) { v0[i] = t < 20 ? v0[i] : ones; v1[i] = t < 20 ? v1[i] : ones; }
#pragma unroll
                    for (int r = 0; r < 16; ++r) s[r] = __builtin_amdgcn_exp2f(s[r]);
                    A = pv16(v0, __builtin_shufflevector(s, s, 0, 1, 2, 3, 4, 5, 6, 7), A);
                    A = pv16(v1, __builtin_shufflevector(s, s, 8, 9, 10, 11, 12, 13, 14, 15), A);
                }
            }
#pragma unroll
            for (int r = 0; r < 12; ++r) o[r] = A[r];
        }
    }
    const float inv = __builtin_amdgcn_rcpf(swap32(o[8], o[8])[1]);
    st16(rws, ov, ob, u32x4{pk2(o[0] * inv, o[1] * inv), pk2(o[2] * inv, o[3] * inv), pk2(o[4] * inv, o[5] * inv), pk2(o[6] * inv, o[7] * inv)});
    st8(rws, ow, oc, u32x2{pk2(o[8] * inv, o[9] * inv), pk2(o[10] * inv, o[11] * inv)});
}

#define MF16(A, W, B) "v_mfma_f32_16x16x32_bf16 v[" A ":" A "+3], v[" W ":" W "+3], v[" B ":" B "+3], v[" A ":" A "+3]\n\t"
#define MF0(A, W, B) "v_mfma_f32_16x16x32_bf16 v[" A ":" A "+3], v[" W ":" W "+3], v[" B ":" B "+3], 0\n\t"
#define RD5(W, V, O) RD1(W, V, O) RD1(W "+4", V, O "+1024") RD1(W "+8", V, O "+2048") RD1(W "+12", V, O "+3072") RD1(W "+16", V, O "+4096")
#define WR1(V, R, O) "ds_write_b128 " V ", v[" R ":" R "+3] offset:" O "\n\t"
#define MX3(B) "v_max3_u32 %[t], v[" B "], v[" B "+1], v[" B "+2]\n\t"
#define MX2(B) "v_max3_u32 %[t], v[" B "], v[" B "+1], %[t]\n\t"
#define MXE(B) "v_max_u32 %[t], v[" B "], %[t]\n\tv_cmp_eq_u32 vcc, -1, %[t]\n\ts_and_b64 vcc, vcc, s[60:61]\n\t"
#define CK4(B) MX3(B) MXE(B "+3")
#define CK8(B) MX3(B) MX2(B "+3") MX2(B "+5") MXE(B "+7")
#define CK16(B) MX3(B) MX2(B "+3") MX2(B "+5") MX2(B "+7") MX2(B "+9") MX2(B "+11") MX2(B "+13") MXE(B "+15")
#define CK28(B) MX3(B) MX2(B "+3") MX2(B "+5") MX2(B "+7") MX2(B "+9") MX2(B "+11") MX2(B "+13") MX2(B "+15") MX2(B "+17") MX2(B "+19") MX2(B "+21") MX2(B "+23") MX2(B "+25") MXE(B "+27")
#define SPIN0 "s_mov_b32 s59, " SPINS "\n\ts_mov_b64 s[60:61], -1\n\t"
#define SPIN1 "s_sub_u32 s59, s59, 1\n\ts_cselect_b64 s[60:61], 0, s[60:61]\n\t"
#define UNP(D, S) "v_lshlrev_b32 v[" #D "], 16, v" #S "\n\tv_and_b32 v[" #D "+1], 0xffff0000, v" #S "\n\t"
#define PKADD(D, S) "v_pk_add_f32 v[" D ":" D "+1], v[" D ":" D "+1], v[" S ":" S "+1]\n\t"
#define CVT(D, S) "v_cvt_pk_bf16_f32 v[" D "], v[" S "], v[" S "+1]\n\t"
#define WON(n, W, B, R) MF16("72+4*" #n, W "+4*" #n, B) R
#define WOK(W, B, V, O) WON(0, W, B, RD1(W, V, O)) WON(1, W, B, RD1(W "+4", V, O "+1024")) WON(2, W, B, RD1(W "+8", V, O "+2048")) WON(3, W, B, RD1(W "+12", V, O "+3072")) WON(4, W, B, RD1(W "+16", V, O "+4096"))
#define WOE(W, B) WON(0, W, B, "") WON(1, W, B, "") WON(2, W, B, "") WON(3, W, B, "") WON(4, W, B, "")
#define WOL(B, S) LD1(#B, "%[l16]", #S, "0") LD1(#B "+4", "%[l16]", #S, "1024")
#define WOC(p, N, B) "1" #p ":\n\t" VM(N) CK8(#B) "s_cbranch_scc1 2" #p "f\n\t" TSI
#define WOT(p) "2" #p ":\n\ts_call_b64 s[56:57], 8f\n\ts_branch 1" #p "b\n"
#define WOR(B, S) CK8(#B) "s_cbranch_scc0 1f\n\ts_mov_b64 exec, vcc\n\t" WOL(B, S) "s_mov_b64 exec, -1\n1:\n\t"
#define PW2(V, R, O) WR1(V, #R, #O) WR1(V, #R "+4", #O "+1024")
#define LD5(R) LD1(#R, "%[l16]", "s51", "0") LD1(#R "+4", "%[l16]", "s51", "1024") LD1(#R "+8", "%[l16]", "s51", "2048") LD1(#R "+12", "%[l16]", "s51", "3072") \
    "s_add_u32 s51, s51, 4096\n\t" LD1(#R "+16", "%[l16]", "s51", "0") "s_add_u32 s51, s51, 6144\n\t"
#define CSL(R) "buffer_load_dword " R ", %[ve], s[40:43], s49 offen sc1\n\ts_add_u32 s49, s49, 14336\n\t"
DI void phase_wo(rsrc_t rws, int w, unsigned lane, int mt, int nt, int ntg, u32x4& r0, u32x2& r1, Park& pk PROFARG) {
    const unsigned tg = w >> 1, kh = w & 1, wl = w & 3, grp = w >> 2, t = lane & 15, g = lane >> 4, l16 = lane * 16, la = kh * 5120 + l16, e = (w ^ 4) * 64 + lane, ro = nt & 3;
    const unsigned ab = WS_O + (mt * 4 + tg) * 20480 + kh * 10240, hb = WS_B + (mt >> 2) * QBS + nt * 256, tk = (mt & 3) * 4 + tg;
    u32x8 sa = {ab + ro * 2048, ab + (ro + 1) * 2048, ab + mod5(ro + 2) * 2048, ab + mod5(ro + 3) * 2048, ab + mod5(ro + 4) * 2048, WS_CS + ntg * 448, hb + tk * 20480,
                WS_WO + nt * 102400 + (ro + 1) * 20480 + kh * 5120};
    const unsigned ma = 40960 + (w & 2) * 30720 + grp * 6144 + l16;
    const unsigned q0 = ((grp ? 0x87827d78u : 0x70423400u) >> (8 * wl)) & 255, q1 = ((grp ? 0x9950148cu : 0x0e4a3c08u) >> (8 * wl)) & 255, q2 = ((grp ? 0x21571b93u : 0x67642b28u) >> (8 * wl)) & 255;
    unsigned tmp, cs;
    asm volatile(
        CSL("v92") CSL("v93") CSL("v94") CSL("v95") CSL("%[cs]") LD5(136) LD5(156) SPIN0
        RD5("96", "%[wa]", "0") RD5("116", "%[wa]", "10240")
        UNP(72, 248) UNP(74, 249) UNP(76, 250) UNP(78, 251) Z8("80") UNP(88, 252) UNP(90, 253)
        "v_mov_b32 %[t], -1\n\ts_cmp_eq_u32 %[grp], 0\n\t" LGK(0) FSET("%[t]", "%[fw]", 140)
        VM(15) SLP(SLO) TSA("20") TSS("45") WOL(32, s44) WOL(40, s45) WOL(48, s46) WOL(56, s47) WOL(64, s48) "s_cbranch_scc1 2f\n"
        "1:\n\t" FGET("%[t]", "%[fs]", 132) LGK(0) FTST("%[t]", "s59", "1b") "v_mov_b32 %[t], -1\n\t"
        PW2("%[p0]", 176, 0) PW2("%[p0]", 184, 2048) WR1("%[p0]", "192", "4096") FSET("%[t]", "%[fw]", 132) "s_mov_b32 s59, " SPINS "\n\t"
        VM(10) "v_add_f32 v92, v92, v93\n\tv_add_f32 v94, v94, v95\n\tv_add_f32 v92, v92, %[cs]\n\tv_add_f32 %[cs], v92, v94\n\tds_write_b32 %[vl], %[cs]\n2:\n"
        WOC(0, 8, 32) TSA("8") WOK("96", "32", "%[wa]", "20480") WOK("116", "36", "%[wa]", "30720")
        WOC(1, 6, 40) WOK("136", "40", "%[wb]", "0") WOK("156", "44", "%[wb]", "10240")
        WOC(2, 4, 48) LGK(10) WOK("96", "48", "%[wc]", "0") WOK("116", "52", "%[wc]", "10240")
        WOC(3, 2, 56) LGK(10) WOE("136", "56") WOE("156", "60")
        WOC(4, 0, 64) LGK(0) WOE("96", "64") WOE("116", "68") TSA("9")
        "s_cmp_eq_u32 %[kh], 0\n\ts_mov_b32 s59, " SPINS "\n\tv_mov_b32 v32, -1\n\ts_nop 3\n\t" WR1("%[xw]", "80", "0") WR1("%[xw]", "84", "1024") "s_cbranch_scc1 1f\n\t" WR1("%[xw]", "88", "2048")
        "1:\n\t" FSET("v32", "%[fw]", 8)
        "1:\n\t" FGET("%[t]", "%[fp]", 8) RD1("80", "%[xr]", "0") RD1("84", "%[xr]", "1024") RD1("92", "%[xr]", "2048") LGK(3) FTST("%[t]", "s59", "1b") "s_cmp_eq_u32 %[kh], 0\n\t"
        LGK(2) PKADD("72", "80") PKADD("74", "82") LGK(1) PKADD("76", "84") PKADD("78", "86") CVT("248", "72") CVT("249", "74") CVT("250", "76") CVT("251", "78")
        "buffer_store_dwordx4 v[248:251], %[h0], s[40:43], s50 offen sc1\n\t" LGK(0) "s_cbranch_scc0 9f\n\t"
        PKADD("88", "92") PKADD("90", "94") CVT("252", "88") CVT("253", "90") "buffer_store_dwordx2 v[252:253], %[h1], s[40:43], s50 offen sc1\n\ts_branch 9f\n"
        WOT(0) WOT(1) WOT(2) WOT(3) WOT(4)
        "8:\n\t" SLP(RO) VM(0) WOR(32, s44) WOR(40, s45) WOR(48, s46) WOR(56, s47) WOR(64, s48) VM(0) SPIN1 "s_setpc_b64 s[56:57]\n9:\n\t"
        TSA("31") FSET("v32", "%[fw]", 12)
        "1:\n\ts_sleep 1\n\t" FGET("%[t]", "%[fs]", 0) LGK(0) FTST("%[t]", "s59", "1b") "s_cmp_eq_u32 %[grp], 0\n\ts_cbranch_scc1 1f\n\t"
        PW2("%[p1]", 196, 0) PW2("%[p1]", 204, 2048) PW2("%[p1]", 212, 4096) WR1("%[p1]", "220", "6144") PW2("%[p2]", 224, 0) PW2("%[p2]", 232, 2048) PW2("%[p2]", 240, 4096)
        FSET("v32", "%[fw]", 136) "s_branch 2f\n1:\n\t"
        PW2("%[p0]", 176, 0) PW2("%[p0]", 184, 2048) PW2("%[p0]", 192, 4096) PW2("%[p0]", 200, 6144) PW2("%[p1]", 208, 0) PW2("%[p1]", 216, 2048) PW2("%[p1]", 224, 4096)
        PW2("%[p2]", 232, 0) WR1("%[p2]", "240", "2048") FSET("v32", "%[fw]", 132) "s_setprio 0\n2:"
        : "+{v[248:251]}"(r0), "+{v[252:253]}"(r1), "+{v[176:207]}"(pk.a), "+{v[208:239]}"(pk.b), "+{v[240:247]}"(pk.c), "+{s[44:51]}"(sa), [t] "=&v"(tmp), [cs] "=&v"(cs) PFIO
        : "{s[40:43]}"(rws), [l16] "v"(l16), [wa] "v"(la + 122880), [wb] "v"(la + 20480), [wc] "v"(la + 81920), [kh] "s"(kh), [grp] "s"(grp),
          [p0] "v"(q0 * 1024 + l16), [p1] "v"(q1 * 1024 + l16), [p2] "v"(q2 * 1024 + l16),
          [fw] "v"(L_FL + w * 16), [fp] "v"(L_FL + (w ^ 1) * 16), [fs] "v"(L_FL + (l16 & 112) + (grp ? 8 : (lane & 4) ? 4 : 12)),
          [ve] "v"(e < 112 ? (e ^ (((lane >> 1) ^ (lane >> 3)) & 1) * 10) * 4 : OOB), [vl] "v"(e < 112 ? L_CS + e * 4 : L_FL + 260),
          [xw] "v"(ma + 3072 - kh * 3072), [xr] "v"(ma + kh * 3072),
          [h0] "v"((2 * g + kh) * 2048 + t * 16), [h1] "v"((8 + (g >> 1)) * 2048 + t * 16 + (g & 1) * 8)
        : "memory", "scc", "vcc", "s56", "s57", "s59", "s60", "s61", VCL32, VCL48, VCL64, VCL80, VCL96, VCL112, VCL128, VCL140, VCL160 PFCL);
    TS(10);
    float s = 0.f, q = 0.f;
#pragma unroll
    for (int k = 0; k < 6; ++k) {
        const bf2v v = __builtin_bit_cast(bf2v, k < 4 ? r0[k & 3] : r1[k & 1]);
        s = __builtin_amdgcn_fdot2_f32_bf16(v, __builtin_bit_cast(bf2v, 0x3f803f80u), s, false);
        q = __builtin_amdgcn_fdot2_f32_bf16(v, v, q, false);
    }
    const f32x2 y = swap32(s, q);
    const float z = sum16(y[0] + y[1]);
    const f32x2 v = swap32(z, z);
    st8(rws, g == 0 ? t * 16 + kh * 8 : OOB, hb + OFF_HS + tk * 2048, u32x2{__float_as_uint(v[0]), __float_as_uint(v[1])});
}

#define GW0 LGK(.Lc-.Lr2)
#define GW1
#define GW2
#define GW3 LGK(.Lc-.Lr4)
#define GW4
#define GW5 LGK(.Lc-.Lr6)
#define GW6
#define GUN(MF, n, H, R, X) GW##n MF("140+8*" #n, "112+4*" #n, H) X MF("144+8*" #n, "112+4*" #n, H "+8") R
#define RDW_(LO, HI, KB) ".if (7*(\\k)+\\n >= " #LO ") && (7*(\\k)+\\n < " #HI ")\n.set .Lwa, (7*(\\k)+\\n-" #LO "+" #KB ")*1024\n.endif\n"
#define LC ".set .Lc, .Lc+1\n\t"
#define RDWDEF ".set .Lc, 0\n.set .Lhi, 0\n.macro RDW k, n, a0, a1\n" RDW_(0, 14, 0) RDW_(14, 17, 40) RDW_(17, 31, 52) RDW_(31, 34, 43) RDW_(34, 48, 66) RDW_(48, 51, 100) RDW_(51, 59, 112) \
    RDW_(59, 65, 14) RDW_(65, 68, 103) RDW_(68, 101, 120) RDW_(101, 114, 20) RDW_(114, 127, 80) RDW_(127, 134, 153) RDW_(134, 140, 33) \
    ".if (\\k) == 20\n.set .Lwa, 65536+52224+64*\\n\n.set .Lhi, 0\n.endif\n" \
    ".if (.Lwa >= 131072) && (.Lhi == 0)\nv_add_u32 \\a1, 0x8000, \\a1\n.set .Lhi, 32768\n.endif\n" \
    ".if (.Lwa >= 65536) && (.Lwa < 98304) && (.Lhi != 0)\nv_add_u32 \\a1, 0xffff8000, \\a1\n.set .Lhi, 0\n.endif\n" \
    ".if .Lwa < 65536\nds_read_b128 v[112+4*\\n:115+4*\\n], \\a0 offset:.Lwa\n.else\nds_read_b128 v[112+4*\\n:115+4*\\n], \\a1 offset:.Lwa-65536-.Lhi\n.endif\n" \
    LC ".set .Lr\\n, .Lc\n.endm\n\t"
#define RDW(K, n) "RDW " K ", " #n ", %[l16], %[wa]\n\t"
#define GUK(MF, H, K, X0, X1, X2, X3, X4, X5, X6) \
    GUN(MF, 0, H, RDW(K, 0), X0) GUN(MF, 1, H, RDW(K, 1), X1) GUN(MF, 2, H, RDW(K, 2), X2) GUN(MF, 3, H, RDW(K, 3), X3) \
    GUN(MF, 4, H, RDW(K, 4), X4) GUN(MF, 5, H, RDW(K, 5), X5) GUN(MF, 6, H, RDW(K, 6), X6)
#define LH0(H, S) LD1(#H, "%[l16]", #S, "0")
#define LH1(H, S) LD1(#H "+4", "%[l16]", #S, "1024")
#define LH2(H, S) LD1(#H "+8", "%[lb]", #S, "0")
#define LH3(H, S) LD1(#H "+12", "%[lb]", #S, "1024")
#define LDH(H, S) LH0(H, S) LH1(H, S) LH2(H, S) LH3(H, S)
#define LH(H, S) LH0(H, S), LH1(H, S), LH2(H, S), LH3(H, S)
#define LS(R, O) LD1(#R, "%[l16]", "s54", #O)
#define LDST LS(32, 0) LS(36, 1024) LS(40, 2048) LS(44, 3072)
#define WL(R, O) LD1(#R, "%[l16]", "s52", #O)
#define WA "s_add_u32 s52, s52, 4096\n\t"
#define W15 WL(196, 0) WL(200, 1024) WL(204, 2048) WL(208, 3072) WA WL(212, 0) WL(216, 1024) WL(220, 2048) WL(224, 3072) WA WL(228, 0) WL(232, 1024) WL(236, 2048) WL(240, 3072) WA \
    WL(244, 0) WL(24, 1024) WL(28, 2048)
#define FCK(L) "v_cmp_ne_u32 vcc, -1, v16\n\ts_cbranch_vccnz 5" #L "f\n6" #L ":\n\t"
#define FWT(L, A, O) "5" #L ":\n\t" FGET("v16", A, O) LGK(0) FTST("v16", "s58", "5" #L "b") "s_branch 6" #L "b\n"
#define WEND "v_add_u32 %[lb], 4096, %[l16]\n\t" WL(32, 3072) LD1("36", "%[lb]", "s52", "0") LD1("40", "%[lb]", "s52", "1024") LD1("44", "%[z4]", "s52", "2048") LD1("96", "%[z4]", "s52", "3072") WA \
    LD1("100", "%[z4]", "s52", "0") LD1("104", "%[z4]", "s52", "1024") LD1("108", "%[z4]", "s52", "2048")
#define CKS(N, B, s, Y1, Y2, Y3, Y4) VM(N) MX3(B), MX2(B "+3") Y1, MX2(B "+5") Y2, MX2(B "+7"), MX2(B "+9") Y3, MX2(B "+11") Y4, MX2(B "+13") MXE(B "+15") "s_cbranch_scc1 4" #s "f\n3" #s ":\n\t"
#define CKS_(...) CKS(__VA_ARGS__)
#define NO4 "", "", "", ""
#define UN(...) __VA_ARGS__
#define GUK_(...) GUK(__VA_ARGS__)
#define TR 8
#define BQ 1
#define SE 0
#define STR_(x) #x
#define STR(x) STR_(x)
#define PR0(s) ".if " #s " == 0\ns_setprio 0\n.endif\n.if " #s " == " STR(TR) "\n" FSET("v21", "v22", 264) LC ".endif\n\t"
#define GUS(MF, s, H, PRE, HN, N, Y, ...) PR0(s) GUK_(MF, #H, "2*" #s "+1", __VA_ARGS__) PRE GUK_(MF16, #H "+4", "2*" #s "+2", CKS_(N, #HN, s, UN Y)) TSI
#define LJ(H, S) LH0(H, S), "", LH1(H, S), "", LH2(H, S), "", LH3(H, S)
#define REDO(s, L, CK) "4" #s ":\n\ts_mov_b64 exec, vcc\n\t" SLP(RF) L "s_mov_b64 exec, -1\n\t" VM(0) SPIN1 CK "s_cbranch_scc1 4" #s "b\n\ts_branch 3" #s "b\n"
#define HNX(A, B) "s_add_u32 s" #B ", s" #A ", 2048\n\ts_cmp_eq_u32 s" #B ", s55\n\ts_cselect_b32 s" #B ", %[hb], s" #B "\n\t"
#define SW32(A, B) "v_permlane32_swap_b32 v[" A "], v[" B "]\n\t"
#define SW16(A, B) "v_permlane16_swap_b32 v[" A "], v[" B "]\n\t"
#define EPF(A, C, T) "v_pk_fma_f32 v[" A ":" A "+1], v[" C ":" C "+1], v[16+4*" #T ":17+4*" #T "], v[" A ":" A "+1]\n\t"
#define EPT(A, C, T, D) EPF(A, C, T) EPF(A "+2", C "+2", T) "v_max_f32 v[" A "], 0, v[" A "]\n\tv_max_f32 v[" A "+1], 0, v[" A "+1]\n\t" \
    "v_pk_mul_f32 v[" A "+2:" A "+3], v[" A "+2:" A "+3], v[18+4*" #T ":19+4*" #T "]\n\t" \
    "v_pk_mul_f32 v[" A ":" A "+1], v[" A ":" A "+1], v[" A "+2:" A "+3]\n\t" CVT(D, A)
#define EPN(n, D) EPT("140+8*" #n, "112+4*" #n, 0, D) EPT("144+8*" #n, "112+4*" #n, 1, D "+1")
#define EP2(n, m, D, N) LGK(N) EPN(n, D) EPN(m, D "+2") SW32(D, D "+1") "s_nop 0\n\t" SW32(D "+2", D "+3") \
    "buffer_store_dwordx4 v[" D ":" D "+3], %[f0], s[40:43], s53 offen sc1\n\ts_add_u32 s53, s53, 15872\n\t"
#define LF0(F, S) LD1(F, "%[l16]", S, "0")
#define LF1(F, S) LD1(F "+4", "%[l16]", S, "1024")
#define LF2(F, S) LD1(F "+8", "%[l16]", S, "2048")
#define LF3(F, S) LD1(F "+12", "%[l16]", S, "3072")
#define LF4(F, S) LD1(F "+16", "%[lb]", S, "0")
#define LF5(F, S) LD1(F "+20", "%[lb]", S, "1024")
#define LF6(F, S) LD1(F "+24", "%[z4]", S, "2048")
#define LDF(F, S) LF0(F, S) LF1(F, S) LF2(F, S) LF3(F, S) LF4(F, S) LF5(F, S) LF6(F, S)
#define DK(MF, F, a, b, c, d, e) MF("168", #a, F) MF("172", #b, F) MF("176", #c, F) MF("180", #d, F) MF("184", #e, F)
#define DT(F, X0, X1, X2, X3, X4, X5, X6) DK(MF0, F, 196, 200, 204, 208, 212) X0 DK(MF16, F "+4", 216, 220, 224, 228, 232) X1 DK(MF16, F "+8", 236, 240, 244, 24, 28) X2 \
    DK(MF16, F "+12", 48, 52, 56, 60, 64) X3 DK(MF16, F "+16", 68, 72, 76, 80, 84) X4 DK(MF16, F "+20", 88, 92, 32, 36, 40) X5 DK(MF16, F "+24", 44, 96, 100, 104, 108) X6 TSI
#define DTL(F, S) DT(F, LF0(F, S), LF1(F, S), LF2(F, S), LF3(F, S), LF4(F, S), LF5(F, S), LF6(F, S))
#define DTE(F) DT(F, "", "", "", "", "", "", "")
#define DW(S) "v_add_u32 %[wa], " S ", %[l16]\n\t" WR1("%[wa]", "168", "0") WR1("%[wa]", "172", "1024") WR1("%[wa]", "176", "2048") WR1("%[wa]", "180", "3072") WR1("%[wa]", "184", "4096")
#define DC(N, F, s) VM(N) CK28(F) "s_cbranch_scc1 4" #s "f\n3" #s ":\n\t"
#define LD0 LDH(32, s64) LDH(48, s65)
DI void phase_ffn(rsrc_t rws, int w, unsigned lane, int mt, int nt, int qg, int ntg, const Park& pk PROFARG) {
    const unsigned t = lane & 15, g = lane >> 4, l16 = lane * 16, qbb = WS_B + qg * QBS, wg = (w & 3) * 2 + (w >> 2), hb = qbb + wg * 40960, k0 = 7 * w - (w == 7);
    const unsigned fb = WS_B + (mt >> 2) * QBS + OFF_F + (mt & 3) * 4 * 55296 + k0 * 1024, fv = (g >> 1) * 55296 + t * 16;
    u32x8 sa; u32x4 sb = {WS_WD + nt * 276480 + k0 * 5120, qbb + OFF_F + wg * 110592 + ntg * 256, qbb + OFF_HS + wg * 4096, hb + 20480};
#pragma unroll
    for (int i = 0; i < 4; ++i) { sa[i] = fb + i * 55296; sa[4 + i] = i * 40960 + w * 5120; }
    unsigned wa = 65536 + l16, h0 = hb + (((ntg & 15) * 5) >> 3) * 2048, tmp, lb = l16 + 20480;
    u32x32 da = pk.a, db = pk.b; u32x8 dc = pk.c;
    asm volatile(
        RDWDEF SPIN0 HNX(64, 65) HNX(65, 66) HNX(66, 67) HNX(67, 68) HNX(68, 69) HNX(69, 70) HNX(70, 71) HNX(71, 72) HNX(72, 73)
        "v_and_b32 v17, 0x70, %[l16]\n\tv_and_b32 v18, 0x30, %[l16]\n\tv_mov_b32 v21, -1\n\tv_mov_b32 v22, %[fw]\n\t"
        "s_mov_b32 s58, s59\n\ts_cmp_eq_u32 %[ng], 31\n\ts_cbranch_scc1 7f\n\t" VM(1) SLP(SH) TSA("21") LD0
        "1:\n\t" FGET("v16", "v17", 52868) LGK(0) FTST("v16", "s58", "1b")
        RDW("0", 0) RDW("0", 1) RDW("0", 2) RDW("0", 3) RDW("0", 4) RDW("0", 5) RDW("0", 6)
        "1:\n\t" VM(4) CK16("32") "s_cbranch_scc1 2f\n\t" TSA("11") TSS("35")
        GUS(MF0, 0, 32, "", 48, 4, (LH(80, s67)), LJ(64, s66))
        GUS(MF16, 1, 48, "", 64, 8, (LH(32, s69)), LJ(96, s68))
        GUS(MF16, 2, 64, "", 80, 12, (WL(196, 0), WL(200, 1024), WL(204, 2048), ""), LJ(48, s70))
        GUS(MF16, 3, 80, "", 96, 15, (WL(208, 3072) WA, WL(212, 0), WL(216, 1024), ""), LJ(64, s71))
        GUS(MF16, 4, 96, "", 32, 18, (WL(220, 2048), WL(224, 3072) WA, WL(228, 0), ""), LJ(80, s72))
        GUS(MF16, 5, 32, FGET("v16", "v18", 52936) LC, 48, 21, (WL(232, 1024), WL(236, 2048), LGK(4) FCK(0) TSA("57"), WL(240, 3072) WA), LJ(96, s73))
        GUS(MF16, 6, 48, "", 64, 21, (WL(244, 0), WL(24, 1024), WL(28, 2048), ""), LS(32, 0), "", LS(36, 1024), "", LS(40, 2048), "", LS(44, 3072))
        GUS(MF16, 7, 64, "", 80, 21, (NO4), WL(48, 3072) WA, "", WL(52, 0), "", WL(56, 1024), "", WL(60, 2048))
        GUS(MF16, 8, 80, "", 96, 18, (NO4), WL(64, 3072) WA, "", WL(68, 0), "", WL(72, 1024), "", WL(76, 2048))
        PR0(9) GUK_(MF16, "96", "19", CKS_(11, "32", 9, NO4))
        "v_lshrrev_b32 %[wa], 4, %[l16]\n\tv_and_b32 %[wa], 48, %[wa]\n\t"
        GUK(MF16, "100", "20",
            WL(80, 3072) WA PKADD("32", "34") PKADD("36", "38") PKADD("40", "42") PKADD("44", "46"),
            WL(84, 0) PKADD("32", "36") PKADD("40", "44"),
            WL(88, 1024) SW32("32", "40") SW32("33", "41") PKADD("32", "40") "v_mov_b64 v[34:35], v[32:33]\n\t",
            WL(92, 2048) SW16("32", "34") SW16("33", "35") PKADD("32", "34") "v_mul_f32 v16, 0xbacccccd, v32\n\t",
            "v_mul_f32 v33, 0x3acccccd, v33\n\tv_fma_f32 v33, -v16, v16, v33\n\tv_max_f32 v33, 0, v33\n\tv_add_f32 v33, 0x3727c5ac, v33\n\t",
            "v_rcp_f32 v18, v33\n\tv_mov_b32 v20, v16\n\tv_mov_b32 v22, v18\n\t",
            SW32("16", "20") SW32("18", "22") "v_mov_b32 v17, v16\n\tv_mov_b32 v21, v20\n\tv_mov_b32 v19, v18\n\tv_mov_b32 v23, v22\n\t")
        TSA("12") WEND EP2(0, 1, "140", 5) EP2(2, 3, "156", 3) EP2(4, 5, "172", 1) LGK(0) EPN(6, "188") "s_nop 1\n\t" SW32("188", "189")
        "buffer_store_dwordx2 v[188:189], %[f1], s[40:43], s53 offen sc1\n"
        TSA("22") "s_bitcmp0_b32 %[fw], 6\n\ts_cbranch_scc1 70f\n\ts_setprio " STR(BQ) "\n6:\n\ts_barrier\n\t" SLP(SF) TSA("23") LDF("112", "s44") LDF("140", "s45")
        "72:\n\t" DC(7, "112", 13) TSA("13") TSS("50")
        DTL("112", "s46") DC(7, "140", 10) DW("s48") DTL("140", "s47")
        "s_nop 5\n\t" DW("s49") "71:\n\t" DC(7, "112", 11) TSA("56") DTE("112") DC(0, "140", 12) DW("s50") DTE("140") "s_nop 6\n\t" DW("s51")
        "s_waitcnt lgkmcnt(0)\n\ts_barrier\n\ts_branch 9f\n"
        "2:\n\ts_mov_b64 exec, vcc\n\t" SLP(RH) LD0 "s_mov_b64 exec, -1\n\t" SPIN1 "s_branch 1b\n"
        FWT(0, "v18", 52936)
        "70:\n\tv_mov_b32 v17, %[fw]\n1:\n\ts_sleep 2\n\t" FGET("v16", "v17", 328) LGK(0) FTST("v16", "s58", "1b") SLP(SE) TSA("24") LDF("112", "s44") LDF("140", "s45")
        "s_barrier\n\t" TSA("23") DC(7, "112", 14) TSA("13") TSS("50") DTE("112") DC(0, "140", 15) DW("s48")
        DT("140", LF0("112", "s46"), LF1("112", "s46"), LF2("112", "s46"), LF3("112", "s46"), LF4("112", "s46"), LF5("112", "s46"), LF6("112", "s46")) LDF("140", "s47") DW("s49") TSA("59")
        "s_branch 71b\n"
        REDO(14, LDF("112", "s44"), CK28("112")) REDO(15, LDF("140", "s45"), CK28("140"))
        REDO(0, LDH(48, s65), CK16("48")) REDO(1, LDH(64, s66), CK16("64")) REDO(2, LDH(80, s67), CK16("80")) REDO(3, LDH(96, s68), CK16("96")) REDO(4, LDH(32, s69), CK16("32"))
        REDO(5, LDH(48, s70), CK16("48")) REDO(6, LDH(64, s71), CK16("64")) REDO(7, LDH(80, s72), CK16("80")) REDO(8, LDH(96, s73), CK16("96")) REDO(9, LDST, CK16("32"))
        REDO(10, LDF("140", "s45"), CK28("140")) REDO(11, LDF("112", "s46"), CK28("112")) REDO(12, LDF("140", "s47"), CK28("140"))
        REDO(13, LDF("112", "s44"), CK28("112"))
        "7:\n\ts_setprio 0\n\t" LGK(0) W15 WL(48, 3072) WA WL(52, 0) WL(56, 1024) WL(60, 2048) WL(64, 3072) WA WL(68, 0) WL(72, 1024) WL(76, 2048) WL(80, 3072) WA WL(84, 0) WL(88, 1024) WL(92, 2048) WEND
        "1:\n\t" SLP(R7) LF0("112", P7) SPIN1 VM(0) CK4("112") "s_cbranch_scc1 1b\n\ts_branch 6b\n9:"
        : "+{v[176:207]}"(da), "+{v[208:239]}"(db), "+{v[240:247]}"(dc), "+{s[52:55]}"(sb), "+{s64}"(h0), [wa] "+&v"(wa), [t] "=&v"(tmp), [lb] "+&v"(lb) PFIO
        : "{s[40:43]}"(rws), "{s[44:51]}"(sa), [l16] "v"(l16), [z4] "v"(w < 6 ? l16 + 4096 : OOB),
          [f0] "v"(fv + (g & 1) * 7936), [f1] "v"(ntg < 30 ? fv + (g & 1) * 8 : OOB), [ng] "s"(ntg), [hb] "s"(hb), [fw] "s"(L_FL + w * 16)
        : "memory", "scc", "vcc", "s58", "s59", "s60", "s61", "s65", "s66", "s67", "s68", "s69", "s70", "s71", "s72", "s73", "v16", "v17", "v18", "v19", "v20", "v21", "v22", "v23",
          "v24", "v25", "v26", "v27", "v28", "v29", "v30", "v31", VCL32, VCL48, VCL64, VCL80, VCL96, VCL112, VCL128, VCL140, VCL160 PFCL);
}
DI f32x4 sum8(unsigned a) {
    f32x4 v[8];
#pragma unroll
    for (int i = 0; i < 8; ++i) v[i] = ldsr<f32x4>(a + i * 5120);
    return ((v[0] + v[1]) + (v[2] + v[3])) + ((v[4] + v[5]) + (v[6] + v[7]));
}
DI u32x2 addres(f32x4 a, unsigned lo, unsigned hi) { return u32x2{pk2(a[0] + bflo(lo), a[1] + bfhi(lo)), pk2(a[2] + bflo(hi), a[3] + bfhi(hi))}; }

#define ROT 0
__global__ void __launch_bounds__(512, 2) block_kernel(Params) {
    u32x16 ka;
    asm volatile("s_load_dwordx16 %0, %1, 0" : "=s"(ka) : "s"(__builtin_amdgcn_kernarg_segment_ptr()) : "memory");
    __shared__ __attribute__((aligned(16))) char lds[163840];
    asm volatile("" :: "v"((LAS char*)lds) : "memory");
    const int lane = threadIdx.x & 63, w = __builtin_amdgcn_readfirstlane(threadIdx.x >> 6), b = blockIdx.x;
    unsigned pre;
    asm volatile("s_cmp_lg_u32 %1, %2\n\ts_cbranch_scc1 1f\n\ts_getpc_b64 s[4:5]\n.Lpc:\n\tv_lshlrev_b32 %0, 7, %3\n\tv_min_u32 %0, .Lend-.Lpc-4, %0\n\t"
                 "global_load_dword %0, %0, s[4:5]\n1:" : "=&{v200}"(pre) : "s"(w), "s"((b >> 3) & 7), "v"(threadIdx.x) : "memory", "scc", "s4", "s5");
    Params p;
    rsrc_t rws;
    const int xcd = b & 7, i = b >> 3;
    Retile rt;
    u32x2 tag;
    unsigned fq;
    {
        const int mt = (xcd >> 1) * 4 + (i & 3), nt = (xcd & 1) * 8 + (i >> 2), rot = ROT;
        QkvSrc c; QkvAdr e; u32x4 row; unsigned rowt, T;
        qkv_start(c, row, rowt, T, rt.t, p, ka, b, mt, nt, rot, w, lane);
        rws = mkrs(p.ws, WS_END);
        TSW(60);
        retile_setup(rt, p, b, lane);
        qkv_setup(e, mt, nt, w, lane);
        fq = lane == 0 ? WS_FQ + (nt * 16 + mt) * 128 : OOB;
        TSW(61);
        asm volatile("s_add_u32 m0, %[dm], 65536\n\ts_cmp_eq_u32 %[nt], 15\n\ts_cbranch_scc1 1f\n\t" QD4 "1:\n\ts_waitcnt vmcnt(12) lgkmcnt(0)\n\ts_add_u32 %0, %0, 1"
                     : "+s"(T), "+{s[60:75]}"(rt.t), "+v"(row), "+v"(rowt), "+v"(fq), "+{v200}"(pre), [sx] "+s"(c.sx), [sw] "+s"(c.sw)
                     : [x0] "v"(c.xo[0]), [x1] "v"(c.xo[1]), [w0] "v"(c.wo[0]), [w1] "v"(c.wo[1]), [rx] "s"(c.rx), [rw] "s"(c.rw), [dm] "s"(w * 4096), [nt] "s"(nt) : "memory", "scc");
        TSW(62);
        const u32x4 gr = row_stats(row, rowt, T);
        tag = u32x2{gr[2], gr[3]};
        TSW(63);
        if (nt == 15) {
            st16(rws, e.go, 0, gr);
            retile_issue(rt, w);
            asm volatile("s_waitcnt vmcnt(0)" ::: "memory");
        } else if (nt >= 10) qkv_body<true>(c, e, rt, rws, gr, tag, w PROFPASS);
        else qkv_body<false>(c, e, rt, rws, gr, tag, w PROFPASS);
    }
    const int mt = (xcd >> 1) * 8 + (i & 7), nt = (xcd & 1) * 4 + (i >> 3);
    const int qg = (xcd & 6) + ((i >> 2) & 1), ntg = (xcd & 1) * 16 + (i & 3) * 4 + (i >> 3);
    const int tg = w >> 1, kh = w & 1;
    auto rv0 = [&](unsigned l) { return (l & 15) * 1280 + (nt & 1) * 32 + kh * 64 + (l >> 4) * 16; };
    auto rv1 = [&](unsigned l) { return kh ? OOB : (l & 15) * 1280 + ((nt & 1) ? 0 : 128) + (l >> 4) * 8; };
    const unsigned rsb = (mt * 64 + tg * 16) * 1280 + nt * 160;
    u32x4 r0; u32x2 r1; Park pk;
    phase_attn(p, rt, rws, tag, fq, b, w, lane, nt, ntg, rv0(lane), rv1(lane), rsb, r0, r1, pk PROFPASS);
    unsigned ln;
    asm volatile("v_mbcnt_lo_u32_b32 %0, -1, 0\n\tv_mbcnt_hi_u32_b32 %0, -1, %0" : "=v"(ln) : "{v154}"(pk.d));
    phase_wo(rws, w, ln, mt, nt, ntg, r0, r1, pk PROFPASS);
    PIN(ln);
    unsigned o0 = rv0(ln), o1 = rv1(ln), a = tg * 40960 + ln * 16;
    PIN(o0); PIN(o1); PIN(a);
    phase_ffn(rws, w, ln, mt, nt, qg, ntg, pk PROFPASS);
    TS(14);
    {
        const rsrc_t ro = mkrs(p.out, 2048 * 1280);
        const u32x2 u = addres(sum8(a + kh * 2048), r0[0], r0[1]), v = addres(sum8(a + kh * 2048 + 1024), r0[2], r0[3]);
        st16(ro, o0, rsb, u32x4{u[0], u[1], v[0], v[1]});
        if (kh == 0) st8(ro, o1, rsb, addres(sum8(a + 4096), r1[0], r1[1]));
    }
    asm volatile(".Lend:" ::: "memory");
}

extern "C" void kernel_launch(void* const* d_in, const int* in_sizes, int n_in, void* d_out, int out_size, void* d_ws, size_t ws_size, hipStream_t stream) {
    Params p;
    p.x = (const char*)d_in[0]; p.wqkv = (const char*)d_in[1]; p.wo = (const char*)d_in[2];
    p.wg = (const char*)d_in[4]; p.wu = (const char*)d_in[5]; p.wd = (const char*)d_in[6];
    p.out = (char*)d_out; p.ws = (char*)d_ws + ((ws_size - WS_END) & ~(size_t)2097151);
    block_kernel<<<256, 512, 0, stream>>>(p);
}
```

```cpp
#include <hip/hip_runtime.h>
#include <hip/hip_bf16.h>
#include <stdint.h>

#define DI __device__ __forceinline__
typedef __attribute__((ext_vector_type(2))) __bf16 bf2v;
typedef __attribute__((ext_vector_type(8))) __bf16 bf8v;
typedef __attribute__((ext_vector_type(16))) __bf16 bf16v;
typedef __attribute__((ext_vector_type(4))) short s4v;
typedef __attribute__((ext_vector_type(2))) float f32x2;
typedef __attribute__((ext_vector_type(4))) float f32x4;
typedef __attribute__((ext_vector_type(8))) float f32x8;
typedef __attribute__((ext_vector_type(16))) float f32x16;
typedef __attribute__((ext_vector_type(32))) float f32x32;
typedef __attribute__((ext_vector_type(2))) unsigned u32x2;
typedef __attribute__((ext_vector_type(4))) unsigned u32x4;
typedef __attribute__((ext_vector_type(8))) unsigned u32x8;
typedef __attribute__((ext_vector_type(16))) unsigned u32x16;
typedef __attribute__((ext_vector_type(32))) unsigned u32x32;
typedef __amdgpu_buffer_rsrc_t rsrc_t;
#define LAS __attribute__((address_space(3)))

constexpr unsigned WS_Q = 0;
constexpr unsigned WS_KV = WS_Q + 32 * 81920;
constexpr unsigned WS_O = WS_KV + 32 * 163840;
constexpr unsigned WS_B = WS_O + 128 * 20480;
constexpr unsigned QBS = 32 * 40960, OFF_HS = 16 * 20480, OFF_F = OFF_HS + 16 * 2048;
constexpr unsigned WS_WO = WS_B + 8 * QBS;
constexpr unsigned WS_WD = WS_WO + 8 * 102400;
constexpr unsigned WS_WGU = WS_WD + 8 * 276480;
constexpr unsigned WS_CS = WS_WGU + 32 * 143360;
constexpr unsigned WS_FQ = WS_CS + 5 * 3584 * 4;
constexpr unsigned WS_FR = WS_FQ + 256 * 128;
constexpr unsigned WS_XS = WS_FR + 2048;
constexpr unsigned WS_E = WS_XS + 2048 * 16;
constexpr unsigned WS_END = WS_E + 128;
constexpr unsigned OOB = 0x40000000u;
constexpr int SPIN = 1 << 14;

struct Params {
    const char* x; const char* wqkv; const char* wo; const char* wg; const char* wu; const char* wd;
    char* out; char* ws;
};

#define SBAR() __builtin_amdgcn_sched_barrier(0)
#define PIN(x) asm volatile("" : "+v"(x))
#define WAITBAR(N) asm volatile("s_waitcnt vmcnt(" #N ") lgkmcnt(0)\n\ts_barrier" ::: "memory")
#define VBAR(N) asm volatile("s_waitcnt vmcnt(" #N ")\n\ts_barrier" ::: "memory")
#define LBAR() asm volatile("s_waitcnt lgkmcnt(0)\n\ts_barrier" ::: "memory")
#define BAR() asm volatile("s_barrier" ::: "memory")

DI rsrc_t mkrs(const char* p, unsigned bytes) { return __builtin_amdgcn_make_buffer_rsrc((void*)p, (short)0, (int)bytes, 0x00020000); }
template <class T> DI T ldsr(unsigned a) { return *(const LAS T*)(uintptr_t)a; }
template <class T> DI void ldsw(unsigned a, T v) { *(LAS T*)(uintptr_t)a = v; }
DI s4v ldstr(unsigned a) { return __builtin_amdgcn_ds_read_tr16_b64_v4i16((LAS s4v*)(uintptr_t)a); }
DI bf8v ldstr2(unsigned a, unsigned d) {
    s4v lo = ldstr(a), hi = ldstr(a + d);
    return __builtin_bit_cast(bf8v, __builtin_shufflevector(lo, hi, 0, 1, 2, 3, 4, 5, 6, 7));
}
DI f32x16 mfma32(bf8v a, bf8v b, f32x16 c) { return __builtin_amdgcn_mfma_f32_32x32x16_bf16(a, b, c, 0, 0, 0); }
DI f32x4 mfma16(bf8v a, bf8v b, f32x4 c) { return __builtin_amdgcn_mfma_f32_16x16x32_bf16(a, b, c, 0, 0, 0); }
DI bf8v asbf(u32x4 v) { return __builtin_bit_cast(bf8v, v); }
DI unsigned pk2(float lo, float hi) { f32x2 v = {lo, hi}; return __builtin_bit_cast(unsigned, __builtin_convertvector(v, bf2v)); }
DI float bflo(unsigned u) { return __uint_as_float(u << 16); }
DI float bfhi(unsigned u) { return __uint_as_float(u & 0xffff0000u); }
DI f32x2 swap32(float a, float b) { auto r = __builtin_amdgcn_permlane32_swap(__float_as_uint(a), __float_as_uint(b), false, false); return f32x2{__uint_as_float(r[0]), __uint_as_float(r[1])}; }
DI float sum16(float a) { auto r = __builtin_amdgcn_permlane16_swap(__float_as_uint(a), __float_as_uint(a), false, false); return __uint_as_float(r[0]) + __uint_as_float(r[1]); }
DI bool anyl(bool c) { return __builtin_amdgcn_ballot_w64(c) != 0; }
DI void st16(rsrc_t r, unsigned voff, unsigned soff, u32x4 v) { asm volatile("buffer_store_dwordx4 %0, %1, %2, %3 offen sc1\n\ts_nop 1" :: "v"(v), "v"(voff), "s"(r), "s"(soff) : "memory"); }
DI void st8(rsrc_t r, unsigned voff, unsigned soff, u32x2 v) { asm volatile("buffer_store_dwordx2 %0, %1, %2, %3 offen sc1" :: "v"(v), "v"(voff), "s"(r), "s"(soff) : "memory"); }
DI void st4(rsrc_t r, unsigned voff, unsigned soff, unsigned v) { asm volatile("buffer_store_dword %0, %1, %2, %3 offen sc1" :: "v"(v), "v"(voff), "s"(r), "s"(soff) : "memory"); }
DI u32x4 ld16c(rsrc_t r, unsigned voff, unsigned soff) { return __builtin_amdgcn_raw_buffer_load_b128(r, voff, soff, 16); }
DI u32x2 ld8c(rsrc_t r, unsigned voff, unsigned soff) { return __builtin_amdgcn_raw_buffer_load_b64(r, voff, soff, 16); }
DI void dma1(unsigned m0v, unsigned voff, rsrc_t r, unsigned soff) {
    asm volatile("s_mov_b32 m0, %0\n\ts_nop 0\n\tbuffer_load_dwordx4 %1, %2, %3 offen lds" :: "s"(m0v), "v"(voff), "s"(r), "s"(soff) : "memory"); }
DI void dma1c(unsigned m0v, unsigned voff, rsrc_t r, unsigned soff) {
    asm volatile("s_mov_b32 m0, %0\n\ts_nop 0\n\tbuffer_load_dwordx4 %1, %2, %3 offen sc1 lds" :: "s"(m0v), "v"(voff), "s"(r), "s"(soff) : "memory"); }
DI void dma3c(unsigned m0v, unsigned voff, rsrc_t r, unsigned soff) {
    asm volatile("s_mov_b32 m0, %0\n\ts_nop 0\n\tbuffer_load_dwordx4 %1, %2, %3 offen sc1 lds\n\tbuffer_load_dwordx4 %1, %2, %3 offen offset:1024 sc1 lds\n\t"
                 "buffer_load_dwordx4 %1, %2, %3 offen offset:2048 sc1 lds" :: "s"(m0v), "v"(voff), "s"(r), "s"(soff) : "memory"); }
DI void dma4c(unsigned m0v, unsigned voff, rsrc_t r, unsigned soff) {
    asm volatile("s_mov_b32 m0, %0\n\ts_nop 0\n\tbuffer_load_dwordx4 %1, %2, %3 offen sc1 lds\n\tbuffer_load_dwordx4 %1, %2, %3 offen offset:1024 sc1 lds\n\t"
                 "buffer_load_dwordx4 %1, %2, %3 offen offset:2048 sc1 lds\n\tbuffer_load_dwordx4 %1, %2, %3 offen offset:3072 sc1 lds" :: "s"(m0v), "v"(voff), "s"(r), "s"(soff) : "memory"); }
#define SPINS "0xfffff"
#define PINS(x) asm volatile("" : "+s"(x))
#define CHK(B, TMP, DST) \
    "v_max3_u32 v" #TMP ", v[" #B "], v[" #B "+1], v[" #B "+2]\n\tv_max3_u32 v" #TMP ", v[" #B "+3], v[" #B "+4], v" #TMP "\n\t" \
    "v_max3_u32 v" #TMP ", v[" #B "+5], v[" #B "+6], v" #TMP "\n\tv_max3_u32 v" #TMP ", v[" #B "+7], v[" #B "+8], v" #TMP "\n\t" \
    "v_max3_u32 v" #TMP ", v[" #B "+9], v[" #B "+10], v" #TMP "\n\tv_max3_u32 v" #TMP ", v[" #B "+11], v[" #B "+12], v" #TMP "\n\t" \
    "v_max3_u32 v" #TMP ", v[" #B "+13], v[" #B "+14], v" #TMP "\n\tv_max_u32 v" #TMP ", v[" #B "+15], v" #TMP "\n\t" \
    "v_cmp_eq_u32 " DST ", -1, v" #TMP "\n\t"
#define LD1(R, V, S, O) "buffer_load_dwordx4 v[" R ":" R "+3], " V ", s[40:43], " S " offen offset:" O " sc1\n\t"
#define DM1(V, S, O) "buffer_load_dwordx4 " V ", s[40:43], " S " offen offset:" #O " sc1 lds\n\t"
#define DM3(V, S) DM1(V, S, 0) DM1(V, S, 1024) DM1(V, S, 2048)
#define DM4(V, S) DM3(V, S) DM1(V, S, 3072)
#define Z2(R) "v_mov_b64 v[" R ":" R "+1], 0\n\t"
#define Z4(R) Z2(R) Z2(R "+2")
#define Z8(R) Z4(R) Z4(R "+4")

#define VCL32 "v32", "v33", "v34", "v35", "v36", "v37", "v38", "v39", "v40", "v41", "v42", "v43", "v44", "v45", "v46", "v47"
#define VCL48 "v48", "v49", "v50", "v51", "v52", "v53", "v54", "v55", "v56", "v57", "v58", "v59", "v60", "v61", "v62", "v63"
#define VCL64 "v64", "v65", "v66", "v67", "v68", "v69", "v70", "v71", "v72", "v73", "v74", "v75", "v76", "v77", "v78", "v79"
#define VCL80 "v80", "v81", "v82", "v83", "v84", "v85", "v86", "v87", "v88", "v89", "v90", "v91", "v92", "v93", "v94", "v95"
#define VCL96 "v96", "v97", "v98", "v99", "v100", "v101", "v102", "v103", "v104", "v105", "v106", "v107", "v108", "v109", "v110", "v111"
#define VCL112 "v112", "v113", "v114", "v115", "v116", "v117", "v118", "v119", "v120", "v121", "v122", "v123", "v124", "v125", "v126", "v127"
#define VCL128 "v128", "v129", "v130", "v131", "v132", "v133", "v134", "v135", "v136", "v137", "v138", "v139"
#define VCL140 "v140", "v141", "v142", "v143", "v144", "v145", "v146", "v147", "v148", "v149", "v150", "v151", "v152", "v153", "v154", "v155", "v156", "v157", "v158", "v159"
#define VCL160 "v160", "v161", "v162", "v163", "v164", "v165", "v166", "v167", "v168", "v169", "v170", "v171", "v172", "v173", "v174", "v175"
#define VCL112L "v112", "v113", "v114", "v115", "v116", "v117", "v118", "v119"

constexpr unsigned L_RT = 0, L_SM = 32768, L_CP = 49152;
struct RtTab { unsigned v[2048][16]; };
constexpr RtTab make_rt() {
    RtTab t{};
    for (unsigned b = 0; b < 256; ++b) for (unsigned w = 0; w < 8; ++w) for (unsigned f = 0; f < 4; ++f) {
        unsigned A = 0, M = 0, SO = 0, R0 = 0, R1 = 0;
        if (b < 135) {
            const unsigned fg = b / 5, ks = (b % 5) * 4 + (w >> 1), j = ks >> 1, o = fg * 8 + (w & 1) * 4 + f;
            A = (320 * (ks & 1) + (j < 8 ? (j & 1) * 32 + (j >> 1) * 8 : 8 * j) + (f & 1) * 160) * 3456 + fg * 128 + (w & 1) * 64;
            M = (j < 8 ? 96 : 16) * 3456;
            SO = WS_WGU + (o / 7) * 143360 + (ks + 20 - (o / 7 % 16 * 5 >> 3) * 2) % 20 * 7168 + (o % 7) * 1024;
            R0 = 16 * f;
        } else {
            const unsigned v = (b - 135) * 8 + w;
            const bool isWo = v < 200;
            const unsigned vv = v < 740 ? (isWo ? v : v - 200) : 0, ks = vv / 10, cu = vv % 10;
            const unsigned c = 4 * ks + f, j = c / 31, ng = c % 31;
            A = (isWo ? (ks >= 16 ? 40 * c - 2544 : 20 * (c >> 1) + 4 * (c & 1)) : 56 * ng + 16 * (j >> 1) + 4 * (j & 1)) * 1280 + cu * 128;
            M = (isWo ? (ks >= 16 ? 20 : 8) : (j < 6 ? 8 : 4)) * 1280;
            const unsigned seg = 2 * cu + (f >> 1), a = seg / 5, sm = seg % 5, fi = f & 1;
            const unsigned tile = 2 * a + (sm == 2 ? fi : sm > 2), blk = sm == 2 ? 4 : (sm == 1 || sm == 4) ? 2 + fi : fi;
            SO = isWo ? WS_WO + (tile * 100 + (ks >= 10 ? (ks - 10) * 10 + 5 + (blk < 4 ? blk ^ 2 : 4) : ks * 10 + blk)) * 1024 : WS_WD + (tile * 270 + ks * 5 + blk) * 1024;
            R0 = (f >> 1) * 64 + (sm == 2 ? 32 : 8) * fi; R1 = sm == 2 ? 8 : 16;
        }
        unsigned* r = t.v[b * 8 + w];
        r[f] = A + 3072 - 1024 * f; r[4 + f] = M; r[8 + f] = SO; r[12 + f] = (L_RT + w * 4096 + R0) | R1 << 16;
    }
    return t;
}
static __device__ const __attribute__((aligned(64))) RtTab RT_TAB = make_rt();
struct Retile { rsrc_t ra, rb; u32x16 t; unsigned vlo, hi; };
DI void retile_setup(Retile& r, const Params& p, unsigned b, unsigned lane) {
    const bool gu = b < 135;
    const char *pa, *pb; unsigned n;
    asm("s_cmpk_lt_u32 %3, 160\n\ts_cselect_b64 %0, %4, %5\n\ts_cselect_b32 %2, %9, %8\n\ts_cmpk_lt_u32 %3, 135\n\ts_cselect_b64 %1, %7, %0\n\ts_cselect_b64 %0, %6, %0\n\ts_cselect_b32 %2, %8, %2"
        : "=&s"(pa), "=&s"(pb), "=&s"(n) : "s"(b), "s"(p.wo), "s"(p.wd), "s"(p.wg), "s"(p.wu), "s"(640 * 3456 + 3072), "n"(640 * 1280 + 3072) : "scc");
    r.ra = mkrs(pa - 3072, n); r.rb = mkrs(pb - 3072, n);
    r.vlo = gu ? ((lane >> 2) & 7) * 3456 + (lane & 3) * 16 : ((lane >> 3) & 3) * 1280 + (lane & 7) * 16;
    r.hi = lane >> 5;
    PIN(r.vlo); PIN(r.hi); PINS(r.ra); PINS(r.rb);
}
DI void retile_issue(const Retile& r, int w) {
    asm volatile("s_mov_b32 m0, %0\n\ts_nop 0\n\tbuffer_load_dwordx4 %1, %5, 0 offen lds\n\tbuffer_load_dwordx4 %2, %5, 0 offen offset:1024 lds\n\t"
                 "buffer_load_dwordx4 %3, %6, 0 offen offset:2048 lds\n\tbuffer_load_dwordx4 %4, %6, 0 offen offset:3072 lds"
                 :: "s"(L_RT + w * 4096), "v"(r.vlo + r.t[0] + r.hi * r.t[4]), "v"(r.vlo + r.t[1] + r.hi * r.t[5]), "v"(r.vlo + r.t[2] + r.hi * r.t[6]),
                    "v"(r.vlo + r.t[3] + r.hi * r.t[7]), "s"(r.ra), "s"(r.rb) : "memory");
}
DI u32x32 retile_read(const Retile& r, unsigned b, int w, unsigned lane) {
    const u32x4 one = {0x3f803f80u, 0x3f803f80u, 0x3f803f80u, 0x3f803f80u};
    const unsigned f = lane & 3, row = 8 * (lane >> 4) + ((lane >> 2) & 3), dl = b < 135 ? 256 : 512;
    const unsigned rb = b < 135 ? (f >> 1) * 2048 + row * 64 + (f & 1) * 8 : row * 128;
    u32x32 o; f32x4 cp[4];
#pragma unroll
    for (int i = 0; i < 4; ++i) {
        const bf8v fr = ldstr2(rb + (r.t[12 + i] & 0xffff) + (r.t[12 + i] >> 16) * f, dl);
        cp[i] = mfma16(fr, asbf(one), f32x4{});
#pragma unroll
        for (int k = 0; k < 4; ++k) o[4 * i + k] = __builtin_bit_cast(u32x4, fr)[k];
    }
    if ((lane & 15) == 0) {
#pragma unroll
        for (int i = 0; i < 4; ++i) ldsw<f32x4>(L_CP + w * 256 + i * 64 + lane, cp[i]);
    }
    return o;
}

struct QkvSrc { rsrc_t rx, rw; unsigned xo[2], wo[2], sx, sw; };
#define QDX(V, O) "buffer_load_dwordx4 %[" #V "], %[rx], %[sx] offen offset:" #O " lds\n\t"
#define QDW(V, O) "buffer_load_dwordx4 %[" #V "], %[rw], %[sw] offen offset:" #O " lds\n\t"
#define QADV "s_add_u32 %[sx], %[sx], 128\n\ts_cmp_eq_u32 %[sx], 1280\n\ts_cselect_b32 %[sx], 0, %[sx]\n\ts_mul_i32 %[sw], %[sx], 1920\n\t"
#define QD4 QDX(x0, 0) QDX(x1, 1024) QDW(w0, 2048) QDW(w1, 3072) QADV "s_add_u32 m0, m0, 32768\n\t"
DI void qkv_start(QkvSrc& c, u32x4& row, unsigned& rowt, unsigned& T, u32x16& tab, Params& p, u32x16 ka, int b, int mt, int nt, int w, int lane) {
    const unsigned rr = lane >> 3, isv = nt >= 10;
    const unsigned tok = mt * 128 + (w >> 1) * 32 + ((w & 1) << (2 * isv)) + (rr & 4) * 2 + ((rr & 3) << (1 - isv));
    c.xo[0] = tok * 1280 + (((lane & 7) ^ (rr >> 1)) << 4);
    c.xo[1] = (c.xo[0] ^ 64) + 16 * 1280 - 1024;
    c.wo[0] = (8 * w + ((lane >> 2) & 3)) * 3840 + nt * 256 + (lane >> 4) * 64 + (((lane & 3) ^ ((w & 1) << 1)) << 4) + 1024;
    c.wo[1] = c.wo[0] + 4 * 3840 - 1024;
    c.sx = 0; c.sw = 0;
    unsigned ro = (mt * 128 + nt * 8 + w) * 1280;
    asm volatile("s_waitcnt lgkmcnt(0)\n\ts_load_dwordx16 %5, %6, %7" : "+s"(ka), "+v"(c.xo[1]), "+v"(c.wo[1]), "+s"(c.sw), "+s"(ro), "=&{s[60:75]}"(tab) : "s"(&RT_TAB), "s"((b * 8 + w) * 64) : "memory");
    p = __builtin_bit_cast(Params, ka);
    asm volatile("s_load_dword %0, %1, %2" : "=&s"(T) : "s"(p.ws), "s"(WS_E) : "memory");
    c.rx = mkrs(p.x, 2048 * 1280); c.rw = mkrs(p.wqkv - 3072, 640 * 3840 + 3072);
    asm volatile("s_mov_b32 m0, %[dm]\n\tbuffer_load_dwordx4 %[r4], %[l16], %[rx], %[ro] offen\n\tbuffer_load_dword %[r1], %[l4], %[rx], %[ro] offen offset:1024\n\t"
                 "s_cmp_eq_u32 %[nt], 15\n\ts_cbranch_scc1 1f\n\t" QD4 "s_barrier\n\t" QD4 "s_branch 2f\n1:\n\ts_waitcnt vmcnt(0)\n2:"
                 : [r4] "=&v"(row), [r1] "=&v"(rowt), [sx] "+&s"(c.sx), [sw] "+&s"(c.sw)
                 : [x0] "v"(c.xo[0]), [x1] "v"(c.xo[1]), [w0] "v"(c.wo[0]), [w1] "v"(c.wo[1]), [rx] "s"(c.rx), [rw] "s"(c.rw), [dm] "s"(w * 4096),
                   [l16] "v"(lane * 16), [l4] "v"(lane * 4), [ro] "s"(ro), [nt] "s"(nt) : "memory", "scc");
}
template <int C> DI float rorsum(float v) { return v + __uint_as_float(__builtin_amdgcn_update_dpp(0u, __float_as_uint(v), 0x120 + C, 0xf, 0xf, true)); }
DI u32x4 row_stats(u32x4 row, unsigned rowt, unsigned T) {
    float s = 0.f, q = 0.f;
#pragma unroll
    for (int i = 0; i < 5; ++i) {
        const bf2v v = __builtin_bit_cast(bf2v, i < 4 ? row[i & 3] : rowt);
        s = __builtin_amdgcn_fdot2_f32_bf16(v, __builtin_bit_cast(bf2v, 0x3f803f80u), s, false);
        q = __builtin_amdgcn_fdot2_f32_bf16(v, v, q, false);
    }
    const f32x2 y = swap32(s, q);
    const float z = rorsum<1>(rorsum<2>(rorsum<4>(rorsum<8>(sum16(y[0] + y[1]))))) * (1.f / 640.f);
    const f32x2 v = swap32(z, z);
    const float ra = __builtin_amdgcn_rsqf(__builtin_fmaxf(v[1] - v[0] * v[0], 0.f) + 1e-5f);
    return u32x4{__float_as_uint(ra), __float_as_uint(-v[0] * ra), T, ~T};
}
#define MFQ(A, W, X) "v_mfma_f32_16x16x32_bf16 v[" A ":" A "+3], v[" W ":" W "+3], v[" X ":" X "+3], v[" A ":" A "+3]\n\t"
#define MFV(A, W, X) MFQ(A, X, W)
#define QTR(R, V, O) "ds_read_b64_tr_b16 v[" R ":" R "+1], %[" #V "] offset:" O "\n\t"
#define QRX(R, V, O) "ds_read_b128 v[" R ":" R "+3], %[" #V "] offset:" O "\n\t"
#define LGK(N) "s_waitcnt lgkmcnt(" #N ")\n\t"
#define QSEC(MF, P, Q, XA, WO, D1, D2, E1, E2) \
    "s_cmp_lt_u32 %[n], 8\n\t" LGK(4) MF("64", P, "68") QTR(Q, wa0, WO) MF("32", P, P "+8") QTR(Q "+2", wa0, WO "+1024") \
    "s_cbranch_scc0 1f\n\t" D1 "1:\n\t" MF("36", P, P "+12") QRX(Q "+8", XA, "0") LGK(5) MF("48", P "+4", P "+8") QRX(Q "+12", XA, "4096") \
    MF("52", P "+4", P "+12") QTR(Q "+4", wa1, WO) LGK(5) MF("40", P, P "+16") QTR(Q "+6", wa1, WO "+1024") \
    "s_cbranch_scc0 1f\n\t" D2 "1:\n\t" MF("56", P "+4", P "+16") QRX(Q "+16", XA, "8192") E1 MF("44", P, P "+20") QRX(Q "+20", XA, "12288") E2 MF("60", P "+4", P "+20")
#define QLDG "buffer_load_dwordx4 v[120:123], %[ga], %[rs], 0 offen sc1\n\t"
#define QADD(r) "v_add_u32 %[" #r "], %[d], %[" #r "]\n\t"
#define QRT(r, A, M) "v_mad_u32_u24 %[" #r "], %[hi], s" #M ", %[vlo]\n\tv_add_u32 %[" #r "], s" #A ", %[" #r "]\n\t"
#define QKV_ASM(MF, STRD) asm volatile( \
    "s_add_u32 m0, %[dm], 98304\n\ts_mov_b32 %[n], 0\n\tbuffer_store_dwordx4 %[gr], %[go], %[rs], 0 offen sc1\n\t" \
    Z8("32") Z8("40") Z8("48") Z8("56") Z4("64") "v_mov_b32 v68, 0x3f803f80\n\tv_mov_b32 v69, v68\n\tv_mov_b64 v[70:71], v[68:69]\n\t" \
    "s_waitcnt vmcnt(9)\n\ts_barrier\n\t" \
    QTR("72", wa0, "0") QTR("74", wa0, "1024") QRX("80", xa0, "0") QRX("84", xa0, "4096") QTR("76", wa1, "0") QTR("78", wa1, "1024") QRX("88", xa0, "8192") QRX("92", xa0, "12288") \
    QDX(x0, 0) QDX(x1, 1024) "s_branch 6f\n" \
    "3:\n\ts_cmp_gt_u32 %[n], 2\n\ts_cbranch_scc1 2f\n\ts_waitcnt vmcnt(9)\n\ts_branch 5f\n" \
    "2:\n\ts_cmp_eq_u32 %[n], 10\n\ts_cbranch_scc1 7f\n\ts_cmp_eq_u32 %[n], 7\n\ts_cbranch_scc1 2f\n\ts_waitcnt vmcnt(5)\n\ts_branch 5f\n" \
    "2:\n\t" QLDG QRT(x0, 60, 64) QRT(x1, 61, 65) QRT(w0, 62, 66) QRT(w1, 63, 67) \
    "s_mov_b64 s[44:45], s[52:53]\n\ts_mov_b32 s46, s54\n\ts_mov_b64 s[48:49], s[56:57]\n\ts_mov_b32 s50, s58\n\ts_mov_b32 %[sx], 0\n\ts_mov_b32 %[sw], 0\n" \
    "4:\n\ts_waitcnt vmcnt(8)\n5:\n\ts_barrier\n\t" \
    QSEC(MF, "96", "72", xa0, "0", QDX(x0, 0), QDX(x1, 1024), "", "") \
    "6:\n\t" \
    QSEC(MF, "72", "96", xa1, "16384", QDW(w0, 2048), QDW(w1, 3072), \
         QADV "s_bitcmp1_b32 0x42, %[n]\n\ts_cselect_b32 %[d], %[wr], %[up]\n\ts_add_u32 m0, m0, %[d]\n\ts_cmp_eq_u32 %[n], 4\n\ts_cselect_b32 %[d], %[wr], %[up]\n\ts_add_u32 %[n], %[n], 1\n\t", \
         QADD(xa0) QADD(xa1) QADD(wa0) QADD(wa1)) \
    "s_bitcmp1_b32 0x686, %[n]\n\ts_cbranch_scc0 4b\n\ts_branch 3b\n" \
    "7:\n\ts_mov_b32 %[n], " SPINS "\n\t" LGK(4) MF("64", "96", "68") MF("32", "96", "104") "s_waitcnt vmcnt(4)\n" \
    "1:\n\tv_cmp_ne_u64 vcc, v[122:123], %[tag]\n\ts_cbranch_vccz 2f\n\t" QLDG "s_waitcnt vmcnt(0)\n\ts_sub_u32 %[n], %[n], 1\n\ts_cbranch_scc0 1b\n" \
    "2:\n\t" MF("36", "96", "108") "v_mul_f32 v120, %[sc], v120\n\tv_mul_f32 v121, %[sc], v121\n\t" LGK(2) MF("48", "100", "104") \
    "ds_write2_b32 %[pa], v120, v121 offset1:64\n\t" MF("52", "100", "108") "s_nop 1\n\tds_write_b128 %[ca], v[64:67]\n\t" LGK(2) \
    MF("40", "96", "112") STRD MF("56", "100", "112") MF("44", "96", "116") MF("60", "100", "116") \
    "s_waitcnt lgkmcnt(0)\n\ts_barrier\n\ts_nop 7" \
    : "=&{v[32:63]}"(A), "=&{v[64:67]}"(cs0), "=&{v[124:155]}"(S), [rx] "+{s[44:47]}"(c.rx), [rw] "+{s[48:51]}"(c.rw), [sx] "+&s"(c.sx), [sw] "+&s"(c.sw), \
      [x0] "+&v"(c.xo[0]), [x1] "+&v"(c.xo[1]), [w0] "+&v"(c.wo[0]), [w1] "+&v"(c.wo[1]), [xa0] "+&v"(e.xa0), [xa1] "+&v"(e.xa1), [wa0] "+&v"(e.wa0), [wa1] "+&v"(e.wa1), \
      [n] "=&s"(n), [d] "=&s"(d) \
    : "{s[52:55]}"(rt.ra), "{s[56:59]}"(rt.rb), [rs] "s"(rws), [dm] "s"(w * 4096), [wr] "s"(-131072), [up] "s"(32768), [sc] "s"(e.sc), [gr] "v"(gr), [go] "v"(e.go), [ga] "v"(e.ga), \
      "{s[60:75]}"(rt.t), [vlo] "v"(rt.vlo), [hi] "v"(rt.hi), [tag] "v"(tag), [pa] "v"(e.pa), [pr] "v"(e.pr), [ca] "v"(e.ca) \
    : "memory", "scc", "vcc", "v68", "v69", "v70", "v71", "v72", "v73", "v74", "v75", "v76", "v77", "v78", "v79", VCL80, VCL96, VCL112L, "v120", "v121", "v122", "v123")
#define QS2(R, O) "ds_read2_b32 v[" #R ":" #R "+1], %[pr] offset0:" #O " offset1:" #O "\n\tds_read2_b32 v[" #R "+2:" #R "+3], %[pr] offset0:" #O "+64 offset1:" #O "+64\n\t"
struct QkvAdr { unsigned xa0, xa1, wa0, wa1, go, ga, pa, pr, ca, so[2], sb, sc; };
DI void qkv_setup(QkvAdr& e, int mt, int nt, int w, int lane) {
    const unsigned t = lane & 15, g = lane >> 4, T = w & 1, cb = w >> 1, g64 = 2 * mt + T;
    const bool isv = nt >= 10, isq = nt < 5;
    e.xa0 = 4 * T * 4096 + t * 128 + ((g ^ (t >> 1)) << 4); e.xa1 = e.xa0 ^ 64;
    e.wa0 = g * 4096 + 2048 + cb * 256 + ((lane >> 2) & 3) * 64 + ((g ^ T) & 1) * 32 + (lane & 3) * 8; e.wa1 = e.wa0 ^ 32;
    e.go = lane == 0 ? WS_XS + (mt * 128 + nt * 8 + w) * 16 : OOB;
    e.ga = WS_XS + (g64 * 64 + (g >> 1) * 32 + (isv ? 8 * (t >> 2) + 4 * (g & 1) + (t & 3) : 2 * t + (g & 1))) * 16;
    e.pa = L_SM + w * 2048 + lane * 4; e.pr = L_SM + w * 2048 + (isv ? g * 16 : t * 4); e.ca = L_SM + w * 2048 + 1024 + lane * 16;
    e.sc = isq ? __builtin_bit_cast(unsigned, 0.22360679774997896f * 1.4426950408889634f) : 0x3f800000u;
    e.sb = isq ? WS_Q + g64 * 2560 : WS_KV + g64 * 5120 + (isv ? 2560 : 0);
#pragma unroll
    for (int n = 0; n < 2; ++n) {
        if (isv) {
            const unsigned col = (nt - 10) * 128 + cb * 32 + 16 * (n ^ T) + t, head = (col * 3277) >> 16;
            e.so[n] = head * (163840 - 320) + g * 320 + col * 16;
        } else {
            const unsigned G = (isq ? nt : nt - 5) * 32 + cb * 8 + 4 * (n ^ T) + g, head = (G * 205) >> 10;
            e.so[n] = head * (isq ? 81920 - 2560 : 163840 - 2560) + G * 512 + t * 16;
        }
        PIN(e.so[n]);
    }
    PIN(e.xa0); PIN(e.xa1); PIN(e.wa0); PIN(e.wa1); PIN(e.go); PIN(e.ga); PIN(e.pa); PIN(e.pr); PIN(e.ca); PINS(e.sb); PINS(e.sc);
}
template <bool ISV>
DI void qkv_body(QkvSrc& c, QkvAdr& e, const Retile& rt, rsrc_t rws, u32x4 gr, u32x2 tag, int w) {
    f32x32 A, S; f32x4 cs0; unsigned n, d;
    if (ISV) QKV_ASM(MFV, QRX("124", pr, "0") QRX("128", pr, "256") QRX("132", pr, "64") QRX("136", pr, "320") QRX("140", pr, "128") QRX("144", pr, "384") QRX("148", pr, "192") QRX("152", pr, "448"));
    else QKV_ASM(MFQ, QS2(124, 0) QS2(128, 16) QS2(132, 32) QS2(136, 48));
    const f32x4 cs1 = ldsr<f32x4>(e.ca ^ 2048);
#pragma unroll
    for (int n = 0; n < 2; ++n) {
        const f32x4 cs = n ? cs1 : cs0;
#pragma unroll
        for (int q = 0; q < 2; ++q) {
            u32x4 o;
#pragma unroll
            for (int i = 0; i < 4; ++i) {
                const int k = 2 * q + (i >> 1), j = 2 * (i & 1), a = 16 * n + 4 * k + j;
                o[i] = ISV ? pk2(A[a] * S[8 * k + j] + S[8 * k + 4 + j] * cs[0], A[a + 1] * S[8 * k + j + 1] + S[8 * k + 5 + j] * cs[0])
                           : pk2(A[a] * S[4 * k] + S[4 * k + 2] * cs[j], A[a + 1] * S[4 * k + 1] + S[4 * k + 3] * cs[j + 1]);
            }
            asm volatile("buffer_store_dwordx4 %0, %1, %2, %3 offen offset:%4 sc1\n\ts_nop 1" :: "v"(o), "v"(e.so[n]), "s"(rws), "s"(e.sb), "n"(q * (ISV ? 1280 : 256)) : "memory");
        }
    }
    asm volatile("s_waitcnt vmcnt(4)" ::: "memory");
}

DI f32x16 qk_tile(u32x4 km, u32x2 kt, u32x4 qm, u32x2 qt, f32x16 c) {
    c = mfma32(asbf(km), asbf(qm), c);
    return __builtin_amdgcn_mfma_f32_32x32x8bf16_1k(__builtin_bit_cast(s4v, kt), __builtin_bit_cast(s4v, qt), c, 0, 0, 0);
}
DI float max16(const f32x16& s, float m) {
#pragma unroll
    for (int r = 0; r < 16; r += 2) m = __builtin_fmaxf(__builtin_fmaxf(s[r], s[r + 1]), m);
    return m;
}
DI f32x16 pv16(u32x4 v, f32x8 pr, f32x16 o) {
    return __builtin_amdgcn_smfmac_f32_32x32x32_bf16(asbf(v), __builtin_bit_cast(bf16v, pr), o, 0xDDDD, 0, 0);
}
#define EX4(P, S) "v_exp_f32 v[" #P "], v[" S "]\n\tv_exp_f32 v[" #P "+1], v[" S "+1]\n\tv_exp_f32 v[" #P "+2], v[" S "+2]\n\tv_exp_f32 v[" #P "+3], v[" S "+3]\n\t"
#define SMF(O, V, P) "v_smfmac_f32_32x32x32_bf16 v[" #O ":" #O "+15], v[" #V ":" #V "+3], v[" #P ":" #P "+7], %[idx]\n\t"
#define QKT(SN, AB) "v_mfma_f32_32x32x4_2b_bf16 v[" #SN ":" #SN "+31], v[156:157], v[152:153], 0 cbsz:1 abid:" #AB "\n\t"
#define QKA(SN) "v_mfma_f32_32x32x16_bf16 v[" #SN ":" #SN "+15], v[160:163], v[144:147], v[" #SN ":" #SN "+15]\n\t"
#define QKB(SN) "v_mfma_f32_32x32x16_bf16 v[" #SN "+16:" #SN "+31], v[160:163], v[148:151], v[" #SN "+16:" #SN "+31]\n\t"
#define STEP_(SC, VP, VC, L0, L1, L2, L3, L4) \
    L0 EX4(128, #SC) EX4(132, #SC "+4") SMF(112, VP, 136) EX4(136, #SC "+16") "s_waitcnt lgkmcnt(0)\n\t" SMF(96, 164, 128) EX4(140, #SC "+20") \
    L1 EX4(128, #SC "+8") SMF(112, 164, 136) L2 EX4(132, #SC "+12") L3 EX4(136, #SC "+24") SMF(96, VC, 128) L4 EX4(140, #SC "+28")
#define STEP(SC, SN, AB, VP, VC, KTRD, VRD, KRD, M1, M2) STEP_(SC, VP, VC, QKT(SN, AB) KTRD, QKA(SN) M1, VRD, QKB(SN) KRD, M2)
#define VR(VH, O) "s_mov_b64 exec, s[56:57]\n\tds_read_b128 v[164:167], %[av] offset:" O "\n\tds_read_b128 v[" #VH ":" #VH "+3], %[av] offset:" O "+640\n\ts_mov_b64 exec, -1\n\t"
#define KR(O) "ds_read_b64 v[160:161], %[a0] offset:" O "\n\tds_read_b64 v[162:163], %[a0] offset:" O "+512\n\t"
#define KT(O) "ds_read_b64 v[156:157], %[at] offset:" O "\n\t"
#define PRIO "s_cbranch_vccz 1f\n\ts_setprio 1\n1:\n\t"
#define PVC "s_mov_b32 vcc_lo, %[grp]\n\ts_mov_b32 vcc_hi, 0\n\t"
#define QUAD3(X, N, A0, B0, A1, B1, A2, B2) \
    STEP(32, 64, 1, 172, 168, KT(#X "+5120"), VR(172, #X "+1280"), KR(#X "+5120"), A0, B0) \
    STEP(64, 32, 0, 168, 172, "", VR(168, #X "+5120"), KR(#X "+5376"), A1, B1) \
    STEP(32, 64, 1, 172, 168, KT(#N), VR(172, #X "+6400"), KR(#N), A2, B2)
#define QUAD(X, N, A0, B0, A1, B1, A2, B2, A3, B3) QUAD3(X, N, A0, B0, A1, B1, A2, B2) STEP(64, 32, 0, 168, 172, "", VR(168, #N), KR(#N "+256"), A3, B3)
#define QUADP(X, N, A0, B0, A1, B1, A2, B2, A3, B3) PRIO QUAD(X, N, A0, B0, A1, B1, A2, B2 "s_setprio 0\n\t", A3, B3)
#define KVP_(D) "buffer_load_dwordx4 %[l16], s[40:43], s44 offen sc1 lds\n\ts_add_u32 m0, m0, " #D "\n\ts_add_u32 s44, s44, 4096\n\t"
#define KVP KVP_(4096)
#define LDQ(R, V, O) "buffer_load_dwordx2 v[" #R ":" #R "+1], %[" #V "], s[40:43], %[sq] offen offset:" #O " sc1\n\t"
#define STW(R, S) "buffer_store_dwordx4 v[" #R ":" #R "+3], %[vs], s[40:43], s" #S " offen sc1\n\t"
#define STM(V, S, O) "buffer_store_dwordx4 v[192:195], %[" #V "], s[40:43], " S " offen offset:" #O " sc1\n\t"
#define WO4 "s_cbranch_vccnz 5f\n\t" DM4("%[l16]", "s48") "s_add_u32 m0, m0, 4096\n\ts_add_u32 s48, s48, 4096\n5:\n\t"
#define PK(R, O) LD1(#R, "%[l16]", "s52", #O)
#define PKA "s_add_u32 s52, s52, 4096\n\t"
#define TCH(O) "buffer_load_dword v154, %[vt], s[40:43], 0 offen offset:" #O " sc1\n\t"
#define LDQ5 LDQ(144, va, 0) LDQ(146, va, 512) LDQ(148, vb, 0) LDQ(150, vb, 512) LDQ(152, vc, 2048)
#define RD1(W, V, O) "ds_read_b128 v[" W ":" W "+3], " V " offset:" O "\n\t"
#define VM_(N) "s_waitcnt vmcnt(" #N ")\n\t"
#define VM(N) VM_(N)
DI unsigned mod5(unsigned v) { return (0x21043210u >> (4 * v)) & 7; }
constexpr unsigned L_CS = 52224, L_FL = L_CS + 512;
#define FSET(V, A, O) "ds_write_b32 " A ", " V " offset:" #O "\n\t"
#define FGET(V, A, O) "ds_read_b32 " V ", " A " offset:" #O "\n\t"
#define FTST(V, C, L) "v_cmp_ne_u32 vcc, -1, " V "\n\ts_sub_u32 " C ", " C ", 1\n\ts_cselect_b64 vcc, 0, vcc\n\ts_cbranch_vccnz " L "\n\t"
struct Park { u32x32 a, b; u32x8 c; unsigned d; };

DI void phase_attn(const Params& p, const Retile& rt, rsrc_t rws, u32x2 tag, unsigned fq, int b, int w, int lane, int nt80, int ntg,
                   unsigned rv0, unsigned rv1, unsigned rsb, u32x4& r0, u32x2& r1, Park& pk) {
    const int t = lane & 31, hh = lane >> 5, wl = w & 3, grp = w >> 2, kh = w & 1;
    const int head = (b & 7) * 4 + (b >> 6), qb = (b >> 3) & 7;
    const unsigned gb = kh * 61440, kvb = WS_KV + head * 163840;
    pk.a = retile_read(rt, b, w, lane);
    const unsigned l16 = lane * 16;
    unsigned va = hh * 1024 + grp * 256 + t * 8 + kh * 2560, dm = gb + (w >> 1) * 1024, sq = WS_Q + head * 81920 + (qb * 4 + (w & 2)) * 2560;
    const int n0 = (head * 20 + ((lane & 16) ? 19 : 0)) >> 7;
    unsigned fo = WS_FQ + (w == 0 ? (5 + 5 * hh + n0) * 16 + (lane & 15) : n0 * 16 + 2 * qb + (lane & 1)) * 128;
    unsigned skv = kvb + kh * 81920 + (w >> 1) * 1024;
    u32x8 qm; u32x2 qt;
    asm volatile("s_cmp_eq_u32 %[w], 2\n\ts_waitcnt vmcnt(0) lgkmcnt(0)\n\ts_barrier\n\ts_cbranch_scc0 1f\n\tbuffer_store_dwordx2 %[tag], %[fq], %[rs], 0 offen sc1\n1:"
                 : "+{v[176:207]}"(pk.a), "+v"(fo), "+v"(va), "+s"(skv), "+s"(sq), "+s"(dm) : [w] "s"(w), [tag] "v"(tag), [fq] "v"(fq), [rs] "s"(rws) : "memory", "scc");
    SBAR();
    const unsigned a0 = gb + hh * 1024 + t * 8, at = gb + lane * 8 + 2048, av = gb + 2560 + hh * 320 + (t < 20 ? t : 0) * 16;
    const unsigned ones = t == 20 ? 0x3f803f80u : 0u;
    const unsigned wob = WS_WO + nt80 * 102400;
    u32x8 sa;
    sa[0] = skv;
    sa[2] = WS_O + b * 10240 + w * 1280;
    sa[1] = WS_B + qb * QBS + (head * 8 + w) * 1408;
    sa[3] = WS_B + qb * QBS + OFF_F + (head * 8 + w) * 3456;
    const unsigned mv = l16 + (lane < 16 ? sa[2] + 1024 : lane < 40 ? sa[1] + 768 : sa[3] + 2432);
    const unsigned ro = nt80 & 3, pb = WS_WGU + ntg * 143360 + (grp ? 68 + 5 * wl : 17 * wl) * 1024;
    sa[4] = wob + mod5(ro + wl + (wl != 0)) * 20480; sa[5] = ((0x4176u >> (4 * wl)) & 7) * 20480;
    sa[6] = pb + (grp ? 20 + 8 * wl : 5) * 1024; sa[7] = L_FL + w * 16;
    u32x2 sb = {pb, rsb};
    const unsigned vt = lane < 17 ? WS_WD + nt80 * 276480 + qb * 34560 + (w * 17 + lane) * 256 : OOB;
    const unsigned vs = b < 135 ? l16 ^ (((lane >> 1) ^ (lane >> 3)) & 1) * 0xa0 : b * 8 + w < 1820 ? l16 : OOB;
    const unsigned vfs = (w == 2 && lane == 0) ? WS_FR + b * 8 : OOB, vfe = (b == 0 && w == 0 && lane == 0) ? WS_E : OOB, vfp = WS_FR + w * 256 + t * 8;
    const unsigned fg = (b * 205) >> 10;
    const unsigned vcs = (b < 135 && w < 2) ? WS_CS + ((b - fg * 5) * 3584 + fg * 128 + w * 64 + lane) * 4 : OOB;
    const unsigned ma = 40960 + (w & 2) * 30720 + grp * 6144 + l16;
    unsigned ov = (t >> 4) * 20480 + (t & 15) * 16, ow = hh ? OOB : ov;
    unsigned ob = WS_O + (qb * 16 + wl * 4 + grp * 2) * 20480, oc = ob + (64 + (head >> 1)) * 256 + (head & 1) * 8;
    ov += hh * 256; ob += head * 512;
    f32x16 O, P;
    asm volatile(
        "ds_read2st64_b32 v[196:197], %[cp] offset1:2\n\tds_read2st64_b32 v[198:199], %[cp] offset0:4 offset1:6\n\t"
        Z8("96") Z8("104") Z8("112") Z8("120") Z8("136") "v_mov_b32 v164, %[one]\n\tv_mov_b32 v165, %[one]\n\tv_mov_b64 v[166:167], v[164:165]\n\tv_mov_b64 v[168:169], v[164:165]\n\t"
        "v_mov_b64 v[170:171], v[164:165]\n\tv_mov_b64 v[172:173], v[164:165]\n\tv_mov_b64 v[174:175], v[164:165]\n\tv_mov_b64 v[192:193], -1\n\tv_mov_b64 v[194:195], -1\n\t"
        "s_mov_b32 s56, 0xfffff\n\ts_mov_b32 s57, s56\n\ts_mov_b32 s58, " SPINS "\n\ts_mov_b32 m0, %[dm]\n\ts_cmp_gt_u32 %[w], 1\n\t" "s_cbranch_scc1 2f\n"
        "1:\n\tbuffer_load_dwordx2 v[32:33], %[fo], s[40:43], 0 offen sc1\n\ts_waitcnt vmcnt(0)\n\tv_cmp_ne_u64 vcc, v[32:33], %[tag]\n\t"
        "s_cbranch_vccz 2f\n\ts_sub_u32 s58, s58, 1\n\ts_cbranch_scc0 1b\n"
        "2:\n\ts_barrier\n\t" KVP LDQ5 KVP "s_barrier\n\t" KVP KVP
        LGK(0) "v_add_f32 v196, v196, v197\n\tv_add_f32 v198, v198, v199\n\tv_add_f32 v196, v196, v198\n\ts_mov_b32 s58, " SPINS "\n\t"
        "s_waitcnt vmcnt(2)\n\ts_barrier\n\t"
        KT("0") KR("0") VR(168, "0") "s_waitcnt lgkmcnt(4)\n\t" QKT(32, 0) "s_waitcnt lgkmcnt(2)\n\ts_nop 7\n\ts_nop 7\n\ts_nop 2\n\t" QKA(32) QKB(32) KR("256")
        PVC "s_waitcnt vmcnt(0)\n\ts_barrier\n\ts_nop 6\n\t"
        QUADP(0, 10240, KVP, KVP STW(176, 68), KVP, STW(180, 69) "buffer_store_dword v196, %[vcs], s[40:43], 0 offen sc1\n\t", KVP, STW(184, 70), KVP, STW(188, 71))
        "s_waitcnt vmcnt(8)\n\ts_barrier\n\t"
        QUADP(10240, 20480, KVP, STM(l16, "s46", 0), KVP, STM(mv, "0", 0), KVP, "", KVP, "")
        "s_waitcnt vmcnt(2)\n\ts_barrier\n\tbuffer_store_dwordx2 %[tag], %[vfs], s[40:43], 0 offen sc1\n\t"
        QUADP(20480, 30720, KVP, STM(l16, "s45", 0), KVP_(-57344), KVP, KVP, STM(l16, "s47", 0), KVP, KVP)
        QUADP(30720, 40960, KVP, "buffer_load_dwordx4 v[248:251], %[rv0], %[rx], s53 offen\n\tbuffer_load_dwordx2 v[252:253], %[rv1], %[rx], s53 offen\n\t", "buffer_load_dwordx2 v[198:199], %[vfp], s[40:43], 0 offen sc1\n\t", STM(l16, "s47", 1024), "", STM(l16, "s47", 2048), "", "")
        "s_mov_b32 m0, s49\n\ts_waitcnt vmcnt(2)\n"
        "3:\n\tv_cmp_ne_u64 vcc, v[198:199], %[tag]\n\ts_cbranch_vccz 4f\n\t"
        "buffer_load_dwordx2 v[198:199], %[vfp], s[40:43], 0 offen sc1\n\ts_sub_u32 s58, s58, 1\n\ts_waitcnt vmcnt(0)\n\ts_cbranch_scc0 3b\n"
        "4:\n\t" PVC "s_barrier\n\t" "buffer_store_dwordx2 %[tag], %[vfe], s[40:43], 0 offen sc1\n\t"

        QUADP(40960, 51200, PK(176, 0), PK(180, 1024), PK(184, 2048) TCH(0), PK(188, 3072) PKA, PK(192, 0) "s_mov_b32 s52, s50\n\t" PK(196, 0), PK(200, 1024), PK(204, 2048), PK(208, 3072) PKA)
        QUAD(51200, 0, PK(212, 0), PK(216, 1024), PK(220, 2048) TCH(128), PK(224, 3072) PKA, PK(228, 0) PK(232, 1024), PK(236, 2048), PK(240, 3072) PKA, PK(244, 0))
        "s_cbranch_vccz 5f\n\ts_waitcnt vmcnt(20)\n\ts_barrier\n\t" "5:\n\t"
        QUAD(0, 10240, WO4, "", WO4, "", WO4, "", WO4, "")
        QUAD3(10240, 0, WO4, "", "", "", "", "")
        STEP_(64, 168, 172, "", "v_mov_b32 v45, s51\n\t", "v_mov_b32 v41, 0\n\t", "v_mov_b64 v[42:43], 0\n\t", "v_mov_b32 v44, -1\n\t")
        "v_xor_b32 v46, 16, v45\n\ts_waitcnt vmcnt(0)\n\ts_setprio 1\n\t" SMF(112, 172, 136) "s_cbranch_vccnz 5f\n\ts_barrier\n"
        "5:\n\ts_mov_b32 s58, " SPINS "\n\ts_nop 7\n\ts_nop 4\n\t"
        "ds_write_b128 %[e0], v[112:115]\n\tds_write_b128 %[e0], v[116:119] offset:1024\n\tds_write_b32 %[e0], v120 offset:2048\n\t"
        "s_mov_b32 exec_hi, 0\n\tds_write_b128 %[e0], v[120:123] offset:2048\n\t" FSET("v44", "v45", 4)
        "5:\n\t" FGET("v47", "v46", 4) RD1("40", "%[e1]", "2048") "s_mov_b32 exec_hi, -1\n\t" RD1("32", "%[e1]", "0") RD1("36", "%[e1]", "1024") "ds_read_b32 v40, %[e1] offset:2048\n\t"
        "s_mov_b32 exec_hi, 0\n\t" LGK(4) FTST("v47", "s58", "5b") "s_mov_b32 exec_hi, -1\n\t" LGK(0)
        : "=&{v[96:111]}"(O), "=&{v[32:47]}"(P), "=&{v[144:151]}"(qm), "=&{v[152:153]}"(qt), "+{v[176:207]}"(pk.a), "=&{v[208:239]}"(pk.b), "=&{v[240:247]}"(pk.c), "=&{v[248:251]}"(r0), "=&{v[252:253]}"(r1), "=&{v154}"(pk.d),
          "+{s[44:51]}"(sa), "+{s[52:53]}"(sb), "+v"(ov), "+v"(ow), "+s"(ob), "+s"(oc)
        : "{s[40:43]}"(rws), "{s[60:75]}"(rt.t), [a0] "v"(a0), [at] "v"(at), [av] "v"(av), [l16] "v"(l16), [tag] "v"(tag), [vcs] "v"(vcs), [fo] "v"(fo), [sq] "s"(sq),
          [cp] "v"(L_CP + (w & 1) * 256 + lane * 4), [mv] "v"(mv), [vs] "v"(vs), [vt] "v"(vt), [one] "v"(ones), [idx] "v"(0xDDDD),
          [va] "v"(va), [vb] "v"(va + 2560 - kh * 5120), [vc] "v"(grp * 256 + t * 8 + (hh ^ kh) * 2560), [e0] "v"(ma + kh * 3072), [e1] "v"(ma + 3072 - kh * 3072), [vfs] "v"(vfs), [vfe] "v"(vfe), [vfp] "v"(vfp), [rv0] "v"(rv0), [rv1] "v"(rv1),
          [dm] "s"(dm), [w] "s"(w), [grp] "s"(grp), [rx] "s"(mkrs(p.x, 2048 * 1280))
        : "memory", "scc", "vcc", "s56", "s57", "s58", VCL48, VCL64, VCL80, VCL112, VCL128, "v140", "v141", "v142", "v143", "v156", "v157", VCL160);
    float o[12];
#pragma unroll
    for (int i = 0; i < 12; ++i) o[i] = O[i] + P[i];
    {
        f32x2 z = {0.f, 0.f};
#pragma unroll
        for (int r = 0; r < 12; r += 2) z += f32x2{o[r], o[r + 1]} * 0.f;
        const float l = (hh ? o[8] : 1.f) + (z[0] + z[1]);
        const bool bad = !(l < 1.8446744e19f && l > 8.6736174e-19f);
        if (__builtin_expect(anyl(bad), 0)) {
            float m = -__builtin_inff();
            const unsigned ko = hh * 1024 + t * 8, vo = 2560 + hh * 320 + (t < 20 ? t : 0) * 16;
            const u32x4 qa = {qm[0], qm[1], qm[2], qm[3]}; const u32x2 qe = {hh ? 0u : qt[0], hh ? 0u : qt[1]};
            f32x16 c = {}, A = {};
#pragma unroll 1
            for (int it = 0; it < 128; ++it) {
                const unsigned sb = kvb + ((it & 63) >> 1) * 5120;
                const u32x2 k0 = ld8c(rws, ko, sb + (it & 1) * 256), k1 = ld8c(rws, ko + 512, sb + (it & 1) * 256), k2 = ld8c(rws, t * 8 + 2048, sb + (it & 1) * 256);
                f32x16 s = qk_tile(u32x4{k0[0], k0[1], k1[0], k1[1]}, k2, qa, qe, c);
                if (it < 64) {
                    m = max16(s, m);
                    if (it == 63) {
                        const f32x2 mm = swap32(m, m);
                        m = __builtin_fmaxf(mm[0], mm[1]);
#pragma unroll
                        for (int r = 0; r < 16; ++r) c[r] = -m;
                    }
                } else {
                    u32x4 v0 = ld16c(rws, vo, sb + (it & 1) * 1280), v1 = ld16c(rws, vo + 640, sb + (it & 1) * 1280);
#pragma unroll
                    for (int i = 0; i < 4; ++i) { v0[i] = t < 20 ? v0[i] : ones; v1[i] = t < 20 ? v1[i] : ones; }
#pragma unroll
                    for (int r = 0; r < 16; ++r) s[r] = __builtin_amdgcn_exp2f(s[r]);
                    A = pv16(v0, __builtin_shufflevector(s, s, 0, 1, 2, 3, 4, 5, 6, 7), A);
                    A = pv16(v1, __builtin_shufflevector(s, s, 8, 9, 10, 11, 12, 13, 14, 15), A);
                }
            }
#pragma unroll
            for (int r = 0; r < 12; ++r) o[r] = A[r];
        }
    }
    const float inv = __builtin_amdgcn_rcpf(swap32(o[8], o[8])[1]);
    st16(rws, ov, ob, u32x4{pk2(o[0] * inv, o[1] * inv), pk2(o[2] * inv, o[3] * inv), pk2(o[4] * inv, o[5] * inv), pk2(o[6] * inv, o[7] * inv)});
    st8(rws, ow, oc, u32x2{pk2(o[8] * inv, o[9] * inv), pk2(o[10] * inv, o[11] * inv)});
}

#define MF16(A, W, B) "v_mfma_f32_16x16x32_bf16 v[" A ":" A "+3], v[" W ":" W "+3], v[" B ":" B "+3], v[" A ":" A "+3]\n\t"
#define MF0(A, W, B) "v_mfma_f32_16x16x32_bf16 v[" A ":" A "+3], v[" W ":" W "+3], v[" B ":" B "+3], 0\n\t"
#define RD5(W, V, O) RD1(W, V, O) RD1(W "+4", V, O "+1024") RD1(W "+8", V, O "+2048") RD1(W "+12", V, O "+3072") RD1(W "+16", V, O "+4096")
#define WR1(V, R, O) "ds_write_b128 " V ", v[" R ":" R "+3] offset:" O "\n\t"
#define MX3(B) "v_max3_u32 %[t], v[" B "], v[" B "+1], v[" B "+2]\n\t"
#define MX2(B) "v_max3_u32 %[t], v[" B "], v[" B "+1], %[t]\n\t"
#define MXE(B) "v_max_u32 %[t], v[" B "], %[t]\n\tv_cmp_eq_u32 vcc, -1, %[t]\n\ts_and_b64 vcc, vcc, s[60:61]\n\t"
#define CK4(B) MX3(B) MXE(B "+3")
#define CK8(B) MX3(B) MX2(B "+3") MX2(B "+5") MXE(B "+7")
#define CK16(B) MX3(B) MX2(B "+3") MX2(B "+5") MX2(B "+7") MX2(B "+9") MX2(B "+11") MX2(B "+13") MXE(B "+15")
#define CK28(B) MX3(B) MX2(B "+3") MX2(B "+5") MX2(B "+7") MX2(B "+9") MX2(B "+11") MX2(B "+13") MX2(B "+15") MX2(B "+17") MX2(B "+19") MX2(B "+21") MX2(B "+23") MX2(B "+25") MXE(B "+27")
#define SPIN0 "s_mov_b32 s59, " SPINS "\n\ts_mov_b64 s[60:61], -1\n\t"
#define SPIN1 "s_sub_u32 s59, s59, 1\n\ts_cselect_b64 s[60:61], 0, s[60:61]\n\t"
#define UNP(D, S) "v_lshlrev_b32 v[" #D "], 16, v" #S "\n\tv_and_b32 v[" #D "+1], 0xffff0000, v" #S "\n\t"
#define PKADD(D, S) "v_pk_add_f32 v[" D ":" D "+1], v[" D ":" D "+1], v[" S ":" S "+1]\n\t"
#define CVT(D, S) "v_cvt_pk_bf16_f32 v[" D "], v[" S "], v[" S "+1]\n\t"
#define WON(n, W, B, R) MF16("72+4*" #n, W "+4*" #n, B) R
#define WOK(W, B, V, O) WON(0, W, B, RD1(W, V, O)) WON(1, W, B, RD1(W "+4", V, O "+1024")) WON(2, W, B, RD1(W "+8", V, O "+2048")) WON(3, W, B, RD1(W "+12", V, O "+3072")) WON(4, W, B, RD1(W "+16", V, O "+4096"))
#define WOE(W, B) WON(0, W, B, "") WON(1, W, B, "") WON(2, W, B, "") WON(3, W, B, "") WON(4, W, B, "")
#define WOL(B, S) LD1(#B, "%[l16]", #S, "0") LD1(#B "+4", "%[l16]", #S, "1024")
#define WOC(p, N, B) "1" #p ":\n\t" VM(N) CK8(#B) "s_cbranch_scc1 2" #p "f\n\t"
#define WOT(p) "2" #p ":\n\ts_call_b64 s[56:57], 8f\n\ts_branch 1" #p "b\n"
#define WOR(B, S) CK8(#B) "s_cbranch_scc0 1f\n\ts_mov_b64 exec, vcc\n\t" WOL(B, S) "s_mov_b64 exec, -1\n1:\n\t"
#define PW2(V, R, O) WR1(V, #R, #O) WR1(V, #R "+4", #O "+1024")
#define LD5(R) LD1(#R, "%[l16]", "s51", "0") LD1(#R "+4", "%[l16]", "s51", "1024") LD1(#R "+8", "%[l16]", "s51", "2048") LD1(#R "+12", "%[l16]", "s51", "3072") \
    "s_add_u32 s51, s51, 4096\n\t" LD1(#R "+16", "%[l16]", "s51", "0") "s_add_u32 s51, s51, 6144\n\t"
#define CSL(R) "buffer_load_dword " R ", %[ve], s[40:43], s49 offen sc1\n\ts_add_u32 s49, s49, 14336\n\t"
DI void phase_wo(rsrc_t rws, int w, unsigned lane, int mt, int nt, int ntg, u32x4& r0, u32x2& r1, Park& pk) {
    const unsigned tg = w >> 1, kh = w & 1, wl = w & 3, grp = w >> 2, t = lane & 15, g = lane >> 4, l16 = lane * 16, la = kh * 5120 + l16, e = (w ^ 4) * 64 + lane, ro = nt & 3;
    const unsigned ab = WS_O + (mt * 4 + tg) * 20480 + kh * 10240, hb = WS_B + (mt >> 2) * QBS + nt * 256, tk = (mt & 3) * 4 + tg;
    u32x8 sa = {ab + ro * 2048, ab + (ro + 1) * 2048, ab + mod5(ro + 2) * 2048, ab + mod5(ro + 3) * 2048, ab + mod5(ro + 4) * 2048, WS_CS + ntg * 448, hb + tk * 20480,
                WS_WO + nt * 102400 + (ro + 1) * 20480 + kh * 5120};
    const unsigned ma = 40960 + (w & 2) * 30720 + grp * 6144 + l16;
    const unsigned q0 = ((grp ? 0x87827d78u : 0x70423400u) >> (8 * wl)) & 255, q1 = ((grp ? 0x9950148cu : 0x0e4a3c08u) >> (8 * wl)) & 255, q2 = ((grp ? 0x21571b93u : 0x67642b28u) >> (8 * wl)) & 255;
    unsigned tmp, cs;
    asm volatile(
        CSL("v92") CSL("v93") CSL("v94") CSL("v95") CSL("%[cs]") LD5(136) LD5(156) SPIN0
        RD5("96", "%[wa]", "0") RD5("116", "%[wa]", "10240")
        UNP(72, 248) UNP(74, 249) UNP(76, 250) UNP(78, 251) Z8("80") UNP(88, 252) UNP(90, 253)
        "v_mov_b32 %[t], -1\n\ts_cmp_eq_u32 %[grp], 0\n\t" LGK(0) FSET("%[t]", "%[fw]", 140)
        VM(15) WOL(32, s44) WOL(40, s45) WOL(48, s46) WOL(56, s47) WOL(64, s48) "s_cbranch_scc1 2f\n"
        "1:\n\t" FGET("%[t]", "%[fs]", 132) LGK(0) FTST("%[t]", "s59", "1b") "v_mov_b32 %[t], -1\n\t"
        PW2("%[p0]", 176, 0) PW2("%[p0]", 184, 2048) WR1("%[p0]", "192", "4096") FSET("%[t]", "%[fw]", 132) "s_mov_b32 s59, " SPINS "\n\t"
        VM(10) "v_add_f32 v92, v92, v93\n\tv_add_f32 v94, v94, v95\n\tv_add_f32 v92, v92, %[cs]\n\tv_add_f32 %[cs], v92, v94\n\tds_write_b32 %[vl], %[cs]\n2:\n"
        WOC(0, 8, 32) WOK("96", "32", "%[wa]", "20480") WOK("116", "36", "%[wa]", "30720")
        WOC(1, 6, 40) WOK("136", "40", "%[wb]", "0") WOK("156", "44", "%[wb]", "10240")
        WOC(2, 4, 48) LGK(10) WOK("96", "48", "%[wc]", "0") WOK("116", "52", "%[wc]", "10240")
        WOC(3, 2, 56) LGK(10) WOE("136", "56") WOE("156", "60")
        WOC(4, 0, 64) LGK(0) WOE("96", "64") WOE("116", "68")
        "s_cmp_eq_u32 %[kh], 0\n\ts_mov_b32 s59, " SPINS "\n\tv_mov_b32 v32, -1\n\ts_nop 3\n\t" WR1("%[xw]", "80", "0") WR1("%[xw]", "84", "1024") "s_cbranch_scc1 1f\n\t" WR1("%[xw]", "88", "2048")
        "1:\n\t" FSET("v32", "%[fw]", 8)
        "1:\n\t" FGET("%[t]", "%[fp]", 8) RD1("80", "%[xr]", "0") RD1("84", "%[xr]", "1024") RD1("92", "%[xr]", "2048") LGK(3) FTST("%[t]", "s59", "1b") "s_cmp_eq_u32 %[kh], 0\n\t"
        LGK(2) PKADD("72", "80") PKADD("74", "82") LGK(1) PKADD("76", "84") PKADD("78", "86") CVT("248", "72") CVT("249", "74") CVT("250", "76") CVT("251", "78")
        "buffer_store_dwordx4 v[248:251], %[h0], s[40:43], s50 offen sc1\n\t" LGK(0) "s_cbranch_scc0 9f\n\t"
        PKADD("88", "92") PKADD("90", "94") CVT("252", "88") CVT("253", "90") "buffer_store_dwordx2 v[252:253], %[h1], s[40:43], s50 offen sc1\n\ts_branch 9f\n"
        WOT(0) WOT(1) WOT(2) WOT(3) WOT(4)
        "8:\n\t" VM(0) WOR(32, s44) WOR(40, s45) WOR(48, s46) WOR(56, s47) WOR(64, s48) VM(0) SPIN1 "s_setpc_b64 s[56:57]\n9:\n\t"
        FSET("v32", "%[fw]", 12)
        "1:\n\ts_sleep 1\n\t" FGET("%[t]", "%[fs]", 0) LGK(0) FTST("%[t]", "s59", "1b") "s_cmp_eq_u32 %[grp], 0\n\ts_cbranch_scc1 1f\n\t"
        PW2("%[p1]", 196, 0) PW2("%[p1]", 204, 2048) PW2("%[p1]", 212, 4096) WR1("%[p1]", "220", "6144") PW2("%[p2]", 224, 0) PW2("%[p2]", 232, 2048) PW2("%[p2]", 240, 4096)
        FSET("v32", "%[fw]", 136) "s_branch 2f\n1:\n\t"
        PW2("%[p0]", 176, 0) PW2("%[p0]", 184, 2048) PW2("%[p0]", 192, 4096) PW2("%[p0]", 200, 6144) PW2("%[p1]", 208, 0) PW2("%[p1]", 216, 2048) PW2("%[p1]", 224, 4096)
        PW2("%[p2]", 232, 0) WR1("%[p2]", "240", "2048") FSET("v32", "%[fw]", 132) "s_setprio 0\n2:"
        : "+{v[248:251]}"(r0), "+{v[252:253]}"(r1), "+{v[176:207]}"(pk.a), "+{v[208:239]}"(pk.b), "+{v[240:247]}"(pk.c), "+{s[44:51]}"(sa), [t] "=&v"(tmp), [cs] "=&v"(cs)
        : "{s[40:43]}"(rws), [l16] "v"(l16), [wa] "v"(la + 122880), [wb] "v"(la + 20480), [wc] "v"(la + 81920), [kh] "s"(kh), [grp] "s"(grp),
          [p0] "v"(q0 * 1024 + l16), [p1] "v"(q1 * 1024 + l16), [p2] "v"(q2 * 1024 + l16),
          [fw] "v"(L_FL + w * 16), [fp] "v"(L_FL + (w ^ 1) * 16), [fs] "v"(L_FL + (l16 & 112) + (grp ? 8 : (lane & 4) ? 4 : 12)),
          [ve] "v"(e < 112 ? (e ^ (((lane >> 1) ^ (lane >> 3)) & 1) * 10) * 4 : OOB), [vl] "v"(e < 112 ? L_CS + e * 4 : L_FL + 260),
          [xw] "v"(ma + 3072 - kh * 3072), [xr] "v"(ma + kh * 3072),
          [h0] "v"((2 * g + kh) * 2048 + t * 16), [h1] "v"((8 + (g >> 1)) * 2048 + t * 16 + (g & 1) * 8)
        : "memory", "scc", "vcc", "s56", "s57", "s59", "s60", "s61", VCL32, VCL48, VCL64, VCL80, VCL96, VCL112, VCL128, VCL140, VCL160);
    float s = 0.f, q = 0.f;
#pragma unroll
    for (int k = 0; k < 6; ++k) {
        const bf2v v = __builtin_bit_cast(bf2v, k < 4 ? r0[k & 3] : r1[k & 1]);
        s = __builtin_amdgcn_fdot2_f32_bf16(v, __builtin_bit_cast(bf2v, 0x3f803f80u), s, false);
        q = __builtin_amdgcn_fdot2_f32_bf16(v, v, q, false);
    }
    const f32x2 y = swap32(s, q);
    const float z = sum16(y[0] + y[1]);
    const f32x2 v = swap32(z, z);
    st8(rws, g == 0 ? t * 16 + kh * 8 : OOB, hb + OFF_HS + tk * 2048, u32x2{__float_as_uint(v[0]), __float_as_uint(v[1])});
}

#define GW0 LGK(.Lc-.Lr2)
#define GW1
#define GW2
#define GW3 LGK(.Lc-.Lr4)
#define GW4
#define GW5 LGK(.Lc-.Lr6)
#define GW6
#define GUN(MF, n, H, R, X) GW##n MF("140+8*" #n, "112+4*" #n, H) X MF("144+8*" #n, "112+4*" #n, H "+8") R
#define RDW_(LO, HI, KB) ".if (7*(\\k)+\\n >= " #LO ") && (7*(\\k)+\\n < " #HI ")\n.set .Lwa, (7*(\\k)+\\n-" #LO "+" #KB ")*1024\n.endif\n"
#define LC ".set .Lc, .Lc+1\n\t"
#define RDWDEF ".set .Lc, 0\n.set .Lhi, 0\n.macro RDW k, n, a0, a1\n" RDW_(0, 14, 0) RDW_(14, 17, 40) RDW_(17, 31, 52) RDW_(31, 34, 43) RDW_(34, 48, 66) RDW_(48, 51, 100) RDW_(51, 59, 112) \
    RDW_(59, 65, 14) RDW_(65, 68, 103) RDW_(68, 101, 120) RDW_(101, 114, 20) RDW_(114, 127, 80) RDW_(127, 134, 153) RDW_(134, 140, 33) \
    ".if (\\k) == 20\n.set .Lwa, 65536+52224+64*\\n\n.set .Lhi, 0\n.endif\n" \
    ".if (.Lwa >= 131072) && (.Lhi == 0)\nv_add_u32 \\a1, 0x8000, \\a1\n.set .Lhi, 32768\n.endif\n" \
    ".if (.Lwa >= 65536) && (.Lwa < 98304) && (.Lhi != 0)\nv_add_u32 \\a1, 0xffff8000, \\a1\n.set .Lhi, 0\n.endif\n" \
    ".if .Lwa < 65536\nds_read_b128 v[112+4*\\n:115+4*\\n], \\a0 offset:.Lwa\n.else\nds_read_b128 v[112+4*\\n:115+4*\\n], \\a1 offset:.Lwa-65536-.Lhi\n.endif\n" \
    LC ".set .Lr\\n, .Lc\n.endm\n\t"
#define RDW(K, n) "RDW " K ", " #n ", %[l16], %[wa]\n\t"
#define GUK(MF, H, K, X0, X1, X2, X3, X4, X5, X6) \
    GUN(MF, 0, H, RDW(K, 0), X0) GUN(MF, 1, H, RDW(K, 1), X1) GUN(MF, 2, H, RDW(K, 2), X2) GUN(MF, 3, H, RDW(K, 3), X3) \
    GUN(MF, 4, H, RDW(K, 4), X4) GUN(MF, 5, H, RDW(K, 5), X5) GUN(MF, 6, H, RDW(K, 6), X6)
#define LH0(H, S) LD1(#H, "%[l16]", #S, "0")
#define LH1(H, S) LD1(#H "+4", "%[l16]", #S, "1024")
#define LH2(H, S) LD1(#H "+8", "%[lb]", #S, "0")
#define LH3(H, S) LD1(#H "+12", "%[lb]", #S, "1024")
#define LDH(H, S) LH0(H, S) LH1(H, S) LH2(H, S) LH3(H, S)
#define LH(H, S) LH0(H, S), LH1(H, S), LH2(H, S), LH3(H, S)
#define LS(R, O) LD1(#R, "%[l16]", "s54", #O)
#define LDST LS(32, 0) LS(36, 1024) LS(40, 2048) LS(44, 3072)
#define WL(R, O) LD1(#R, "%[l16]", "s52", #O)
#define WA "s_add_u32 s52, s52, 4096\n\t"
#define W15 WL(196, 0) WL(200, 1024) WL(204, 2048) WL(208, 3072) WA WL(212, 0) WL(216, 1024) WL(220, 2048) WL(224, 3072) WA WL(228, 0) WL(232, 1024) WL(236, 2048) WL(240, 3072) WA \
    WL(244, 0) WL(24, 1024) WL(28, 2048)
#define FCK(L) "v_cmp_ne_u32 vcc, -1, v16\n\ts_cbranch_vccnz 5" #L "f\n6" #L ":\n\t"
#define FWT(L, A, O) "5" #L ":\n\t" FGET("v16", A, O) LGK(0) FTST("v16", "s58", "5" #L "b") "s_branch 6" #L "b\n"
#define WEND "v_add_u32 %[lb], 4096, %[l16]\n\t" WL(32, 3072) LD1("36", "%[lb]", "s52", "0") LD1("40", "%[lb]", "s52", "1024") LD1("44", "%[z4]", "s52", "2048") LD1("96", "%[z4]", "s52", "3072") WA \
    LD1("100", "%[z4]", "s52", "0") LD1("104", "%[z4]", "s52", "1024") LD1("108", "%[z4]", "s52", "2048")
#define CKS(N, B, s, Y1, Y2, Y3, Y4) VM(N) MX3(B), MX2(B "+3") Y1, MX2(B "+5") Y2, MX2(B "+7"), MX2(B "+9") Y3, MX2(B "+11") Y4, MX2(B "+13") MXE(B "+15") "s_cbranch_scc1 4" #s "f\n3" #s ":\n\t"
#define CKS_(...) CKS(__VA_ARGS__)
#define NO4 "", "", "", ""
#define UN(...) __VA_ARGS__
#define GUK_(...) GUK(__VA_ARGS__)
#define PR0(s) ".if " #s " == 0\ns_setprio 0\n.endif\n.if " #s " == 8\n" FSET("v21", "v22", 264) LC ".endif\n\t"
#define GUS(MF, s, H, PRE, HN, N, Y, ...) PR0(s) GUK_(MF, #H, "2*" #s "+1", __VA_ARGS__) PRE GUK_(MF16, #H "+4", "2*" #s "+2", CKS_(N, #HN, s, UN Y))
#define LJ(H, S) LH0(H, S), "", LH1(H, S), "", LH2(H, S), "", LH3(H, S)
#define REDO(s, L, CK) "4" #s ":\n\ts_mov_b64 exec, vcc\n\t" L "s_mov_b64 exec, -1\n\t" VM(0) SPIN1 CK "s_cbranch_scc1 4" #s "b\n\ts_branch 3" #s "b\n"
#define HNX(A, B) "s_add_u32 s" #B ", s" #A ", 2048\n\ts_cmp_eq_u32 s" #B ", s55\n\ts_cselect_b32 s" #B ", %[hb], s" #B "\n\t"
#define SW32(A, B) "v_permlane32_swap_b32 v[" A "], v[" B "]\n\t"
#define SW16(A, B) "v_permlane16_swap_b32 v[" A "], v[" B "]\n\t"
#define EPF(A, C, T) "v_pk_fma_f32 v[" A ":" A "+1], v[" C ":" C "+1], v[16+4*" #T ":17+4*" #T "], v[" A ":" A "+1]\n\t"
#define EPT(A, C, T, D) EPF(A, C, T) EPF(A "+2", C "+2", T) "v_max_f32 v[" A "], 0, v[" A "]\n\tv_max_f32 v[" A "+1], 0, v[" A "+1]\n\t" \
    "v_pk_mul_f32 v[" A "+2:" A "+3], v[" A "+2:" A "+3], v[18+4*" #T ":19+4*" #T "]\n\t" \
    "v_pk_mul_f32 v[" A ":" A "+1], v[" A ":" A "+1], v[" A "+2:" A "+3]\n\t" CVT(D, A)
#define EPN(n, D) EPT("140+8*" #n, "112+4*" #n, 0, D) EPT("144+8*" #n, "112+4*" #n, 1, D "+1")
#define EP2(n, m, D, N) LGK(N) EPN(n, D) EPN(m, D "+2") SW32(D, D "+1") "s_nop 0\n\t" SW32(D "+2", D "+3") \
    "buffer_store_dwordx4 v[" D ":" D "+3], %[f0], s[40:43], s53 offen sc1\n\ts_add_u32 s53, s53, 15872\n\t"
#define LF0(F, S) LD1(F, "%[l16]", S, "0")
#define LF1(F, S) LD1(F "+4", "%[l16]", S, "1024")
#define LF2(F, S) LD1(F "+8", "%[l16]", S, "2048")
#define LF3(F, S) LD1(F "+12", "%[l16]", S, "3072")
#define LF4(F, S) LD1(F "+16", "%[lb]", S, "0")
#define LF5(F, S) LD1(F "+20", "%[lb]", S, "1024")
#define LF6(F, S) LD1(F "+24", "%[z4]", S, "2048")
#define LDF(F, S) LF0(F, S) LF1(F, S) LF2(F, S) LF3(F, S) LF4(F, S) LF5(F, S) LF6(F, S)
#define DK(MF, F, a, b, c, d, e) MF("168", #a, F) MF("172", #b, F) MF("176", #c, F) MF("180", #d, F) MF("184", #e, F)
#define DT(F, X0, X1, X2, X3, X4, X5, X6) DK(MF0, F, 196, 200, 204, 208, 212) X0 DK(MF16, F "+4", 216, 220, 224, 228, 232) X1 DK(MF16, F "+8", 236, 240, 244, 24, 28) X2 \
    DK(MF16, F "+12", 48, 52, 56, 60, 64) X3 DK(MF16, F "+16", 68, 72, 76, 80, 84) X4 DK(MF16, F "+20", 88, 92, 32, 36, 40) X5 DK(MF16, F "+24", 44, 96, 100, 104, 108) X6
#define DTL(F, S) DT(F, LF0(F, S), LF1(F, S), LF2(F, S), LF3(F, S), LF4(F, S), LF5(F, S), LF6(F, S))
#define DTE(F) DT(F, "", "", "", "", "", "", "")
#define DW(S) "v_add_u32 %[wa], " S ", %[l16]\n\t" WR1("%[wa]", "168", "0") WR1("%[wa]", "172", "1024") WR1("%[wa]", "176", "2048") WR1("%[wa]", "180", "3072") WR1("%[wa]", "184", "4096")
#define DC(N, F, s) VM(N) CK28(F) "s_cbranch_scc1 4" #s "f\n3" #s ":\n\t"
#define LD0 LDH(32, s64) LDH(48, s65)
DI void phase_ffn(rsrc_t rws, int w, unsigned lane, int mt, int nt, int qg, int ntg, const Park& pk) {
    const unsigned t = lane & 15, g = lane >> 4, l16 = lane * 16, qbb = WS_B + qg * QBS, wg = (w & 3) * 2 + (w >> 2), hb = qbb + wg * 40960, k0 = 7 * w - (w == 7);
    const unsigned fb = WS_B + (mt >> 2) * QBS + OFF_F + (mt & 3) * 4 * 55296 + k0 * 1024, fv = (g >> 1) * 55296 + t * 16;
    u32x8 sa; u32x4 sb = {WS_WD + nt * 276480 + k0 * 5120, qbb + OFF_F + wg * 110592 + ntg * 256, qbb + OFF_HS + wg * 4096, hb + 20480};
#pragma unroll
    for (int i = 0; i < 4; ++i) { sa[i] = fb + i * 55296; sa[4 + i] = i * 40960 + w * 5120; }
    unsigned wa = 65536 + l16, h0 = hb + (((ntg & 15) * 5) >> 3) * 2048, tmp, lb = l16 + 20480;
    u32x32 da = pk.a, db = pk.b; u32x8 dc = pk.c;
    asm volatile(
        RDWDEF SPIN0 HNX(64, 65) HNX(65, 66) HNX(66, 67) HNX(67, 68) HNX(68, 69) HNX(69, 70) HNX(70, 71) HNX(71, 72) HNX(72, 73)
        "v_and_b32 v17, 0x70, %[l16]\n\tv_and_b32 v18, 0x30, %[l16]\n\tv_mov_b32 v21, -1\n\tv_mov_b32 v22, %[fw]\n\t"
        "s_mov_b32 s58, s59\n\ts_cmp_eq_u32 %[ng], 31\n\ts_cbranch_scc1 7f\n\t" VM(1) LD0
        "1:\n\t" FGET("v16", "v17", 52868) LGK(0) FTST("v16", "s58", "1b")
        RDW("0", 0) RDW("0", 1) RDW("0", 2) RDW("0", 3) RDW("0", 4) RDW("0", 5) RDW("0", 6)
        "1:\n\t" VM(4) CK16("32") "s_cbranch_scc1 2f\n\t"
        GUS(MF0, 0, 32, "", 48, 4, (LH(80, s67)), LJ(64, s66))
        GUS(MF16, 1, 48, "", 64, 8, (LH(32, s69)), LJ(96, s68))
        GUS(MF16, 2, 64, "", 80, 12, (WL(196, 0), WL(200, 1024), WL(204, 2048), ""), LJ(48, s70))
        GUS(MF16, 3, 80, "", 96, 15, (WL(208, 3072) WA, WL(212, 0), WL(216, 1024), ""), LJ(64, s71))
        GUS(MF16, 4, 96, "", 32, 18, (WL(220, 2048), WL(224, 3072) WA, WL(228, 0), ""), LJ(80, s72))
        GUS(MF16, 5, 32, FGET("v16", "v18", 52936) LC, 48, 21, (WL(232, 1024), WL(236, 2048), LGK(4) FCK(0), WL(240, 3072) WA), LJ(96, s73))
        GUS(MF16, 6, 48, "", 64, 21, (WL(244, 0), WL(24, 1024), WL(28, 2048), ""), LS(32, 0), "", LS(36, 1024), "", LS(40, 2048), "", LS(44, 3072))
        GUS(MF16, 7, 64, "", 80, 21, (NO4), WL(48, 3072) WA, "", WL(52, 0), "", WL(56, 1024), "", WL(60, 2048))
        GUS(MF16, 8, 80, "", 96, 18, (NO4), WL(64, 3072) WA, "", WL(68, 0), "", WL(72, 1024), "", WL(76, 2048))
        PR0(9) GUK_(MF16, "96", "19", CKS_(11, "32", 9, NO4))
        "v_lshrrev_b32 %[wa], 4, %[l16]\n\tv_and_b32 %[wa], 48, %[wa]\n\t"
        GUK(MF16, "100", "20",
            WL(80, 3072) WA PKADD("32", "34") PKADD("36", "38") PKADD("40", "42") PKADD("44", "46"),
            WL(84, 0) PKADD("32", "36") PKADD("40", "44"),
            WL(88, 1024) SW32("32", "40") SW32("33", "41") PKADD("32", "40") "v_mov_b64 v[34:35], v[32:33]\n\t",
            WL(92, 2048) SW16("32", "34") SW16("33", "35") PKADD("32", "34") "v_mul_f32 v16, 0xbacccccd, v32\n\t",
            "v_mul_f32 v33, 0x3acccccd, v33\n\tv_fma_f32 v33, -v16, v16, v33\n\tv_max_f32 v33, 0, v33\n\tv_add_f32 v33, 0x3727c5ac, v33\n\t",
            "v_rcp_f32 v18, v33\n\tv_mov_b32 v20, v16\n\tv_mov_b32 v22, v18\n\t",
            SW32("16", "20") SW32("18", "22") "v_mov_b32 v17, v16\n\tv_mov_b32 v21, v20\n\tv_mov_b32 v19, v18\n\tv_mov_b32 v23, v22\n\t")
        WEND EP2(0, 1, "140", 5) EP2(2, 3, "156", 3) EP2(4, 5, "172", 1) LGK(0) EPN(6, "188") "s_nop 1\n\t" SW32("188", "189")
        "buffer_store_dwordx2 v[188:189], %[f1], s[40:43], s53 offen sc1\n"
        "s_bitcmp0_b32 %[fw], 6\n\ts_cbranch_scc1 70f\n\ts_setprio 1\n6:\n\ts_barrier\n\t" LDF("112", "s44") LDF("140", "s45")
        DC(7, "112", 13)
        DTL("112", "s46") DC(7, "140", 10) DW("s48") DTL("140", "s47")
        "s_nop 5\n\t" DW("s49") "71:\n\t" DC(7, "112", 11) DTE("112") DC(0, "140", 12) DW("s50") DTE("140") "s_nop 6\n\t" DW("s51")
        "s_waitcnt lgkmcnt(0)\n\ts_barrier\n\ts_branch 9f\n"
        "2:\n\ts_mov_b64 exec, vcc\n\t" LD0 "s_mov_b64 exec, -1\n\t" SPIN1 "s_branch 1b\n"
        FWT(0, "v18", 52936)
        "70:\n\tv_mov_b32 v17, %[fw]\n1:\n\ts_sleep 2\n\t" FGET("v16", "v17", 328) LGK(0) FTST("v16", "s58", "1b") LDF("112", "s44") LDF("140", "s45")
        "s_barrier\n\t" DC(7, "112", 14) DTE("112") DC(0, "140", 15) DW("s48")
        DT("140", LF0("112", "s46"), LF1("112", "s46"), LF2("112", "s46"), LF3("112", "s46"), LF4("112", "s46"), LF5("112", "s46"), LF6("112", "s46")) LDF("140", "s47") DW("s49")
        "s_branch 71b\n"
        REDO(14, LDF("112", "s44"), CK28("112")) REDO(15, LDF("140", "s45"), CK28("140"))
        REDO(0, LDH(48, s65), CK16("48")) REDO(1, LDH(64, s66), CK16("64")) REDO(2, LDH(80, s67), CK16("80")) REDO(3, LDH(96, s68), CK16("96")) REDO(4, LDH(32, s69), CK16("32"))
        REDO(5, LDH(48, s70), CK16("48")) REDO(6, LDH(64, s71), CK16("64")) REDO(7, LDH(80, s72), CK16("80")) REDO(8, LDH(96, s73), CK16("96")) REDO(9, LDST, CK16("32"))
        REDO(10, LDF("140", "s45"), CK28("140")) REDO(11, LDF("112", "s46"), CK28("112")) REDO(12, LDF("140", "s47"), CK28("140"))
        REDO(13, LDF("112", "s44"), CK28("112"))
        "7:\n\ts_setprio 0\n\t" LGK(0) W15 WL(48, 3072) WA WL(52, 0) WL(56, 1024) WL(60, 2048) WL(64, 3072) WA WL(68, 0) WL(72, 1024) WL(76, 2048) WL(80, 3072) WA WL(84, 0) WL(88, 1024) WL(92, 2048) WEND
        "1:\n\t" "s_sleep 4\n\t" LF0("112", "s44") SPIN1 VM(0) CK4("112") "s_cbranch_scc1 1b\n\ts_branch 6b\n9:"
        : "+{v[176:207]}"(da), "+{v[208:239]}"(db), "+{v[240:247]}"(dc), "+{s[52:55]}"(sb), "+{s64}"(h0), [wa] "+&v"(wa), [t] "=&v"(tmp), [lb] "+&v"(lb)
        : "{s[40:43]}"(rws), "{s[44:51]}"(sa), [l16] "v"(l16), [z4] "v"(w < 6 ? l16 + 4096 : OOB),
          [f0] "v"(fv + (g & 1) * 7936), [f1] "v"(ntg < 30 ? fv + (g & 1) * 8 : OOB), [ng] "s"(ntg), [hb] "s"(hb), [fw] "s"(L_FL + w * 16)
        : "memory", "scc", "vcc", "s58", "s59", "s60", "s61", "s65", "s66", "s67", "s68", "s69", "s70", "s71", "s72", "s73", "v16", "v17", "v18", "v19", "v20", "v21", "v22", "v23",
          "v24", "v25", "v26", "v27", "v28", "v29", "v30", "v31", VCL32, VCL48, VCL64, VCL80, VCL96, VCL112, VCL128, VCL140, VCL160);
}
DI f32x4 sum8(unsigned a) {
    f32x4 v[8];
#pragma unroll
    for (int i = 0; i < 8; ++i) v[i] = ldsr<f32x4>(a + i * 5120);
    return ((v[0] + v[1]) + (v[2] + v[3])) + ((v[4] + v[5]) + (v[6] + v[7]));
}
DI u32x2 addres(f32x4 a, unsigned lo, unsigned hi) { return u32x2{pk2(a[0] + bflo(lo), a[1] + bfhi(lo)), pk2(a[2] + bflo(hi), a[3] + bfhi(hi))}; }

__global__ void __launch_bounds__(512, 2) block_kernel(Params) {
    u32x16 ka;
    asm volatile("s_load_dwordx16 %0, %1, 0" : "=s"(ka) : "s"(__builtin_amdgcn_kernarg_segment_ptr()) : "memory");
    __shared__ __attribute__((aligned(16))) char lds[163840];
    asm volatile("" :: "v"((LAS char*)lds) : "memory");
    const int lane = threadIdx.x & 63, w = __builtin_amdgcn_readfirstlane(threadIdx.x >> 6), b = blockIdx.x;
    unsigned pre;
    asm volatile("s_cmp_lg_u32 %1, %2\n\ts_cbranch_scc1 1f\n\ts_getpc_b64 s[4:5]\n.Lpc:\n\tv_lshlrev_b32 %0, 7, %3\n\tv_min_u32 %0, .Lend-.Lpc-4, %0\n\t"
                 "global_load_dword %0, %0, s[4:5]\n1:" : "=&{v200}"(pre) : "s"(w), "s"((b >> 3) & 7), "v"(threadIdx.x) : "memory", "scc", "s4", "s5");
    Params p;
    rsrc_t rws;
    const int xcd = b & 7, i = b >> 3;
    Retile rt;
    u32x2 tag;
    unsigned fq;
    {
        const int mt = (xcd >> 1) * 4 + (i & 3), nt = (xcd & 1) * 8 + (i >> 2);
        QkvSrc c; QkvAdr e; u32x4 row; unsigned rowt, T;
        qkv_start(c, row, rowt, T, rt.t, p, ka, b, mt, nt, w, lane);
        rws = mkrs(p.ws, WS_END);
        retile_setup(rt, p, b, lane);
        qkv_setup(e, mt, nt, w, lane);
        fq = lane == 0 ? WS_FQ + (nt * 16 + mt) * 128 : OOB;
        asm volatile("s_add_u32 m0, %[dm], 65536\n\ts_cmp_eq_u32 %[nt], 15\n\ts_cbranch_scc1 1f\n\t" QD4 "1:\n\ts_waitcnt vmcnt(12) lgkmcnt(0)\n\ts_add_u32 %0, %0, 1"
                     : "+s"(T), "+{s[60:75]}"(rt.t), "+v"(row), "+v"(rowt), "+v"(fq), "+{v200}"(pre), [sx] "+s"(c.sx), [sw] "+s"(c.sw)
                     : [x0] "v"(c.xo[0]), [x1] "v"(c.xo[1]), [w0] "v"(c.wo[0]), [w1] "v"(c.wo[1]), [rx] "s"(c.rx), [rw] "s"(c.rw), [dm] "s"(w * 4096), [nt] "s"(nt) : "memory", "scc");
        const u32x4 gr = row_stats(row, rowt, T);
        tag = u32x2{gr[2], gr[3]};
        if (nt == 15) {
            st16(rws, e.go, 0, gr);
            retile_issue(rt, w);
            asm volatile("s_waitcnt vmcnt(0)" ::: "memory");
        } else if (nt >= 10) qkv_body<true>(c, e, rt, rws, gr, tag, w);
        else qkv_body<false>(c, e, rt, rws, gr, tag, w);
    }
    const int mt = (xcd >> 1) * 8 + (i & 7), nt = (xcd & 1) * 4 + (i >> 3);
    const int qg = (xcd & 6) + ((i >> 2) & 1), ntg = (xcd & 1) * 16 + (i & 3) * 4 + (i >> 3);
    const int tg = w >> 1, kh = w & 1;
    auto rv0 = [&](unsigned l) { return (l & 15) * 1280 + (nt & 1) * 32 + kh * 64 + (l >> 4) * 16; };
    auto rv1 = [&](unsigned l) { return kh ? OOB : (l & 15) * 1280 + ((nt & 1) ? 0 : 128) + (l >> 4) * 8; };
    const unsigned rsb = (mt * 64 + tg * 16) * 1280 + nt * 160;
    u32x4 r0; u32x2 r1; Park pk;
    phase_attn(p, rt, rws, tag, fq, b, w, lane, nt, ntg, rv0(lane), rv1(lane), rsb, r0, r1, pk);
    unsigned ln;
    asm volatile("v_mbcnt_lo_u32_b32 %0, -1, 0\n\tv_mbcnt_hi_u32_b32 %0, -1, %0" : "=v"(ln) : "{v154}"(pk.d));
    phase_wo(rws, w, ln, mt, nt, ntg, r0, r1, pk);
    PIN(ln);
    unsigned o0 = rv0(ln), o1 = rv1(ln), a = tg * 40960 + ln * 16;
    PIN(o0); PIN(o1); PIN(a);
    phase_ffn(rws, w, ln, mt, nt, qg, ntg, pk);
    {
        const rsrc_t ro = mkrs(p.out, 2048 * 1280);
        const u32x2 u = addres(sum8(a + kh * 2048), r0[0], r0[1]), v = addres(sum8(a + kh * 2048 + 1024), r0[2], r0[3]);
        st16(ro, o0, rsb, u32x4{u[0], u[1], v[0], v[1]});
        if (kh == 0) st8(ro, o1, rsb, addres(sum8(a + 4096), r1[0], r1[1]));
    }
    asm volatile(".Lend:" ::: "memory");
}

extern "C" void kernel_launch(void* const* d_in, const int* in_sizes, int n_in, void* d_out, int out_size, void* d_ws, size_t ws_size, hipStream_t stream) {
    Params p;
    p.x = (const char*)d_in[0]; p.wqkv = (const char*)d_in[1]; p.wo = (const char*)d_in[2];
    p.wg = (const char*)d_in[4]; p.wu = (const char*)d_in[5]; p.wd = (const char*)d_in[6];
    p.out = (char*)d_out; p.ws = (char*)d_ws + ((ws_size - WS_END) & ~(size_t)2097151);
    block_kernel<<<256, 512, 0, stream>>>(p);
}
```
